```python
import torch

def _softmax(x, axis=-1):
    return torch.softmax(x, dim=axis)
def _gelu(x):
    return torch.nn.functional.gelu(x, approximate="tanh")
def _silu(x):
    return torch.nn.functional.silu(x)
def _sigmoid(x):
    return torch.sigmoid(x)
def _layernorm(x, axis=-1, eps=1e-5):
    m = x.mean(dim=axis, keepdim=True)
    v = x.var(dim=axis, keepdim=True, unbiased=False)
    return (x - m) / torch.sqrt(v + eps)
def _scatter_add(tgt, idx, src, axis=0):
    return tgt.clone().index_add_(axis, idx, src)
def _segment_sum(src, seg, n):
    out = torch.zeros((n,) + src.shape[1:], dtype=src.dtype)
    return out.index_add_(0, seg, src)
def _pad(x, pw):
    flat = tuple(v for pair in reversed(pw) for v in pair)
    return torch.nn.functional.pad(x, flat)

kernel_name = 'concat_heads__mha_block_cached__gqa_block_cached__near_ridge'

def setup_inputs():

    _w = (lambda a: (lambda t: t.to(torch.bfloat16) if t.is_floating_point() else t)(torch.as_tensor(a)))
    def _A_setup():
        B, Sq, D = 8, 144, 640
        H, Sk, Dh = 32, 1024, 128
        return {
            "x": _w((lambda *s: torch.randn(*s, dtype=torch.bfloat16) * 0.8)(B, Sq, D)),
            "k_cache": _w((lambda *s: torch.randn(*s, dtype=torch.bfloat16) * 0.8)(B, H, Sk, Dh)),
            "v_cache": _w((lambda *s: torch.randn(*s, dtype=torch.bfloat16) * 0.05)(B, H, Sk, Dh)),
            "wq": _w((lambda *s: torch.randn(*s, dtype=torch.bfloat16) * 0.8)(D, H * Dh) * (D ** -0.5)),
            "wo": _w((lambda *s: torch.randn(*s, dtype=torch.bfloat16) * 0.05)(H * Dh, D) * ((H * Dh) ** -0.5)),
        }
    _A_in = _A_setup()

    def _B_setup():
        B, Sq, D = 8, 144, 640
        Hq, Hkv, Sk, Dh = 32, 32, 1024, 128
        assert Hq % Hkv == 0, (Hq, Hkv)
        return {
            "x": _w((lambda *s: torch.randn(*s, dtype=torch.bfloat16) * 0.8)(B, Sq, D)),
            "k_cache": _w((lambda *s: torch.randn(*s, dtype=torch.bfloat16) * 0.8)(B, Hkv, Sk, Dh)),
            "v_cache": _w((lambda *s: torch.randn(*s, dtype=torch.bfloat16) * 0.05)(B, Hkv, Sk, Dh)),
            "wq": _w((lambda *s: torch.randn(*s, dtype=torch.bfloat16) * 0.8)(D, Hq * Dh) * (D ** -0.5)),
            "wo": _w((lambda *s: torch.randn(*s, dtype=torch.bfloat16) * 0.05)(Hq * Dh, D) * ((Hq * Dh) ** -0.5)),
        }
    _B_in = _B_setup()

    return {
        "A_x": _A_in["x"],
        "A_k_cache": _A_in["k_cache"],
        "A_v_cache": _A_in["v_cache"],
        "A_wq": _A_in["wq"],
        "A_wo": _A_in["wo"],
        "B_x": _B_in["x"],
        "B_k_cache": _B_in["k_cache"],
        "B_v_cache": _B_in["v_cache"],
        "B_wq": _B_in["wq"],
        "B_wo": _B_in["wo"],
    }

def reference(A_x, A_k_cache, A_v_cache, A_wq, A_wo, B_x, B_k_cache, B_v_cache, B_wq, B_wo):
    def _A_ref(x, k_cache, v_cache, wq, wo):
        B, Sq, D = x.shape
        H, Dh = 32, 128
        q = (lambda x, *d: x.permute(*d))((x @ wq).reshape(B, Sq, H, Dh), 0, 2, 1, 3)
        scores = torch.einsum("bhqd,bhkd->bhqk", q, k_cache) * (Dh ** -0.5)
        p = _softmax(scores, axis=-1)
        o = torch.einsum("bhqk,bhkd->bhqd", p, v_cache)
        return (lambda x, *d: x.permute(*d))(o, 0, 2, 1, 3).reshape(B, Sq, H * Dh) @ wo

    def _B_ref(x, k_cache, v_cache, wq, wo):
        B, Sq, D = x.shape
        Hq, Dh = 32, 128
        Hkv = k_cache.shape[1]
        rep = Hq // Hkv
        q = (lambda x, *d: x.permute(*d))((x @ wq).reshape(B, Sq, Hq, Dh), 0, 2, 1, 3)
        k = (lambda x, n, axis: torch.repeat_interleave(x, n, dim=axis))(k_cache, rep, axis=1)
        v = (lambda x, n, axis: torch.repeat_interleave(x, n, dim=axis))(v_cache, rep, axis=1)
        scores = torch.einsum("bhqd,bhkd->bhqk", q, k) * (Dh ** -0.5)
        p = _softmax(scores, axis=-1)
        o = torch.einsum("bhqk,bhkd->bhqd", p, v)
        return (lambda x, *d: x.permute(*d))(o, 0, 2, 1, 3).reshape(B, Sq, Hq * Dh) @ wo

    a = _A_ref(A_x, A_k_cache, A_v_cache, A_wq, A_wo)
    b = _B_ref(B_x, B_k_cache, B_v_cache, B_wq, B_wo)
    return torch.concatenate([a, b], axis=-1)
```

```cpp
#include <hip/hip_runtime.h>
#include <hip/hip_bf16.h>
typedef unsigned short u16;
__device__ __forceinline__ float bf2f(u16 x){ return __uint_as_float(((unsigned)x)<<16); }
__device__ __forceinline__ u16 f2bf(float f){ __hip_bfloat16 b = __float2bfloat16(f); return *(u16*)&b; }
__global__ void gemm_naive(const u16* A, const u16* B, u16* C, int M, int N, int K, int ldc){
  __shared__ float As[16][17], Bs[16][17];
  int tx=threadIdx.x&15, ty=threadIdx.x>>4; int row=blockIdx.y*16+ty, col=blockIdx.x*16+tx; float acc=0;
  for(int k0=0;k0<K;k0+=16){
    As[ty][tx]=bf2f(A[(size_t)row*K+k0+tx]); Bs[ty][tx]=bf2f(B[(size_t)(k0+ty)*N+col]);
    __syncthreads();
    for(int k=0;k<16;k++) acc+=As[ty][k]*Bs[k][tx];
    __syncthreads();
  }
  C[(size_t)row*ldc+col]=f2bf(acc);
}
__global__ void attn_naive(const u16* Q, const u16* Kc, const u16* Vc, u16* O){
  int row=blockIdx.x, h=blockIdx.y, b=blockIdx.z; int t=threadIdx.x;
  __shared__ float q[128], s[1024], red[128];
  q[t]=bf2f(Q[((size_t)(b*144+row))*4096+h*128+t]); __syncthreads();
  const u16* K=Kc+((size_t)(b*32+h))*1024*128; const u16* V=Vc+((size_t)(b*32+h))*1024*128;
  float mx=-1e30f;
  for(int k=t;k<1024;k+=128){ float a=0; for(int d=0;d<128;d++) a+=q[d]*bf2f(K[k*128+d]); a*=0.08838834764831845f; s[k]=a; mx=fmaxf(mx,a);}
  red[t]=mx; __syncthreads(); for(int o=64;o>0;o>>=1){ if(t<o) red[t]=fmaxf(red[t],red[t+o]); __syncthreads(); } mx=red[0]; __syncthreads();
  float sum=0; for(int k=t;k<1024;k+=128){ float e=__expf(s[k]-mx); s[k]=e; sum+=e; }
  red[t]=sum; __syncthreads(); for(int o=64;o>0;o>>=1){ if(t<o) red[t]+=red[t+o]; __syncthreads(); } sum=red[0];
  float acc=0; for(int k=0;k<1024;k++) acc+=s[k]*bf2f(V[k*128+t]);
  O[((size_t)(b*144+row))*4096+h*128+t]=f2bf(acc/sum);
}
extern "C" void kernel_launch(void* const* d_in, const int* in_sizes, int n_in, void* d_out, int out_size, void* d_ws, size_t ws_size, hipStream_t stream){
  u16* q=(u16*)d_ws; u16* o=q+1152*4096;
  for(int br=0;br<2;br++){
    const u16* x=(const u16*)d_in[br*5+0]; const u16* k=(const u16*)d_in[br*5+1]; const u16* v=(const u16*)d_in[br*5+2];
    const u16* wq=(const u16*)d_in[br*5+3]; const u16* wo=(const u16*)d_in[br*5+4];
    gemm_naive<<<dim3(4096/16,1152/16),256,0,stream>>>(x,wq,q,1152,4096,640,4096);
    attn_naive<<<dim3(144,32,8),128,0,stream>>>(q,k,v,o);
    gemm_naive<<<dim3(640/16,1152/16),256,0,stream>>>(o,wo,(u16*)d_out+br*640,1152,640,4096,1280);
  }
}
```

```cpp
#include <hip/hip_runtime.h>

struct Args { const void* p[12]; };

__global__ __launch_bounds__(512, 2) void fused_attn_block(Args a) {
  unsigned tid = threadIdx.x;
  asm volatile(
    "s_mov_b64 s[8:9], %1\n"
    "s_mov_b32 s10, %2\n"
    "v_mov_b32 v1, %0\n"
    "s_and_b32 s60, s10, 7\n"
    "s_lshr_b32 s89, s60, 2\n"
    "s_mul_i32 s61, s89, 40\n"
    "s_add_u32 s62, s8, s61\n"
    "s_addc_u32 s63, s9, 0\n"
    "s_load_dwordx8 s[76:83], s[62:63], 0x0\n"
    "s_load_dwordx2 s[92:93], s[62:63], 0x20\n"
    "s_load_dwordx4 s[84:87], s[8:9], 0x50\n"
    "v_lshrrev_b32 v2, 6, v1\n"
    "s_nop 0\n"
    "v_readfirstlane_b32 s11, v2\n"
    "v_and_b32 v1, 63, v1\n"
    "s_and_b32 s90, s10, 3\n"
    "s_lshr_b32 s61, s10, 3\n"
    "s_lshr_b32 s13, s61, 2\n"
    "s_and_b32 s64, s61, 3\n"
    "s_lshl_b32 s14, s90, 2\n"
    "s_add_u32 s14, s14, s64\n"
    "s_lshl_b32 s14, s14, 1\n"
    "s_and_b32 s12, s11, 3\n"
    "s_lshl_b32 s73, s12, 11\n"
    "s_and_b32 s88, s12, 1\n"
    "s_mul_i32 s65, s13, 5\n"
    "s_mul_i32 s64, s64, 10\n"
    "s_add_u32 s65, s65, s64\n"
    "s_lshr_b32 s72, s65, 2\n"
    "s_sub_u32 s67, s72, 10\n"
    "s_cselect_b32 s72, s72, s67\n"
    "s_mul_i32 s64, s61, 52\n"
    "s_lshr_b32 s91, s64, 8\n"
    "s_mul_i32 s65, s91, 5\n"
    "s_sub_u32 s99, s61, s65\n"
    "v_lshrrev_b32 v249, 2, v1\n"
    "v_and_b32 v249, 7, v249\n"
    "v_lshrrev_b32 v248, 5, v1\n"
    "v_lshlrev_b32 v248, 6, v248\n"
    "v_and_b32 v252, 3, v1\n"
    "v_lshrrev_b32 v251, 4, v1\n"
    "v_and_b32 v251, 1, v251\n"
    "v_add_u32 v251, 0, v251\n"
    "v_xor_b32 v252, v252, v251\n"
    "v_lshlrev_b32 v252, 4, v252\n"
    "v_add_u32 v248, v248, v252\n"
    "v_lshl_add_u32 v89, v249, 13, v248\n"
    "s_lshl_b32 s60, s12, 17\n"
    "v_add_u32 v89, s60, v89\n"
    "v_add_u32 v88, 0x380, v89\n"
    "v_lshrrev_b32 v248, 5, v1\n"
    "v_lshlrev_b32 v248, 6, v248\n"
    "v_and_b32 v252, 3, v1\n"
    "v_lshrrev_b32 v251, 4, v1\n"
    "v_and_b32 v251, 1, v251\n"
    "v_add_u32 v251, 2, v251\n"
    "v_xor_b32 v252, v252, v251\n"
    "v_lshlrev_b32 v252, 4, v252\n"
    "v_add_u32 v248, v248, v252\n"
    "v_lshl_add_u32 v91, v249, 13, v248\n"
    "s_add_u32 s60, s60, 0xf800\n"
    "v_add_u32 v91, s60, v91\n"
    "v_add_u32 v90, 0x380, v91\n"
    "s_mul_i32 s60, s12, 0xa000\n"
    "v_lshrrev_b32 v94, 3, v1\n"
    "v_mul_u32_u24 v94, 0x500, v94\n"
    "v_and_b32 v252, 7, v1\n"
    "v_lshrrev_b32 v251, 4, v1\n"
    "v_add_u32 v251, 0, v251\n"
    "v_xor_b32 v252, v252, v251\n"
    "v_lshlrev_b32 v252, 4, v252\n"
    "v_add_u32 v94, v94, v252\n"
    "v_lshrrev_b32 v95, 3, v1\n"
    "v_mul_u32_u24 v95, 0x500, v95\n"
    "v_and_b32 v252, 7, v1\n"
    "v_lshrrev_b32 v251, 4, v1\n"
    "v_add_u32 v251, 4, v251\n"
    "v_xor_b32 v252, v252, v251\n"
    "v_lshlrev_b32 v252, 4, v252\n"
    "v_add_u32 v95, v95, v252\n"
    "s_cmp_eq_u32 s88, 1\n"
    "s_cselect_b64 vcc, -1, 0\n"
    "v_cndmask_b32 v96, v94, v95, vcc\n"
    "v_add_u32 v92, s60, v94\n"
    "s_add_u32 s60, s60, 0x2400\n"
    "v_add_u32 v93, s60, v95\n"
    "v_add_u32 v94, 0x4800, v92\n"
    "v_add_u32 v95, 0x4800, v93\n"
    "s_mul_i32 s60, s12, 0x2800\n"
    "s_add_u32 s60, s60, 0x28000\n"
    "v_add_u32 v96, s60, v96\n"
    "s_lshl_b32 s96, s72, 7\n"
    "s_lshl_b32 s97, s72, 19\n"
    "s_lshl_b32 s98, s12, 12\n"
    "s_lshl_b32 s72, s12, 10\n"
    "s_add_u32 s72, s72, 0x4000\n"
    "s_lshl_b32 s60, s88, 1\n"
    "v_lshrrev_b32 v248, 5, v1\n"
    "v_lshlrev_b32 v248, 6, v248\n"
    "v_and_b32 v252, 3, v1\n"
    "v_lshrrev_b32 v251, 4, v1\n"
    "v_and_b32 v251, 1, v251\n"
    "v_add_u32 v251, s60, v251\n"
    "v_xor_b32 v252, v252, v251\n"
    "v_lshlrev_b32 v252, 4, v252\n"
    "v_add_u32 v248, v248, v252\n"
    "v_lshl_add_u32 v3, v249, 8, v248\n"
    "v_add_u32 v2, 0x380, v3\n"
    "v_lshrrev_b32 v248, 5, v1\n"
    "v_lshlrev_b32 v248, 6, v248\n"
    "v_and_b32 v252, 3, v1\n"
    "v_xor_b32 v252, s60, v252\n"
    "v_lshlrev_b32 v252, 4, v252\n"
    "v_add_u32 v248, v248, v252\n"
    "v_lshl_add_u32 v8, v249, 8, v248\n"
    "v_add_u32 v7, 0x380, v8\n"
    "s_mov_b32 s15, 0\n"
    "s_mul_i32 s64, s13, 0x2d000\n"
    "s_lshl_b32 s65, s14, 8\n"
    "s_waitcnt lgkmcnt(0)\n"
    "s_add_u32 s28, s82, s65\n"
    "s_addc_u32 s29, s83, 0\n"
    "s_sub_u32 s28, s28, 0x380\n"
    "s_subb_u32 s29, s29, 0\n"
    "s_and_b32 s29, s29, 0xffff\n"
    "s_mov_b32 s30, 0\n"
    "s_mov_b32 s31, 0x20000\n"
    "s_sub_u32 s30, 0x500380, s65\n"
    "s_add_u32 s24, s76, s64\n"
    "s_addc_u32 s25, s77, 0\n"
    "s_and_b32 s25, s25, 0xffff\n"
    "s_mov_b32 s26, 0x2d000\n"
    "s_mov_b32 s27, 0x20000\n"
    "s_cmp_ge_u32 s11, 4\n"
    "s_cbranch_scc1 .LkH_1\n"
    "s_add_u32 s62, s98, 0x16000\n"
    "s_add_u32 m0, s62, 0x4800\n"
    "s_nop 0\n"
    "buffer_load_dwordx4 v88, s[28:31], s97 offen lds\n"
    "buffer_load_dwordx4 v89, s[28:31], s97 offen offset:1024 lds\n"
    "buffer_load_dwordx4 v90, s[28:31], s97 offen offset:2048 lds\n"
    "buffer_load_dwordx4 v91, s[28:31], s97 offen offset:3072 lds\n"
    "s_mov_b32 m0, s62\n"
    "s_add_u32 s62, s72, 0x16000\n"
    "buffer_load_dwordx4 v92, s[24:27], s96 offen lds\n"
    "buffer_load_dwordx4 v93, s[24:27], s96 offen offset:1024 lds\n"
    "buffer_load_dwordx4 v94, s[24:27], s96 offen offset:2048 lds\n"
    "buffer_load_dwordx4 v95, s[24:27], s96 offen offset:3072 lds\n"
    "s_mov_b32 m0, s62\n"
    "s_cmp_ge_u32 s12, 2\n"
    "s_cbranch_scc1 .Lkx5_3\n"
    "buffer_load_dwordx4 v96, s[24:27], s96 offen lds\n"
    ".Lkx5_3:\n"
    "s_add_u32 s96, s96, 0x80\n"
    "s_cmp_lg_u32 s96, 0x500\n"
    "s_cselect_b32 s96, s96, 0\n"
    "s_lshl_b32 s97, s96, 12\n"
    "s_load_dwordx2 s[68:69], s[86:87], 0x0\n"
    "s_lshl_b32 s64, s13, 5\n"
    "s_add_u32 s64, s64, s14\n"
    "s_lshl_b32 s64, s64, 18\n"
    "s_add_u32 s16, s78, s64\n"
    "s_addc_u32 s17, s79, 0\n"
    "s_sub_u32 s16, s16, 0x380\n"
    "s_subb_u32 s17, s17, 0\n"
    "s_and_b32 s17, s17, 0xffff\n"
    "s_mov_b32 s18, 0x80380\n"
    "s_mov_b32 s19, 0x20000\n"
    "s_add_u32 s20, s80, s64\n"
    "s_addc_u32 s21, s81, 0\n"
    "s_sub_u32 s20, s20, 0x380\n"
    "s_subb_u32 s21, s21, 0\n"
    "s_and_b32 s21, s21, 0xffff\n"
    "s_mov_b32 s22, 0x80380\n"
    "s_mov_b32 s23, 0x20000\n"
    "s_lshl_b32 s64, s99, 8\n"
    "s_add_u32 s32, s92, s64\n"
    "s_addc_u32 s33, s93, 0\n"
    "s_sub_u32 s32, s32, 0x400\n"
    "s_subb_u32 s33, s33, 0\n"
    "s_and_b32 s33, s33, 0xffff\n"
    "s_mov_b32 s34, 0\n"
    "s_mov_b32 s35, 0x20000\n"
    "s_sub_u32 s34, 0x500400, s64\n"
    "s_mul_i32 s64, s89, 0x900000\n"
    "s_add_u32 s64, s64, 0x10000\n"
    "s_add_u32 s36, s86, s64\n"
    "s_addc_u32 s37, s87, 0\n"
    "s_and_b32 s37, s37, 0xffff\n"
    "s_mov_b32 s38, 0x900000\n"
    "s_mov_b32 s39, 0x20000\n"
    "s_mov_b32 s40, s84\n"
    "s_and_b32 s41, s85, 0xffff\n"
    "s_mov_b32 s42, 0x2d0000\n"
    "s_mov_b32 s43, 0x20000\n"
    "s_mov_b32 s44, s86\n"
    "s_and_b32 s45, s87, 0xffff\n"
    "s_mov_b32 s46, 0x10000\n"
    "s_mov_b32 s47, 0x20000\n"
    "s_getpc_b64 s[48:49]\n"
    ".Lkhere_4:\n"
    "s_and_b32 s49, s49, 0xffff\n"
    "s_mov_b32 s50, .Lkhotend_2-.Lkhere_4\n"
    "s_mov_b32 s51, 0x20000\n"
    "s_lshr_b32 s64, s10, 3\n"
    "s_and_b32 s64, s64, 15\n"
    "s_lshl2_add_u32 s64, s64, s12\n"
    "s_lshl_b32 s64, s64, 10\n"
    "v_lshlrev_b32 v244, 4, v1\n"
    "buffer_load_dwordx4 v[232:235], v244, s[48:51], s64 offen\n"
    "s_add_u32 s62, s98, 0x1e800\n"
    "s_add_u32 m0, s62, 0x4800\n"
    "s_nop 0\n"
    "buffer_load_dwordx4 v88, s[28:31], s97 offen lds\n"
    "buffer_load_dwordx4 v89, s[28:31], s97 offen offset:1024 lds\n"
    "buffer_load_dwordx4 v90, s[28:31], s97 offen offset:2048 lds\n"
    "buffer_load_dwordx4 v91, s[28:31], s97 offen offset:3072 lds\n"
    "s_mov_b32 m0, s62\n"
    "s_add_u32 s62, s72, 0x1e800\n"
    "buffer_load_dwordx4 v92, s[24:27], s96 offen lds\n"
    "buffer_load_dwordx4 v93, s[24:27], s96 offen offset:1024 lds\n"
    "buffer_load_dwordx4 v94, s[24:27], s96 offen offset:2048 lds\n"
    "buffer_load_dwordx4 v95, s[24:27], s96 offen offset:3072 lds\n"
    "s_mov_b32 m0, s62\n"
    "s_cmp_ge_u32 s12, 2\n"
    "s_cbranch_scc1 .Lkx5_5\n"
    "buffer_load_dwordx4 v96, s[24:27], s96 offen lds\n"
    ".Lkx5_5:\n"
    "s_add_u32 s96, s96, 0x80\n"
    "s_cmp_lg_u32 s96, 0x500\n"
    "s_cselect_b32 s96, s96, 0\n"
    "s_lshl_b32 s97, s96, 12\n"
    "v_lshrrev_b32 v120, 2, v1\n"
    "v_and_b32 v120, 3, v120\n"
    "v_lshlrev_b32 v120, 11, v120\n"
    "v_and_b32 v252, 3, v1\n"
    "v_add_u32 v252, 0, v252\n"
    "v_lshlrev_b32 v252, 6, v252\n"
    "v_add_u32 v120, v120, v252\n"
    "v_lshrrev_b32 v252, 4, v1\n"
    "v_lshrrev_b32 v251, 2, v1\n"
    "v_and_b32 v251, 1, v251\n"
    "v_lshlrev_b32 v251, 1, v251\n"
    "v_add_u32 v251, 0, v251\n"
    "v_xor_b32 v252, v252, v251\n"
    "v_lshlrev_b32 v252, 4, v252\n"
    "v_add_u32 v120, v120, v252\n"
    "v_lshrrev_b32 v122, 4, v1\n"
    "v_lshlrev_b32 v122, 11, v122\n"
    "v_lshrrev_b32 v252, 2, v1\n"
    "v_and_b32 v252, 3, v252\n"
    "v_lshlrev_b32 v252, 6, v252\n"
    "v_add_u32 v122, v122, v252\n"
    "v_lshrrev_b32 v252, 4, v1\n"
    "v_and_b32 v252, 1, v252\n"
    "v_xor_b32 v252, 0, v252\n"
    "v_lshlrev_b32 v252, 5, v252\n"
    "v_add_u32 v122, v122, v252\n"
    "v_and_b32 v252, 3, v1\n"
    "v_lshlrev_b32 v252, 3, v252\n"
    "v_add_u32 v122, v122, v252\n"
    "v_lshrrev_b32 v121, 2, v1\n"
    "v_and_b32 v121, 3, v121\n"
    "v_lshlrev_b32 v121, 11, v121\n"
    "v_and_b32 v252, 3, v1\n"
    "v_add_u32 v252, 4, v252\n"
    "v_lshlrev_b32 v252, 6, v252\n"
    "v_add_u32 v121, v121, v252\n"
    "v_lshrrev_b32 v252, 4, v1\n"
    "v_lshrrev_b32 v251, 2, v1\n"
    "v_and_b32 v251, 1, v251\n"
    "v_lshlrev_b32 v251, 1, v251\n"
    "v_add_u32 v251, 1, v251\n"
    "v_xor_b32 v252, v252, v251\n"
    "v_lshlrev_b32 v252, 4, v252\n"
    "v_add_u32 v121, v121, v252\n"
    "v_lshrrev_b32 v123, 4, v1\n"
    "v_lshlrev_b32 v123, 11, v123\n"
    "v_lshrrev_b32 v252, 2, v1\n"
    "v_and_b32 v252, 3, v252\n"
    "v_lshlrev_b32 v252, 6, v252\n"
    "v_add_u32 v123, v123, v252\n"
    "v_lshrrev_b32 v252, 4, v1\n"
    "v_and_b32 v252, 1, v252\n"
    "v_xor_b32 v252, 1, v252\n"
    "v_lshlrev_b32 v252, 5, v252\n"
    "v_add_u32 v123, v123, v252\n"
    "v_and_b32 v252, 3, v1\n"
    "v_lshlrev_b32 v252, 3, v252\n"
    "v_add_u32 v123, v123, v252\n"
    "v_lshlrev_b32 v6, 4, v1\n"
    "v_mov_b32 v208, 0\n"
    "v_mov_b32 v209, 0\n"
    "v_mov_b32 v210, 0\n"
    "v_mov_b32 v211, 0\n"
    "s_nop 1\n"
    "v_mfma_f32_32x32x16_bf16 v[16:31], v[208:211], v[208:211], 0\n"
    "v_mfma_f32_32x32x16_bf16 v[32:47], v[208:211], v[208:211], 0\n"
    "v_mfma_f32_32x32x16_bf16 v[48:63], v[208:211], v[208:211], 0\n"
    "v_mfma_f32_32x32x16_bf16 v[64:79], v[208:211], v[208:211], 0\n"
    "v_mov_b32 v228, 0\n"
    "v_mov_b32 v229, 0\n"
    "s_mov_b32 s70, 0x44800000\n"
    "s_mov_b32 s52, 0\n"
    "s_mov_b32 s53, 0x2000\n"
    "s_mov_b32 s54, 0x4000\n"
    "s_mov_b32 s55, 0x6000\n"
    "s_mul_i32 s65, s13, 9\n"
    "s_lshl1_add_u32 s65, s12, s65\n"
    "s_lshl_b32 s65, s65, 5\n"
    "s_add_u32 s65, s65, s14\n"
    "s_lshl_b32 s84, s65, 12\n"
    "s_add_u32 s85, s84, 0x20000\n"
    "s_lshl_b32 s65, s12, 13\n"
    "s_add_u32 s65, s65, 0x1e800\n"
    "v_lshrrev_b32 v11, 3, v1\n"
    "v_and_b32 v11, 1, v11\n"
    "v_lshlrev_b32 v11, 11, v11\n"
    "v_and_b32 v252, 7, v1\n"
    "v_lshlrev_b32 v252, 6, v252\n"
    "v_add_u32 v11, v11, v252\n"
    "v_lshrrev_b32 v252, 4, v1\n"
    "v_lshrrev_b32 v251, 2, v1\n"
    "v_and_b32 v251, 3, v251\n"
    "v_xor_b32 v252, v252, v251\n"
    "v_lshlrev_b32 v252, 4, v252\n"
    "v_add_u32 v11, v11, v252\n"
    "v_add_u32 v11, s65, v11\n"
    "v_add_u32 v124, s52, v120\n"
    "v_add_u32 v125, s52, v121\n"
    "v_add_u32 v224, s53, v120\n"
    "v_add_u32 v225, s53, v121\n"
    "v_add_u32 v126, s54, v122\n"
    "v_add_u32 v127, s54, v123\n"
    "v_add_u32 v226, s55, v122\n"
    "v_add_u32 v227, s55, v123\n"
    "v_add_u32 v246, s54, v120\n"
    "v_add_u32 v247, s54, v121\n"
    "s_waitcnt vmcnt(9)\n"
    "s_barrier\n"
    "s_add_u32 s62, s98, 0xc000\n"
    "s_add_u32 m0, s62, 0x4800\n"
    "s_nop 0\n"
    "buffer_load_dwordx4 v88, s[28:31], s97 offen lds\n"
    "buffer_load_dwordx4 v89, s[28:31], s97 offen offset:1024 lds\n"
    "buffer_load_dwordx4 v90, s[28:31], s97 offen offset:2048 lds\n"
    "buffer_load_dwordx4 v91, s[28:31], s97 offen offset:3072 lds\n"
    "s_mov_b32 m0, s62\n"
    "s_add_u32 s62, s72, 0xc000\n"
    "buffer_load_dwordx4 v92, s[24:27], s96 offen lds\n"
    "buffer_load_dwordx4 v93, s[24:27], s96 offen offset:1024 lds\n"
    "buffer_load_dwordx4 v94, s[24:27], s96 offen offset:2048 lds\n"
    "buffer_load_dwordx4 v95, s[24:27], s96 offen offset:3072 lds\n"
    "s_mov_b32 m0, s62\n"
    "s_cmp_ge_u32 s12, 2\n"
    "s_cbranch_scc1 .Lkx5_6\n"
    "buffer_load_dwordx4 v96, s[24:27], s96 offen lds\n"
    ".Lkx5_6:\n"
    "s_add_u32 s96, s96, 0x80\n"
    "s_cmp_lg_u32 s96, 0x500\n"
    "s_cselect_b32 s96, s96, 0\n"
    "s_lshl_b32 s97, s96, 12\n"
    "s_waitcnt vmcnt(8)\n"
    "s_barrier\n"
    "s_add_u32 s62, s98, 0x16000\n"
    "s_add_u32 m0, s62, 0x4800\n"
    "s_nop 0\n"
    "buffer_load_dwordx4 v88, s[28:31], s97 offen lds\n"
    "buffer_load_dwordx4 v89, s[28:31], s97 offen offset:1024 lds\n"
    "buffer_load_dwordx4 v90, s[28:31], s97 offen offset:2048 lds\n"
    "buffer_load_dwordx4 v91, s[28:31], s97 offen offset:3072 lds\n"
    "s_mov_b32 m0, s62\n"
    "s_add_u32 s62, s72, 0x16000\n"
    "buffer_load_dwordx4 v92, s[24:27], s96 offen lds\n"
    "buffer_load_dwordx4 v93, s[24:27], s96 offen offset:1024 lds\n"
    "buffer_load_dwordx4 v94, s[24:27], s96 offen offset:2048 lds\n"
    "buffer_load_dwordx4 v95, s[24:27], s96 offen offset:3072 lds\n"
    "s_mov_b32 m0, s62\n"
    "s_cmp_ge_u32 s12, 2\n"
    "s_cbranch_scc1 .Lkx5_7\n"
    "buffer_load_dwordx4 v96, s[24:27], s96 offen lds\n"
    ".Lkx5_7:\n"
    "s_add_u32 s96, s96, 0x80\n"
    "s_cmp_lg_u32 s96, 0x500\n"
    "s_cselect_b32 s96, s96, 0\n"
    "s_lshl_b32 s97, s96, 12\n"
    "s_waitcnt vmcnt(8)\n"
    "s_barrier\n"
    "s_add_u32 s62, s98, 0x1e800\n"
    "s_add_u32 m0, s62, 0x4800\n"
    "s_nop 0\n"
    "buffer_load_dwordx4 v88, s[28:31], s97 offen lds\n"
    "buffer_load_dwordx4 v89, s[28:31], s97 offen offset:1024 lds\n"
    "buffer_load_dwordx4 v90, s[28:31], s97 offen offset:2048 lds\n"
    "buffer_load_dwordx4 v91, s[28:31], s97 offen offset:3072 lds\n"
    "s_mov_b32 m0, s62\n"
    "s_add_u32 s62, s72, 0x1e800\n"
    "buffer_load_dwordx4 v92, s[24:27], s96 offen lds\n"
    "buffer_load_dwordx4 v93, s[24:27], s96 offen offset:1024 lds\n"
    "buffer_load_dwordx4 v94, s[24:27], s96 offen offset:2048 lds\n"
    "buffer_load_dwordx4 v95, s[24:27], s96 offen offset:3072 lds\n"
    "s_mov_b32 m0, s62\n"
    "s_cmp_ge_u32 s12, 2\n"
    "s_cbranch_scc1 .Lkx5_8\n"
    "buffer_load_dwordx4 v96, s[24:27], s96 offen lds\n"
    ".Lkx5_8:\n"
    "s_add_u32 s96, s96, 0x80\n"
    "s_cmp_lg_u32 s96, 0x500\n"
    "s_cselect_b32 s96, s96, 0\n"
    "s_lshl_b32 s97, s96, 12\n"
    "s_waitcnt vmcnt(8)\n"
    "s_barrier\n"
    "s_add_u32 s62, s98, 0xc000\n"
    "s_add_u32 m0, s62, 0x4800\n"
    "s_nop 0\n"
    "buffer_load_dwordx4 v88, s[28:31], s97 offen lds\n"
    "buffer_load_dwordx4 v89, s[28:31], s97 offen offset:1024 lds\n"
    "buffer_load_dwordx4 v90, s[28:31], s97 offen offset:2048 lds\n"
    "buffer_load_dwordx4 v91, s[28:31], s97 offen offset:3072 lds\n"
    "s_mov_b32 m0, s62\n"
    "s_add_u32 s62, s72, 0xc000\n"
    "buffer_load_dwordx4 v92, s[24:27], s96 offen lds\n"
    "buffer_load_dwordx4 v93, s[24:27], s96 offen offset:1024 lds\n"
    "buffer_load_dwordx4 v94, s[24:27], s96 offen offset:2048 lds\n"
    "buffer_load_dwordx4 v95, s[24:27], s96 offen offset:3072 lds\n"
    "s_mov_b32 m0, s62\n"
    "s_cmp_ge_u32 s12, 2\n"
    "s_cbranch_scc1 .Lkx5_9\n"
    "buffer_load_dwordx4 v96, s[24:27], s96 offen lds\n"
    ".Lkx5_9:\n"
    "s_add_u32 s96, s96, 0x80\n"
    "s_cmp_lg_u32 s96, 0x500\n"
    "s_cselect_b32 s96, s96, 0\n"
    "s_lshl_b32 s97, s96, 12\n"
    "s_waitcnt vmcnt(8)\n"
    "s_barrier\n"
    "s_add_u32 s62, s98, 0x16000\n"
    "s_add_u32 m0, s62, 0x4800\n"
    "s_nop 0\n"
    "buffer_load_dwordx4 v88, s[28:31], s97 offen lds\n"
    "buffer_load_dwordx4 v89, s[28:31], s97 offen offset:1024 lds\n"
    "buffer_load_dwordx4 v90, s[28:31], s97 offen offset:2048 lds\n"
    "buffer_load_dwordx4 v91, s[28:31], s97 offen offset:3072 lds\n"
    "s_mov_b32 m0, s62\n"
    "s_add_u32 s62, s72, 0x16000\n"
    "buffer_load_dwordx4 v92, s[24:27], s96 offen lds\n"
    "buffer_load_dwordx4 v93, s[24:27], s96 offen offset:1024 lds\n"
    "buffer_load_dwordx4 v94, s[24:27], s96 offen offset:2048 lds\n"
    "buffer_load_dwordx4 v95, s[24:27], s96 offen offset:3072 lds\n"
    "s_mov_b32 m0, s62\n"
    "s_cmp_ge_u32 s12, 2\n"
    "s_cbranch_scc1 .Lkx5_10\n"
    "buffer_load_dwordx4 v96, s[24:27], s96 offen lds\n"
    ".Lkx5_10:\n"
    "s_add_u32 s96, s96, 0x80\n"
    "s_cmp_lg_u32 s96, 0x500\n"
    "s_cselect_b32 s96, s96, 0\n"
    "s_lshl_b32 s97, s96, 12\n"
    "s_waitcnt vmcnt(8)\n"
    "s_barrier\n"
    "s_add_u32 s62, s98, 0x1e800\n"
    "s_add_u32 m0, s62, 0x4800\n"
    "s_nop 0\n"
    "buffer_load_dwordx4 v88, s[28:31], s97 offen lds\n"
    "buffer_load_dwordx4 v89, s[28:31], s97 offen offset:1024 lds\n"
    "buffer_load_dwordx4 v90, s[28:31], s97 offen offset:2048 lds\n"
    "buffer_load_dwordx4 v91, s[28:31], s97 offen offset:3072 lds\n"
    "s_mov_b32 m0, s62\n"
    "s_add_u32 s62, s72, 0x1e800\n"
    "buffer_load_dwordx4 v92, s[24:27], s96 offen lds\n"
    "buffer_load_dwordx4 v93, s[24:27], s96 offen offset:1024 lds\n"
    "buffer_load_dwordx4 v94, s[24:27], s96 offen offset:2048 lds\n"
    "buffer_load_dwordx4 v95, s[24:27], s96 offen offset:3072 lds\n"
    "s_mov_b32 m0, s62\n"
    "s_cmp_ge_u32 s12, 2\n"
    "s_cbranch_scc1 .Lkx5_11\n"
    "buffer_load_dwordx4 v96, s[24:27], s96 offen lds\n"
    ".Lkx5_11:\n"
    "s_add_u32 s96, s96, 0x80\n"
    "s_cmp_lg_u32 s96, 0x500\n"
    "s_cselect_b32 s96, s96, 0\n"
    "s_lshl_b32 s97, s96, 12\n"
    "s_add_u32 m0, s73, 0\n"
    "s_add_u32 s64, s73, 0x2000\n"
    "buffer_load_dwordx4 v2, s[16:19], s64 offen nt lds\n"
    "buffer_load_dwordx4 v3, s[16:19], s64 offen offset:1024 nt lds\n"
    "s_add_u32 m0, s73, 0x2000\n"
    "s_add_u32 s64, s73, 0x4000\n"
    "buffer_load_dwordx4 v2, s[16:19], s64 offen nt lds\n"
    "buffer_load_dwordx4 v3, s[16:19], s64 offen offset:1024 nt lds\n"
    "s_waitcnt vmcnt(12)\n"
    "s_barrier\n"
    "s_add_u32 s62, s98, 0xc000\n"
    "s_add_u32 m0, s62, 0x4800\n"
    "s_nop 0\n"
    "buffer_load_dwordx4 v88, s[28:31], s97 offen lds\n"
    "buffer_load_dwordx4 v89, s[28:31], s97 offen offset:1024 lds\n"
    "buffer_load_dwordx4 v90, s[28:31], s97 offen offset:2048 lds\n"
    "buffer_load_dwordx4 v91, s[28:31], s97 offen offset:3072 lds\n"
    "s_mov_b32 m0, s62\n"
    "s_add_u32 s62, s72, 0xc000\n"
    "buffer_load_dwordx4 v92, s[24:27], s96 offen lds\n"
    "buffer_load_dwordx4 v93, s[24:27], s96 offen offset:1024 lds\n"
    "buffer_load_dwordx4 v94, s[24:27], s96 offen offset:2048 lds\n"
    "buffer_load_dwordx4 v95, s[24:27], s96 offen offset:3072 lds\n"
    "s_mov_b32 m0, s62\n"
    "s_cmp_ge_u32 s12, 2\n"
    "s_cbranch_scc1 .Lkx5_12\n"
    "buffer_load_dwordx4 v96, s[24:27], s96 offen lds\n"
    ".Lkx5_12:\n"
    "s_add_u32 s96, s96, 0x80\n"
    "s_cmp_lg_u32 s96, 0x500\n"
    "s_cselect_b32 s96, s96, 0\n"
    "s_lshl_b32 s97, s96, 12\n"
    "s_add_u32 m0, s73, 0x4000\n"
    "s_add_u32 s64, s73, 0\n"
    "buffer_load_dwordx4 v2, s[16:19], s64 offen nt lds\n"
    "buffer_load_dwordx4 v3, s[16:19], s64 offen offset:1024 nt lds\n"
    "s_add_u32 m0, s73, 0x6000\n"
    "s_add_u32 s64, s73, 0\n"
    "buffer_load_dwordx4 v7, s[20:23], s64 offen nt lds\n"
    "buffer_load_dwordx4 v8, s[20:23], s64 offen offset:1024 nt lds\n"
    "s_waitcnt vmcnt(16)\n"
    "s_barrier\n"
    "s_add_u32 s62, s98, 0x16000\n"
    "s_add_u32 m0, s62, 0x4800\n"
    "s_nop 0\n"
    "buffer_load_dwordx4 v88, s[28:31], s97 offen lds\n"
    "buffer_load_dwordx4 v89, s[28:31], s97 offen offset:1024 lds\n"
    "buffer_load_dwordx4 v90, s[28:31], s97 offen offset:2048 lds\n"
    "buffer_load_dwordx4 v91, s[28:31], s97 offen offset:3072 lds\n"
    "s_mov_b32 m0, s62\n"
    "s_add_u32 s62, s72, 0x16000\n"
    "buffer_load_dwordx4 v92, s[24:27], s96 offen lds\n"
    "buffer_load_dwordx4 v93, s[24:27], s96 offen offset:1024 lds\n"
    "buffer_load_dwordx4 v94, s[24:27], s96 offen offset:2048 lds\n"
    "buffer_load_dwordx4 v95, s[24:27], s96 offen offset:3072 lds\n"
    "s_mov_b32 m0, s62\n"
    "s_cmp_ge_u32 s12, 2\n"
    "s_cbranch_scc1 .Lkx5_13\n"
    "buffer_load_dwordx4 v96, s[24:27], s96 offen lds\n"
    ".Lkx5_13:\n"
    "s_add_u32 s96, s96, 0x80\n"
    "s_cmp_lg_u32 s96, 0x500\n"
    "s_cselect_b32 s96, s96, 0\n"
    "s_lshl_b32 s97, s96, 12\n"
    "s_add_u32 m0, s73, 0x8000\n"
    "s_add_u32 s64, s73, 0x6000\n"
    "buffer_load_dwordx4 v2, s[16:19], s64 offen nt lds\n"
    "buffer_load_dwordx4 v3, s[16:19], s64 offen offset:1024 nt lds\n"
    "s_add_u32 m0, s73, 0xa000\n"
    "s_add_u32 s64, s73, 0x8000\n"
    "buffer_load_dwordx4 v2, s[16:19], s64 offen nt lds\n"
    "buffer_load_dwordx4 v3, s[16:19], s64 offen offset:1024 nt lds\n"
    "s_waitcnt vmcnt(16)\n"
    "s_barrier\n"
    "s_waitcnt vmcnt(4)\n"
    "s_barrier\n"
    "ds_read_b128 v[176:179], v246\n"
    "ds_read_b128 v[180:183], v246 offset:512\n"
    "ds_read_b128 v[184:187], v246 offset:1024\n"
    "ds_read_b128 v[188:191], v246 offset:1536\n"
    "ds_read_b128 v[192:195], v247\n"
    "ds_read_b128 v[196:199], v247 offset:512\n"
    "ds_read_b128 v[200:203], v247 offset:1024\n"
    "ds_read_b128 v[204:207], v247 offset:1536\n"
    "s_waitcnt vmcnt(0) lgkmcnt(0)\n"
    "s_add_u32 s68, s68, 1\n"
    "s_addc_u32 s69, s69, 0\n"
    "s_barrier\n"
    "ds_read_b128 v[128:131], v11\n"
    "ds_read_b128 v[132:135], v11 offset:512\n"
    "ds_read_b128 v[136:139], v11 offset:1024\n"
    "ds_read_b128 v[140:143], v11 offset:1536\n"
    "ds_read_b128 v[144:147], v11 offset:4096\n"
    "ds_read_b128 v[148:151], v11 offset:4608\n"
    "ds_read_b128 v[152:155], v11 offset:5120\n"
    "ds_read_b128 v[156:159], v11 offset:5632\n"
    "s_waitcnt lgkmcnt(7)\n"
    "v_mfma_f32_16x16x32_bf16 v[80:83], v[176:179], v[128:131], 0\n"
    "s_waitcnt lgkmcnt(6)\n"
    "v_mfma_f32_16x16x32_bf16 v[80:83], v[180:183], v[132:135], v[80:83]\n"
    "s_waitcnt lgkmcnt(5)\n"
    "v_mfma_f32_16x16x32_bf16 v[80:83], v[184:187], v[136:139], v[80:83]\n"
    "s_waitcnt lgkmcnt(4)\n"
    "v_mfma_f32_16x16x32_bf16 v[80:83], v[188:191], v[140:143], v[80:83]\n"
    "s_waitcnt lgkmcnt(0)\n"
    "v_mfma_f32_16x16x32_bf16 v[88:91], v[188:191], v[156:159], 0\n"
    "v_mfma_f32_16x16x32_bf16 v[88:91], v[184:187], v[152:155], v[88:91]\n"
    "v_mfma_f32_16x16x32_bf16 v[88:91], v[180:183], v[148:151], v[88:91]\n"
    "v_mfma_f32_16x16x32_bf16 v[88:91], v[176:179], v[144:147], v[88:91]\n"
    "v_mfma_f32_16x16x32_bf16 v[84:87], v[192:195], v[128:131], 0\n"
    "v_mfma_f32_16x16x32_bf16 v[84:87], v[196:199], v[132:135], v[84:87]\n"
    "v_mfma_f32_16x16x32_bf16 v[84:87], v[200:203], v[136:139], v[84:87]\n"
    "v_mfma_f32_16x16x32_bf16 v[84:87], v[204:207], v[140:143], v[84:87]\n"
    "v_mfma_f32_16x16x32_bf16 v[92:95], v[204:207], v[156:159], 0\n"
    "v_mfma_f32_16x16x32_bf16 v[92:95], v[200:203], v[152:155], v[92:95]\n"
    "v_mfma_f32_16x16x32_bf16 v[92:95], v[196:199], v[148:151], v[92:95]\n"
    "v_mfma_f32_16x16x32_bf16 v[92:95], v[192:195], v[144:147], v[92:95]\n"
    "ds_read_b128 v[176:179], v124\n"
    "ds_read_b128 v[180:183], v124 offset:512\n"
    "ds_read_b128 v[184:187], v124 offset:1024\n"
    "ds_read_b128 v[188:191], v124 offset:1536\n"
    "ds_read_b128 v[192:195], v125\n"
    "ds_read_b128 v[196:199], v125 offset:512\n"
    "ds_read_b128 v[200:203], v125 offset:1024\n"
    "ds_read_b128 v[204:207], v125 offset:1536\n"
    "v_max3_f32 v249, v80, v81, v82\n"
    "v_max3_f32 v0, v88, v89, v90\n"
    "v_max3_f32 v246, v83, v84, v85\n"
    "v_max3_f32 v247, v91, v92, v93\n"
    "v_max3_f32 v249, v249, v86, v87\n"
    "v_max3_f32 v0, v0, v94, v95\n"
    "v_max_f32 v249, v249, v246\n"
    "v_max_f32 v0, v0, v247\n"
    "v_mov_b32 v246, v249\n"
    "v_mov_b32 v247, v0\n"
    "s_nop 0\n"
    "v_permlane16_swap_b32 v249, v246\n"
    "v_permlane16_swap_b32 v0, v247\n"
    "v_max_f32 v249, v249, v246\n"
    "v_max_f32 v0, v0, v247\n"
    "v_mov_b32 v246, v249\n"
    "v_mov_b32 v247, v0\n"
    "s_nop 0\n"
    "v_permlane32_swap_b32 v249, v246\n"
    "v_permlane32_swap_b32 v0, v247\n"
    "v_max_f32 v249, v249, v246\n"
    "v_max_f32 v0, v0, v247\n"
    "v_sub_f32 v112, 0, v249\n"
    "v_sub_f32 v113, 0, v249\n"
    "v_sub_f32 v114, 0, v249\n"
    "v_sub_f32 v115, 0, v249\n"
    "v_sub_f32 v116, 0, v0\n"
    "v_sub_f32 v117, 0, v0\n"
    "v_sub_f32 v118, 0, v0\n"
    "v_sub_f32 v119, 0, v0\n"
    "v_sub_f32 v80, v80, v249\n"
    "v_sub_f32 v81, v81, v249\n"
    "v_sub_f32 v82, v82, v249\n"
    "v_sub_f32 v83, v83, v249\n"
    "v_sub_f32 v84, v84, v249\n"
    "v_sub_f32 v85, v85, v249\n"
    "v_sub_f32 v86, v86, v249\n"
    "v_sub_f32 v87, v87, v249\n"
    "v_sub_f32 v88, v88, v0\n"
    "v_sub_f32 v89, v89, v0\n"
    "v_sub_f32 v90, v90, v0\n"
    "v_sub_f32 v91, v91, v0\n"
    "v_sub_f32 v92, v92, v0\n"
    "v_sub_f32 v93, v93, v0\n"
    "v_sub_f32 v94, v94, v0\n"
    "v_sub_f32 v95, v95, v0\n"
    "s_waitcnt lgkmcnt(4)\n"
    "v_mfma_f32_16x16x32_bf16 v[96:99], v[176:179], v[128:131], v[112:115]\n"
    "v_exp_f32 v12, v80\n"
    "v_mfma_f32_16x16x32_bf16 v[96:99], v[180:183], v[132:135], v[96:99]\n"
    "v_exp_f32 v13, v81\n"
    "v_mfma_f32_16x16x32_bf16 v[96:99], v[184:187], v[136:139], v[96:99]\n"
    "v_exp_f32 v14, v82\n"
    "v_mfma_f32_16x16x32_bf16 v[96:99], v[188:191], v[140:143], v[96:99]\n"
    "v_add_f32 v230, v12, v13\n"
    "v_cvt_pk_bf16_f32 v160, v12, v13\n"
    "v_exp_f32 v15, v83\n"
    "v_add_f32 v230, v230, v14\n"
    "v_exp_f32 v12, v84\n"
    "v_add_f32 v230, v230, v15\n"
    "v_cvt_pk_bf16_f32 v161, v14, v15\n"
    "v_exp_f32 v13, v85\n"
    "v_add_f32 v230, v230, v12\n"
    "v_exp_f32 v14, v86\n"
    "s_add_u32 s66, s52, 0x8000\n"
    "s_sub_u32 s52, s52, 0xe000\n"
    "s_cselect_b32 s52, s66, s52\n"
    "v_mfma_f32_16x16x32_bf16 v[104:107], v[188:191], v[156:159], v[116:119]\n"
    "ds_read_b128 v[208:211], v224\n"
    "v_mfma_f32_16x16x32_bf16 v[104:107], v[184:187], v[152:155], v[104:107]\n"
    "ds_read_b128 v[212:215], v224 offset:512\n"
    "v_add_f32 v230, v230, v13\n"
    "v_mfma_f32_16x16x32_bf16 v[104:107], v[180:183], v[148:151], v[104:107]\n"
    "ds_read_b128 v[216:219], v224 offset:1024\n"
    "v_cvt_pk_bf16_f32 v162, v12, v13\n"
    "v_mfma_f32_16x16x32_bf16 v[104:107], v[176:179], v[144:147], v[104:107]\n"
    "ds_read_b128 v[220:223], v224 offset:1536\n"
    "v_exp_f32 v15, v87\n"
    "v_add_f32 v230, v230, v14\n"
    "v_exp_f32 v12, v88\n"
    "v_add_f32 v230, v230, v15\n"
    "v_cvt_pk_bf16_f32 v163, v14, v15\n"
    "v_exp_f32 v13, v89\n"
    "v_exp_f32 v14, v90\n"
    "v_add_f32 v231, v12, v13\n"
    "v_cvt_pk_bf16_f32 v164, v12, v13\n"
    "s_add_u32 s66, s53, 0x8000\n"
    "s_sub_u32 s53, s53, 0xe000\n"
    "s_cselect_b32 s53, s66, s53\n"
    "s_waitcnt lgkmcnt(4)\n"
    "v_mfma_f32_16x16x32_bf16 v[100:103], v[192:195], v[128:131], v[112:115]\n"
    "v_exp_f32 v15, v91\n"
    "v_mfma_f32_16x16x32_bf16 v[100:103], v[196:199], v[132:135], v[100:103]\n"
    "v_add_f32 v231, v231, v14\n"
    "v_mfma_f32_16x16x32_bf16 v[100:103], v[200:203], v[136:139], v[100:103]\n"
    "v_exp_f32 v12, v92\n"
    "v_mfma_f32_16x16x32_bf16 v[100:103], v[204:207], v[140:143], v[100:103]\n"
    "v_add_f32 v231, v231, v15\n"
    "v_cvt_pk_bf16_f32 v165, v14, v15\n"
    "v_exp_f32 v13, v93\n"
    "v_add_f32 v231, v231, v12\n"
    "v_exp_f32 v14, v94\n"
    "v_add_f32 v231, v231, v13\n"
    "v_cvt_pk_bf16_f32 v166, v12, v13\n"
    "v_exp_f32 v15, v95\n"
    "v_add_f32 v231, v231, v14\n"
    "v_add_f32 v231, v231, v15\n"
    "v_cvt_pk_bf16_f32 v167, v14, v15\n"
    "s_add_u32 s66, s54, 0x8000\n"
    "s_sub_u32 s54, s54, 0xe000\n"
    "s_cselect_b32 s54, s66, s54\n"
    "v_mfma_f32_16x16x32_bf16 v[108:111], v[204:207], v[156:159], v[116:119]\n"
    "ds_read_b128 v[188:191], v225\n"
    "v_max_f32 v248, v230, v231\n"
    "v_mfma_f32_16x16x32_bf16 v[108:111], v[200:203], v[152:155], v[108:111]\n"
    "ds_read_b128 v[184:187], v225 offset:512\n"
    "v_cmp_nge_f32 vcc, s70, v248\n"
    "v_mfma_f32_16x16x32_bf16 v[108:111], v[196:199], v[148:151], v[108:111]\n"
    "ds_read_b128 v[180:183], v225 offset:1024\n"
    "v_mfma_f32_16x16x32_bf16 v[108:111], v[192:195], v[144:147], v[108:111]\n"
    "s_cbranch_vccnz .Lkrare_14\n"
    ".Lkrret_15:\n"
    "ds_read_b128 v[176:179], v225 offset:1536\n"
    "s_waitcnt lgkmcnt(4)\n"
    "v_mfma_f32_16x16x32_bf16 v[80:83], v[208:211], v[128:131], v[112:115]\n"
    "v_add_f32 v228, v228, v230\n"
    "v_add_f32 v229, v229, v231\n"
    "v_mfma_f32_16x16x32_bf16 v[80:83], v[212:215], v[132:135], v[80:83]\n"
    "v_exp_f32 v12, v96\n"
    "v_mfma_f32_16x16x32_bf16 v[80:83], v[216:219], v[136:139], v[80:83]\n"
    "v_exp_f32 v13, v97\n"
    "v_mfma_f32_16x16x32_bf16 v[80:83], v[220:223], v[140:143], v[80:83]\n"
    "v_exp_f32 v14, v98\n"
    "s_add_u32 s66, s55, 0x8000\n"
    "s_sub_u32 s55, s55, 0xe000\n"
    "s_cselect_b32 s55, s66, s55\n"
    "v_mfma_f32_16x16x32_bf16 v[88:91], v[220:223], v[156:159], v[116:119]\n"
    "ds_read_b64_tr_b16 v[204:205], v226\n"
    "ds_read_b64_tr_b16 v[206:207], v226 offset:256\n"
    "v_mfma_f32_16x16x32_bf16 v[88:91], v[216:219], v[152:155], v[88:91]\n"
    "v_add_f32 v230, v12, v13\n"
    "v_mfma_f32_16x16x32_bf16 v[88:91], v[212:215], v[148:151], v[88:91]\n"
    "ds_read_b64_tr_b16 v[200:201], v227\n"
    "ds_read_b64_tr_b16 v[202:203], v227 offset:256\n"
    "v_mfma_f32_16x16x32_bf16 v[88:91], v[208:211], v[144:147], v[88:91]\n"
    "v_cvt_pk_bf16_f32 v168, v12, v13\n"
    "v_exp_f32 v15, v99\n"
    "v_add_u32 v124, s52, v120\n"
    "s_add_u32 s15, s15, 1\n"
    "s_waitcnt lgkmcnt(4)\n"
    "v_mfma_f32_16x16x32_bf16 v[84:87], v[188:191], v[128:131], v[112:115]\n"
    "ds_read_b64_tr_b16 v[196:197], v226 offset:512\n"
    "ds_read_b64_tr_b16 v[198:199], v226 offset:768\n"
    "v_mfma_f32_16x16x32_bf16 v[84:87], v[184:187], v[132:135], v[84:87]\n"
    "v_add_f32 v230, v230, v14\n"
    "v_mfma_f32_16x16x32_bf16 v[84:87], v[180:183], v[136:139], v[84:87]\n"
    "ds_read_b64_tr_b16 v[192:193], v227 offset:512\n"
    "ds_read_b64_tr_b16 v[194:195], v227 offset:768\n"
    "v_mfma_f32_16x16x32_bf16 v[84:87], v[176:179], v[140:143], v[84:87]\n"
    "ds_read_b64_tr_b16 v[208:209], v226 offset:1024\n"
    "ds_read_b64_tr_b16 v[210:211], v226 offset:1280\n"
    "v_exp_f32 v12, v100\n"
    "v_add_f32 v230, v230, v15\n"
    "v_mfma_f32_16x16x32_bf16 v[92:95], v[176:179], v[156:159], v[116:119]\n"
    "ds_read_b64_tr_b16 v[212:213], v227 offset:1024\n"
    "ds_read_b64_tr_b16 v[214:215], v227 offset:1280\n"
    "v_mfma_f32_16x16x32_bf16 v[92:95], v[180:183], v[152:155], v[92:95]\n"
    "v_cvt_pk_bf16_f32 v169, v14, v15\n"
    "v_mfma_f32_16x16x32_bf16 v[92:95], v[184:187], v[148:151], v[92:95]\n"
    "ds_read_b64_tr_b16 v[216:217], v226 offset:1536\n"
    "ds_read_b64_tr_b16 v[218:219], v226 offset:1792\n"
    "v_mfma_f32_16x16x32_bf16 v[92:95], v[188:191], v[144:147], v[92:95]\n"
    "v_exp_f32 v13, v101\n"
    "v_add_f32 v230, v230, v12\n"
    "v_exp_f32 v14, v102\n"
    "v_add_u32 v125, s52, v121\n"
    "s_waitcnt lgkmcnt(10)\n"
    "v_mfma_f32_16x16x32_bf16 v[16:19], v[204:207], v[160:163], v[16:19]\n"
    "ds_read_b64_tr_b16 v[220:221], v227 offset:1536\n"
    "ds_read_b64_tr_b16 v[222:223], v227 offset:1792\n"
    "v_add_f32 v230, v230, v13\n"
    "v_cvt_pk_bf16_f32 v170, v12, v13\n"
    "v_mfma_f32_16x16x32_bf16 v[20:23], v[204:207], v[164:167], v[20:23]\n"
    "v_exp_f32 v15, v103\n"
    "v_add_f32 v230, v230, v14\n"
    "v_add_u32 v224, s53, v120\n"
    "v_mfma_f32_16x16x32_bf16 v[28:31], v[200:203], v[164:167], v[28:31]\n"
    "v_exp_f32 v12, v104\n"
    "v_add_f32 v230, v230, v15\n"
    "v_cvt_pk_bf16_f32 v171, v14, v15\n"
    "v_add_u32 v225, s53, v121\n"
    "v_mfma_f32_16x16x32_bf16 v[24:27], v[200:203], v[160:163], v[24:27]\n"
    "v_exp_f32 v13, v105\n"
    "v_exp_f32 v14, v106\n"
    "v_add_u32 v126, s54, v122\n"
    "s_waitcnt lgkmcnt(8)\n"
    "v_mfma_f32_16x16x32_bf16 v[32:35], v[196:199], v[160:163], v[32:35]\n"
    "v_add_f32 v231, v12, v13\n"
    "v_cvt_pk_bf16_f32 v172, v12, v13\n"
    "v_exp_f32 v15, v107\n"
    "v_add_u32 v127, s54, v123\n"
    "v_mfma_f32_16x16x32_bf16 v[36:39], v[196:199], v[164:167], v[36:39]\n"
    "v_add_f32 v231, v231, v14\n"
    "v_exp_f32 v12, v108\n"
    "v_add_u32 v226, s55, v122\n"
    "v_mfma_f32_16x16x32_bf16 v[44:47], v[192:195], v[164:167], v[44:47]\n"
    "v_add_f32 v231, v231, v15\n"
    "v_cvt_pk_bf16_f32 v173, v14, v15\n"
    "v_exp_f32 v13, v109\n"
    "v_add_u32 v227, s55, v123\n"
    "v_mfma_f32_16x16x32_bf16 v[40:43], v[192:195], v[160:163], v[40:43]\n"
    "s_waitcnt lgkmcnt(0)\n"
    "s_barrier\n"
    ".Lkbody_16:\n"
    "v_mfma_f32_16x16x32_bf16 v[48:51], v[208:211], v[160:163], v[48:51]\n"
    "ds_read_b128 v[176:179], v124\n"
    "ds_read_b128 v[180:183], v124 offset:512\n"
    "v_add_f32 v231, v231, v12\n"
    "v_mfma_f32_16x16x32_bf16 v[52:55], v[208:211], v[164:167], v[52:55]\n"
    "ds_read_b128 v[184:187], v124 offset:1024\n"
    "ds_read_b128 v[188:191], v124 offset:1536\n"
    "v_exp_f32 v14, v110\n"
    "v_mfma_f32_16x16x32_bf16 v[60:63], v[212:215], v[164:167], v[60:63]\n"
    "v_add_f32 v231, v231, v13\n"
    "s_add_u32 s66, s52, 0x8000\n"
    "s_sub_u32 s52, s52, 0xe000\n"
    "s_cselect_b32 s52, s66, s52\n"
    "v_mfma_f32_16x16x32_bf16 v[56:59], v[212:215], v[160:163], v[56:59]\n"
    "v_cvt_pk_bf16_f32 v174, v12, v13\n"
    "v_exp_f32 v15, v111\n"
    "v_mfma_f32_16x16x32_bf16 v[64:67], v[216:219], v[160:163], v[64:67]\n"
    "ds_read_b128 v[192:195], v125\n"
    "v_add_f32 v231, v231, v14\n"
    "v_add_f32 v231, v231, v15\n"
    "v_cvt_pk_bf16_f32 v175, v14, v15\n"
    "v_mfma_f32_16x16x32_bf16 v[68:71], v[216:219], v[164:167], v[68:71]\n"
    "ds_read_b128 v[196:199], v125 offset:512\n"
    "v_max_f32 v248, v230, v231\n"
    "v_cmp_nge_f32 vcc, s70, v248\n"
    "v_mfma_f32_16x16x32_bf16 v[76:79], v[220:223], v[164:167], v[76:79]\n"
    "ds_read_b128 v[200:203], v125 offset:1024\n"
    "v_mfma_f32_16x16x32_bf16 v[72:75], v[220:223], v[160:163], v[72:75]\n"
    "s_cbranch_vccnz .Lkrare_17\n"
    ".Lkrret_18:\n"
    "ds_read_b128 v[204:207], v125 offset:1536\n"
    "s_waitcnt lgkmcnt(4)\n"
    "v_mfma_f32_16x16x32_bf16 v[96:99], v[176:179], v[128:131], v[112:115]\n"
    "v_add_f32 v228, v228, v230\n"
    "v_add_f32 v229, v229, v231\n"
    "v_mfma_f32_16x16x32_bf16 v[96:99], v[180:183], v[132:135], v[96:99]\n"
    "v_exp_f32 v12, v80\n"
    "v_mfma_f32_16x16x32_bf16 v[96:99], v[184:187], v[136:139], v[96:99]\n"
    "v_exp_f32 v13, v81\n"
    "v_mfma_f32_16x16x32_bf16 v[96:99], v[188:191], v[140:143], v[96:99]\n"
    "s_add_u32 s66, s53, 0x8000\n"
    "s_sub_u32 s53, s53, 0xe000\n"
    "s_cselect_b32 s53, s66, s53\n"
    "v_mfma_f32_16x16x32_bf16 v[104:107], v[188:191], v[156:159], v[116:119]\n"
    "ds_read_b64_tr_b16 v[208:209], v126\n"
    "ds_read_b64_tr_b16 v[210:211], v126 offset:256\n"
    "v_mfma_f32_16x16x32_bf16 v[104:107], v[184:187], v[152:155], v[104:107]\n"
    "v_exp_f32 v14, v82\n"
    "v_mfma_f32_16x16x32_bf16 v[104:107], v[180:183], v[148:151], v[104:107]\n"
    "ds_read_b64_tr_b16 v[212:213], v127\n"
    "ds_read_b64_tr_b16 v[214:215], v127 offset:256\n"
    "v_mfma_f32_16x16x32_bf16 v[104:107], v[176:179], v[144:147], v[104:107]\n"
    "v_add_f32 v230, v12, v13\n"
    "v_cvt_pk_bf16_f32 v160, v12, v13\n"
    "v_exp_f32 v15, v83\n"
    "s_waitcnt lgkmcnt(4)\n"
    "v_mfma_f32_16x16x32_bf16 v[100:103], v[192:195], v[128:131], v[112:115]\n"
    "ds_read_b64_tr_b16 v[216:217], v126 offset:512\n"
    "ds_read_b64_tr_b16 v[218:219], v126 offset:768\n"
    "v_mfma_f32_16x16x32_bf16 v[100:103], v[196:199], v[132:135], v[100:103]\n"
    "v_add_f32 v230, v230, v14\n"
    "v_exp_f32 v12, v84\n"
    "v_mfma_f32_16x16x32_bf16 v[100:103], v[200:203], v[136:139], v[100:103]\n"
    "ds_read_b64_tr_b16 v[220:221], v127 offset:512\n"
    "ds_read_b64_tr_b16 v[222:223], v127 offset:768\n"
    "v_mfma_f32_16x16x32_bf16 v[100:103], v[204:207], v[140:143], v[100:103]\n"
    "s_add_u32 s66, s54, 0x8000\n"
    "s_sub_u32 s54, s54, 0xe000\n"
    "s_cselect_b32 s54, s66, s54\n"
    "v_mfma_f32_16x16x32_bf16 v[108:111], v[204:207], v[156:159], v[116:119]\n"
    "ds_read_b64_tr_b16 v[188:189], v126 offset:1024\n"
    "ds_read_b64_tr_b16 v[190:191], v126 offset:1280\n"
    "v_mfma_f32_16x16x32_bf16 v[108:111], v[200:203], v[152:155], v[108:111]\n"
    "v_add_f32 v230, v230, v15\n"
    "v_cvt_pk_bf16_f32 v161, v14, v15\n"
    "v_mfma_f32_16x16x32_bf16 v[108:111], v[196:199], v[148:151], v[108:111]\n"
    "ds_read_b64_tr_b16 v[184:185], v127 offset:1024\n"
    "ds_read_b64_tr_b16 v[186:187], v127 offset:1280\n"
    "v_mfma_f32_16x16x32_bf16 v[108:111], v[192:195], v[144:147], v[108:111]\n"
    "v_exp_f32 v13, v85\n"
    "v_add_f32 v230, v230, v12\n"
    "s_waitcnt lgkmcnt(8)\n"
    "v_mfma_f32_16x16x32_bf16 v[16:19], v[208:211], v[168:171], v[16:19]\n"
    "ds_read_b64_tr_b16 v[180:181], v126 offset:1536\n"
    "ds_read_b64_tr_b16 v[182:183], v126 offset:1792\n"
    "v_exp_f32 v14, v86\n"
    "v_add_f32 v230, v230, v13\n"
    "v_mfma_f32_16x16x32_bf16 v[20:23], v[208:211], v[172:175], v[20:23]\n"
    "s_add_u32 s66, s55, 0x8000\n"
    "s_sub_u32 s55, s55, 0xe000\n"
    "s_cselect_b32 s55, s66, s55\n"
    "v_mfma_f32_16x16x32_bf16 v[28:31], v[212:215], v[172:175], v[28:31]\n"
    "ds_read_b64_tr_b16 v[176:177], v127 offset:1536\n"
    "ds_read_b64_tr_b16 v[178:179], v127 offset:1792\n"
    "v_cvt_pk_bf16_f32 v162, v12, v13\n"
    "v_exp_f32 v15, v87\n"
    "v_mfma_f32_16x16x32_bf16 v[24:27], v[212:215], v[168:171], v[24:27]\n"
    "v_add_f32 v230, v230, v14\n"
    "v_exp_f32 v12, v88\n"
    "s_waitcnt lgkmcnt(8)\n"
    "v_mfma_f32_16x16x32_bf16 v[32:35], v[216:219], v[168:171], v[32:35]\n"
    "ds_read_b128 v[204:207], v224\n"
    "v_add_f32 v230, v230, v15\n"
    "v_cvt_pk_bf16_f32 v163, v14, v15\n"
    "v_exp_f32 v13, v89\n"
    "v_mfma_f32_16x16x32_bf16 v[36:39], v[216:219], v[172:175], v[36:39]\n"
    "ds_read_b128 v[200:203], v224 offset:512\n"
    "v_exp_f32 v14, v90\n"
    "v_mfma_f32_16x16x32_bf16 v[44:47], v[220:223], v[172:175], v[44:47]\n"
    "ds_read_b128 v[196:199], v224 offset:1024\n"
    "v_add_f32 v231, v12, v13\n"
    "v_cvt_pk_bf16_f32 v164, v12, v13\n"
    "v_mfma_f32_16x16x32_bf16 v[40:43], v[220:223], v[168:171], v[40:43]\n"
    "ds_read_b128 v[192:195], v224 offset:1536\n"
    "v_exp_f32 v15, v91\n"
    "v_add_f32 v231, v231, v14\n"
    "s_waitcnt lgkmcnt(8)\n"
    "v_mfma_f32_16x16x32_bf16 v[48:51], v[188:191], v[168:171], v[48:51]\n"
    "v_exp_f32 v12, v92\n"
    "v_add_f32 v231, v231, v15\n"
    "v_cvt_pk_bf16_f32 v165, v14, v15\n"
    "v_add_u32 v124, s52, v120\n"
    "v_mfma_f32_16x16x32_bf16 v[52:55], v[188:191], v[172:175], v[52:55]\n"
    "v_exp_f32 v13, v93\n"
    "v_add_u32 v125, s52, v121\n"
    "v_mfma_f32_16x16x32_bf16 v[60:63], v[184:187], v[172:175], v[60:63]\n"
    "v_add_f32 v231, v231, v12\n"
    "v_exp_f32 v14, v94\n"
    "v_add_u32 v126, s54, v122\n"
    "v_mfma_f32_16x16x32_bf16 v[56:59], v[184:187], v[168:171], v[56:59]\n"
    "v_add_f32 v231, v231, v13\n"
    "v_cvt_pk_bf16_f32 v166, v12, v13\n"
    "v_exp_f32 v15, v95\n"
    "v_add_u32 v127, s54, v123\n"
    "s_waitcnt lgkmcnt(4)\n"
    "v_mfma_f32_16x16x32_bf16 v[64:67], v[180:183], v[168:171], v[64:67]\n"
    "ds_read_b128 v[220:223], v225\n"
    "v_add_f32 v231, v231, v14\n"
    "v_add_f32 v231, v231, v15\n"
    "v_mfma_f32_16x16x32_bf16 v[68:71], v[180:183], v[172:175], v[68:71]\n"
    "ds_read_b128 v[188:191], v225 offset:512\n"
    "v_cvt_pk_bf16_f32 v167, v14, v15\n"
    "v_max_f32 v248, v230, v231\n"
    "v_cmp_nge_f32 vcc, s70, v248\n"
    "v_mfma_f32_16x16x32_bf16 v[76:79], v[176:179], v[172:175], v[76:79]\n"
    "ds_read_b128 v[216:219], v225 offset:1024\n"
    "v_mfma_f32_16x16x32_bf16 v[72:75], v[176:179], v[168:171], v[72:75]\n"
    "s_cbranch_vccnz .Lkrare_19\n"
    ".Lkrret_20:\n"
    "ds_read_b128 v[184:187], v225 offset:1536\n"
    "s_waitcnt lgkmcnt(4)\n"
    "v_mfma_f32_16x16x32_bf16 v[80:83], v[204:207], v[128:131], v[112:115]\n"
    "v_add_f32 v228, v228, v230\n"
    "v_add_f32 v229, v229, v231\n"
    "v_mfma_f32_16x16x32_bf16 v[80:83], v[200:203], v[132:135], v[80:83]\n"
    "v_exp_f32 v12, v96\n"
    "v_mfma_f32_16x16x32_bf16 v[80:83], v[196:199], v[136:139], v[80:83]\n"
    "v_exp_f32 v13, v97\n"
    "v_mfma_f32_16x16x32_bf16 v[80:83], v[192:195], v[140:143], v[80:83]\n"
    "v_exp_f32 v14, v98\n"
    "v_add_f32 v230, v12, v13\n"
    "v_add_u32 v224, s53, v120\n"
    "v_mfma_f32_16x16x32_bf16 v[88:91], v[192:195], v[156:159], v[116:119]\n"
    "ds_read_b64_tr_b16 v[180:181], v226\n"
    "ds_read_b64_tr_b16 v[182:183], v226 offset:256\n"
    "v_mfma_f32_16x16x32_bf16 v[88:91], v[196:199], v[152:155], v[88:91]\n"
    "v_cvt_pk_bf16_f32 v168, v12, v13\n"
    "v_mfma_f32_16x16x32_bf16 v[88:91], v[200:203], v[148:151], v[88:91]\n"
    "ds_read_b64_tr_b16 v[176:177], v227\n"
    "ds_read_b64_tr_b16 v[178:179], v227 offset:256\n"
    "v_mfma_f32_16x16x32_bf16 v[88:91], v[204:207], v[144:147], v[88:91]\n"
    "v_exp_f32 v15, v99\n"
    "v_add_f32 v230, v230, v14\n"
    "v_add_u32 v225, s53, v121\n"
    "s_waitcnt lgkmcnt(4)\n"
    "v_mfma_f32_16x16x32_bf16 v[84:87], v[220:223], v[128:131], v[112:115]\n"
    "ds_read_b64_tr_b16 v[192:193], v226 offset:512\n"
    "ds_read_b64_tr_b16 v[194:195], v226 offset:768\n"
    "v_mfma_f32_16x16x32_bf16 v[84:87], v[188:191], v[132:135], v[84:87]\n"
    "v_exp_f32 v12, v100\n"
    "v_mfma_f32_16x16x32_bf16 v[84:87], v[216:219], v[136:139], v[84:87]\n"
    "ds_read_b64_tr_b16 v[196:197], v227 offset:512\n"
    "ds_read_b64_tr_b16 v[198:199], v227 offset:768\n"
    "v_mfma_f32_16x16x32_bf16 v[84:87], v[184:187], v[140:143], v[84:87]\n"
    "ds_read_b64_tr_b16 v[208:209], v226 offset:1024\n"
    "ds_read_b64_tr_b16 v[210:211], v226 offset:1280\n"
    "v_add_f32 v230, v230, v15\n"
    "v_cvt_pk_bf16_f32 v169, v14, v15\n"
    "v_mfma_f32_16x16x32_bf16 v[92:95], v[184:187], v[156:159], v[116:119]\n"
    "ds_read_b64_tr_b16 v[212:213], v227 offset:1024\n"
    "ds_read_b64_tr_b16 v[214:215], v227 offset:1280\n"
    "v_mfma_f32_16x16x32_bf16 v[92:95], v[216:219], v[152:155], v[92:95]\n"
    "v_exp_f32 v13, v101\n"
    "v_mfma_f32_16x16x32_bf16 v[92:95], v[188:191], v[148:151], v[92:95]\n"
    "ds_read_b64_tr_b16 v[216:217], v226 offset:1536\n"
    "ds_read_b64_tr_b16 v[218:219], v226 offset:1792\n"
    "v_mfma_f32_16x16x32_bf16 v[92:95], v[220:223], v[144:147], v[92:95]\n"
    "v_add_f32 v230, v230, v12\n"
    "v_exp_f32 v14, v102\n"
    "s_add_u32 s15, s15, 1\n"
    "s_waitcnt lgkmcnt(10)\n"
    "v_mfma_f32_16x16x32_bf16 v[16:19], v[180:183], v[160:163], v[16:19]\n"
    "ds_read_b64_tr_b16 v[220:221], v227 offset:1536\n"
    "ds_read_b64_tr_b16 v[222:223], v227 offset:1792\n"
    "v_add_f32 v230, v230, v13\n"
    "v_cvt_pk_bf16_f32 v170, v12, v13\n"
    "v_mfma_f32_16x16x32_bf16 v[20:23], v[180:183], v[164:167], v[20:23]\n"
    "v_exp_f32 v15, v103\n"
    "v_add_f32 v230, v230, v14\n"
    "v_add_u32 v226, s55, v122\n"
    "v_mfma_f32_16x16x32_bf16 v[28:31], v[176:179], v[164:167], v[28:31]\n"
    "v_exp_f32 v12, v104\n"
    "v_add_f32 v230, v230, v15\n"
    "v_add_u32 v227, s55, v123\n"
    "v_mfma_f32_16x16x32_bf16 v[24:27], v[176:179], v[160:163], v[24:27]\n"
    "v_cvt_pk_bf16_f32 v171, v14, v15\n"
    "v_exp_f32 v13, v105\n"
    "v_exp_f32 v14, v106\n"
    "s_waitcnt lgkmcnt(8)\n"
    "v_mfma_f32_16x16x32_bf16 v[32:35], v[192:195], v[160:163], v[32:35]\n"
    "v_add_f32 v231, v12, v13\n"
    "v_cvt_pk_bf16_f32 v172, v12, v13\n"
    "v_exp_f32 v15, v107\n"
    "v_mfma_f32_16x16x32_bf16 v[36:39], v[192:195], v[164:167], v[36:39]\n"
    "v_add_f32 v231, v231, v14\n"
    "v_exp_f32 v12, v108\n"
    "v_mfma_f32_16x16x32_bf16 v[44:47], v[196:199], v[164:167], v[44:47]\n"
    "v_add_f32 v231, v231, v15\n"
    "v_cvt_pk_bf16_f32 v173, v14, v15\n"
    "v_exp_f32 v13, v109\n"
    "v_mfma_f32_16x16x32_bf16 v[40:43], v[196:199], v[160:163], v[40:43]\n"
    "s_cmp_lt_u32 s15, 15\n"
    "s_waitcnt lgkmcnt(0)\n"
    "s_barrier\n"
    "s_cbranch_scc1 .Lkbody_16\n"
    "v_mfma_f32_16x16x32_bf16 v[48:51], v[208:211], v[160:163], v[48:51]\n"
    "ds_read_b128 v[176:179], v124\n"
    "ds_read_b128 v[180:183], v124 offset:512\n"
    "v_add_f32 v231, v231, v12\n"
    "v_mfma_f32_16x16x32_bf16 v[52:55], v[208:211], v[164:167], v[52:55]\n"
    "ds_read_b128 v[184:187], v124 offset:1024\n"
    "ds_read_b128 v[188:191], v124 offset:1536\n"
    "v_exp_f32 v14, v110\n"
    "v_mfma_f32_16x16x32_bf16 v[60:63], v[212:215], v[164:167], v[60:63]\n"
    "v_add_f32 v231, v231, v13\n"
    "s_add_u32 s66, s52, 0x8000\n"
    "s_sub_u32 s52, s52, 0xe000\n"
    "s_cselect_b32 s52, s66, s52\n"
    "v_mfma_f32_16x16x32_bf16 v[56:59], v[212:215], v[160:163], v[56:59]\n"
    "v_cvt_pk_bf16_f32 v174, v12, v13\n"
    "v_exp_f32 v15, v111\n"
    "v_mfma_f32_16x16x32_bf16 v[64:67], v[216:219], v[160:163], v[64:67]\n"
    "ds_read_b128 v[192:195], v125\n"
    "v_add_f32 v231, v231, v14\n"
    "v_add_f32 v231, v231, v15\n"
    "v_cvt_pk_bf16_f32 v175, v14, v15\n"
    "v_mfma_f32_16x16x32_bf16 v[68:71], v[216:219], v[164:167], v[68:71]\n"
    "ds_read_b128 v[196:199], v125 offset:512\n"
    "v_max_f32 v248, v230, v231\n"
    "v_cmp_nge_f32 vcc, s70, v248\n"
    "v_mfma_f32_16x16x32_bf16 v[76:79], v[220:223], v[164:167], v[76:79]\n"
    "ds_read_b128 v[200:203], v125 offset:1024\n"
    "v_mfma_f32_16x16x32_bf16 v[72:75], v[220:223], v[160:163], v[72:75]\n"
    "s_cbranch_vccnz .Lkrare_21\n"
    ".Lkrret_22:\n"
    "ds_read_b128 v[204:207], v125 offset:1536\n"
    "s_waitcnt lgkmcnt(4)\n"
    "v_mfma_f32_16x16x32_bf16 v[96:99], v[176:179], v[128:131], v[112:115]\n"
    "v_add_f32 v228, v228, v230\n"
    "v_add_f32 v229, v229, v231\n"
    "v_mfma_f32_16x16x32_bf16 v[96:99], v[180:183], v[132:135], v[96:99]\n"
    "v_exp_f32 v12, v80\n"
    "v_mfma_f32_16x16x32_bf16 v[96:99], v[184:187], v[136:139], v[96:99]\n"
    "v_exp_f32 v13, v81\n"
    "v_mfma_f32_16x16x32_bf16 v[96:99], v[188:191], v[140:143], v[96:99]\n"
    "s_add_u32 s66, s53, 0x8000\n"
    "s_sub_u32 s53, s53, 0xe000\n"
    "s_cselect_b32 s53, s66, s53\n"
    "v_mfma_f32_16x16x32_bf16 v[104:107], v[188:191], v[156:159], v[116:119]\n"
    "ds_read_b64_tr_b16 v[208:209], v126\n"
    "ds_read_b64_tr_b16 v[210:211], v126 offset:256\n"
    "v_mfma_f32_16x16x32_bf16 v[104:107], v[184:187], v[152:155], v[104:107]\n"
    "v_exp_f32 v14, v82\n"
    "v_add_f32 v230, v12, v13\n"
    "v_mfma_f32_16x16x32_bf16 v[104:107], v[180:183], v[148:151], v[104:107]\n"
    "ds_read_b64_tr_b16 v[212:213], v127\n"
    "ds_read_b64_tr_b16 v[214:215], v127 offset:256\n"
    "v_mfma_f32_16x16x32_bf16 v[104:107], v[176:179], v[144:147], v[104:107]\n"
    "v_cvt_pk_bf16_f32 v160, v12, v13\n"
    "v_exp_f32 v15, v83\n"
    "v_add_f32 v230, v230, v14\n"
    "s_waitcnt lgkmcnt(4)\n"
    "v_mfma_f32_16x16x32_bf16 v[100:103], v[192:195], v[128:131], v[112:115]\n"
    "ds_read_b64_tr_b16 v[216:217], v126 offset:512\n"
    "ds_read_b64_tr_b16 v[218:219], v126 offset:768\n"
    "v_mfma_f32_16x16x32_bf16 v[100:103], v[196:199], v[132:135], v[100:103]\n"
    "v_exp_f32 v12, v84\n"
    "v_mfma_f32_16x16x32_bf16 v[100:103], v[200:203], v[136:139], v[100:103]\n"
    "ds_read_b64_tr_b16 v[220:221], v127 offset:512\n"
    "ds_read_b64_tr_b16 v[222:223], v127 offset:768\n"
    "v_mfma_f32_16x16x32_bf16 v[100:103], v[204:207], v[140:143], v[100:103]\n"
    "v_add_f32 v230, v230, v15\n"
    "s_add_u32 s66, s54, 0x8000\n"
    "s_sub_u32 s54, s54, 0xe000\n"
    "s_cselect_b32 s54, s66, s54\n"
    "v_mfma_f32_16x16x32_bf16 v[108:111], v[204:207], v[156:159], v[116:119]\n"
    "ds_read_b64_tr_b16 v[188:189], v126 offset:1024\n"
    "ds_read_b64_tr_b16 v[190:191], v126 offset:1280\n"
    "v_mfma_f32_16x16x32_bf16 v[108:111], v[200:203], v[152:155], v[108:111]\n"
    "v_cvt_pk_bf16_f32 v161, v14, v15\n"
    "v_mfma_f32_16x16x32_bf16 v[108:111], v[196:199], v[148:151], v[108:111]\n"
    "ds_read_b64_tr_b16 v[184:185], v127 offset:1024\n"
    "ds_read_b64_tr_b16 v[186:187], v127 offset:1280\n"
    "v_mfma_f32_16x16x32_bf16 v[108:111], v[192:195], v[144:147], v[108:111]\n"
    "v_exp_f32 v13, v85\n"
    "v_add_f32 v230, v230, v12\n"
    "v_exp_f32 v14, v86\n"
    "s_waitcnt lgkmcnt(8)\n"
    "v_mfma_f32_16x16x32_bf16 v[16:19], v[208:211], v[168:171], v[16:19]\n"
    "ds_read_b64_tr_b16 v[180:181], v126 offset:1536\n"
    "ds_read_b64_tr_b16 v[182:183], v126 offset:1792\n"
    "v_add_f32 v230, v230, v13\n"
    "v_cvt_pk_bf16_f32 v162, v12, v13\n"
    "v_mfma_f32_16x16x32_bf16 v[20:23], v[208:211], v[172:175], v[20:23]\n"
    "ds_read_b128 v[128:131], v11\n"
    "s_add_u32 s66, s55, 0x8000\n"
    "s_sub_u32 s55, s55, 0xe000\n"
    "s_cselect_b32 s55, s66, s55\n"
    "v_mfma_f32_16x16x32_bf16 v[28:31], v[212:215], v[172:175], v[28:31]\n"
    "ds_read_b64_tr_b16 v[176:177], v127 offset:1536\n"
    "ds_read_b64_tr_b16 v[178:179], v127 offset:1792\n"
    "v_exp_f32 v15, v87\n"
    "v_mfma_f32_16x16x32_bf16 v[24:27], v[212:215], v[168:171], v[24:27]\n"
    "v_add_f32 v230, v230, v14\n"
    "v_exp_f32 v12, v88\n"
    "ds_read_b128 v[132:135], v11 offset:512\n"
    "s_waitcnt lgkmcnt(10)\n"
    "v_mfma_f32_16x16x32_bf16 v[32:35], v[216:219], v[168:171], v[32:35]\n"
    "ds_read_b128 v[204:207], v224\n"
    "v_add_f32 v230, v230, v15\n"
    "v_cvt_pk_bf16_f32 v163, v14, v15\n"
    "ds_read_b128 v[136:139], v11 offset:1024\n"
    "v_mfma_f32_16x16x32_bf16 v[36:39], v[216:219], v[172:175], v[36:39]\n"
    "ds_read_b128 v[200:203], v224 offset:512\n"
    "v_exp_f32 v13, v89\n"
    "v_exp_f32 v14, v90\n"
    "v_mfma_f32_16x16x32_bf16 v[44:47], v[220:223], v[172:175], v[44:47]\n"
    "ds_read_b128 v[196:199], v224 offset:1024\n"
    "v_add_f32 v231, v12, v13\n"
    "v_cvt_pk_bf16_f32 v164, v12, v13\n"
    "ds_read_b128 v[140:143], v11 offset:1536\n"
    "v_mfma_f32_16x16x32_bf16 v[40:43], v[220:223], v[168:171], v[40:43]\n"
    "s_waitcnt lgkmcnt(13)\n"
    "ds_read_b128 v[192:195], v224 offset:1536\n"
    "v_exp_f32 v15, v91\n"
    "v_add_f32 v231, v231, v14\n"
    "v_mfma_f32_16x16x32_bf16 v[48:51], v[188:191], v[168:171], v[48:51]\n"
    "v_exp_f32 v12, v92\n"
    "v_add_f32 v231, v231, v15\n"
    "ds_read_b128 v[144:147], v11 offset:4096\n"
    "v_mfma_f32_16x16x32_bf16 v[52:55], v[188:191], v[172:175], v[52:55]\n"
    "v_cvt_pk_bf16_f32 v165, v14, v15\n"
    "v_exp_f32 v13, v93\n"
    "v_add_u32 v124, s52, v120\n"
    "s_waitcnt lgkmcnt(11)\n"
    "v_mfma_f32_16x16x32_bf16 v[60:63], v[184:187], v[172:175], v[60:63]\n"
    "v_add_f32 v231, v231, v12\n"
    "v_exp_f32 v14, v94\n"
    "v_add_f32 v231, v231, v13\n"
    "ds_read_b128 v[148:151], v11 offset:4608\n"
    "v_mfma_f32_16x16x32_bf16 v[56:59], v[184:187], v[168:171], v[56:59]\n"
    "v_cvt_pk_bf16_f32 v166, v12, v13\n"
    "v_exp_f32 v15, v95\n"
    "v_add_u32 v125, s52, v121\n"
    "v_mfma_f32_16x16x32_bf16 v[64:67], v[180:183], v[168:171], v[64:67]\n"
    "ds_read_b128 v[220:223], v225\n"
    "v_add_f32 v231, v231, v14\n"
    "v_add_f32 v231, v231, v15\n"
    "ds_read_b128 v[152:155], v11 offset:5120\n"
    "v_mfma_f32_16x16x32_bf16 v[68:71], v[180:183], v[172:175], v[68:71]\n"
    "ds_read_b128 v[188:191], v225 offset:512\n"
    "v_cvt_pk_bf16_f32 v167, v14, v15\n"
    "v_max_f32 v248, v230, v231\n"
    "v_cmp_nge_f32 vcc, s70, v248\n"
    "s_waitcnt lgkmcnt(8)\n"
    "v_mfma_f32_16x16x32_bf16 v[76:79], v[176:179], v[172:175], v[76:79]\n"
    "ds_read_b128 v[216:219], v225 offset:1024\n"
    "ds_read_b128 v[156:159], v11 offset:5632\n"
    "v_mfma_f32_16x16x32_bf16 v[72:75], v[176:179], v[168:171], v[72:75]\n"
    "s_cbranch_vccnz .Lkrare_23\n"
    ".Lkrret_24:\n"
    "ds_read_b128 v[184:187], v225 offset:1536\n"
    "v_mfma_f32_16x16x32_bf16 v[80:83], v[204:207], v[128:131], 0\n"
    "v_add_f32 v228, v228, v230\n"
    "v_add_f32 v229, v229, v231\n"
    "v_mfma_f32_16x16x32_bf16 v[80:83], v[200:203], v[132:135], v[80:83]\n"
    "v_exp_f32 v12, v96\n"
    "s_waitcnt lgkmcnt(1)\n"
    "v_mfma_f32_16x16x32_bf16 v[80:83], v[196:199], v[136:139], v[80:83]\n"
    "v_exp_f32 v13, v97\n"
    "v_mfma_f32_16x16x32_bf16 v[80:83], v[192:195], v[140:143], v[80:83]\n"
    "v_exp_f32 v14, v98\n"
    "v_add_f32 v230, v12, v13\n"
    "v_cvt_pk_bf16_f32 v168, v12, v13\n"
    "v_add_u32 v224, s53, v120\n"
    "v_mfma_f32_16x16x32_bf16 v[88:91], v[192:195], v[156:159], 0\n"
    "ds_read_b64_tr_b16 v[180:181], v226\n"
    "ds_read_b64_tr_b16 v[182:183], v226 offset:256\n"
    "v_mfma_f32_16x16x32_bf16 v[88:91], v[196:199], v[152:155], v[88:91]\n"
    "v_exp_f32 v15, v99\n"
    "v_mfma_f32_16x16x32_bf16 v[88:91], v[200:203], v[148:151], v[88:91]\n"
    "ds_read_b64_tr_b16 v[176:177], v227\n"
    "ds_read_b64_tr_b16 v[178:179], v227 offset:256\n"
    "v_mfma_f32_16x16x32_bf16 v[88:91], v[204:207], v[144:147], v[88:91]\n"
    "v_add_f32 v230, v230, v14\n"
    "v_exp_f32 v12, v100\n"
    "v_add_f32 v230, v230, v15\n"
    "v_cvt_pk_bf16_f32 v169, v14, v15\n"
    "v_add_u32 v225, s53, v121\n"
    "v_mfma_f32_16x16x32_bf16 v[84:87], v[220:223], v[128:131], 0\n"
    "ds_read_b64_tr_b16 v[192:193], v226 offset:512\n"
    "ds_read_b64_tr_b16 v[194:195], v226 offset:768\n"
    "v_mfma_f32_16x16x32_bf16 v[84:87], v[188:191], v[132:135], v[84:87]\n"
    "v_exp_f32 v13, v101\n"
    "v_mfma_f32_16x16x32_bf16 v[84:87], v[216:219], v[136:139], v[84:87]\n"
    "ds_read_b64_tr_b16 v[196:197], v227 offset:512\n"
    "ds_read_b64_tr_b16 v[198:199], v227 offset:768\n"
    "s_waitcnt lgkmcnt(8)\n"
    "v_mfma_f32_16x16x32_bf16 v[84:87], v[184:187], v[140:143], v[84:87]\n"
    "ds_read_b64_tr_b16 v[208:209], v226 offset:1024\n"
    "ds_read_b64_tr_b16 v[210:211], v226 offset:1280\n"
    "v_add_f32 v230, v230, v12\n"
    "v_exp_f32 v14, v102\n"
    "v_add_f32 v230, v230, v13\n"
    "v_mfma_f32_16x16x32_bf16 v[92:95], v[184:187], v[156:159], 0\n"
    "ds_read_b64_tr_b16 v[212:213], v227 offset:1024\n"
    "ds_read_b64_tr_b16 v[214:215], v227 offset:1280\n"
    "v_mfma_f32_16x16x32_bf16 v[92:95], v[216:219], v[152:155], v[92:95]\n"
    "v_cvt_pk_bf16_f32 v170, v12, v13\n"
    "v_mfma_f32_16x16x32_bf16 v[92:95], v[188:191], v[148:151], v[92:95]\n"
    "ds_read_b64_tr_b16 v[216:217], v226 offset:1536\n"
    "ds_read_b64_tr_b16 v[218:219], v226 offset:1792\n"
    "v_mfma_f32_16x16x32_bf16 v[92:95], v[220:223], v[144:147], v[92:95]\n"
    "v_exp_f32 v15, v103\n"
    "v_add_f32 v230, v230, v14\n"
    "v_exp_f32 v12, v104\n"
    "v_add_u32 v126, s54, v122\n"
    "s_waitcnt lgkmcnt(10)\n"
    "v_mfma_f32_16x16x32_bf16 v[16:19], v[180:183], v[160:163], v[16:19]\n"
    "ds_read_b64_tr_b16 v[220:221], v227 offset:1536\n"
    "ds_read_b64_tr_b16 v[222:223], v227 offset:1792\n"
    "v_add_f32 v230, v230, v15\n"
    "v_cvt_pk_bf16_f32 v171, v14, v15\n"
    "v_exp_f32 v13, v105\n"
    "v_mfma_f32_16x16x32_bf16 v[20:23], v[180:183], v[164:167], v[20:23]\n"
    "v_exp_f32 v14, v106\n"
    "v_add_f32 v231, v12, v13\n"
    "v_cvt_pk_bf16_f32 v172, v12, v13\n"
    "v_exp_f32 v15, v107\n"
    "v_add_u32 v127, s54, v123\n"
    "v_mfma_f32_16x16x32_bf16 v[28:31], v[176:179], v[164:167], v[28:31]\n"
    "v_max3_f32 v249, v80, v81, v82\n"
    "v_max3_f32 v0, v88, v89, v90\n"
    "v_max3_f32 v246, v83, v84, v85\n"
    "v_max3_f32 v247, v91, v92, v93\n"
    "v_add_u32 v226, s55, v122\n"
    "v_mfma_f32_16x16x32_bf16 v[24:27], v[176:179], v[160:163], v[24:27]\n"
    "v_max3_f32 v249, v249, v86, v87\n"
    "v_max3_f32 v0, v0, v94, v95\n"
    "v_max_f32 v249, v249, v246\n"
    "v_add_f32 v231, v231, v14\n"
    "v_add_u32 v227, s55, v123\n"
    "s_waitcnt lgkmcnt(8)\n"
    "v_mfma_f32_16x16x32_bf16 v[32:35], v[192:195], v[160:163], v[32:35]\n"
    "v_max_f32 v0, v0, v247\n"
    "v_mov_b32 v246, v249\n"
    "v_mov_b32 v247, v0\n"
    "s_nop 0\n"
    "v_permlane16_swap_b32 v249, v246\n"
    "v_exp_f32 v12, v108\n"
    "s_add_u32 s15, s15, 1\n"
    "v_mfma_f32_16x16x32_bf16 v[36:39], v[192:195], v[164:167], v[36:39]\n"
    "v_permlane16_swap_b32 v0, v247\n"
    "v_max_f32 v249, v249, v246\n"
    "v_max_f32 v0, v0, v247\n"
    "v_mov_b32 v246, v249\n"
    "v_add_f32 v231, v231, v15\n"
    "v_mfma_f32_16x16x32_bf16 v[44:47], v[196:199], v[164:167], v[44:47]\n"
    "v_mov_b32 v247, v0\n"
    "v_permlane32_swap_b32 v249, v246\n"
    "s_nop 0\n"
    "v_permlane32_swap_b32 v0, v247\n"
    "v_max_f32 v249, v249, v246\n"
    "v_max_f32 v0, v0, v247\n"
    "v_cvt_pk_bf16_f32 v173, v14, v15\n"
    "v_mfma_f32_16x16x32_bf16 v[40:43], v[196:199], v[160:163], v[40:43]\n"
    "v_exp_f32 v13, v109\n"
    "s_waitcnt lgkmcnt(0)\n"
    "s_barrier\n"
    "v_mfma_f32_16x16x32_bf16 v[48:51], v[208:211], v[160:163], v[48:51]\n"
    "ds_read_b128 v[176:179], v124\n"
    "ds_read_b128 v[180:183], v124 offset:512\n"
    "v_add_f32 v231, v231, v12\n"
    "v_exp_f32 v14, v110\n"
    "v_add_f32 v231, v231, v13\n"
    "v_mfma_f32_16x16x32_bf16 v[52:55], v[208:211], v[164:167], v[52:55]\n"
    "ds_read_b128 v[184:187], v124 offset:1024\n"
    "ds_read_b128 v[188:191], v124 offset:1536\n"
    "v_cvt_pk_bf16_f32 v174, v12, v13\n"
    "v_exp_f32 v15, v111\n"
    "v_add_f32 v231, v231, v14\n"
    "v_mfma_f32_16x16x32_bf16 v[60:63], v[212:215], v[164:167], v[60:63]\n"
    "v_add_f32 v231, v231, v15\n"
    "v_cvt_pk_bf16_f32 v175, v14, v15\n"
    "v_max_f32 v248, v230, v231\n"
    "s_add_u32 s66, s52, 0x8000\n"
    "s_sub_u32 s52, s52, 0xe000\n"
    "s_cselect_b32 s52, s66, s52\n"
    "v_mfma_f32_16x16x32_bf16 v[56:59], v[212:215], v[160:163], v[56:59]\n"
    "v_cmp_nge_f32 vcc, s70, v248\n"
    "v_sub_f32 v112, 0, v249\n"
    "v_sub_f32 v113, 0, v249\n"
    "v_sub_f32 v114, 0, v249\n"
    "v_sub_f32 v115, 0, v249\n"
    "v_sub_f32 v116, 0, v0\n"
    "v_sub_f32 v117, 0, v0\n"
    "v_mfma_f32_16x16x32_bf16 v[64:67], v[216:219], v[160:163], v[64:67]\n"
    "ds_read_b128 v[192:195], v125\n"
    "v_sub_f32 v118, 0, v0\n"
    "v_sub_f32 v119, 0, v0\n"
    "v_sub_f32 v80, v80, v249\n"
    "v_sub_f32 v81, v81, v249\n"
    "v_sub_f32 v82, v82, v249\n"
    "v_mfma_f32_16x16x32_bf16 v[68:71], v[216:219], v[164:167], v[68:71]\n"
    "ds_read_b128 v[196:199], v125 offset:512\n"
    "v_sub_f32 v83, v83, v249\n"
    "v_sub_f32 v84, v84, v249\n"
    "v_sub_f32 v85, v85, v249\n"
    "v_sub_f32 v86, v86, v249\n"
    "v_sub_f32 v87, v87, v249\n"
    "v_mfma_f32_16x16x32_bf16 v[76:79], v[220:223], v[164:167], v[76:79]\n"
    "ds_read_b128 v[200:203], v125 offset:1024\n"
    "v_sub_f32 v88, v88, v0\n"
    "v_sub_f32 v89, v89, v0\n"
    "v_sub_f32 v90, v90, v0\n"
    "v_sub_f32 v91, v91, v0\n"
    "v_sub_f32 v92, v92, v0\n"
    "v_mfma_f32_16x16x32_bf16 v[72:75], v[220:223], v[160:163], v[72:75]\n"
    "v_sub_f32 v93, v93, v0\n"
    "v_sub_f32 v94, v94, v0\n"
    "v_sub_f32 v95, v95, v0\n"
    "s_cbranch_vccnz .Lkrare_25\n"
    ".Lkrret_26:\n"
    "ds_read_b128 v[204:207], v125 offset:1536\n"
    "s_waitcnt lgkmcnt(4)\n"
    "v_mfma_f32_16x16x32_bf16 v[96:99], v[176:179], v[128:131], v[112:115]\n"
    "v_add_f32 v4, v228, v230\n"
    "v_add_f32 v5, v229, v231\n"
    "v_mfma_f32_16x16x32_bf16 v[96:99], v[180:183], v[132:135], v[96:99]\n"
    "v_mov_b32 v228, 0\n"
    "v_mov_b32 v229, 0\n"
    "v_mfma_f32_16x16x32_bf16 v[96:99], v[184:187], v[136:139], v[96:99]\n"
    "v_exp_f32 v12, v80\n"
    "v_mfma_f32_16x16x32_bf16 v[96:99], v[188:191], v[140:143], v[96:99]\n"
    "s_add_u32 s66, s53, 0x8000\n"
    "s_sub_u32 s53, s53, 0xe000\n"
    "s_cselect_b32 s53, s66, s53\n"
    "v_mfma_f32_16x16x32_bf16 v[104:107], v[188:191], v[156:159], v[116:119]\n"
    "ds_read_b64_tr_b16 v[208:209], v126\n"
    "ds_read_b64_tr_b16 v[210:211], v126 offset:256\n"
    "v_mfma_f32_16x16x32_bf16 v[104:107], v[184:187], v[152:155], v[104:107]\n"
    "v_exp_f32 v13, v81\n"
    "v_mfma_f32_16x16x32_bf16 v[104:107], v[180:183], v[148:151], v[104:107]\n"
    "ds_read_b64_tr_b16 v[212:213], v127\n"
    "ds_read_b64_tr_b16 v[214:215], v127 offset:256\n"
    "v_mfma_f32_16x16x32_bf16 v[104:107], v[176:179], v[144:147], v[104:107]\n"
    "v_exp_f32 v14, v82\n"
    "v_add_f32 v230, v12, v13\n"
    "v_cvt_pk_bf16_f32 v160, v12, v13\n"
    "v_mov_b32 v250, v4\n"
    "s_waitcnt lgkmcnt(4)\n"
    "v_mfma_f32_16x16x32_bf16 v[100:103], v[192:195], v[128:131], v[112:115]\n"
    "ds_read_b64_tr_b16 v[216:217], v126 offset:512\n"
    "ds_read_b64_tr_b16 v[218:219], v126 offset:768\n"
    "v_mfma_f32_16x16x32_bf16 v[100:103], v[196:199], v[132:135], v[100:103]\n"
    "v_exp_f32 v15, v83\n"
    "v_mfma_f32_16x16x32_bf16 v[100:103], v[200:203], v[136:139], v[100:103]\n"
    "ds_read_b64_tr_b16 v[220:221], v127 offset:512\n"
    "ds_read_b64_tr_b16 v[222:223], v127 offset:768\n"
    "v_mfma_f32_16x16x32_bf16 v[100:103], v[204:207], v[140:143], v[100:103]\n"
    "v_add_f32 v230, v230, v14\n"
    "v_mov_b32 v252, v5\n"
    "v_permlane16_swap_b32 v4, v250\n"
    "s_add_u32 s66, s54, 0x8000\n"
    "s_sub_u32 s54, s54, 0xe000\n"
    "s_cselect_b32 s54, s66, s54\n"
    "v_mfma_f32_16x16x32_bf16 v[108:111], v[204:207], v[156:159], v[116:119]\n"
    "ds_read_b64_tr_b16 v[188:189], v126 offset:1024\n"
    "ds_read_b64_tr_b16 v[190:191], v126 offset:1280\n"
    "v_mfma_f32_16x16x32_bf16 v[108:111], v[200:203], v[152:155], v[108:111]\n"
    "v_exp_f32 v12, v84\n"
    "v_mfma_f32_16x16x32_bf16 v[108:111], v[196:199], v[148:151], v[108:111]\n"
    "ds_read_b64_tr_b16 v[184:185], v127 offset:1024\n"
    "ds_read_b64_tr_b16 v[186:187], v127 offset:1280\n"
    "v_mfma_f32_16x16x32_bf16 v[108:111], v[192:195], v[144:147], v[108:111]\n"
    "v_add_f32 v230, v230, v15\n"
    "v_cvt_pk_bf16_f32 v161, v14, v15\n"
    "v_exp_f32 v13, v85\n"
    "v_permlane16_swap_b32 v5, v252\n"
    "s_waitcnt lgkmcnt(8)\n"
    "v_mfma_f32_16x16x32_bf16 v[16:19], v[208:211], v[168:171], v[16:19]\n"
    "ds_read_b64_tr_b16 v[180:181], v126 offset:1536\n"
    "ds_read_b64_tr_b16 v[182:183], v126 offset:1792\n"
    "v_add_f32 v230, v230, v12\n"
    "v_exp_f32 v14, v86\n"
    "v_add_f32 v4, v4, v250\n"
    "v_mfma_f32_16x16x32_bf16 v[20:23], v[208:211], v[172:175], v[20:23]\n"
    "v_add_f32 v5, v5, v252\n"
    "v_mov_b32 v250, v4\n"
    "s_add_u32 s66, s55, 0x8000\n"
    "s_sub_u32 s55, s55, 0xe000\n"
    "s_cselect_b32 s55, s66, s55\n"
    "v_mfma_f32_16x16x32_bf16 v[28:31], v[212:215], v[172:175], v[28:31]\n"
    "ds_read_b64_tr_b16 v[176:177], v127 offset:1536\n"
    "ds_read_b64_tr_b16 v[178:179], v127 offset:1792\n"
    "v_add_f32 v230, v230, v13\n"
    "v_cvt_pk_bf16_f32 v162, v12, v13\n"
    "v_mov_b32 v252, v5\n"
    "v_permlane32_swap_b32 v4, v250\n"
    "v_mfma_f32_16x16x32_bf16 v[24:27], v[212:215], v[168:171], v[24:27]\n"
    "v_exp_f32 v15, v87\n"
    "v_add_f32 v230, v230, v14\n"
    "v_exp_f32 v12, v88\n"
    "s_waitcnt lgkmcnt(8)\n"
    "v_mfma_f32_16x16x32_bf16 v[32:35], v[216:219], v[168:171], v[32:35]\n"
    "ds_read_b128 v[204:207], v224\n"
    "v_add_f32 v230, v230, v15\n"
    "v_cvt_pk_bf16_f32 v163, v14, v15\n"
    "v_permlane32_swap_b32 v5, v252\n"
    "v_add_f32 v4, v4, v250\n"
    "v_add_f32 v5, v5, v252\n"
    "v_mfma_f32_16x16x32_bf16 v[36:39], v[216:219], v[172:175], v[36:39]\n"
    "ds_read_b128 v[200:203], v224 offset:512\n"
    "v_exp_f32 v13, v89\n"
    "v_exp_f32 v14, v90\n"
    "v_mfma_f32_16x16x32_bf16 v[44:47], v[220:223], v[172:175], v[44:47]\n"
    "ds_read_b128 v[196:199], v224 offset:1024\n"
    "v_add_f32 v231, v12, v13\n"
    "v_cvt_pk_bf16_f32 v164, v12, v13\n"
    "v_rcp_f32 v250, v4\n"
    "v_rcp_f32 v252, v5\n"
    "v_mov_b32 v251, v250\n"
    "v_mfma_f32_16x16x32_bf16 v[40:43], v[220:223], v[168:171], v[40:43]\n"
    "ds_read_b128 v[192:195], v224 offset:1536\n"
    "v_exp_f32 v15, v91\n"
    "v_add_f32 v231, v231, v14\n"
    "v_mov_b32 v253, v252\n"
    "s_waitcnt lgkmcnt(8)\n"
    "v_mfma_f32_16x16x32_bf16 v[48:51], v[188:191], v[168:171], v[48:51]\n"
    "v_exp_f32 v12, v92\n"
    "v_add_f32 v231, v231, v15\n"
    "v_add_u32 v124, s52, v120\n"
    "v_mfma_f32_16x16x32_bf16 v[52:55], v[188:191], v[172:175], v[52:55]\n"
    "v_cvt_pk_bf16_f32 v165, v14, v15\n"
    "v_exp_f32 v13, v93\n"
    "v_add_u32 v125, s52, v121\n"
    "v_mfma_f32_16x16x32_bf16 v[60:63], v[184:187], v[172:175], v[60:63]\n"
    "v_add_f32 v231, v231, v12\n"
    "v_exp_f32 v14, v94\n"
    "v_add_u32 v126, s54, v122\n"
    "v_mfma_f32_16x16x32_bf16 v[56:59], v[184:187], v[168:171], v[56:59]\n"
    "v_add_f32 v231, v231, v13\n"
    "v_cvt_pk_bf16_f32 v166, v12, v13\n"
    "v_exp_f32 v15, v95\n"
    "v_add_u32 v127, s54, v123\n"
    "s_waitcnt lgkmcnt(4)\n"
    "v_mfma_f32_16x16x32_bf16 v[64:67], v[180:183], v[168:171], v[64:67]\n"
    "ds_read_b128 v[220:223], v225\n"
    "v_add_f32 v231, v231, v14\n"
    "v_add_f32 v231, v231, v15\n"
    "v_mfma_f32_16x16x32_bf16 v[68:71], v[180:183], v[172:175], v[68:71]\n"
    "ds_read_b128 v[188:191], v225 offset:512\n"
    "v_cvt_pk_bf16_f32 v167, v14, v15\n"
    "v_max_f32 v248, v230, v231\n"
    "v_cmp_nge_f32 vcc, s70, v248\n"
    "v_mfma_f32_16x16x32_bf16 v[76:79], v[176:179], v[172:175], v[76:79]\n"
    "ds_read_b128 v[216:219], v225 offset:1024\n"
    "v_mfma_f32_16x16x32_bf16 v[72:75], v[176:179], v[168:171], v[72:75]\n"
    "s_cbranch_vccnz .Lkrare_27\n"
    ".Lkrret_28:\n"
    "ds_read_b128 v[184:187], v225 offset:1536\n"
    "s_waitcnt lgkmcnt(4)\n"
    "v_mfma_f32_16x16x32_bf16 v[80:83], v[204:207], v[128:131], v[112:115]\n"
    "v_mul_f32 v16, v16, v250\n"
    "v_mul_f32 v17, v17, v250\n"
    "v_mfma_f32_16x16x32_bf16 v[80:83], v[200:203], v[132:135], v[80:83]\n"
    "v_mul_f32 v18, v18, v250\n"
    "v_mul_f32 v19, v19, v250\n"
    "v_mfma_f32_16x16x32_bf16 v[80:83], v[196:199], v[136:139], v[80:83]\n"
    "v_mul_f32 v24, v24, v250\n"
    "v_mul_f32 v25, v25, v250\n"
    "v_mfma_f32_16x16x32_bf16 v[80:83], v[192:195], v[140:143], v[80:83]\n"
    "v_mul_f32 v26, v26, v250\n"
    "v_mul_f32 v27, v27, v250\n"
    "v_cvt_pk_bf16_f32 v232, v16, v17\n"
    "v_cvt_pk_bf16_f32 v233, v18, v19\n"
    "v_cvt_pk_bf16_f32 v234, v24, v25\n"
    "v_cvt_pk_bf16_f32 v235, v26, v27\n"
    "buffer_store_dwordx4 v[232:235], v6, s[36:39], s84 offen sc1\n"
    "v_mul_f32 v20, v20, v252\n"
    "v_mul_f32 v21, v21, v252\n"
    "v_mul_f32 v22, v22, v252\n"
    "v_add_f32 v228, v228, v230\n"
    "v_add_f32 v229, v229, v231\n"
    "v_add_u32 v224, s53, v120\n"
    "v_mfma_f32_16x16x32_bf16 v[88:91], v[192:195], v[156:159], v[116:119]\n"
    "ds_read_b64_tr_b16 v[180:181], v226\n"
    "ds_read_b64_tr_b16 v[182:183], v226 offset:256\n"
    "v_mfma_f32_16x16x32_bf16 v[88:91], v[196:199], v[152:155], v[88:91]\n"
    "v_mul_f32 v23, v23, v252\n"
    "v_mul_f32 v28, v28, v252\n"
    "v_mfma_f32_16x16x32_bf16 v[88:91], v[200:203], v[148:151], v[88:91]\n"
    "ds_read_b64_tr_b16 v[176:177], v227\n"
    "ds_read_b64_tr_b16 v[178:179], v227 offset:256\n"
    "v_mfma_f32_16x16x32_bf16 v[88:91], v[204:207], v[144:147], v[88:91]\n"
    "v_mul_f32 v29, v29, v252\n"
    "v_mul_f32 v30, v30, v252\n"
    "v_mul_f32 v31, v31, v252\n"
    "v_cvt_pk_bf16_f32 v236, v20, v21\n"
    "v_cvt_pk_bf16_f32 v237, v22, v23\n"
    "v_cvt_pk_bf16_f32 v238, v28, v29\n"
    "v_cvt_pk_bf16_f32 v239, v30, v31\n"
    "buffer_store_dwordx4 v[236:239], v6, s[36:39], s85 offen sc1\n"
    "v_mul_f32 v32, v32, v250\n"
    "v_mul_f32 v33, v33, v250\n"
    "v_exp_f32 v12, v96\n"
    "v_add_u32 v225, s53, v121\n"
    "s_waitcnt lgkmcnt(4)\n"
    "v_mfma_f32_16x16x32_bf16 v[84:87], v[220:223], v[128:131], v[112:115]\n"
    "ds_read_b64_tr_b16 v[192:193], v226 offset:512\n"
    "ds_read_b64_tr_b16 v[194:195], v226 offset:768\n"
    "v_mfma_f32_16x16x32_bf16 v[84:87], v[188:191], v[132:135], v[84:87]\n"
    "v_mul_f32 v34, v34, v250\n"
    "v_mul_f32 v35, v35, v250\n"
    "v_mfma_f32_16x16x32_bf16 v[84:87], v[216:219], v[136:139], v[84:87]\n"
    "ds_read_b64_tr_b16 v[196:197], v227 offset:512\n"
    "ds_read_b64_tr_b16 v[198:199], v227 offset:768\n"
    "v_mfma_f32_16x16x32_bf16 v[84:87], v[184:187], v[140:143], v[84:87]\n"
    "ds_read_b64_tr_b16 v[208:209], v226 offset:1024\n"
    "ds_read_b64_tr_b16 v[210:211], v226 offset:1280\n"
    "v_mul_f32 v40, v40, v250\n"
    "v_mul_f32 v41, v41, v250\n"
    "v_mul_f32 v42, v42, v250\n"
    "v_mul_f32 v43, v43, v250\n"
    "v_cvt_pk_bf16_f32 v240, v32, v33\n"
    "v_cvt_pk_bf16_f32 v241, v34, v35\n"
    "v_cvt_pk_bf16_f32 v242, v40, v41\n"
    "v_cvt_pk_bf16_f32 v243, v42, v43\n"
    "buffer_store_dwordx4 v[240:243], v6, s[36:39], s84 offen offset:1024 sc1\n"
    "v_mul_f32 v36, v36, v252\n"
    "v_mfma_f32_16x16x32_bf16 v[92:95], v[184:187], v[156:159], v[116:119]\n"
    "ds_read_b64_tr_b16 v[212:213], v227 offset:1024\n"
    "ds_read_b64_tr_b16 v[214:215], v227 offset:1280\n"
    "v_mfma_f32_16x16x32_bf16 v[92:95], v[216:219], v[152:155], v[92:95]\n"
    "v_mul_f32 v37, v37, v252\n"
    "v_mul_f32 v38, v38, v252\n"
    "v_mfma_f32_16x16x32_bf16 v[92:95], v[188:191], v[148:151], v[92:95]\n"
    "ds_read_b64_tr_b16 v[216:217], v226 offset:1536\n"
    "ds_read_b64_tr_b16 v[218:219], v226 offset:1792\n"
    "s_barrier\n"
    "v_mfma_f32_16x16x32_bf16 v[92:95], v[220:223], v[144:147], v[92:95]\n"
    "v_mul_f32 v39, v39, v252\n"
    "v_mul_f32 v44, v44, v252\n"
    "v_mul_f32 v45, v45, v252\n"
    "v_mul_f32 v46, v46, v252\n"
    "v_mul_f32 v47, v47, v252\n"
    "v_cvt_pk_bf16_f32 v232, v36, v37\n"
    "v_cvt_pk_bf16_f32 v233, v38, v39\n"
    "v_cvt_pk_bf16_f32 v234, v44, v45\n"
    "v_cvt_pk_bf16_f32 v235, v46, v47\n"
    "buffer_store_dwordx4 v[232:235], v6, s[36:39], s85 offen offset:1024 sc1\n"
    "v_exp_f32 v13, v97\n"
    "s_add_u32 s15, s15, 1\n"
    "s_waitcnt lgkmcnt(10)\n"
    "v_mfma_f32_16x16x32_bf16 v[16:19], v[180:183], v[160:163], 0\n"
    "ds_read_b64_tr_b16 v[220:221], v227 offset:1536\n"
    "ds_read_b64_tr_b16 v[222:223], v227 offset:1792\n"
    "v_mul_f32 v48, v48, v250\n"
    "v_mul_f32 v49, v49, v250\n"
    "v_mul_f32 v50, v50, v250\n"
    "v_mul_f32 v51, v51, v250\n"
    "v_mul_f32 v56, v56, v250\n"
    "v_mul_f32 v57, v57, v250\n"
    "v_exp_f32 v14, v98\n"
    "v_add_f32 v230, v12, v13\n"
    "v_cvt_pk_bf16_f32 v168, v12, v13\n"
    "v_mfma_f32_16x16x32_bf16 v[20:23], v[180:183], v[164:167], 0\n"
    "v_mul_f32 v58, v58, v250\n"
    "v_mul_f32 v59, v59, v250\n"
    "v_cvt_pk_bf16_f32 v236, v48, v49\n"
    "v_cvt_pk_bf16_f32 v237, v50, v51\n"
    "v_cvt_pk_bf16_f32 v238, v56, v57\n"
    "v_cvt_pk_bf16_f32 v239, v58, v59\n"
    "v_exp_f32 v15, v99\n"
    "v_add_f32 v230, v230, v14\n"
    "v_exp_f32 v12, v100\n"
    "v_add_f32 v230, v230, v15\n"
    "v_add_u32 v226, s55, v122\n"
    "v_mfma_f32_16x16x32_bf16 v[28:31], v[176:179], v[164:167], 0\n"
    "buffer_store_dwordx4 v[236:239], v6, s[36:39], s84 offen offset:2048 sc1\n"
    "v_mul_f32 v52, v52, v252\n"
    "v_mul_f32 v53, v53, v252\n"
    "v_mul_f32 v54, v54, v252\n"
    "v_mul_f32 v55, v55, v252\n"
    "v_mul_f32 v60, v60, v252\n"
    "v_mul_f32 v61, v61, v252\n"
    "v_cvt_pk_bf16_f32 v169, v14, v15\n"
    "v_exp_f32 v13, v101\n"
    "v_add_f32 v230, v230, v12\n"
    "v_add_u32 v227, s55, v123\n"
    "v_mfma_f32_16x16x32_bf16 v[24:27], v[176:179], v[160:163], 0\n"
    "v_mul_f32 v62, v62, v252\n"
    "v_mul_f32 v63, v63, v252\n"
    "v_cvt_pk_bf16_f32 v240, v52, v53\n"
    "v_cvt_pk_bf16_f32 v241, v54, v55\n"
    "v_cvt_pk_bf16_f32 v242, v60, v61\n"
    "v_cvt_pk_bf16_f32 v243, v62, v63\n"
    "buffer_store_dwordx4 v[240:243], v6, s[36:39], s85 offen offset:2048 sc1\n"
    "v_exp_f32 v14, v102\n"
    "v_add_f32 v230, v230, v13\n"
    "v_cvt_pk_bf16_f32 v170, v12, v13\n"
    "v_exp_f32 v15, v103\n"
    "s_waitcnt lgkmcnt(8)\n"
    "v_mfma_f32_16x16x32_bf16 v[32:35], v[192:195], v[160:163], 0\n"
    "v_mul_f32 v64, v64, v250\n"
    "v_mul_f32 v65, v65, v250\n"
    "v_mul_f32 v66, v66, v250\n"
    "v_mul_f32 v67, v67, v250\n"
    "v_mul_f32 v72, v72, v250\n"
    "v_mul_f32 v73, v73, v250\n"
    "v_mul_f32 v74, v74, v250\n"
    "v_mul_f32 v75, v75, v250\n"
    "v_add_f32 v230, v230, v14\n"
    "v_exp_f32 v12, v104\n"
    "v_add_f32 v230, v230, v15\n"
    "v_mfma_f32_16x16x32_bf16 v[36:39], v[192:195], v[164:167], 0\n"
    "v_cvt_pk_bf16_f32 v232, v64, v65\n"
    "v_cvt_pk_bf16_f32 v233, v66, v67\n"
    "v_cvt_pk_bf16_f32 v234, v72, v73\n"
    "v_cvt_pk_bf16_f32 v235, v74, v75\n"
    "buffer_store_dwordx4 v[232:235], v6, s[36:39], s84 offen offset:3072 sc1\n"
    "v_mul_f32 v68, v68, v252\n"
    "v_mul_f32 v69, v69, v252\n"
    "v_cvt_pk_bf16_f32 v171, v14, v15\n"
    "v_exp_f32 v13, v105\n"
    "v_exp_f32 v14, v106\n"
    "v_add_f32 v231, v12, v13\n"
    "v_mfma_f32_16x16x32_bf16 v[44:47], v[196:199], v[164:167], 0\n"
    "v_mul_f32 v70, v70, v252\n"
    "v_mul_f32 v71, v71, v252\n"
    "v_mul_f32 v76, v76, v252\n"
    "v_mul_f32 v77, v77, v252\n"
    "v_mul_f32 v78, v78, v252\n"
    "v_mul_f32 v79, v79, v252\n"
    "v_cvt_pk_bf16_f32 v236, v68, v69\n"
    "v_cvt_pk_bf16_f32 v237, v70, v71\n"
    "v_cvt_pk_bf16_f32 v172, v12, v13\n"
    "v_exp_f32 v15, v107\n"
    "v_add_f32 v231, v231, v14\n"
    "v_mfma_f32_16x16x32_bf16 v[40:43], v[196:199], v[160:163], 0\n"
    "v_cvt_pk_bf16_f32 v238, v76, v77\n"
    "v_cvt_pk_bf16_f32 v239, v78, v79\n"
    "buffer_store_dwordx4 v[236:239], v6, s[36:39], s85 offen offset:3072 sc1\n"
    "v_exp_f32 v12, v108\n"
    "v_add_f32 v231, v231, v15\n"
    "v_cvt_pk_bf16_f32 v173, v14, v15\n"
    "v_exp_f32 v13, v109\n"
    "s_add_u32 s84, s84, 0x1000\n"
    "s_add_u32 s85, s85, 0x1000\n"
    "s_waitcnt lgkmcnt(0)\n"
    "s_barrier\n"
    "v_mfma_f32_16x16x32_bf16 v[48:51], v[208:211], v[160:163], 0\n"
    "ds_read_b128 v[176:179], v124\n"
    "ds_read_b128 v[180:183], v124 offset:512\n"
    "v_add_f32 v231, v231, v12\n"
    "v_mfma_f32_16x16x32_bf16 v[52:55], v[208:211], v[164:167], 0\n"
    "ds_read_b128 v[184:187], v124 offset:1024\n"
    "ds_read_b128 v[188:191], v124 offset:1536\n"
    "v_exp_f32 v14, v110\n"
    "v_mfma_f32_16x16x32_bf16 v[60:63], v[212:215], v[164:167], 0\n"
    "v_add_f32 v231, v231, v13\n"
    "s_add_u32 s66, s52, 0x8000\n"
    "s_sub_u32 s52, s52, 0xe000\n"
    "s_cselect_b32 s52, s66, s52\n"
    "v_mfma_f32_16x16x32_bf16 v[56:59], v[212:215], v[160:163], 0\n"
    "v_cvt_pk_bf16_f32 v174, v12, v13\n"
    "v_exp_f32 v15, v111\n"
    "v_mfma_f32_16x16x32_bf16 v[64:67], v[216:219], v[160:163], 0\n"
    "ds_read_b128 v[192:195], v125\n"
    "v_add_f32 v231, v231, v14\n"
    "v_add_f32 v231, v231, v15\n"
    "v_cvt_pk_bf16_f32 v175, v14, v15\n"
    "v_mfma_f32_16x16x32_bf16 v[68:71], v[216:219], v[164:167], 0\n"
    "ds_read_b128 v[196:199], v125 offset:512\n"
    "v_max_f32 v248, v230, v231\n"
    "v_cmp_nge_f32 vcc, s70, v248\n"
    "v_mfma_f32_16x16x32_bf16 v[76:79], v[220:223], v[164:167], 0\n"
    "ds_read_b128 v[200:203], v125 offset:1024\n"
    "v_mfma_f32_16x16x32_bf16 v[72:75], v[220:223], v[160:163], 0\n"
    "s_cbranch_vccnz .Lkrare_29\n"
    ".Lkrret_30:\n"
    "ds_read_b128 v[204:207], v125 offset:1536\n"
    "s_waitcnt lgkmcnt(4)\n"
    "v_mfma_f32_16x16x32_bf16 v[96:99], v[176:179], v[128:131], v[112:115]\n"
    "v_add_f32 v228, v228, v230\n"
    "v_add_f32 v229, v229, v231\n"
    "v_mfma_f32_16x16x32_bf16 v[96:99], v[180:183], v[132:135], v[96:99]\n"
    "v_exp_f32 v12, v80\n"
    "v_mfma_f32_16x16x32_bf16 v[96:99], v[184:187], v[136:139], v[96:99]\n"
    "v_exp_f32 v13, v81\n"
    "v_mfma_f32_16x16x32_bf16 v[96:99], v[188:191], v[140:143], v[96:99]\n"
    "s_add_u32 s66, s53, 0x8000\n"
    "s_sub_u32 s53, s53, 0xe000\n"
    "s_cselect_b32 s53, s66, s53\n"
    "v_mfma_f32_16x16x32_bf16 v[104:107], v[188:191], v[156:159], v[116:119]\n"
    "ds_read_b64_tr_b16 v[208:209], v126\n"
    "ds_read_b64_tr_b16 v[210:211], v126 offset:256\n"
    "v_mfma_f32_16x16x32_bf16 v[104:107], v[184:187], v[152:155], v[104:107]\n"
    "v_exp_f32 v14, v82\n"
    "v_mfma_f32_16x16x32_bf16 v[104:107], v[180:183], v[148:151], v[104:107]\n"
    "ds_read_b64_tr_b16 v[212:213], v127\n"
    "ds_read_b64_tr_b16 v[214:215], v127 offset:256\n"
    "v_mfma_f32_16x16x32_bf16 v[104:107], v[176:179], v[144:147], v[104:107]\n"
    "v_add_f32 v230, v12, v13\n"
    "v_cvt_pk_bf16_f32 v160, v12, v13\n"
    "v_exp_f32 v15, v83\n"
    "s_waitcnt lgkmcnt(4)\n"
    "v_mfma_f32_16x16x32_bf16 v[100:103], v[192:195], v[128:131], v[112:115]\n"
    "ds_read_b64_tr_b16 v[216:217], v126 offset:512\n"
    "ds_read_b64_tr_b16 v[218:219], v126 offset:768\n"
    "v_mfma_f32_16x16x32_bf16 v[100:103], v[196:199], v[132:135], v[100:103]\n"
    "v_add_f32 v230, v230, v14\n"
    "v_exp_f32 v12, v84\n"
    "v_mfma_f32_16x16x32_bf16 v[100:103], v[200:203], v[136:139], v[100:103]\n"
    "ds_read_b64_tr_b16 v[220:221], v127 offset:512\n"
    "ds_read_b64_tr_b16 v[222:223], v127 offset:768\n"
    "v_mfma_f32_16x16x32_bf16 v[100:103], v[204:207], v[140:143], v[100:103]\n"
    "s_add_u32 s66, s54, 0x8000\n"
    "s_sub_u32 s54, s54, 0xe000\n"
    "s_cselect_b32 s54, s66, s54\n"
    "v_mfma_f32_16x16x32_bf16 v[108:111], v[204:207], v[156:159], v[116:119]\n"
    "ds_read_b64_tr_b16 v[188:189], v126 offset:1024\n"
    "ds_read_b64_tr_b16 v[190:191], v126 offset:1280\n"
    "v_mfma_f32_16x16x32_bf16 v[108:111], v[200:203], v[152:155], v[108:111]\n"
    "v_add_f32 v230, v230, v15\n"
    "v_cvt_pk_bf16_f32 v161, v14, v15\n"
    "v_mfma_f32_16x16x32_bf16 v[108:111], v[196:199], v[148:151], v[108:111]\n"
    "ds_read_b64_tr_b16 v[184:185], v127 offset:1024\n"
    "ds_read_b64_tr_b16 v[186:187], v127 offset:1280\n"
    "v_mfma_f32_16x16x32_bf16 v[108:111], v[192:195], v[144:147], v[108:111]\n"
    "v_exp_f32 v13, v85\n"
    "v_add_f32 v230, v230, v12\n"
    "s_waitcnt lgkmcnt(8)\n"
    "v_mfma_f32_16x16x32_bf16 v[16:19], v[208:211], v[168:171], v[16:19]\n"
    "ds_read_b64_tr_b16 v[180:181], v126 offset:1536\n"
    "ds_read_b64_tr_b16 v[182:183], v126 offset:1792\n"
    "v_exp_f32 v14, v86\n"
    "v_add_f32 v230, v230, v13\n"
    "v_mfma_f32_16x16x32_bf16 v[20:23], v[208:211], v[172:175], v[20:23]\n"
    "s_add_u32 s66, s55, 0x8000\n"
    "s_sub_u32 s55, s55, 0xe000\n"
    "s_cselect_b32 s55, s66, s55\n"
    "v_mfma_f32_16x16x32_bf16 v[28:31], v[212:215], v[172:175], v[28:31]\n"
    "ds_read_b64_tr_b16 v[176:177], v127 offset:1536\n"
    "ds_read_b64_tr_b16 v[178:179], v127 offset:1792\n"
    "v_cvt_pk_bf16_f32 v162, v12, v13\n"
    "v_exp_f32 v15, v87\n"
    "v_mfma_f32_16x16x32_bf16 v[24:27], v[212:215], v[168:171], v[24:27]\n"
    "v_add_f32 v230, v230, v14\n"
    "v_exp_f32 v12, v88\n"
    "s_waitcnt lgkmcnt(8)\n"
    "v_mfma_f32_16x16x32_bf16 v[32:35], v[216:219], v[168:171], v[32:35]\n"
    "ds_read_b128 v[204:207], v224\n"
    "v_add_f32 v230, v230, v15\n"
    "v_cvt_pk_bf16_f32 v163, v14, v15\n"
    "v_exp_f32 v13, v89\n"
    "v_mfma_f32_16x16x32_bf16 v[36:39], v[216:219], v[172:175], v[36:39]\n"
    "ds_read_b128 v[200:203], v224 offset:512\n"
    "v_exp_f32 v14, v90\n"
    "v_mfma_f32_16x16x32_bf16 v[44:47], v[220:223], v[172:175], v[44:47]\n"
    "ds_read_b128 v[196:199], v224 offset:1024\n"
    "v_add_f32 v231, v12, v13\n"
    "v_cvt_pk_bf16_f32 v164, v12, v13\n"
    "v_mfma_f32_16x16x32_bf16 v[40:43], v[220:223], v[168:171], v[40:43]\n"
    "ds_read_b128 v[192:195], v224 offset:1536\n"
    "v_exp_f32 v15, v91\n"
    "v_add_f32 v231, v231, v14\n"
    "s_waitcnt lgkmcnt(8)\n"
    "v_mfma_f32_16x16x32_bf16 v[48:51], v[188:191], v[168:171], v[48:51]\n"
    "v_exp_f32 v12, v92\n"
    "v_add_f32 v231, v231, v15\n"
    "v_cvt_pk_bf16_f32 v165, v14, v15\n"
    "v_add_u32 v124, s52, v120\n"
    "v_mfma_f32_16x16x32_bf16 v[52:55], v[188:191], v[172:175], v[52:55]\n"
    "v_exp_f32 v13, v93\n"
    "v_add_u32 v125, s52, v121\n"
    "v_mfma_f32_16x16x32_bf16 v[60:63], v[184:187], v[172:175], v[60:63]\n"
    "v_add_f32 v231, v231, v12\n"
    "v_exp_f32 v14, v94\n"
    "v_add_u32 v126, s54, v122\n"
    "v_mfma_f32_16x16x32_bf16 v[56:59], v[184:187], v[168:171], v[56:59]\n"
    "v_add_f32 v231, v231, v13\n"
    "v_cvt_pk_bf16_f32 v166, v12, v13\n"
    "v_exp_f32 v15, v95\n"
    "v_add_u32 v127, s54, v123\n"
    "s_waitcnt lgkmcnt(4)\n"
    "v_mfma_f32_16x16x32_bf16 v[64:67], v[180:183], v[168:171], v[64:67]\n"
    "ds_read_b128 v[220:223], v225\n"
    "v_add_f32 v231, v231, v14\n"
    "v_add_f32 v231, v231, v15\n"
    "v_mfma_f32_16x16x32_bf16 v[68:71], v[180:183], v[172:175], v[68:71]\n"
    "ds_read_b128 v[188:191], v225 offset:512\n"
    "v_cvt_pk_bf16_f32 v167, v14, v15\n"
    "v_max_f32 v248, v230, v231\n"
    "v_cmp_nge_f32 vcc, s70, v248\n"
    "v_mfma_f32_16x16x32_bf16 v[76:79], v[176:179], v[172:175], v[76:79]\n"
    "ds_read_b128 v[216:219], v225 offset:1024\n"
    "v_mfma_f32_16x16x32_bf16 v[72:75], v[176:179], v[168:171], v[72:75]\n"
    "s_cbranch_vccnz .Lkrare_31\n"
    ".Lkrret_32:\n"
    "ds_read_b128 v[184:187], v225 offset:1536\n"
    "s_waitcnt lgkmcnt(4)\n"
    "v_mfma_f32_16x16x32_bf16 v[80:83], v[204:207], v[128:131], v[112:115]\n"
    "v_add_f32 v228, v228, v230\n"
    "v_add_f32 v229, v229, v231\n"
    "v_mfma_f32_16x16x32_bf16 v[80:83], v[200:203], v[132:135], v[80:83]\n"
    "v_exp_f32 v12, v96\n"
    "v_mfma_f32_16x16x32_bf16 v[80:83], v[196:199], v[136:139], v[80:83]\n"
    "v_exp_f32 v13, v97\n"
    "v_mfma_f32_16x16x32_bf16 v[80:83], v[192:195], v[140:143], v[80:83]\n"
    "v_exp_f32 v14, v98\n"
    "v_add_f32 v230, v12, v13\n"
    "v_add_u32 v224, s53, v120\n"
    "v_mfma_f32_16x16x32_bf16 v[88:91], v[192:195], v[156:159], v[116:119]\n"
    "ds_read_b64_tr_b16 v[180:181], v226\n"
    "ds_read_b64_tr_b16 v[182:183], v226 offset:256\n"
    "v_mfma_f32_16x16x32_bf16 v[88:91], v[196:199], v[152:155], v[88:91]\n"
    "v_cvt_pk_bf16_f32 v168, v12, v13\n"
    "v_mfma_f32_16x16x32_bf16 v[88:91], v[200:203], v[148:151], v[88:91]\n"
    "ds_read_b64_tr_b16 v[176:177], v227\n"
    "ds_read_b64_tr_b16 v[178:179], v227 offset:256\n"
    "v_mfma_f32_16x16x32_bf16 v[88:91], v[204:207], v[144:147], v[88:91]\n"
    "v_exp_f32 v15, v99\n"
    "v_add_f32 v230, v230, v14\n"
    "v_add_u32 v225, s53, v121\n"
    "s_waitcnt lgkmcnt(4)\n"
    "v_mfma_f32_16x16x32_bf16 v[84:87], v[220:223], v[128:131], v[112:115]\n"
    "ds_read_b64_tr_b16 v[192:193], v226 offset:512\n"
    "ds_read_b64_tr_b16 v[194:195], v226 offset:768\n"
    "v_mfma_f32_16x16x32_bf16 v[84:87], v[188:191], v[132:135], v[84:87]\n"
    "v_exp_f32 v12, v100\n"
    "v_mfma_f32_16x16x32_bf16 v[84:87], v[216:219], v[136:139], v[84:87]\n"
    "ds_read_b64_tr_b16 v[196:197], v227 offset:512\n"
    "ds_read_b64_tr_b16 v[198:199], v227 offset:768\n"
    "v_mfma_f32_16x16x32_bf16 v[84:87], v[184:187], v[140:143], v[84:87]\n"
    "ds_read_b64_tr_b16 v[208:209], v226 offset:1024\n"
    "ds_read_b64_tr_b16 v[210:211], v226 offset:1280\n"
    "v_add_f32 v230, v230, v15\n"
    "v_cvt_pk_bf16_f32 v169, v14, v15\n"
    "v_mfma_f32_16x16x32_bf16 v[92:95], v[184:187], v[156:159], v[116:119]\n"
    "ds_read_b64_tr_b16 v[212:213], v227 offset:1024\n"
    "ds_read_b64_tr_b16 v[214:215], v227 offset:1280\n"
    "v_mfma_f32_16x16x32_bf16 v[92:95], v[216:219], v[152:155], v[92:95]\n"
    "v_exp_f32 v13, v101\n"
    "v_mfma_f32_16x16x32_bf16 v[92:95], v[188:191], v[148:151], v[92:95]\n"
    "ds_read_b64_tr_b16 v[216:217], v226 offset:1536\n"
    "ds_read_b64_tr_b16 v[218:219], v226 offset:1792\n"
    "v_mfma_f32_16x16x32_bf16 v[92:95], v[220:223], v[144:147], v[92:95]\n"
    "v_add_f32 v230, v230, v12\n"
    "v_exp_f32 v14, v102\n"
    "s_add_u32 s15, s15, 1\n"
    "s_waitcnt lgkmcnt(10)\n"
    "v_mfma_f32_16x16x32_bf16 v[16:19], v[180:183], v[160:163], v[16:19]\n"
    "ds_read_b64_tr_b16 v[220:221], v227 offset:1536\n"
    "ds_read_b64_tr_b16 v[222:223], v227 offset:1792\n"
    "v_add_f32 v230, v230, v13\n"
    "v_cvt_pk_bf16_f32 v170, v12, v13\n"
    "v_mfma_f32_16x16x32_bf16 v[20:23], v[180:183], v[164:167], v[20:23]\n"
    "v_exp_f32 v15, v103\n"
    "v_add_f32 v230, v230, v14\n"
    "v_add_u32 v226, s55, v122\n"
    "v_mfma_f32_16x16x32_bf16 v[28:31], v[176:179], v[164:167], v[28:31]\n"
    "v_exp_f32 v12, v104\n"
    "v_add_f32 v230, v230, v15\n"
    "v_add_u32 v227, s55, v123\n"
    "v_mfma_f32_16x16x32_bf16 v[24:27], v[176:179], v[160:163], v[24:27]\n"
    "v_cvt_pk_bf16_f32 v171, v14, v15\n"
    "v_exp_f32 v13, v105\n"
    "v_exp_f32 v14, v106\n"
    "s_waitcnt lgkmcnt(8)\n"
    "v_mfma_f32_16x16x32_bf16 v[32:35], v[192:195], v[160:163], v[32:35]\n"
    "v_add_f32 v231, v12, v13\n"
    "v_cvt_pk_bf16_f32 v172, v12, v13\n"
    "v_exp_f32 v15, v107\n"
    "v_mfma_f32_16x16x32_bf16 v[36:39], v[192:195], v[164:167], v[36:39]\n"
    "v_add_f32 v231, v231, v14\n"
    "v_exp_f32 v12, v108\n"
    "v_mfma_f32_16x16x32_bf16 v[44:47], v[196:199], v[164:167], v[44:47]\n"
    "v_add_f32 v231, v231, v15\n"
    "v_cvt_pk_bf16_f32 v173, v14, v15\n"
    "v_exp_f32 v13, v109\n"
    "v_mfma_f32_16x16x32_bf16 v[40:43], v[196:199], v[160:163], v[40:43]\n"
    "s_waitcnt vmcnt(0) lgkmcnt(0)\n"
    "s_barrier\n"
    "s_cmp_lg_u32 s12, 0\n"
    "s_cbranch_scc1 .Lknf_33\n"
    "v_mov_b32 v244, s68\n"
    "v_mov_b32 v245, s69\n"
    "v_mov_b32 v246, 0\n"
    "s_lshl_b32 s60, s10, 6\n"
    "s_add_u32 s60, s60, 0x1000\n"
    "s_mov_b64 exec, 1\n"
    "s_nop 0\n"
    "buffer_store_dwordx2 v[244:245], v246, s[44:47], s60 offen sc1\n"
    "s_mov_b64 exec, -1\n"
    ".Lknf_33:\n"
    ".Lkbody2_34:\n"
    "s_nop 0\n"
    "v_mfma_f32_16x16x32_bf16 v[48:51], v[208:211], v[160:163], v[48:51]\n"
    "ds_read_b128 v[176:179], v124\n"
    "ds_read_b128 v[180:183], v124 offset:512\n"
    "v_add_f32 v231, v231, v12\n"
    "v_mfma_f32_16x16x32_bf16 v[52:55], v[208:211], v[164:167], v[52:55]\n"
    "ds_read_b128 v[184:187], v124 offset:1024\n"
    "ds_read_b128 v[188:191], v124 offset:1536\n"
    "v_exp_f32 v14, v110\n"
    "v_mfma_f32_16x16x32_bf16 v[60:63], v[212:215], v[164:167], v[60:63]\n"
    "v_add_f32 v231, v231, v13\n"
    "s_add_u32 s66, s52, 0x8000\n"
    "s_sub_u32 s52, s52, 0xe000\n"
    "s_cselect_b32 s52, s66, s52\n"
    "v_mfma_f32_16x16x32_bf16 v[56:59], v[212:215], v[160:163], v[56:59]\n"
    "v_cvt_pk_bf16_f32 v174, v12, v13\n"
    "v_exp_f32 v15, v111\n"
    "v_mfma_f32_16x16x32_bf16 v[64:67], v[216:219], v[160:163], v[64:67]\n"
    "ds_read_b128 v[192:195], v125\n"
    "v_add_f32 v231, v231, v14\n"
    "v_add_f32 v231, v231, v15\n"
    "v_cvt_pk_bf16_f32 v175, v14, v15\n"
    "v_mfma_f32_16x16x32_bf16 v[68:71], v[216:219], v[164:167], v[68:71]\n"
    "ds_read_b128 v[196:199], v125 offset:512\n"
    "v_max_f32 v248, v230, v231\n"
    "v_cmp_nge_f32 vcc, s70, v248\n"
    "v_mfma_f32_16x16x32_bf16 v[76:79], v[220:223], v[164:167], v[76:79]\n"
    "ds_read_b128 v[200:203], v125 offset:1024\n"
    "v_mfma_f32_16x16x32_bf16 v[72:75], v[220:223], v[160:163], v[72:75]\n"
    "s_cbranch_vccnz .Lkrare_35\n"
    ".Lkrret_36:\n"
    "ds_read_b128 v[204:207], v125 offset:1536\n"
    "s_waitcnt lgkmcnt(4)\n"
    "v_mfma_f32_16x16x32_bf16 v[96:99], v[176:179], v[128:131], v[112:115]\n"
    "v_add_f32 v228, v228, v230\n"
    "v_add_f32 v229, v229, v231\n"
    "v_mfma_f32_16x16x32_bf16 v[96:99], v[180:183], v[132:135], v[96:99]\n"
    "v_exp_f32 v12, v80\n"
    "v_mfma_f32_16x16x32_bf16 v[96:99], v[184:187], v[136:139], v[96:99]\n"
    "v_exp_f32 v13, v81\n"
    "v_mfma_f32_16x16x32_bf16 v[96:99], v[188:191], v[140:143], v[96:99]\n"
    "s_add_u32 s66, s53, 0x8000\n"
    "s_sub_u32 s53, s53, 0xe000\n"
    "s_cselect_b32 s53, s66, s53\n"
    "v_mfma_f32_16x16x32_bf16 v[104:107], v[188:191], v[156:159], v[116:119]\n"
    "ds_read_b64_tr_b16 v[208:209], v126\n"
    "ds_read_b64_tr_b16 v[210:211], v126 offset:256\n"
    "v_mfma_f32_16x16x32_bf16 v[104:107], v[184:187], v[152:155], v[104:107]\n"
    "v_exp_f32 v14, v82\n"
    "v_mfma_f32_16x16x32_bf16 v[104:107], v[180:183], v[148:151], v[104:107]\n"
    "ds_read_b64_tr_b16 v[212:213], v127\n"
    "ds_read_b64_tr_b16 v[214:215], v127 offset:256\n"
    "v_mfma_f32_16x16x32_bf16 v[104:107], v[176:179], v[144:147], v[104:107]\n"
    "v_add_f32 v230, v12, v13\n"
    "v_cvt_pk_bf16_f32 v160, v12, v13\n"
    "v_exp_f32 v15, v83\n"
    "s_waitcnt lgkmcnt(4)\n"
    "v_mfma_f32_16x16x32_bf16 v[100:103], v[192:195], v[128:131], v[112:115]\n"
    "ds_read_b64_tr_b16 v[216:217], v126 offset:512\n"
    "ds_read_b64_tr_b16 v[218:219], v126 offset:768\n"
    "v_mfma_f32_16x16x32_bf16 v[100:103], v[196:199], v[132:135], v[100:103]\n"
    "v_add_f32 v230, v230, v14\n"
    "v_exp_f32 v12, v84\n"
    "v_mfma_f32_16x16x32_bf16 v[100:103], v[200:203], v[136:139], v[100:103]\n"
    "ds_read_b64_tr_b16 v[220:221], v127 offset:512\n"
    "ds_read_b64_tr_b16 v[222:223], v127 offset:768\n"
    "v_mfma_f32_16x16x32_bf16 v[100:103], v[204:207], v[140:143], v[100:103]\n"
    "s_add_u32 s66, s54, 0x8000\n"
    "s_sub_u32 s54, s54, 0xe000\n"
    "s_cselect_b32 s54, s66, s54\n"
    "v_mfma_f32_16x16x32_bf16 v[108:111], v[204:207], v[156:159], v[116:119]\n"
    "ds_read_b64_tr_b16 v[188:189], v126 offset:1024\n"
    "ds_read_b64_tr_b16 v[190:191], v126 offset:1280\n"
    "v_mfma_f32_16x16x32_bf16 v[108:111], v[200:203], v[152:155], v[108:111]\n"
    "v_add_f32 v230, v230, v15\n"
    "v_cvt_pk_bf16_f32 v161, v14, v15\n"
    "v_mfma_f32_16x16x32_bf16 v[108:111], v[196:199], v[148:151], v[108:111]\n"
    "ds_read_b64_tr_b16 v[184:185], v127 offset:1024\n"
    "ds_read_b64_tr_b16 v[186:187], v127 offset:1280\n"
    "v_mfma_f32_16x16x32_bf16 v[108:111], v[192:195], v[144:147], v[108:111]\n"
    "v_exp_f32 v13, v85\n"
    "v_add_f32 v230, v230, v12\n"
    "s_waitcnt lgkmcnt(8)\n"
    "v_mfma_f32_16x16x32_bf16 v[16:19], v[208:211], v[168:171], v[16:19]\n"
    "ds_read_b64_tr_b16 v[180:181], v126 offset:1536\n"
    "ds_read_b64_tr_b16 v[182:183], v126 offset:1792\n"
    "v_exp_f32 v14, v86\n"
    "v_add_f32 v230, v230, v13\n"
    "v_mfma_f32_16x16x32_bf16 v[20:23], v[208:211], v[172:175], v[20:23]\n"
    "s_add_u32 s66, s55, 0x8000\n"
    "s_sub_u32 s55, s55, 0xe000\n"
    "s_cselect_b32 s55, s66, s55\n"
    "v_mfma_f32_16x16x32_bf16 v[28:31], v[212:215], v[172:175], v[28:31]\n"
    "ds_read_b64_tr_b16 v[176:177], v127 offset:1536\n"
    "ds_read_b64_tr_b16 v[178:179], v127 offset:1792\n"
    "v_cvt_pk_bf16_f32 v162, v12, v13\n"
    "v_exp_f32 v15, v87\n"
    "v_mfma_f32_16x16x32_bf16 v[24:27], v[212:215], v[168:171], v[24:27]\n"
    "v_add_f32 v230, v230, v14\n"
    "v_exp_f32 v12, v88\n"
    "s_waitcnt lgkmcnt(8)\n"
    "v_mfma_f32_16x16x32_bf16 v[32:35], v[216:219], v[168:171], v[32:35]\n"
    "ds_read_b128 v[204:207], v224\n"
    "v_add_f32 v230, v230, v15\n"
    "v_cvt_pk_bf16_f32 v163, v14, v15\n"
    "v_exp_f32 v13, v89\n"
    "v_mfma_f32_16x16x32_bf16 v[36:39], v[216:219], v[172:175], v[36:39]\n"
    "ds_read_b128 v[200:203], v224 offset:512\n"
    "v_exp_f32 v14, v90\n"
    "v_mfma_f32_16x16x32_bf16 v[44:47], v[220:223], v[172:175], v[44:47]\n"
    "ds_read_b128 v[196:199], v224 offset:1024\n"
    "v_add_f32 v231, v12, v13\n"
    "v_cvt_pk_bf16_f32 v164, v12, v13\n"
    "v_mfma_f32_16x16x32_bf16 v[40:43], v[220:223], v[168:171], v[40:43]\n"
    "ds_read_b128 v[192:195], v224 offset:1536\n"
    "v_exp_f32 v15, v91\n"
    "v_add_f32 v231, v231, v14\n"
    "s_waitcnt lgkmcnt(8)\n"
    "v_mfma_f32_16x16x32_bf16 v[48:51], v[188:191], v[168:171], v[48:51]\n"
    "v_exp_f32 v12, v92\n"
    "v_add_f32 v231, v231, v15\n"
    "v_cvt_pk_bf16_f32 v165, v14, v15\n"
    "v_add_u32 v124, s52, v120\n"
    "v_mfma_f32_16x16x32_bf16 v[52:55], v[188:191], v[172:175], v[52:55]\n"
    "v_exp_f32 v13, v93\n"
    "v_add_u32 v125, s52, v121\n"
    "v_mfma_f32_16x16x32_bf16 v[60:63], v[184:187], v[172:175], v[60:63]\n"
    "v_add_f32 v231, v231, v12\n"
    "v_exp_f32 v14, v94\n"
    "v_add_u32 v126, s54, v122\n"
    "v_mfma_f32_16x16x32_bf16 v[56:59], v[184:187], v[168:171], v[56:59]\n"
    "v_add_f32 v231, v231, v13\n"
    "v_cvt_pk_bf16_f32 v166, v12, v13\n"
    "v_exp_f32 v15, v95\n"
    "v_add_u32 v127, s54, v123\n"
    "s_waitcnt lgkmcnt(4)\n"
    "v_mfma_f32_16x16x32_bf16 v[64:67], v[180:183], v[168:171], v[64:67]\n"
    "ds_read_b128 v[220:223], v225\n"
    "v_add_f32 v231, v231, v14\n"
    "v_add_f32 v231, v231, v15\n"
    "v_mfma_f32_16x16x32_bf16 v[68:71], v[180:183], v[172:175], v[68:71]\n"
    "ds_read_b128 v[188:191], v225 offset:512\n"
    "v_cvt_pk_bf16_f32 v167, v14, v15\n"
    "v_max_f32 v248, v230, v231\n"
    "v_cmp_nge_f32 vcc, s70, v248\n"
    "v_mfma_f32_16x16x32_bf16 v[76:79], v[176:179], v[172:175], v[76:79]\n"
    "ds_read_b128 v[216:219], v225 offset:1024\n"
    "v_mfma_f32_16x16x32_bf16 v[72:75], v[176:179], v[168:171], v[72:75]\n"
    "s_cbranch_vccnz .Lkrare_37\n"
    ".Lkrret_38:\n"
    "ds_read_b128 v[184:187], v225 offset:1536\n"
    "s_waitcnt lgkmcnt(4)\n"
    "v_mfma_f32_16x16x32_bf16 v[80:83], v[204:207], v[128:131], v[112:115]\n"
    "v_add_f32 v228, v228, v230\n"
    "v_add_f32 v229, v229, v231\n"
    "v_mfma_f32_16x16x32_bf16 v[80:83], v[200:203], v[132:135], v[80:83]\n"
    "v_exp_f32 v12, v96\n"
    "v_mfma_f32_16x16x32_bf16 v[80:83], v[196:199], v[136:139], v[80:83]\n"
    "v_exp_f32 v13, v97\n"
    "v_mfma_f32_16x16x32_bf16 v[80:83], v[192:195], v[140:143], v[80:83]\n"
    "v_exp_f32 v14, v98\n"
    "v_add_f32 v230, v12, v13\n"
    "v_add_u32 v224, s53, v120\n"
    "v_mfma_f32_16x16x32_bf16 v[88:91], v[192:195], v[156:159], v[116:119]\n"
    "ds_read_b64_tr_b16 v[180:181], v226\n"
    "ds_read_b64_tr_b16 v[182:183], v226 offset:256\n"
    "v_mfma_f32_16x16x32_bf16 v[88:91], v[196:199], v[152:155], v[88:91]\n"
    "v_cvt_pk_bf16_f32 v168, v12, v13\n"
    "v_mfma_f32_16x16x32_bf16 v[88:91], v[200:203], v[148:151], v[88:91]\n"
    "ds_read_b64_tr_b16 v[176:177], v227\n"
    "ds_read_b64_tr_b16 v[178:179], v227 offset:256\n"
    "v_mfma_f32_16x16x32_bf16 v[88:91], v[204:207], v[144:147], v[88:91]\n"
    "v_exp_f32 v15, v99\n"
    "v_add_f32 v230, v230, v14\n"
    "v_add_u32 v225, s53, v121\n"
    "s_waitcnt lgkmcnt(4)\n"
    "v_mfma_f32_16x16x32_bf16 v[84:87], v[220:223], v[128:131], v[112:115]\n"
    "ds_read_b64_tr_b16 v[192:193], v226 offset:512\n"
    "ds_read_b64_tr_b16 v[194:195], v226 offset:768\n"
    "v_mfma_f32_16x16x32_bf16 v[84:87], v[188:191], v[132:135], v[84:87]\n"
    "v_exp_f32 v12, v100\n"
    "v_mfma_f32_16x16x32_bf16 v[84:87], v[216:219], v[136:139], v[84:87]\n"
    "ds_read_b64_tr_b16 v[196:197], v227 offset:512\n"
    "ds_read_b64_tr_b16 v[198:199], v227 offset:768\n"
    "v_mfma_f32_16x16x32_bf16 v[84:87], v[184:187], v[140:143], v[84:87]\n"
    "ds_read_b64_tr_b16 v[208:209], v226 offset:1024\n"
    "ds_read_b64_tr_b16 v[210:211], v226 offset:1280\n"
    "v_add_f32 v230, v230, v15\n"
    "v_cvt_pk_bf16_f32 v169, v14, v15\n"
    "v_mfma_f32_16x16x32_bf16 v[92:95], v[184:187], v[156:159], v[116:119]\n"
    "ds_read_b64_tr_b16 v[212:213], v227 offset:1024\n"
    "ds_read_b64_tr_b16 v[214:215], v227 offset:1280\n"
    "v_mfma_f32_16x16x32_bf16 v[92:95], v[216:219], v[152:155], v[92:95]\n"
    "v_exp_f32 v13, v101\n"
    "v_mfma_f32_16x16x32_bf16 v[92:95], v[188:191], v[148:151], v[92:95]\n"
    "ds_read_b64_tr_b16 v[216:217], v226 offset:1536\n"
    "ds_read_b64_tr_b16 v[218:219], v226 offset:1792\n"
    "v_mfma_f32_16x16x32_bf16 v[92:95], v[220:223], v[144:147], v[92:95]\n"
    "v_add_f32 v230, v230, v12\n"
    "v_exp_f32 v14, v102\n"
    "s_add_u32 s15, s15, 1\n"
    "s_waitcnt lgkmcnt(10)\n"
    "v_mfma_f32_16x16x32_bf16 v[16:19], v[180:183], v[160:163], v[16:19]\n"
    "ds_read_b64_tr_b16 v[220:221], v227 offset:1536\n"
    "ds_read_b64_tr_b16 v[222:223], v227 offset:1792\n"
    "v_add_f32 v230, v230, v13\n"
    "v_cvt_pk_bf16_f32 v170, v12, v13\n"
    "v_mfma_f32_16x16x32_bf16 v[20:23], v[180:183], v[164:167], v[20:23]\n"
    "v_exp_f32 v15, v103\n"
    "v_add_f32 v230, v230, v14\n"
    "v_add_u32 v226, s55, v122\n"
    "v_mfma_f32_16x16x32_bf16 v[28:31], v[176:179], v[164:167], v[28:31]\n"
    "v_exp_f32 v12, v104\n"
    "v_add_f32 v230, v230, v15\n"
    "v_add_u32 v227, s55, v123\n"
    "v_mfma_f32_16x16x32_bf16 v[24:27], v[176:179], v[160:163], v[24:27]\n"
    "v_cvt_pk_bf16_f32 v171, v14, v15\n"
    "v_exp_f32 v13, v105\n"
    "v_exp_f32 v14, v106\n"
    "s_waitcnt lgkmcnt(8)\n"
    "v_mfma_f32_16x16x32_bf16 v[32:35], v[192:195], v[160:163], v[32:35]\n"
    "v_add_f32 v231, v12, v13\n"
    "v_cvt_pk_bf16_f32 v172, v12, v13\n"
    "v_exp_f32 v15, v107\n"
    "v_mfma_f32_16x16x32_bf16 v[36:39], v[192:195], v[164:167], v[36:39]\n"
    "v_add_f32 v231, v231, v14\n"
    "v_exp_f32 v12, v108\n"
    "v_mfma_f32_16x16x32_bf16 v[44:47], v[196:199], v[164:167], v[44:47]\n"
    "v_add_f32 v231, v231, v15\n"
    "v_cvt_pk_bf16_f32 v173, v14, v15\n"
    "v_exp_f32 v13, v109\n"
    "v_mfma_f32_16x16x32_bf16 v[40:43], v[196:199], v[160:163], v[40:43]\n"
    "s_cmp_lt_u32 s15, 31\n"
    "s_waitcnt lgkmcnt(0)\n"
    "s_barrier\n"
    "s_cbranch_scc1 .Lkbody2_34\n"
    "v_add_u32 v9, s53, v122\n"
    "v_add_u32 v10, s53, v123\n"
    "v_mfma_f32_16x16x32_bf16 v[48:51], v[208:211], v[160:163], v[48:51]\n"
    "ds_read_b128 v[176:179], v124\n"
    "ds_read_b128 v[180:183], v124 offset:512\n"
    "v_add_f32 v231, v231, v12\n"
    "v_mfma_f32_16x16x32_bf16 v[52:55], v[208:211], v[164:167], v[52:55]\n"
    "ds_read_b128 v[184:187], v124 offset:1024\n"
    "ds_read_b128 v[188:191], v124 offset:1536\n"
    "v_exp_f32 v14, v110\n"
    "v_mfma_f32_16x16x32_bf16 v[60:63], v[212:215], v[164:167], v[60:63]\n"
    "v_add_f32 v231, v231, v13\n"
    "v_cvt_pk_bf16_f32 v174, v12, v13\n"
    "v_mfma_f32_16x16x32_bf16 v[56:59], v[212:215], v[160:163], v[56:59]\n"
    "v_exp_f32 v15, v111\n"
    "v_add_f32 v231, v231, v14\n"
    "v_mfma_f32_16x16x32_bf16 v[64:67], v[216:219], v[160:163], v[64:67]\n"
    "ds_read_b128 v[192:195], v125\n"
    "v_add_f32 v231, v231, v15\n"
    "v_cvt_pk_bf16_f32 v175, v14, v15\n"
    "v_mfma_f32_16x16x32_bf16 v[68:71], v[216:219], v[164:167], v[68:71]\n"
    "ds_read_b128 v[196:199], v125 offset:512\n"
    "v_max_f32 v248, v230, v231\n"
    "v_cmp_nge_f32 vcc, s70, v248\n"
    "v_mfma_f32_16x16x32_bf16 v[76:79], v[220:223], v[164:167], v[76:79]\n"
    "ds_read_b128 v[200:203], v125 offset:1024\n"
    "v_mfma_f32_16x16x32_bf16 v[72:75], v[220:223], v[160:163], v[72:75]\n"
    "s_cbranch_vccnz .Lkrare_39\n"
    ".Lkrret_40:\n"
    "ds_read_b128 v[204:207], v125 offset:1536\n"
    "s_waitcnt lgkmcnt(4)\n"
    "v_mfma_f32_16x16x32_bf16 v[96:99], v[176:179], v[128:131], v[112:115]\n"
    "v_add_f32 v228, v228, v230\n"
    "v_add_f32 v229, v229, v231\n"
    "v_mfma_f32_16x16x32_bf16 v[96:99], v[180:183], v[132:135], v[96:99]\n"
    "v_exp_f32 v12, v80\n"
    "v_mfma_f32_16x16x32_bf16 v[96:99], v[184:187], v[136:139], v[96:99]\n"
    "v_exp_f32 v13, v81\n"
    "v_mfma_f32_16x16x32_bf16 v[96:99], v[188:191], v[140:143], v[96:99]\n"
    "v_exp_f32 v14, v82\n"
    "v_add_f32 v230, v12, v13\n"
    "v_mfma_f32_16x16x32_bf16 v[104:107], v[188:191], v[156:159], v[116:119]\n"
    "ds_read_b64_tr_b16 v[208:209], v126\n"
    "ds_read_b64_tr_b16 v[210:211], v126 offset:256\n"
    "v_mfma_f32_16x16x32_bf16 v[104:107], v[184:187], v[152:155], v[104:107]\n"
    "v_cvt_pk_bf16_f32 v160, v12, v13\n"
    "v_mfma_f32_16x16x32_bf16 v[104:107], v[180:183], v[148:151], v[104:107]\n"
    "ds_read_b64_tr_b16 v[212:213], v127\n"
    "ds_read_b64_tr_b16 v[214:215], v127 offset:256\n"
    "v_mfma_f32_16x16x32_bf16 v[104:107], v[176:179], v[144:147], v[104:107]\n"
    "v_exp_f32 v15, v83\n"
    "v_add_f32 v230, v230, v14\n"
    "v_exp_f32 v12, v84\n"
    "s_waitcnt lgkmcnt(4)\n"
    "v_mfma_f32_16x16x32_bf16 v[100:103], v[192:195], v[128:131], v[112:115]\n"
    "ds_read_b64_tr_b16 v[216:217], v126 offset:512\n"
    "ds_read_b64_tr_b16 v[218:219], v126 offset:768\n"
    "v_mfma_f32_16x16x32_bf16 v[100:103], v[196:199], v[132:135], v[100:103]\n"
    "v_add_f32 v230, v230, v15\n"
    "v_mfma_f32_16x16x32_bf16 v[100:103], v[200:203], v[136:139], v[100:103]\n"
    "ds_read_b64_tr_b16 v[220:221], v127 offset:512\n"
    "ds_read_b64_tr_b16 v[222:223], v127 offset:768\n"
    "v_mfma_f32_16x16x32_bf16 v[100:103], v[204:207], v[140:143], v[100:103]\n"
    "v_cvt_pk_bf16_f32 v161, v14, v15\n"
    "v_exp_f32 v13, v85\n"
    "v_mfma_f32_16x16x32_bf16 v[108:111], v[204:207], v[156:159], v[116:119]\n"
    "ds_read_b64_tr_b16 v[188:189], v126 offset:1024\n"
    "ds_read_b64_tr_b16 v[190:191], v126 offset:1280\n"
    "v_mfma_f32_16x16x32_bf16 v[108:111], v[200:203], v[152:155], v[108:111]\n"
    "v_add_f32 v230, v230, v12\n"
    "v_exp_f32 v14, v86\n"
    "v_mfma_f32_16x16x32_bf16 v[108:111], v[196:199], v[148:151], v[108:111]\n"
    "ds_read_b64_tr_b16 v[184:185], v127 offset:1024\n"
    "ds_read_b64_tr_b16 v[186:187], v127 offset:1280\n"
    "v_mfma_f32_16x16x32_bf16 v[108:111], v[192:195], v[144:147], v[108:111]\n"
    "v_add_f32 v230, v230, v13\n"
    "v_cvt_pk_bf16_f32 v162, v12, v13\n"
    "s_waitcnt lgkmcnt(8)\n"
    "v_mfma_f32_16x16x32_bf16 v[16:19], v[208:211], v[168:171], v[16:19]\n"
    "ds_read_b64_tr_b16 v[180:181], v126 offset:1536\n"
    "ds_read_b64_tr_b16 v[182:183], v126 offset:1792\n"
    "v_exp_f32 v15, v87\n"
    "v_mfma_f32_16x16x32_bf16 v[20:23], v[208:211], v[172:175], v[20:23]\n"
    "v_add_f32 v230, v230, v14\n"
    "v_exp_f32 v12, v88\n"
    "v_mfma_f32_16x16x32_bf16 v[28:31], v[212:215], v[172:175], v[28:31]\n"
    "ds_read_b64_tr_b16 v[176:177], v127 offset:1536\n"
    "ds_read_b64_tr_b16 v[178:179], v127 offset:1792\n"
    "v_add_f32 v230, v230, v15\n"
    "v_cvt_pk_bf16_f32 v163, v14, v15\n"
    "v_mfma_f32_16x16x32_bf16 v[24:27], v[212:215], v[168:171], v[24:27]\n"
    "v_exp_f32 v13, v89\n"
    "v_exp_f32 v14, v90\n"
    "s_waitcnt lgkmcnt(8)\n"
    "v_mfma_f32_16x16x32_bf16 v[32:35], v[216:219], v[168:171], v[32:35]\n"
    "ds_read_b64_tr_b16 v[204:205], v226\n"
    "ds_read_b64_tr_b16 v[206:207], v226 offset:256\n"
    "v_add_f32 v231, v12, v13\n"
    "v_mfma_f32_16x16x32_bf16 v[36:39], v[216:219], v[172:175], v[36:39]\n"
    "v_cvt_pk_bf16_f32 v164, v12, v13\n"
    "v_exp_f32 v15, v91\n"
    "v_mfma_f32_16x16x32_bf16 v[44:47], v[220:223], v[172:175], v[44:47]\n"
    "ds_read_b64_tr_b16 v[200:201], v227\n"
    "ds_read_b64_tr_b16 v[202:203], v227 offset:256\n"
    "v_add_f32 v231, v231, v14\n"
    "v_mfma_f32_16x16x32_bf16 v[40:43], v[220:223], v[168:171], v[40:43]\n"
    "v_exp_f32 v12, v92\n"
    "v_add_f32 v231, v231, v15\n"
    "v_cvt_pk_bf16_f32 v165, v14, v15\n"
    "s_waitcnt lgkmcnt(8)\n"
    "v_mfma_f32_16x16x32_bf16 v[48:51], v[188:191], v[168:171], v[48:51]\n"
    "ds_read_b64_tr_b16 v[196:197], v226 offset:512\n"
    "ds_read_b64_tr_b16 v[198:199], v226 offset:768\n"
    "v_exp_f32 v13, v93\n"
    "v_mfma_f32_16x16x32_bf16 v[52:55], v[188:191], v[172:175], v[52:55]\n"
    "v_add_f32 v231, v231, v12\n"
    "v_exp_f32 v14, v94\n"
    "v_mfma_f32_16x16x32_bf16 v[60:63], v[184:187], v[172:175], v[60:63]\n"
    "ds_read_b64_tr_b16 v[192:193], v227 offset:512\n"
    "ds_read_b64_tr_b16 v[194:195], v227 offset:768\n"
    "v_add_f32 v231, v231, v13\n"
    "v_mfma_f32_16x16x32_bf16 v[56:59], v[184:187], v[168:171], v[56:59]\n"
    "v_cvt_pk_bf16_f32 v166, v12, v13\n"
    "v_exp_f32 v15, v95\n"
    "s_waitcnt lgkmcnt(8)\n"
    "v_mfma_f32_16x16x32_bf16 v[64:67], v[180:183], v[168:171], v[64:67]\n"
    "ds_read_b64_tr_b16 v[208:209], v226 offset:1024\n"
    "ds_read_b64_tr_b16 v[210:211], v226 offset:1280\n"
    "v_add_f32 v231, v231, v14\n"
    "v_add_f32 v231, v231, v15\n"
    "v_mfma_f32_16x16x32_bf16 v[68:71], v[180:183], v[172:175], v[68:71]\n"
    "v_cvt_pk_bf16_f32 v167, v14, v15\n"
    "v_max_f32 v248, v230, v231\n"
    "v_cmp_nge_f32 vcc, s70, v248\n"
    "v_mfma_f32_16x16x32_bf16 v[76:79], v[176:179], v[172:175], v[76:79]\n"
    "ds_read_b64_tr_b16 v[212:213], v227 offset:1024\n"
    "ds_read_b64_tr_b16 v[214:215], v227 offset:1280\n"
    "v_mfma_f32_16x16x32_bf16 v[72:75], v[176:179], v[168:171], v[72:75]\n"
    "s_cbranch_vccnz .Lkrare_41\n"
    ".Lkrret_42:\n"
    "s_waitcnt lgkmcnt(8)\n"
    "v_mfma_f32_16x16x32_bf16 v[16:19], v[204:207], v[160:163], v[16:19]\n"
    "ds_read_b64_tr_b16 v[216:217], v226 offset:1536\n"
    "ds_read_b64_tr_b16 v[218:219], v226 offset:1792\n"
    "v_add_f32 v228, v228, v230\n"
    "v_add_f32 v229, v229, v231\n"
    "v_exp_f32 v12, v96\n"
    "v_mfma_f32_16x16x32_bf16 v[20:23], v[204:207], v[164:167], v[20:23]\n"
    "v_exp_f32 v13, v97\n"
    "v_exp_f32 v14, v98\n"
    "v_mfma_f32_16x16x32_bf16 v[28:31], v[200:203], v[164:167], v[28:31]\n"
    "ds_read_b64_tr_b16 v[220:221], v227 offset:1536\n"
    "ds_read_b64_tr_b16 v[222:223], v227 offset:1792\n"
    "v_add_f32 v230, v12, v13\n"
    "v_cvt_pk_bf16_f32 v168, v12, v13\n"
    "v_exp_f32 v15, v99\n"
    "v_mfma_f32_16x16x32_bf16 v[24:27], v[200:203], v[160:163], v[24:27]\n"
    "v_add_f32 v230, v230, v14\n"
    "v_exp_f32 v12, v100\n"
    "v_add_f32 v230, v230, v15\n"
    "v_cvt_pk_bf16_f32 v169, v14, v15\n"
    "s_waitcnt lgkmcnt(8)\n"
    "v_mfma_f32_16x16x32_bf16 v[32:35], v[196:199], v[160:163], v[32:35]\n"
    "ds_read_b64_tr_b16 v[188:189], v9\n"
    "ds_read_b64_tr_b16 v[190:191], v9 offset:256\n"
    "v_exp_f32 v13, v101\n"
    "v_add_f32 v230, v230, v12\n"
    "v_mfma_f32_16x16x32_bf16 v[36:39], v[196:199], v[164:167], v[36:39]\n"
    "v_exp_f32 v14, v102\n"
    "v_add_f32 v230, v230, v13\n"
    "v_cvt_pk_bf16_f32 v170, v12, v13\n"
    "v_mfma_f32_16x16x32_bf16 v[44:47], v[192:195], v[164:167], v[44:47]\n"
    "ds_read_b64_tr_b16 v[184:185], v10\n"
    "ds_read_b64_tr_b16 v[186:187], v10 offset:256\n"
    "v_exp_f32 v15, v103\n"
    "v_add_f32 v230, v230, v14\n"
    "v_exp_f32 v12, v104\n"
    "v_mfma_f32_16x16x32_bf16 v[40:43], v[192:195], v[160:163], v[40:43]\n"
    "v_add_f32 v230, v230, v15\n"
    "v_cvt_pk_bf16_f32 v171, v14, v15\n"
    "v_exp_f32 v13, v105\n"
    "s_waitcnt lgkmcnt(8)\n"
    "v_mfma_f32_16x16x32_bf16 v[48:51], v[208:211], v[160:163], v[48:51]\n"
    "ds_read_b64_tr_b16 v[180:181], v9 offset:512\n"
    "ds_read_b64_tr_b16 v[182:183], v9 offset:768\n"
    "v_exp_f32 v14, v106\n"
    "v_add_f32 v231, v12, v13\n"
    "v_mfma_f32_16x16x32_bf16 v[52:55], v[208:211], v[164:167], v[52:55]\n"
    "v_cvt_pk_bf16_f32 v172, v12, v13\n"
    "v_exp_f32 v15, v107\n"
    "v_add_f32 v231, v231, v14\n"
    "v_exp_f32 v12, v108\n"
    "v_mfma_f32_16x16x32_bf16 v[60:63], v[212:215], v[164:167], v[60:63]\n"
    "ds_read_b64_tr_b16 v[176:177], v10 offset:512\n"
    "ds_read_b64_tr_b16 v[178:179], v10 offset:768\n"
    "v_add_f32 v231, v231, v15\n"
    "v_cvt_pk_bf16_f32 v173, v14, v15\n"
    "s_barrier\n"
    "v_mfma_f32_16x16x32_bf16 v[56:59], v[212:215], v[160:163], v[56:59]\n"
    "v_exp_f32 v13, v109\n"
    "v_add_f32 v231, v231, v12\n"
    "v_exp_f32 v14, v110\n"
    "v_add_f32 v231, v231, v13\n"
    "s_waitcnt lgkmcnt(8)\n"
    "v_mfma_f32_16x16x32_bf16 v[64:67], v[216:219], v[160:163], v[64:67]\n"
    "ds_read_b64_tr_b16 v[204:205], v9 offset:1024\n"
    "ds_read_b64_tr_b16 v[206:207], v9 offset:1280\n"
    "v_cvt_pk_bf16_f32 v174, v12, v13\n"
    "v_exp_f32 v15, v111\n"
    "v_mfma_f32_16x16x32_bf16 v[68:71], v[216:219], v[164:167], v[68:71]\n"
    "v_add_f32 v231, v231, v14\n"
    "v_add_f32 v231, v231, v15\n"
    "v_cvt_pk_bf16_f32 v175, v14, v15\n"
    "v_max_f32 v248, v230, v231\n"
    "v_cmp_nge_f32 vcc, s70, v248\n"
    "v_mfma_f32_16x16x32_bf16 v[76:79], v[220:223], v[164:167], v[76:79]\n"
    "ds_read_b64_tr_b16 v[200:201], v10 offset:1024\n"
    "ds_read_b64_tr_b16 v[202:203], v10 offset:1280\n"
    "v_mfma_f32_16x16x32_bf16 v[72:75], v[220:223], v[160:163], v[72:75]\n"
    "s_cbranch_vccnz .Lkrare_43\n"
    ".Lkrret_44:\n"
    "s_waitcnt lgkmcnt(8)\n"
    "v_mfma_f32_16x16x32_bf16 v[16:19], v[188:191], v[168:171], v[16:19]\n"
    "ds_read_b64_tr_b16 v[196:197], v9 offset:1536\n"
    "ds_read_b64_tr_b16 v[198:199], v9 offset:1792\n"
    "v_add_f32 v4, v228, v230\n"
    "v_add_f32 v5, v229, v231\n"
    "v_mfma_f32_16x16x32_bf16 v[20:23], v[188:191], v[172:175], v[20:23]\n"
    "v_mov_b32 v250, v4\n"
    "v_mov_b32 v252, v5\n"
    "s_nop 0\n"
    "v_permlane16_swap_b32 v4, v250\n"
    "v_mfma_f32_16x16x32_bf16 v[28:31], v[184:187], v[172:175], v[28:31]\n"
    "ds_read_b64_tr_b16 v[192:193], v10 offset:1536\n"
    "ds_read_b64_tr_b16 v[194:195], v10 offset:1792\n"
    "v_permlane16_swap_b32 v5, v252\n"
    "v_add_f32 v4, v4, v250\n"
    "v_mfma_f32_16x16x32_bf16 v[24:27], v[184:187], v[168:171], v[24:27]\n"
    "v_add_f32 v5, v5, v252\n"
    "v_mov_b32 v250, v4\n"
    "v_mov_b32 v252, v5\n"
    "s_nop 0\n"
    "v_permlane32_swap_b32 v4, v250\n"
    "s_waitcnt lgkmcnt(8)\n"
    "v_mfma_f32_16x16x32_bf16 v[32:35], v[180:183], v[168:171], v[32:35]\n"
    "v_permlane32_swap_b32 v5, v252\n"
    "v_add_f32 v4, v4, v250\n"
    "v_add_f32 v5, v5, v252\n"
    "v_mfma_f32_16x16x32_bf16 v[36:39], v[180:183], v[172:175], v[36:39]\n"
    "v_rcp_f32 v250, v4\n"
    "v_rcp_f32 v252, v5\n"
    "v_mov_b32 v251, v250\n"
    "v_mov_b32 v253, v252\n"
    "v_mfma_f32_16x16x32_bf16 v[44:47], v[176:179], v[172:175], v[44:47]\n"
    "v_mul_f32 v16, v16, v250\n"
    "v_mul_f32 v17, v17, v250\n"
    "v_mul_f32 v18, v18, v250\n"
    "v_mul_f32 v19, v19, v250\n"
    "v_mul_f32 v24, v24, v250\n"
    "v_mul_f32 v25, v25, v250\n"
    "v_mul_f32 v26, v26, v250\n"
    "v_mfma_f32_16x16x32_bf16 v[40:43], v[176:179], v[168:171], v[40:43]\n"
    "v_mul_f32 v27, v27, v250\n"
    "v_cvt_pk_bf16_f32 v232, v16, v17\n"
    "v_cvt_pk_bf16_f32 v233, v18, v19\n"
    "v_cvt_pk_bf16_f32 v234, v24, v25\n"
    "v_cvt_pk_bf16_f32 v235, v26, v27\n"
    "buffer_store_dwordx4 v[232:235], v6, s[36:39], s84 offen sc1\n"
    "s_waitcnt lgkmcnt(4)\n"
    "v_mfma_f32_16x16x32_bf16 v[48:51], v[204:207], v[168:171], v[48:51]\n"
    "v_mul_f32 v20, v20, v252\n"
    "v_mul_f32 v21, v21, v252\n"
    "v_mul_f32 v22, v22, v252\n"
    "v_mul_f32 v23, v23, v252\n"
    "v_mul_f32 v28, v28, v252\n"
    "v_mul_f32 v29, v29, v252\n"
    "v_mul_f32 v30, v30, v252\n"
    "v_mfma_f32_16x16x32_bf16 v[52:55], v[204:207], v[172:175], v[52:55]\n"
    "v_mul_f32 v31, v31, v252\n"
    "v_cvt_pk_bf16_f32 v236, v20, v21\n"
    "v_cvt_pk_bf16_f32 v237, v22, v23\n"
    "v_cvt_pk_bf16_f32 v238, v28, v29\n"
    "v_cvt_pk_bf16_f32 v239, v30, v31\n"
    "buffer_store_dwordx4 v[236:239], v6, s[36:39], s85 offen sc1\n"
    "v_mfma_f32_16x16x32_bf16 v[60:63], v[200:203], v[172:175], v[60:63]\n"
    "v_mul_f32 v32, v32, v250\n"
    "v_mul_f32 v33, v33, v250\n"
    "v_mul_f32 v34, v34, v250\n"
    "v_mul_f32 v35, v35, v250\n"
    "v_mul_f32 v40, v40, v250\n"
    "v_mul_f32 v41, v41, v250\n"
    "v_mul_f32 v42, v42, v250\n"
    "v_mfma_f32_16x16x32_bf16 v[56:59], v[200:203], v[168:171], v[56:59]\n"
    "v_mul_f32 v43, v43, v250\n"
    "v_cvt_pk_bf16_f32 v240, v32, v33\n"
    "v_cvt_pk_bf16_f32 v241, v34, v35\n"
    "v_cvt_pk_bf16_f32 v242, v40, v41\n"
    "v_cvt_pk_bf16_f32 v243, v42, v43\n"
    "buffer_store_dwordx4 v[240:243], v6, s[36:39], s84 offen offset:1024 sc1\n"
    "s_waitcnt lgkmcnt(0)\n"
    "v_mfma_f32_16x16x32_bf16 v[64:67], v[196:199], v[168:171], v[64:67]\n"
    "v_mul_f32 v36, v36, v252\n"
    "v_mul_f32 v37, v37, v252\n"
    "v_mul_f32 v38, v38, v252\n"
    "v_mul_f32 v39, v39, v252\n"
    "v_mul_f32 v44, v44, v252\n"
    "v_mul_f32 v45, v45, v252\n"
    "v_mul_f32 v46, v46, v252\n"
    "v_mfma_f32_16x16x32_bf16 v[68:71], v[196:199], v[172:175], v[68:71]\n"
    "v_mul_f32 v47, v47, v252\n"
    "v_cvt_pk_bf16_f32 v232, v36, v37\n"
    "v_cvt_pk_bf16_f32 v233, v38, v39\n"
    "v_cvt_pk_bf16_f32 v234, v44, v45\n"
    "v_cvt_pk_bf16_f32 v235, v46, v47\n"
    "buffer_store_dwordx4 v[232:235], v6, s[36:39], s85 offen offset:1024 sc1\n"
    "v_mfma_f32_16x16x32_bf16 v[76:79], v[192:195], v[172:175], v[76:79]\n"
    "v_mul_f32 v48, v48, v250\n"
    "v_mul_f32 v49, v49, v250\n"
    "v_mul_f32 v50, v50, v250\n"
    "v_mul_f32 v51, v51, v250\n"
    "v_mul_f32 v56, v56, v250\n"
    "v_mul_f32 v57, v57, v250\n"
    "v_mul_f32 v58, v58, v250\n"
    "v_mul_f32 v59, v59, v250\n"
    "v_cvt_pk_bf16_f32 v236, v48, v49\n"
    "v_cvt_pk_bf16_f32 v237, v50, v51\n"
    "v_cvt_pk_bf16_f32 v238, v56, v57\n"
    "v_cvt_pk_bf16_f32 v239, v58, v59\n"
    "buffer_store_dwordx4 v[236:239], v6, s[36:39], s84 offen offset:2048 sc1\n"
    "v_mul_f32 v52, v52, v252\n"
    "v_mul_f32 v53, v53, v252\n"
    "v_mfma_f32_16x16x32_bf16 v[72:75], v[192:195], v[168:171], v[72:75]\n"
    "v_mul_f32 v54, v54, v252\n"
    "v_mul_f32 v55, v55, v252\n"
    "v_mul_f32 v60, v60, v252\n"
    "v_mul_f32 v61, v61, v252\n"
    "v_mul_f32 v62, v62, v252\n"
    "v_mul_f32 v63, v63, v252\n"
    "v_cvt_pk_bf16_f32 v240, v52, v53\n"
    "v_cvt_pk_bf16_f32 v241, v54, v55\n"
    "v_cvt_pk_bf16_f32 v242, v60, v61\n"
    "v_cvt_pk_bf16_f32 v243, v62, v63\n"
    "buffer_store_dwordx4 v[240:243], v6, s[36:39], s85 offen offset:2048 sc1\n"
    "v_mul_f32 v64, v64, v250\n"
    "v_mul_f32 v65, v65, v250\n"
    "v_mul_f32 v66, v66, v250\n"
    "v_mul_f32 v67, v67, v250\n"
    "v_mul_f32 v72, v72, v250\n"
    "v_mul_f32 v73, v73, v250\n"
    "v_mul_f32 v74, v74, v250\n"
    "v_mul_f32 v75, v75, v250\n"
    "v_cvt_pk_bf16_f32 v232, v64, v65\n"
    "v_cvt_pk_bf16_f32 v233, v66, v67\n"
    "v_cvt_pk_bf16_f32 v234, v72, v73\n"
    "v_cvt_pk_bf16_f32 v235, v74, v75\n"
    "buffer_store_dwordx4 v[232:235], v6, s[36:39], s84 offen offset:3072 sc1\n"
    "v_mul_f32 v68, v68, v252\n"
    "v_mul_f32 v69, v69, v252\n"
    "v_mul_f32 v70, v70, v252\n"
    "v_mul_f32 v71, v71, v252\n"
    "v_mul_f32 v76, v76, v252\n"
    "v_mul_f32 v77, v77, v252\n"
    "v_mul_f32 v78, v78, v252\n"
    "v_mul_f32 v79, v79, v252\n"
    "v_cvt_pk_bf16_f32 v236, v68, v69\n"
    "v_cvt_pk_bf16_f32 v237, v70, v71\n"
    "v_cvt_pk_bf16_f32 v238, v76, v77\n"
    "v_cvt_pk_bf16_f32 v239, v78, v79\n"
    "buffer_store_dwordx4 v[236:239], v6, s[36:39], s85 offen offset:3072 sc1\n"
    "s_waitcnt vmcnt(0) lgkmcnt(0)\n"
    "s_barrier\n"
    "s_cmp_lg_u32 s12, 0\n"
    "s_cbranch_scc1 .Lknf_45\n"
    "v_mov_b32 v244, s68\n"
    "v_mov_b32 v245, s69\n"
    "v_mov_b32 v246, 0\n"
    "s_lshl_b32 s60, s10, 6\n"
    "s_add_u32 s60, s60, 0x5000\n"
    "s_mov_b64 exec, 1\n"
    "s_nop 0\n"
    "buffer_store_dwordx2 v[244:245], v246, s[44:47], s60 offen sc1\n"
    "s_mov_b64 exec, -1\n"
    ".Lknf_45:\n"
    "s_endpgm\n"
    ".LkH_1:\n"
    "s_load_dwordx2 s[68:69], s[86:87], 0x0\n"
    "s_lshl_b32 s64, s13, 5\n"
    "s_add_u32 s64, s64, s14\n"
    "s_lshl_b32 s64, s64, 18\n"
    "s_add_u32 s16, s78, s64\n"
    "s_addc_u32 s17, s79, 0\n"
    "s_sub_u32 s16, s16, 0x380\n"
    "s_subb_u32 s17, s17, 0\n"
    "s_and_b32 s17, s17, 0xffff\n"
    "s_mov_b32 s18, 0x80380\n"
    "s_mov_b32 s19, 0x20000\n"
    "s_add_u32 s20, s80, s64\n"
    "s_addc_u32 s21, s81, 0\n"
    "s_sub_u32 s20, s20, 0x380\n"
    "s_subb_u32 s21, s21, 0\n"
    "s_and_b32 s21, s21, 0xffff\n"
    "s_mov_b32 s22, 0x80380\n"
    "s_mov_b32 s23, 0x20000\n"
    "s_lshl_b32 s64, s99, 8\n"
    "s_add_u32 s32, s92, s64\n"
    "s_addc_u32 s33, s93, 0\n"
    "s_sub_u32 s32, s32, 0x400\n"
    "s_subb_u32 s33, s33, 0\n"
    "s_and_b32 s33, s33, 0xffff\n"
    "s_mov_b32 s34, 0\n"
    "s_mov_b32 s35, 0x20000\n"
    "s_sub_u32 s34, 0x500400, s64\n"
    "s_mul_i32 s64, s89, 0x900000\n"
    "s_add_u32 s64, s64, 0x10000\n"
    "s_add_u32 s36, s86, s64\n"
    "s_addc_u32 s37, s87, 0\n"
    "s_and_b32 s37, s37, 0xffff\n"
    "s_mov_b32 s38, 0x900000\n"
    "s_mov_b32 s39, 0x20000\n"
    "s_mov_b32 s40, s84\n"
    "s_and_b32 s41, s85, 0xffff\n"
    "s_mov_b32 s42, 0x2d0000\n"
    "s_mov_b32 s43, 0x20000\n"
    "s_mov_b32 s44, s86\n"
    "s_and_b32 s45, s87, 0xffff\n"
    "s_mov_b32 s46, 0x10000\n"
    "s_mov_b32 s47, 0x20000\n"
    "s_setprio 1\n"
    "v_and_b32 v240, 15, v1\n"
    "v_lshlrev_b32 v240, 7, v240\n"
    "v_lshrrev_b32 v252, 4, v1\n"
    "v_add_u32 v252, 0, v252\n"
    "v_lshrrev_b32 v251, 1, v1\n"
    "v_and_b32 v251, 7, v251\n"
    "v_xor_b32 v252, v252, v251\n"
    "v_lshlrev_b32 v252, 4, v252\n"
    "v_add_u32 v240, v240, v252\n"
    "v_and_b32 v241, 15, v1\n"
    "v_lshlrev_b32 v241, 7, v241\n"
    "v_lshrrev_b32 v252, 4, v1\n"
    "v_add_u32 v252, 4, v252\n"
    "v_lshrrev_b32 v251, 1, v1\n"
    "v_and_b32 v251, 7, v251\n"
    "v_xor_b32 v252, v252, v251\n"
    "v_lshlrev_b32 v252, 4, v252\n"
    "v_add_u32 v241, v241, v252\n"
    "s_lshl_b32 s65, s12, 9\n"
    "s_add_u32 s65, s65, 0x4800\n"
    "v_lshrrev_b32 v228, 4, v1\n"
    "v_lshlrev_b32 v228, 11, v228\n"
    "v_lshrrev_b32 v252, 2, v1\n"
    "v_and_b32 v252, 3, v252\n"
    "v_add_u32 v252, 0, v252\n"
    "v_lshlrev_b32 v252, 6, v252\n"
    "v_add_u32 v228, v228, v252\n"
    "v_lshrrev_b32 v252, 1, v1\n"
    "v_and_b32 v252, 1, v252\n"
    "v_add_u32 v252, 0, v252\n"
    "v_lshrrev_b32 v251, 4, v1\n"
    "v_and_b32 v251, 1, v251\n"
    "v_lshlrev_b32 v251, 1, v251\n"
    "v_add_u32 v251, 0, v251\n"
    "v_xor_b32 v252, v252, v251\n"
    "v_lshlrev_b32 v252, 4, v252\n"
    "v_add_u32 v228, v228, v252\n"
    "v_and_b32 v252, 1, v1\n"
    "v_lshlrev_b32 v252, 3, v252\n"
    "v_add_u32 v228, v228, v252\n"
    "v_add_u32 v228, s65, v228\n"
    "v_lshrrev_b32 v229, 4, v1\n"
    "v_lshlrev_b32 v229, 11, v229\n"
    "v_lshrrev_b32 v252, 2, v1\n"
    "v_and_b32 v252, 3, v252\n"
    "v_add_u32 v252, 4, v252\n"
    "v_lshlrev_b32 v252, 6, v252\n"
    "v_add_u32 v229, v229, v252\n"
    "v_lshrrev_b32 v252, 1, v1\n"
    "v_and_b32 v252, 1, v252\n"
    "v_add_u32 v252, 0, v252\n"
    "v_lshrrev_b32 v251, 4, v1\n"
    "v_and_b32 v251, 1, v251\n"
    "v_lshlrev_b32 v251, 1, v251\n"
    "v_add_u32 v251, 1, v251\n"
    "v_xor_b32 v252, v252, v251\n"
    "v_lshlrev_b32 v252, 4, v252\n"
    "v_add_u32 v229, v229, v252\n"
    "v_and_b32 v252, 1, v1\n"
    "v_lshlrev_b32 v252, 3, v252\n"
    "v_add_u32 v229, v229, v252\n"
    "v_add_u32 v229, s65, v229\n"
    "v_lshrrev_b32 v230, 4, v1\n"
    "v_lshlrev_b32 v230, 11, v230\n"
    "v_lshrrev_b32 v252, 2, v1\n"
    "v_and_b32 v252, 3, v252\n"
    "v_add_u32 v252, 0, v252\n"
    "v_lshlrev_b32 v252, 6, v252\n"
    "v_add_u32 v230, v230, v252\n"
    "v_lshrrev_b32 v252, 1, v1\n"
    "v_and_b32 v252, 1, v252\n"
    "v_add_u32 v252, 2, v252\n"
    "v_lshrrev_b32 v251, 4, v1\n"
    "v_and_b32 v251, 1, v251\n"
    "v_lshlrev_b32 v251, 1, v251\n"
    "v_add_u32 v251, 0, v251\n"
    "v_xor_b32 v252, v252, v251\n"
    "v_lshlrev_b32 v252, 4, v252\n"
    "v_add_u32 v230, v230, v252\n"
    "v_and_b32 v252, 1, v1\n"
    "v_lshlrev_b32 v252, 3, v252\n"
    "v_add_u32 v230, v230, v252\n"
    "v_add_u32 v230, s65, v230\n"
    "v_lshrrev_b32 v231, 4, v1\n"
    "v_lshlrev_b32 v231, 11, v231\n"
    "v_lshrrev_b32 v252, 2, v1\n"
    "v_and_b32 v252, 3, v252\n"
    "v_add_u32 v252, 4, v252\n"
    "v_lshlrev_b32 v252, 6, v252\n"
    "v_add_u32 v231, v231, v252\n"
    "v_lshrrev_b32 v252, 1, v1\n"
    "v_and_b32 v252, 1, v252\n"
    "v_add_u32 v252, 2, v252\n"
    "v_lshrrev_b32 v251, 4, v1\n"
    "v_and_b32 v251, 1, v251\n"
    "v_lshlrev_b32 v251, 1, v251\n"
    "v_add_u32 v251, 1, v251\n"
    "v_xor_b32 v252, v252, v251\n"
    "v_lshlrev_b32 v252, 4, v252\n"
    "v_add_u32 v231, v231, v252\n"
    "v_and_b32 v252, 1, v1\n"
    "v_lshlrev_b32 v252, 3, v252\n"
    "v_add_u32 v231, v231, v252\n"
    "v_add_u32 v231, s65, v231\n"
    "v_mov_b32 v176, 0\n"
    "v_mov_b32 v177, 0\n"
    "v_mov_b32 v178, 0\n"
    "v_mov_b32 v179, 0\n"
    "s_nop 1\n"
    "v_mfma_f32_32x32x16_bf16 v[16:31], v[176:179], v[176:179], 0\n"
    "v_mfma_f32_32x32x16_bf16 v[32:47], v[176:179], v[176:179], 0\n"
    "v_mfma_f32_32x32x16_bf16 v[48:63], v[176:179], v[176:179], 0\n"
    "v_mfma_f32_32x32x16_bf16 v[64:79], v[176:179], v[176:179], 0\n"
    "v_mfma_f32_16x16x32_bf16 v[80:83], v[176:179], v[176:179], 0\n"
    "v_mfma_f32_16x16x32_bf16 v[84:87], v[176:179], v[176:179], 0\n"
    "v_lshrrev_b32 v236, 2, v1\n"
    "v_and_b32 v236, 3, v236\n"
    "v_lshlrev_b32 v236, 11, v236\n"
    "v_and_b32 v252, 3, v1\n"
    "v_add_u32 v252, 0, v252\n"
    "v_lshlrev_b32 v252, 6, v252\n"
    "v_add_u32 v236, v236, v252\n"
    "v_lshrrev_b32 v252, 4, v1\n"
    "v_lshrrev_b32 v251, 2, v1\n"
    "v_and_b32 v251, 1, v251\n"
    "v_lshlrev_b32 v251, 1, v251\n"
    "v_add_u32 v251, 0, v251\n"
    "v_xor_b32 v252, v252, v251\n"
    "v_lshlrev_b32 v252, 4, v252\n"
    "v_add_u32 v236, v236, v252\n"
    "v_lshrrev_b32 v237, 2, v1\n"
    "v_and_b32 v237, 3, v237\n"
    "v_lshlrev_b32 v237, 11, v237\n"
    "v_and_b32 v252, 3, v1\n"
    "v_add_u32 v252, 4, v252\n"
    "v_lshlrev_b32 v252, 6, v252\n"
    "v_add_u32 v237, v237, v252\n"
    "v_lshrrev_b32 v252, 4, v1\n"
    "v_lshrrev_b32 v251, 2, v1\n"
    "v_and_b32 v251, 1, v251\n"
    "v_lshlrev_b32 v251, 1, v251\n"
    "v_add_u32 v251, 1, v251\n"
    "v_xor_b32 v252, v252, v251\n"
    "v_lshlrev_b32 v252, 4, v252\n"
    "v_add_u32 v237, v237, v252\n"
    "v_lshrrev_b32 v0, 4, v1\n"
    "v_lshlrev_b32 v0, 11, v0\n"
    "v_lshrrev_b32 v252, 2, v1\n"
    "v_and_b32 v252, 3, v252\n"
    "v_lshlrev_b32 v252, 6, v252\n"
    "v_add_u32 v0, v0, v252\n"
    "v_lshrrev_b32 v252, 4, v1\n"
    "v_and_b32 v252, 1, v252\n"
    "v_xor_b32 v252, 0, v252\n"
    "v_lshlrev_b32 v252, 5, v252\n"
    "v_add_u32 v0, v0, v252\n"
    "v_and_b32 v252, 3, v1\n"
    "v_lshlrev_b32 v252, 3, v252\n"
    "v_add_u32 v0, v0, v252\n"
    "v_lshlrev_b32 v11, 4, v1\n"
    "v_add_u32 v225, s73, v11\n"
    "s_xor_b32 s60, s90, 3\n"
    "s_and_b32 s61, s60, 1\n"
    "s_lshr_b32 s60, s60, 1\n"
    "s_lshl_b32 s60, s60, 7\n"
    "v_lshrrev_b32 v4, 2, v1\n"
    "v_and_b32 v4, 7, v4\n"
    "v_mul_u32_u24 v4, 0x500, v4\n"
    "v_lshrrev_b32 v252, 5, v1\n"
    "v_xor_b32 v252, s61, v252\n"
    "v_lshlrev_b32 v252, 6, v252\n"
    "v_add_u32 v4, v4, v252\n"
    "v_and_b32 v252, 3, v1\n"
    "v_lshrrev_b32 v251, 4, v1\n"
    "v_and_b32 v251, 1, v251\n"
    "v_lshlrev_b32 v251, 1, v251\n"
    "v_xor_b32 v252, v252, v251\n"
    "v_lshlrev_b32 v252, 4, v252\n"
    "v_add_u32 v4, v4, v252\n"
    "v_add_u32 v4, 0x400, v4\n"
    "s_sub_u32 s61, 0xfffffc80, s60\n"
    "v_add_u32 v5, s61, v4\n"
    "v_add_u32 v4, s60, v4\n"
    "v_lshrrev_b32 v12, 3, v1\n"
    "v_and_b32 v12, 1, v12\n"
    "v_lshlrev_b32 v12, 11, v12\n"
    "v_and_b32 v252, 7, v1\n"
    "v_lshlrev_b32 v252, 6, v252\n"
    "v_add_u32 v12, v12, v252\n"
    "v_lshrrev_b32 v252, 4, v1\n"
    "v_lshrrev_b32 v251, 2, v1\n"
    "v_and_b32 v251, 3, v251\n"
    "v_xor_b32 v252, v252, v251\n"
    "v_lshlrev_b32 v252, 4, v252\n"
    "v_add_u32 v12, v12, v252\n"
    "v_add_u32 v12, 0x26800, v12\n"
    "v_lshrrev_b32 v238, 5, v1\n"
    "v_lshlrev_b32 v238, 11, v238\n"
    "v_lshrrev_b32 v252, 2, v1\n"
    "v_and_b32 v252, 7, v252\n"
    "v_lshlrev_b32 v252, 6, v252\n"
    "v_add_u32 v238, v238, v252\n"
    "v_lshrrev_b32 v252, 4, v1\n"
    "v_and_b32 v252, 1, v252\n"
    "v_xor_b32 v252, 0, v252\n"
    "v_lshlrev_b32 v252, 5, v252\n"
    "v_add_u32 v238, v238, v252\n"
    "v_and_b32 v252, 3, v1\n"
    "v_lshlrev_b32 v252, 3, v252\n"
    "v_add_u32 v238, v238, v252\n"
    "v_lshrrev_b32 v239, 5, v1\n"
    "v_lshlrev_b32 v239, 11, v239\n"
    "v_lshrrev_b32 v252, 2, v1\n"
    "v_and_b32 v252, 7, v252\n"
    "v_lshlrev_b32 v252, 6, v252\n"
    "v_add_u32 v239, v239, v252\n"
    "v_lshrrev_b32 v252, 4, v1\n"
    "v_and_b32 v252, 1, v252\n"
    "v_xor_b32 v252, 1, v252\n"
    "v_lshlrev_b32 v252, 5, v252\n"
    "v_add_u32 v239, v239, v252\n"
    "v_and_b32 v252, 3, v1\n"
    "v_lshlrev_b32 v252, 3, v252\n"
    "v_add_u32 v239, v239, v252\n"
    "s_lshl_b32 s60, s12, 9\n"
    "s_add_u32 s60, s60, 0x1e800\n"
    "v_lshrrev_b32 v6, 3, v1\n"
    "v_and_b32 v6, 1, v6\n"
    "v_lshlrev_b32 v6, 11, v6\n"
    "v_and_b32 v252, 7, v1\n"
    "v_lshlrev_b32 v252, 6, v252\n"
    "v_add_u32 v6, v6, v252\n"
    "v_lshrrev_b32 v252, 5, v1\n"
    "v_add_u32 v252, 0, v252\n"
    "v_lshrrev_b32 v251, 2, v1\n"
    "v_and_b32 v251, 3, v251\n"
    "v_xor_b32 v252, v252, v251\n"
    "v_lshlrev_b32 v252, 4, v252\n"
    "v_add_u32 v6, v6, v252\n"
    "v_lshrrev_b32 v252, 4, v1\n"
    "v_and_b32 v252, 1, v252\n"
    "v_lshlrev_b32 v252, 3, v252\n"
    "v_add_u32 v6, v6, v252\n"
    "v_add_u32 v6, s60, v6\n"
    "v_lshrrev_b32 v97, 3, v1\n"
    "v_and_b32 v97, 1, v97\n"
    "v_lshlrev_b32 v97, 11, v97\n"
    "v_and_b32 v252, 7, v1\n"
    "v_lshlrev_b32 v252, 6, v252\n"
    "v_add_u32 v97, v97, v252\n"
    "v_lshrrev_b32 v252, 5, v1\n"
    "v_add_u32 v252, 2, v252\n"
    "v_lshrrev_b32 v251, 2, v1\n"
    "v_and_b32 v251, 3, v251\n"
    "v_xor_b32 v252, v252, v251\n"
    "v_lshlrev_b32 v252, 4, v252\n"
    "v_add_u32 v97, v97, v252\n"
    "v_lshrrev_b32 v252, 4, v1\n"
    "v_and_b32 v252, 1, v252\n"
    "v_lshlrev_b32 v252, 3, v252\n"
    "v_add_u32 v97, v97, v252\n"
    "v_add_u32 v97, s60, v97\n"
    "s_mul_i32 s60, s91, 0xac\n"
    "s_lshr_b32 s60, s60, 7\n"
    "s_lshl_b32 s60, s60, 11\n"
    "s_and_b32 s61, s10, 7\n"
    "s_lshl_b32 s61, s61, 6\n"
    "s_add_u32 s60, s60, s61\n"
    "v_and_b32 v13, 7, v1\n"
    "v_lshlrev_b32 v13, 9, v13\n"
    "v_add_u32 v13, s60, v13\n"
    "v_mov_b32 v168, 0\n"
    "v_mov_b32 v172, 0\n"
    "v_mov_b32 v160, 0\n"
    "v_mov_b32 v164, 0xff800000\n"
    "v_mov_b32 v169, 0\n"
    "v_mov_b32 v173, 0\n"
    "v_mov_b32 v161, 0\n"
    "v_mov_b32 v165, 0xff800000\n"
    "v_mov_b32 v170, 0\n"
    "v_mov_b32 v174, 0\n"
    "v_mov_b32 v162, 0\n"
    "v_mov_b32 v166, 0xff800000\n"
    "v_mov_b32 v171, 0\n"
    "v_mov_b32 v175, 0\n"
    "v_mov_b32 v163, 0\n"
    "v_mov_b32 v167, 0xff800000\n"
    "v_mov_b32 v14, 0\n"
    "v_mov_b32 v9, v2\n"
    "v_mov_b32 v10, v3\n"
    "s_mov_b32 s70, 0xff800000\n"
    "s_mov_b32 s71, 0xff800000\n"
    "s_mov_b32 s94, 0x3e0293ee\n"
    "s_mov_b32 s95, s94\n"
    "s_cmp_eq_u32 s12, 2\n"
    "s_cselect_b32 s76, -1, 0\n"
    "s_cmp_lt_u32 s91, 6\n"
    "s_cselect_b32 s82, 1, 0\n"
    "s_mov_b32 s79, 0\n"
    "s_mov_b32 s77, 0\n"
    "s_mov_b32 s86, s73\n"
    "s_mov_b32 s87, 0xc000\n"
    "s_lshl2_add_u32 s60, s91, s12\n"
    "s_mul_i32 s60, s60, 0x60000\n"
    "s_lshl_b32 s61, s90, 15\n"
    "s_add_u32 s81, s60, s61\n"
    "s_add_u32 s60, s99, s91\n"
    "s_mul_i32 s60, s60, 1\n"
    "s_and_b32 s80, s60, 3\n"
    "s_mul_i32 s65, s13, 9\n"
    "s_add_u32 s65, s65, 8\n"
    "s_lshl_b32 s65, s65, 5\n"
    "s_add_u32 s65, s65, s14\n"
    "s_lshl_b32 s65, s65, 2\n"
    "s_add_u32 s65, s65, s12\n"
    "s_lshl_b32 s84, s65, 10\n"
    "s_mov_b32 s48, s16\n"
    "s_mov_b32 s49, s17\n"
    "s_mov_b32 s50, 0x80380\n"
    "s_mov_b32 s51, 0x20000\n"
    "s_cmp_lt_u32 s12, 2\n"
    "s_cselect_b32 s60, 0, 0xe000\n"
    "s_cselect_b32 s61, 0xe000, 0\n"
    "s_add_u32 s52, s60, 0\n"
    "s_add_u32 s56, s61, 0\n"
    "s_add_u32 s53, s60, 0x2000\n"
    "s_add_u32 s57, s61, 0x2000\n"
    "s_add_u32 s54, s60, 0x4000\n"
    "s_add_u32 s58, s61, 0x4000\n"
    "s_add_u32 s55, s60, 0x6000\n"
    "s_add_u32 s59, s61, 0x6000\n"
    "s_waitcnt lgkmcnt(0)\n"
    "s_add_u32 s68, s68, 1\n"
    "s_addc_u32 s69, s69, 0\n"
    "s_barrier\n"
    "v_add_u32 v232, 0x16000, v228\n"
    "v_add_u32 v233, 0x16000, v229\n"
    "v_add_u32 v234, 0x16000, v230\n"
    "v_add_u32 v235, 0x16000, v231\n"
    "v_add_u32 v242, 0x16000, v240\n"
    "v_add_u32 v243, 0x16000, v241\n"
    "ds_read_b64_tr_b16 v[176:177], v232\n"
    "ds_read_b64_tr_b16 v[178:179], v233\n"
    "ds_read_b128 v[192:195], v242\n"
    "ds_read_b64_tr_b16 v[184:185], v232 offset:8192\n"
    "ds_read_b64_tr_b16 v[186:187], v233 offset:8192\n"
    "ds_read_b128 v[196:199], v243\n"
    "ds_read_b64_tr_b16 v[188:189], v234 offset:8192\n"
    "ds_read_b64_tr_b16 v[190:191], v235 offset:8192\n"
    "ds_read_b64_tr_b16 v[180:181], v234\n"
    "ds_read_b64_tr_b16 v[182:183], v235\n"
    "ds_read_b128 v[200:203], v242 offset:2048\n"
    "ds_read_b128 v[204:207], v243 offset:2048\n"
    "s_waitcnt lgkmcnt(2)\n"
    "v_mfma_f32_16x16x32_bf16 v[16:19], v[176:179], v[192:195], v[16:19]\n"
    "v_mfma_f32_16x16x32_bf16 v[16:19], v[184:187], v[196:199], v[16:19]\n"
    "ds_read_b128 v[208:211], v242 offset:4096\n"
    "v_mfma_f32_16x16x32_bf16 v[52:55], v[188:191], v[196:199], v[52:55]\n"
    "v_mfma_f32_16x16x32_bf16 v[52:55], v[180:183], v[192:195], v[52:55]\n"
    "ds_read_b128 v[212:215], v243 offset:4096\n"
    "s_waitcnt lgkmcnt(2)\n"
    "v_mfma_f32_16x16x32_bf16 v[20:23], v[176:179], v[200:203], v[20:23]\n"
    "v_mfma_f32_16x16x32_bf16 v[20:23], v[184:187], v[204:207], v[20:23]\n"
    "ds_read_b128 v[192:195], v242 offset:6144\n"
    "v_mfma_f32_16x16x32_bf16 v[56:59], v[188:191], v[204:207], v[56:59]\n"
    "v_mfma_f32_16x16x32_bf16 v[56:59], v[180:183], v[200:203], v[56:59]\n"
    "ds_read_b128 v[196:199], v243 offset:6144\n"
    "s_waitcnt lgkmcnt(2)\n"
    "v_mfma_f32_16x16x32_bf16 v[24:27], v[176:179], v[208:211], v[24:27]\n"
    "v_mfma_f32_16x16x32_bf16 v[24:27], v[184:187], v[212:215], v[24:27]\n"
    "ds_read_b128 v[200:203], v242 offset:8192\n"
    "v_mfma_f32_16x16x32_bf16 v[60:63], v[188:191], v[212:215], v[60:63]\n"
    "v_mfma_f32_16x16x32_bf16 v[60:63], v[180:183], v[208:211], v[60:63]\n"
    "ds_read_b128 v[204:207], v243 offset:8192\n"
    "s_waitcnt lgkmcnt(2)\n"
    "v_mfma_f32_16x16x32_bf16 v[28:31], v[176:179], v[192:195], v[28:31]\n"
    "v_mfma_f32_16x16x32_bf16 v[28:31], v[184:187], v[196:199], v[28:31]\n"
    "ds_read_b128 v[208:211], v242 offset:10240\n"
    "v_mfma_f32_16x16x32_bf16 v[64:67], v[188:191], v[196:199], v[64:67]\n"
    "v_mfma_f32_16x16x32_bf16 v[64:67], v[180:183], v[192:195], v[64:67]\n"
    "ds_read_b128 v[212:215], v243 offset:10240\n"
    "s_waitcnt lgkmcnt(2)\n"
    "v_mfma_f32_16x16x32_bf16 v[32:35], v[176:179], v[200:203], v[32:35]\n"
    "v_mfma_f32_16x16x32_bf16 v[32:35], v[184:187], v[204:207], v[32:35]\n"
    "ds_read_b128 v[192:195], v242 offset:12288\n"
    "v_mfma_f32_16x16x32_bf16 v[68:71], v[188:191], v[204:207], v[68:71]\n"
    "v_mfma_f32_16x16x32_bf16 v[68:71], v[180:183], v[200:203], v[68:71]\n"
    "ds_read_b128 v[196:199], v243 offset:12288\n"
    "s_waitcnt lgkmcnt(2)\n"
    "v_mfma_f32_16x16x32_bf16 v[36:39], v[176:179], v[208:211], v[36:39]\n"
    "v_mfma_f32_16x16x32_bf16 v[36:39], v[184:187], v[212:215], v[36:39]\n"
    "ds_read_b128 v[200:203], v242 offset:14336\n"
    "v_mfma_f32_16x16x32_bf16 v[72:75], v[188:191], v[212:215], v[72:75]\n"
    "v_mfma_f32_16x16x32_bf16 v[72:75], v[180:183], v[208:211], v[72:75]\n"
    "ds_read_b128 v[204:207], v243 offset:14336\n"
    "s_waitcnt lgkmcnt(2)\n"
    "v_mfma_f32_16x16x32_bf16 v[40:43], v[176:179], v[192:195], v[40:43]\n"
    "v_mfma_f32_16x16x32_bf16 v[40:43], v[184:187], v[196:199], v[40:43]\n"
    "ds_read_b128 v[208:211], v242 offset:16384\n"
    "v_mfma_f32_16x16x32_bf16 v[76:79], v[188:191], v[196:199], v[76:79]\n"
    "v_mfma_f32_16x16x32_bf16 v[76:79], v[180:183], v[192:195], v[76:79]\n"
    "ds_read_b128 v[212:215], v243 offset:16384\n"
    "s_waitcnt lgkmcnt(2)\n"
    "v_mfma_f32_16x16x32_bf16 v[44:47], v[176:179], v[200:203], v[44:47]\n"
    "v_mfma_f32_16x16x32_bf16 v[44:47], v[184:187], v[204:207], v[44:47]\n"
    "v_mfma_f32_16x16x32_bf16 v[80:83], v[188:191], v[204:207], v[80:83]\n"
    "v_mfma_f32_16x16x32_bf16 v[80:83], v[180:183], v[200:203], v[80:83]\n"
    "s_waitcnt lgkmcnt(0)\n"
    "s_barrier\n"
    "v_add_u32 v232, 0x1e800, v228\n"
    "v_add_u32 v233, 0x1e800, v229\n"
    "v_add_u32 v234, 0x1e800, v230\n"
    "v_add_u32 v235, 0x1e800, v231\n"
    "v_add_u32 v242, 0x1e800, v240\n"
    "v_add_u32 v243, 0x1e800, v241\n"
    "ds_read_b64_tr_b16 v[112:113], v232\n"
    "ds_read_b64_tr_b16 v[114:115], v233\n"
    "ds_read_b128 v[192:195], v242\n"
    "ds_read_b64_tr_b16 v[120:121], v232 offset:8192\n"
    "ds_read_b64_tr_b16 v[122:123], v233 offset:8192\n"
    "ds_read_b128 v[196:199], v243\n"
    "ds_read_b64_tr_b16 v[124:125], v234 offset:8192\n"
    "ds_read_b64_tr_b16 v[126:127], v235 offset:8192\n"
    "ds_read_b64_tr_b16 v[116:117], v234\n"
    "ds_read_b64_tr_b16 v[118:119], v235\n"
    "ds_read_b128 v[200:203], v242 offset:2048\n"
    "ds_read_b128 v[204:207], v243 offset:2048\n"
    "v_mfma_f32_16x16x32_bf16 v[48:51], v[176:179], v[208:211], v[48:51]\n"
    "v_mfma_f32_16x16x32_bf16 v[48:51], v[184:187], v[212:215], v[48:51]\n"
    "v_mfma_f32_16x16x32_bf16 v[84:87], v[188:191], v[212:215], v[84:87]\n"
    "v_mfma_f32_16x16x32_bf16 v[84:87], v[180:183], v[208:211], v[84:87]\n"
    "s_waitcnt lgkmcnt(2)\n"
    "v_mfma_f32_16x16x32_bf16 v[16:19], v[112:115], v[192:195], v[16:19]\n"
    "v_mfma_f32_16x16x32_bf16 v[16:19], v[120:123], v[196:199], v[16:19]\n"
    "ds_read_b128 v[208:211], v242 offset:4096\n"
    "v_mfma_f32_16x16x32_bf16 v[52:55], v[124:127], v[196:199], v[52:55]\n"
    "v_mfma_f32_16x16x32_bf16 v[52:55], v[116:119], v[192:195], v[52:55]\n"
    "ds_read_b128 v[212:215], v243 offset:4096\n"
    "s_waitcnt lgkmcnt(2)\n"
    "v_mfma_f32_16x16x32_bf16 v[20:23], v[112:115], v[200:203], v[20:23]\n"
    "v_mfma_f32_16x16x32_bf16 v[20:23], v[120:123], v[204:207], v[20:23]\n"
    "ds_read_b128 v[192:195], v242 offset:6144\n"
    "v_mfma_f32_16x16x32_bf16 v[56:59], v[124:127], v[204:207], v[56:59]\n"
    "v_mfma_f32_16x16x32_bf16 v[56:59], v[116:119], v[200:203], v[56:59]\n"
    "ds_read_b128 v[196:199], v243 offset:6144\n"
    "s_waitcnt lgkmcnt(2)\n"
    "v_mfma_f32_16x16x32_bf16 v[24:27], v[112:115], v[208:211], v[24:27]\n"
    "v_mfma_f32_16x16x32_bf16 v[24:27], v[120:123], v[212:215], v[24:27]\n"
    "ds_read_b128 v[200:203], v242 offset:8192\n"
    "v_mfma_f32_16x16x32_bf16 v[60:63], v[124:127], v[212:215], v[60:63]\n"
    "v_mfma_f32_16x16x32_bf16 v[60:63], v[116:119], v[208:211], v[60:63]\n"
    "ds_read_b128 v[204:207], v243 offset:8192\n"
    "s_waitcnt lgkmcnt(2)\n"
    "v_mfma_f32_16x16x32_bf16 v[28:31], v[112:115], v[192:195], v[28:31]\n"
    "v_mfma_f32_16x16x32_bf16 v[28:31], v[120:123], v[196:199], v[28:31]\n"
    "ds_read_b128 v[208:211], v242 offset:10240\n"
    "v_mfma_f32_16x16x32_bf16 v[64:67], v[124:127], v[196:199], v[64:67]\n"
    "v_mfma_f32_16x16x32_bf16 v[64:67], v[116:119], v[192:195], v[64:67]\n"
    "ds_read_b128 v[212:215], v243 offset:10240\n"
    "s_waitcnt lgkmcnt(2)\n"
    "v_mfma_f32_16x16x32_bf16 v[32:35], v[112:115], v[200:203], v[32:35]\n"
    "v_mfma_f32_16x16x32_bf16 v[32:35], v[120:123], v[204:207], v[32:35]\n"
    "ds_read_b128 v[192:195], v242 offset:12288\n"
    "v_mfma_f32_16x16x32_bf16 v[68:71], v[124:127], v[204:207], v[68:71]\n"
    "v_mfma_f32_16x16x32_bf16 v[68:71], v[116:119], v[200:203], v[68:71]\n"
    "ds_read_b128 v[196:199], v243 offset:12288\n"
    "s_waitcnt lgkmcnt(2)\n"
    "v_mfma_f32_16x16x32_bf16 v[36:39], v[112:115], v[208:211], v[36:39]\n"
    "v_mfma_f32_16x16x32_bf16 v[36:39], v[120:123], v[212:215], v[36:39]\n"
    "ds_read_b128 v[200:203], v242 offset:14336\n"
    "v_mfma_f32_16x16x32_bf16 v[72:75], v[124:127], v[212:215], v[72:75]\n"
    "v_mfma_f32_16x16x32_bf16 v[72:75], v[116:119], v[208:211], v[72:75]\n"
    "ds_read_b128 v[204:207], v243 offset:14336\n"
    "s_waitcnt lgkmcnt(2)\n"
    "v_mfma_f32_16x16x32_bf16 v[40:43], v[112:115], v[192:195], v[40:43]\n"
    "v_mfma_f32_16x16x32_bf16 v[40:43], v[120:123], v[196:199], v[40:43]\n"
    "ds_read_b128 v[208:211], v242 offset:16384\n"
    "v_mfma_f32_16x16x32_bf16 v[76:79], v[124:127], v[196:199], v[76:79]\n"
    "v_mfma_f32_16x16x32_bf16 v[76:79], v[116:119], v[192:195], v[76:79]\n"
    "ds_read_b128 v[212:215], v243 offset:16384\n"
    "s_waitcnt lgkmcnt(2)\n"
    "v_mfma_f32_16x16x32_bf16 v[44:47], v[112:115], v[200:203], v[44:47]\n"
    "v_mfma_f32_16x16x32_bf16 v[44:47], v[120:123], v[204:207], v[44:47]\n"
    "v_mfma_f32_16x16x32_bf16 v[80:83], v[124:127], v[204:207], v[80:83]\n"
    "v_mfma_f32_16x16x32_bf16 v[80:83], v[116:119], v[200:203], v[80:83]\n"
    "s_waitcnt lgkmcnt(0)\n"
    "s_barrier\n"
    "v_add_u32 v232, 0xc000, v228\n"
    "v_add_u32 v233, 0xc000, v229\n"
    "v_add_u32 v234, 0xc000, v230\n"
    "v_add_u32 v235, 0xc000, v231\n"
    "v_add_u32 v242, 0xc000, v240\n"
    "v_add_u32 v243, 0xc000, v241\n"
    "ds_read_b64_tr_b16 v[176:177], v232\n"
    "ds_read_b64_tr_b16 v[178:179], v233\n"
    "ds_read_b128 v[192:195], v242\n"
    "ds_read_b64_tr_b16 v[184:185], v232 offset:8192\n"
    "ds_read_b64_tr_b16 v[186:187], v233 offset:8192\n"
    "ds_read_b128 v[196:199], v243\n"
    "ds_read_b64_tr_b16 v[188:189], v234 offset:8192\n"
    "ds_read_b64_tr_b16 v[190:191], v235 offset:8192\n"
    "ds_read_b64_tr_b16 v[180:181], v234\n"
    "ds_read_b64_tr_b16 v[182:183], v235\n"
    "ds_read_b128 v[200:203], v242 offset:2048\n"
    "ds_read_b128 v[204:207], v243 offset:2048\n"
    "v_mfma_f32_16x16x32_bf16 v[48:51], v[112:115], v[208:211], v[48:51]\n"
    "v_mfma_f32_16x16x32_bf16 v[48:51], v[120:123], v[212:215], v[48:51]\n"
    "v_mfma_f32_16x16x32_bf16 v[84:87], v[124:127], v[212:215], v[84:87]\n"
    "v_mfma_f32_16x16x32_bf16 v[84:87], v[116:119], v[208:211], v[84:87]\n"
    "s_waitcnt lgkmcnt(2)\n"
    "v_mfma_f32_16x16x32_bf16 v[16:19], v[176:179], v[192:195], v[16:19]\n"
    "v_mfma_f32_16x16x32_bf16 v[16:19], v[184:187], v[196:199], v[16:19]\n"
    "ds_read_b128 v[208:211], v242 offset:4096\n"
    "v_mfma_f32_16x16x32_bf16 v[52:55], v[188:191], v[196:199], v[52:55]\n"
    "v_mfma_f32_16x16x32_bf16 v[52:55], v[180:183], v[192:195], v[52:55]\n"
    "ds_read_b128 v[212:215], v243 offset:4096\n"
    "s_waitcnt lgkmcnt(2)\n"
    "v_mfma_f32_16x16x32_bf16 v[20:23], v[176:179], v[200:203], v[20:23]\n"
    "v_mfma_f32_16x16x32_bf16 v[20:23], v[184:187], v[204:207], v[20:23]\n"
    "ds_read_b128 v[192:195], v242 offset:6144\n"
    "v_mfma_f32_16x16x32_bf16 v[56:59], v[188:191], v[204:207], v[56:59]\n"
    "v_mfma_f32_16x16x32_bf16 v[56:59], v[180:183], v[200:203], v[56:59]\n"
    "ds_read_b128 v[196:199], v243 offset:6144\n"
    "s_waitcnt lgkmcnt(2)\n"
    "v_mfma_f32_16x16x32_bf16 v[24:27], v[176:179], v[208:211], v[24:27]\n"
    "v_mfma_f32_16x16x32_bf16 v[24:27], v[184:187], v[212:215], v[24:27]\n"
    "ds_read_b128 v[200:203], v242 offset:8192\n"
    "v_mfma_f32_16x16x32_bf16 v[60:63], v[188:191], v[212:215], v[60:63]\n"
    "v_mfma_f32_16x16x32_bf16 v[60:63], v[180:183], v[208:211], v[60:63]\n"
    "ds_read_b128 v[204:207], v243 offset:8192\n"
    "s_waitcnt lgkmcnt(2)\n"
    "v_mfma_f32_16x16x32_bf16 v[28:31], v[176:179], v[192:195], v[28:31]\n"
    "v_mfma_f32_16x16x32_bf16 v[28:31], v[184:187], v[196:199], v[28:31]\n"
    "ds_read_b128 v[208:211], v242 offset:10240\n"
    "v_mfma_f32_16x16x32_bf16 v[64:67], v[188:191], v[196:199], v[64:67]\n"
    "v_mfma_f32_16x16x32_bf16 v[64:67], v[180:183], v[192:195], v[64:67]\n"
    "ds_read_b128 v[212:215], v243 offset:10240\n"
    "s_waitcnt lgkmcnt(2)\n"
    "v_mfma_f32_16x16x32_bf16 v[32:35], v[176:179], v[200:203], v[32:35]\n"
    "v_mfma_f32_16x16x32_bf16 v[32:35], v[184:187], v[204:207], v[32:35]\n"
    "ds_read_b128 v[192:195], v242 offset:12288\n"
    "v_mfma_f32_16x16x32_bf16 v[68:71], v[188:191], v[204:207], v[68:71]\n"
    "v_mfma_f32_16x16x32_bf16 v[68:71], v[180:183], v[200:203], v[68:71]\n"
    "ds_read_b128 v[196:199], v243 offset:12288\n"
    "s_waitcnt lgkmcnt(2)\n"
    "v_mfma_f32_16x16x32_bf16 v[36:39], v[176:179], v[208:211], v[36:39]\n"
    "v_mfma_f32_16x16x32_bf16 v[36:39], v[184:187], v[212:215], v[36:39]\n"
    "ds_read_b128 v[200:203], v242 offset:14336\n"
    "v_mfma_f32_16x16x32_bf16 v[72:75], v[188:191], v[212:215], v[72:75]\n"
    "v_mfma_f32_16x16x32_bf16 v[72:75], v[180:183], v[208:211], v[72:75]\n"
    "ds_read_b128 v[204:207], v243 offset:14336\n"
    "s_waitcnt lgkmcnt(2)\n"
    "v_mfma_f32_16x16x32_bf16 v[40:43], v[176:179], v[192:195], v[40:43]\n"
    "v_mfma_f32_16x16x32_bf16 v[40:43], v[184:187], v[196:199], v[40:43]\n"
    "ds_read_b128 v[208:211], v242 offset:16384\n"
    "v_mfma_f32_16x16x32_bf16 v[76:79], v[188:191], v[196:199], v[76:79]\n"
    "v_mfma_f32_16x16x32_bf16 v[76:79], v[180:183], v[192:195], v[76:79]\n"
    "ds_read_b128 v[212:215], v243 offset:16384\n"
    "s_waitcnt lgkmcnt(2)\n"
    "v_mfma_f32_16x16x32_bf16 v[44:47], v[176:179], v[200:203], v[44:47]\n"
    "v_mfma_f32_16x16x32_bf16 v[44:47], v[184:187], v[204:207], v[44:47]\n"
    "v_mfma_f32_16x16x32_bf16 v[80:83], v[188:191], v[204:207], v[80:83]\n"
    "v_mfma_f32_16x16x32_bf16 v[80:83], v[180:183], v[200:203], v[80:83]\n"
    "s_waitcnt lgkmcnt(0)\n"
    "s_barrier\n"
    "v_add_u32 v232, 0x16000, v228\n"
    "v_add_u32 v233, 0x16000, v229\n"
    "v_add_u32 v234, 0x16000, v230\n"
    "v_add_u32 v235, 0x16000, v231\n"
    "v_add_u32 v242, 0x16000, v240\n"
    "v_add_u32 v243, 0x16000, v241\n"
    "ds_read_b64_tr_b16 v[112:113], v232\n"
    "ds_read_b64_tr_b16 v[114:115], v233\n"
    "ds_read_b128 v[192:195], v242\n"
    "ds_read_b64_tr_b16 v[120:121], v232 offset:8192\n"
    "ds_read_b64_tr_b16 v[122:123], v233 offset:8192\n"
    "ds_read_b128 v[196:199], v243\n"
    "ds_read_b64_tr_b16 v[124:125], v234 offset:8192\n"
    "ds_read_b64_tr_b16 v[126:127], v235 offset:8192\n"
    "ds_read_b64_tr_b16 v[116:117], v234\n"
    "ds_read_b64_tr_b16 v[118:119], v235\n"
    "ds_read_b128 v[200:203], v242 offset:2048\n"
    "ds_read_b128 v[204:207], v243 offset:2048\n"
    "v_mfma_f32_16x16x32_bf16 v[48:51], v[176:179], v[208:211], v[48:51]\n"
    "v_mfma_f32_16x16x32_bf16 v[48:51], v[184:187], v[212:215], v[48:51]\n"
    "v_mfma_f32_16x16x32_bf16 v[84:87], v[188:191], v[212:215], v[84:87]\n"
    "v_mfma_f32_16x16x32_bf16 v[84:87], v[180:183], v[208:211], v[84:87]\n"
    "s_waitcnt lgkmcnt(2)\n"
    "v_mfma_f32_16x16x32_bf16 v[16:19], v[112:115], v[192:195], v[16:19]\n"
    "v_mfma_f32_16x16x32_bf16 v[16:19], v[120:123], v[196:199], v[16:19]\n"
    "ds_read_b128 v[208:211], v242 offset:4096\n"
    "v_mfma_f32_16x16x32_bf16 v[52:55], v[124:127], v[196:199], v[52:55]\n"
    "v_mfma_f32_16x16x32_bf16 v[52:55], v[116:119], v[192:195], v[52:55]\n"
    "ds_read_b128 v[212:215], v243 offset:4096\n"
    "s_waitcnt lgkmcnt(2)\n"
    "v_mfma_f32_16x16x32_bf16 v[20:23], v[112:115], v[200:203], v[20:23]\n"
    "v_mfma_f32_16x16x32_bf16 v[20:23], v[120:123], v[204:207], v[20:23]\n"
    "ds_read_b128 v[192:195], v242 offset:6144\n"
    "v_mfma_f32_16x16x32_bf16 v[56:59], v[124:127], v[204:207], v[56:59]\n"
    "v_mfma_f32_16x16x32_bf16 v[56:59], v[116:119], v[200:203], v[56:59]\n"
    "ds_read_b128 v[196:199], v243 offset:6144\n"
    "s_waitcnt lgkmcnt(2)\n"
    "v_mfma_f32_16x16x32_bf16 v[24:27], v[112:115], v[208:211], v[24:27]\n"
    "v_mfma_f32_16x16x32_bf16 v[24:27], v[120:123], v[212:215], v[24:27]\n"
    "ds_read_b128 v[200:203], v242 offset:8192\n"
    "v_mfma_f32_16x16x32_bf16 v[60:63], v[124:127], v[212:215], v[60:63]\n"
    "v_mfma_f32_16x16x32_bf16 v[60:63], v[116:119], v[208:211], v[60:63]\n"
    "ds_read_b128 v[204:207], v243 offset:8192\n"
    "s_waitcnt lgkmcnt(2)\n"
    "v_mfma_f32_16x16x32_bf16 v[28:31], v[112:115], v[192:195], v[28:31]\n"
    "v_mfma_f32_16x16x32_bf16 v[28:31], v[120:123], v[196:199], v[28:31]\n"
    "ds_read_b128 v[208:211], v242 offset:10240\n"
    "v_mfma_f32_16x16x32_bf16 v[64:67], v[124:127], v[196:199], v[64:67]\n"
    "v_mfma_f32_16x16x32_bf16 v[64:67], v[116:119], v[192:195], v[64:67]\n"
    "ds_read_b128 v[212:215], v243 offset:10240\n"
    "s_waitcnt lgkmcnt(2)\n"
    "v_mfma_f32_16x16x32_bf16 v[32:35], v[112:115], v[200:203], v[32:35]\n"
    "v_mfma_f32_16x16x32_bf16 v[32:35], v[120:123], v[204:207], v[32:35]\n"
    "ds_read_b128 v[192:195], v242 offset:12288\n"
    "v_mfma_f32_16x16x32_bf16 v[68:71], v[124:127], v[204:207], v[68:71]\n"
    "v_mfma_f32_16x16x32_bf16 v[68:71], v[116:119], v[200:203], v[68:71]\n"
    "ds_read_b128 v[196:199], v243 offset:12288\n"
    "s_waitcnt lgkmcnt(2)\n"
    "v_mfma_f32_16x16x32_bf16 v[36:39], v[112:115], v[208:211], v[36:39]\n"
    "v_mfma_f32_16x16x32_bf16 v[36:39], v[120:123], v[212:215], v[36:39]\n"
    "ds_read_b128 v[200:203], v242 offset:14336\n"
    "v_mfma_f32_16x16x32_bf16 v[72:75], v[124:127], v[212:215], v[72:75]\n"
    "v_mfma_f32_16x16x32_bf16 v[72:75], v[116:119], v[208:211], v[72:75]\n"
    "ds_read_b128 v[204:207], v243 offset:14336\n"
    "s_waitcnt lgkmcnt(2)\n"
    "v_mfma_f32_16x16x32_bf16 v[40:43], v[112:115], v[192:195], v[40:43]\n"
    "v_mfma_f32_16x16x32_bf16 v[40:43], v[120:123], v[196:199], v[40:43]\n"
    "ds_read_b128 v[208:211], v242 offset:16384\n"
    "v_mfma_f32_16x16x32_bf16 v[76:79], v[124:127], v[196:199], v[76:79]\n"
    "v_mfma_f32_16x16x32_bf16 v[76:79], v[116:119], v[192:195], v[76:79]\n"
    "ds_read_b128 v[212:215], v243 offset:16384\n"
    "s_waitcnt lgkmcnt(2)\n"
    "v_mfma_f32_16x16x32_bf16 v[44:47], v[112:115], v[200:203], v[44:47]\n"
    "v_mfma_f32_16x16x32_bf16 v[44:47], v[120:123], v[204:207], v[44:47]\n"
    "v_mfma_f32_16x16x32_bf16 v[80:83], v[124:127], v[204:207], v[80:83]\n"
    "v_mfma_f32_16x16x32_bf16 v[80:83], v[116:119], v[200:203], v[80:83]\n"
    "s_waitcnt lgkmcnt(0)\n"
    "s_barrier\n"
    "v_add_u32 v232, 0x1e800, v228\n"
    "v_add_u32 v233, 0x1e800, v229\n"
    "v_add_u32 v234, 0x1e800, v230\n"
    "v_add_u32 v235, 0x1e800, v231\n"
    "v_add_u32 v242, 0x1e800, v240\n"
    "v_add_u32 v243, 0x1e800, v241\n"
    "ds_read_b64_tr_b16 v[176:177], v232\n"
    "ds_read_b64_tr_b16 v[178:179], v233\n"
    "ds_read_b128 v[192:195], v242\n"
    "ds_read_b64_tr_b16 v[184:185], v232 offset:8192\n"
    "ds_read_b64_tr_b16 v[186:187], v233 offset:8192\n"
    "ds_read_b128 v[196:199], v243\n"
    "ds_read_b64_tr_b16 v[188:189], v234 offset:8192\n"
    "ds_read_b64_tr_b16 v[190:191], v235 offset:8192\n"
    "ds_read_b64_tr_b16 v[180:181], v234\n"
    "ds_read_b64_tr_b16 v[182:183], v235\n"
    "ds_read_b128 v[200:203], v242 offset:2048\n"
    "ds_read_b128 v[204:207], v243 offset:2048\n"
    "v_mfma_f32_16x16x32_bf16 v[48:51], v[112:115], v[208:211], v[48:51]\n"
    "v_mfma_f32_16x16x32_bf16 v[48:51], v[120:123], v[212:215], v[48:51]\n"
    "v_mfma_f32_16x16x32_bf16 v[84:87], v[124:127], v[212:215], v[84:87]\n"
    "v_mfma_f32_16x16x32_bf16 v[84:87], v[116:119], v[208:211], v[84:87]\n"
    "s_waitcnt lgkmcnt(2)\n"
    "v_mfma_f32_16x16x32_bf16 v[16:19], v[176:179], v[192:195], v[16:19]\n"
    "v_mfma_f32_16x16x32_bf16 v[16:19], v[184:187], v[196:199], v[16:19]\n"
    "ds_read_b128 v[208:211], v242 offset:4096\n"
    "v_mfma_f32_16x16x32_bf16 v[52:55], v[188:191], v[196:199], v[52:55]\n"
    "v_mfma_f32_16x16x32_bf16 v[52:55], v[180:183], v[192:195], v[52:55]\n"
    "ds_read_b128 v[212:215], v243 offset:4096\n"
    "s_waitcnt lgkmcnt(2)\n"
    "v_mfma_f32_16x16x32_bf16 v[20:23], v[176:179], v[200:203], v[20:23]\n"
    "v_mfma_f32_16x16x32_bf16 v[20:23], v[184:187], v[204:207], v[20:23]\n"
    "ds_read_b128 v[192:195], v242 offset:6144\n"
    "v_mfma_f32_16x16x32_bf16 v[56:59], v[188:191], v[204:207], v[56:59]\n"
    "v_mfma_f32_16x16x32_bf16 v[56:59], v[180:183], v[200:203], v[56:59]\n"
    "ds_read_b128 v[196:199], v243 offset:6144\n"
    "s_waitcnt lgkmcnt(2)\n"
    "v_mfma_f32_16x16x32_bf16 v[24:27], v[176:179], v[208:211], v[24:27]\n"
    "v_mfma_f32_16x16x32_bf16 v[24:27], v[184:187], v[212:215], v[24:27]\n"
    "ds_read_b128 v[200:203], v242 offset:8192\n"
    "v_mfma_f32_16x16x32_bf16 v[60:63], v[188:191], v[212:215], v[60:63]\n"
    "v_mfma_f32_16x16x32_bf16 v[60:63], v[180:183], v[208:211], v[60:63]\n"
    "ds_read_b128 v[204:207], v243 offset:8192\n"
    "s_waitcnt lgkmcnt(2)\n"
    "v_mfma_f32_16x16x32_bf16 v[28:31], v[176:179], v[192:195], v[28:31]\n"
    "v_mfma_f32_16x16x32_bf16 v[28:31], v[184:187], v[196:199], v[28:31]\n"
    "ds_read_b128 v[208:211], v242 offset:10240\n"
    "v_mfma_f32_16x16x32_bf16 v[64:67], v[188:191], v[196:199], v[64:67]\n"
    "v_mfma_f32_16x16x32_bf16 v[64:67], v[180:183], v[192:195], v[64:67]\n"
    "ds_read_b128 v[212:215], v243 offset:10240\n"
    "s_waitcnt lgkmcnt(2)\n"
    "v_mfma_f32_16x16x32_bf16 v[32:35], v[176:179], v[200:203], v[32:35]\n"
    "v_mfma_f32_16x16x32_bf16 v[32:35], v[184:187], v[204:207], v[32:35]\n"
    "ds_read_b128 v[192:195], v242 offset:12288\n"
    "v_mfma_f32_16x16x32_bf16 v[68:71], v[188:191], v[204:207], v[68:71]\n"
    "v_mfma_f32_16x16x32_bf16 v[68:71], v[180:183], v[200:203], v[68:71]\n"
    "ds_read_b128 v[196:199], v243 offset:12288\n"
    "s_waitcnt lgkmcnt(2)\n"
    "v_mfma_f32_16x16x32_bf16 v[36:39], v[176:179], v[208:211], v[36:39]\n"
    "v_mfma_f32_16x16x32_bf16 v[36:39], v[184:187], v[212:215], v[36:39]\n"
    "ds_read_b128 v[200:203], v242 offset:14336\n"
    "v_mfma_f32_16x16x32_bf16 v[72:75], v[188:191], v[212:215], v[72:75]\n"
    "v_mfma_f32_16x16x32_bf16 v[72:75], v[180:183], v[208:211], v[72:75]\n"
    "ds_read_b128 v[204:207], v243 offset:14336\n"
    "s_waitcnt lgkmcnt(2)\n"
    "v_mfma_f32_16x16x32_bf16 v[40:43], v[176:179], v[192:195], v[40:43]\n"
    "v_mfma_f32_16x16x32_bf16 v[40:43], v[184:187], v[196:199], v[40:43]\n"
    "ds_read_b128 v[208:211], v242 offset:16384\n"
    "v_mfma_f32_16x16x32_bf16 v[76:79], v[188:191], v[196:199], v[76:79]\n"
    "v_mfma_f32_16x16x32_bf16 v[76:79], v[180:183], v[192:195], v[76:79]\n"
    "ds_read_b128 v[212:215], v243 offset:16384\n"
    "s_waitcnt lgkmcnt(2)\n"
    "v_mfma_f32_16x16x32_bf16 v[44:47], v[176:179], v[200:203], v[44:47]\n"
    "v_mfma_f32_16x16x32_bf16 v[44:47], v[184:187], v[204:207], v[44:47]\n"
    "v_mfma_f32_16x16x32_bf16 v[80:83], v[188:191], v[204:207], v[80:83]\n"
    "v_mfma_f32_16x16x32_bf16 v[80:83], v[180:183], v[200:203], v[80:83]\n"
    "s_waitcnt lgkmcnt(0)\n"
    "s_barrier\n"
    "v_add_u32 v232, 0xc000, v228\n"
    "v_add_u32 v233, 0xc000, v229\n"
    "v_add_u32 v234, 0xc000, v230\n"
    "v_add_u32 v235, 0xc000, v231\n"
    "v_add_u32 v242, 0xc000, v240\n"
    "v_add_u32 v243, 0xc000, v241\n"
    "ds_read_b64_tr_b16 v[112:113], v232\n"
    "ds_read_b64_tr_b16 v[114:115], v233\n"
    "ds_read_b128 v[192:195], v242\n"
    "ds_read_b64_tr_b16 v[120:121], v232 offset:8192\n"
    "ds_read_b64_tr_b16 v[122:123], v233 offset:8192\n"
    "ds_read_b128 v[196:199], v243\n"
    "ds_read_b64_tr_b16 v[124:125], v234 offset:8192\n"
    "ds_read_b64_tr_b16 v[126:127], v235 offset:8192\n"
    "ds_read_b64_tr_b16 v[116:117], v234\n"
    "ds_read_b64_tr_b16 v[118:119], v235\n"
    "ds_read_b128 v[200:203], v242 offset:2048\n"
    "ds_read_b128 v[204:207], v243 offset:2048\n"
    "v_mfma_f32_16x16x32_bf16 v[48:51], v[176:179], v[208:211], v[48:51]\n"
    "v_mfma_f32_16x16x32_bf16 v[48:51], v[184:187], v[212:215], v[48:51]\n"
    "v_mfma_f32_16x16x32_bf16 v[84:87], v[188:191], v[212:215], v[84:87]\n"
    "v_mfma_f32_16x16x32_bf16 v[84:87], v[180:183], v[208:211], v[84:87]\n"
    "s_waitcnt lgkmcnt(2)\n"
    "v_mfma_f32_16x16x32_bf16 v[16:19], v[112:115], v[192:195], v[16:19]\n"
    "v_mfma_f32_16x16x32_bf16 v[16:19], v[120:123], v[196:199], v[16:19]\n"
    "ds_read_b128 v[208:211], v242 offset:4096\n"
    "v_mfma_f32_16x16x32_bf16 v[52:55], v[124:127], v[196:199], v[52:55]\n"
    "v_mfma_f32_16x16x32_bf16 v[52:55], v[116:119], v[192:195], v[52:55]\n"
    "ds_read_b128 v[212:215], v243 offset:4096\n"
    "s_waitcnt lgkmcnt(2)\n"
    "v_mfma_f32_16x16x32_bf16 v[20:23], v[112:115], v[200:203], v[20:23]\n"
    "v_mfma_f32_16x16x32_bf16 v[20:23], v[120:123], v[204:207], v[20:23]\n"
    "ds_read_b128 v[192:195], v242 offset:6144\n"
    "v_mfma_f32_16x16x32_bf16 v[56:59], v[124:127], v[204:207], v[56:59]\n"
    "v_mfma_f32_16x16x32_bf16 v[56:59], v[116:119], v[200:203], v[56:59]\n"
    "ds_read_b128 v[196:199], v243 offset:6144\n"
    "s_waitcnt lgkmcnt(2)\n"
    "v_mfma_f32_16x16x32_bf16 v[24:27], v[112:115], v[208:211], v[24:27]\n"
    "v_mfma_f32_16x16x32_bf16 v[24:27], v[120:123], v[212:215], v[24:27]\n"
    "ds_read_b128 v[200:203], v242 offset:8192\n"
    "v_mfma_f32_16x16x32_bf16 v[60:63], v[124:127], v[212:215], v[60:63]\n"
    "v_mfma_f32_16x16x32_bf16 v[60:63], v[116:119], v[208:211], v[60:63]\n"
    "ds_read_b128 v[204:207], v243 offset:8192\n"
    "s_waitcnt lgkmcnt(2)\n"
    "v_mfma_f32_16x16x32_bf16 v[28:31], v[112:115], v[192:195], v[28:31]\n"
    "v_mfma_f32_16x16x32_bf16 v[28:31], v[120:123], v[196:199], v[28:31]\n"
    "ds_read_b128 v[208:211], v242 offset:10240\n"
    "v_mfma_f32_16x16x32_bf16 v[64:67], v[124:127], v[196:199], v[64:67]\n"
    "v_mfma_f32_16x16x32_bf16 v[64:67], v[116:119], v[192:195], v[64:67]\n"
    "ds_read_b128 v[212:215], v243 offset:10240\n"
    "s_waitcnt lgkmcnt(2)\n"
    "v_mfma_f32_16x16x32_bf16 v[32:35], v[112:115], v[200:203], v[32:35]\n"
    "v_mfma_f32_16x16x32_bf16 v[32:35], v[120:123], v[204:207], v[32:35]\n"
    "ds_read_b128 v[192:195], v242 offset:12288\n"
    "v_mfma_f32_16x16x32_bf16 v[68:71], v[124:127], v[204:207], v[68:71]\n"
    "v_mfma_f32_16x16x32_bf16 v[68:71], v[116:119], v[200:203], v[68:71]\n"
    "ds_read_b128 v[196:199], v243 offset:12288\n"
    "s_waitcnt lgkmcnt(2)\n"
    "v_mfma_f32_16x16x32_bf16 v[36:39], v[112:115], v[208:211], v[36:39]\n"
    "v_mfma_f32_16x16x32_bf16 v[36:39], v[120:123], v[212:215], v[36:39]\n"
    "ds_read_b128 v[200:203], v242 offset:14336\n"
    "v_mfma_f32_16x16x32_bf16 v[72:75], v[124:127], v[212:215], v[72:75]\n"
    "v_mfma_f32_16x16x32_bf16 v[72:75], v[116:119], v[208:211], v[72:75]\n"
    "ds_read_b128 v[204:207], v243 offset:14336\n"
    "s_waitcnt lgkmcnt(2)\n"
    "v_mfma_f32_16x16x32_bf16 v[40:43], v[112:115], v[192:195], v[40:43]\n"
    "v_mfma_f32_16x16x32_bf16 v[40:43], v[120:123], v[196:199], v[40:43]\n"
    "ds_read_b128 v[208:211], v242 offset:16384\n"
    "v_mfma_f32_16x16x32_bf16 v[76:79], v[124:127], v[196:199], v[76:79]\n"
    "v_mfma_f32_16x16x32_bf16 v[76:79], v[116:119], v[192:195], v[76:79]\n"
    "ds_read_b128 v[212:215], v243 offset:16384\n"
    "s_waitcnt lgkmcnt(2)\n"
    "v_mfma_f32_16x16x32_bf16 v[44:47], v[112:115], v[200:203], v[44:47]\n"
    "v_mfma_f32_16x16x32_bf16 v[44:47], v[120:123], v[204:207], v[44:47]\n"
    "v_mfma_f32_16x16x32_bf16 v[80:83], v[124:127], v[204:207], v[80:83]\n"
    "v_mfma_f32_16x16x32_bf16 v[80:83], v[116:119], v[200:203], v[80:83]\n"
    "s_waitcnt lgkmcnt(0)\n"
    "s_barrier\n"
    "v_add_u32 v232, 0x16000, v228\n"
    "v_add_u32 v233, 0x16000, v229\n"
    "v_add_u32 v234, 0x16000, v230\n"
    "v_add_u32 v235, 0x16000, v231\n"
    "v_add_u32 v242, 0x16000, v240\n"
    "v_add_u32 v243, 0x16000, v241\n"
    "ds_read_b64_tr_b16 v[176:177], v232\n"
    "ds_read_b64_tr_b16 v[178:179], v233\n"
    "ds_read_b128 v[192:195], v242\n"
    "ds_read_b64_tr_b16 v[184:185], v232 offset:8192\n"
    "ds_read_b64_tr_b16 v[186:187], v233 offset:8192\n"
    "ds_read_b128 v[196:199], v243\n"
    "ds_read_b64_tr_b16 v[188:189], v234 offset:8192\n"
    "ds_read_b64_tr_b16 v[190:191], v235 offset:8192\n"
    "ds_read_b64_tr_b16 v[180:181], v234\n"
    "ds_read_b64_tr_b16 v[182:183], v235\n"
    "ds_read_b128 v[200:203], v242 offset:2048\n"
    "ds_read_b128 v[204:207], v243 offset:2048\n"
    "v_mfma_f32_16x16x32_bf16 v[48:51], v[112:115], v[208:211], v[48:51]\n"
    "v_mfma_f32_16x16x32_bf16 v[48:51], v[120:123], v[212:215], v[48:51]\n"
    "v_mfma_f32_16x16x32_bf16 v[84:87], v[124:127], v[212:215], v[84:87]\n"
    "v_mfma_f32_16x16x32_bf16 v[84:87], v[116:119], v[208:211], v[84:87]\n"
    "s_waitcnt lgkmcnt(2)\n"
    "v_mfma_f32_16x16x32_bf16 v[16:19], v[176:179], v[192:195], v[16:19]\n"
    "v_mfma_f32_16x16x32_bf16 v[16:19], v[184:187], v[196:199], v[16:19]\n"
    "ds_read_b128 v[208:211], v242 offset:4096\n"
    "v_mfma_f32_16x16x32_bf16 v[52:55], v[188:191], v[196:199], v[52:55]\n"
    "v_mfma_f32_16x16x32_bf16 v[52:55], v[180:183], v[192:195], v[52:55]\n"
    "ds_read_b128 v[212:215], v243 offset:4096\n"
    "s_waitcnt lgkmcnt(2)\n"
    "v_mfma_f32_16x16x32_bf16 v[20:23], v[176:179], v[200:203], v[20:23]\n"
    "v_mfma_f32_16x16x32_bf16 v[20:23], v[184:187], v[204:207], v[20:23]\n"
    "ds_read_b128 v[192:195], v242 offset:6144\n"
    "v_mfma_f32_16x16x32_bf16 v[56:59], v[188:191], v[204:207], v[56:59]\n"
    "v_mfma_f32_16x16x32_bf16 v[56:59], v[180:183], v[200:203], v[56:59]\n"
    "ds_read_b128 v[196:199], v243 offset:6144\n"
    "s_waitcnt lgkmcnt(2)\n"
    "v_mfma_f32_16x16x32_bf16 v[24:27], v[176:179], v[208:211], v[24:27]\n"
    "v_mfma_f32_16x16x32_bf16 v[24:27], v[184:187], v[212:215], v[24:27]\n"
    "ds_read_b128 v[200:203], v242 offset:8192\n"
    "v_mfma_f32_16x16x32_bf16 v[60:63], v[188:191], v[212:215], v[60:63]\n"
    "v_mfma_f32_16x16x32_bf16 v[60:63], v[180:183], v[208:211], v[60:63]\n"
    "ds_read_b128 v[204:207], v243 offset:8192\n"
    "s_waitcnt lgkmcnt(2)\n"
    "v_mfma_f32_16x16x32_bf16 v[28:31], v[176:179], v[192:195], v[28:31]\n"
    "v_mfma_f32_16x16x32_bf16 v[28:31], v[184:187], v[196:199], v[28:31]\n"
    "ds_read_b128 v[208:211], v242 offset:10240\n"
    "v_mfma_f32_16x16x32_bf16 v[64:67], v[188:191], v[196:199], v[64:67]\n"
    "v_mfma_f32_16x16x32_bf16 v[64:67], v[180:183], v[192:195], v[64:67]\n"
    "ds_read_b128 v[212:215], v243 offset:10240\n"
    "s_waitcnt lgkmcnt(2)\n"
    "v_mfma_f32_16x16x32_bf16 v[32:35], v[176:179], v[200:203], v[32:35]\n"
    "v_mfma_f32_16x16x32_bf16 v[32:35], v[184:187], v[204:207], v[32:35]\n"
    "ds_read_b128 v[192:195], v242 offset:12288\n"
    "v_mfma_f32_16x16x32_bf16 v[68:71], v[188:191], v[204:207], v[68:71]\n"
    "v_mfma_f32_16x16x32_bf16 v[68:71], v[180:183], v[200:203], v[68:71]\n"
    "ds_read_b128 v[196:199], v243 offset:12288\n"
    "s_waitcnt lgkmcnt(2)\n"
    "v_mfma_f32_16x16x32_bf16 v[36:39], v[176:179], v[208:211], v[36:39]\n"
    "v_mfma_f32_16x16x32_bf16 v[36:39], v[184:187], v[212:215], v[36:39]\n"
    "ds_read_b128 v[200:203], v242 offset:14336\n"
    "v_mfma_f32_16x16x32_bf16 v[72:75], v[188:191], v[212:215], v[72:75]\n"
    "v_mfma_f32_16x16x32_bf16 v[72:75], v[180:183], v[208:211], v[72:75]\n"
    "ds_read_b128 v[204:207], v243 offset:14336\n"
    "s_waitcnt lgkmcnt(2)\n"
    "v_mfma_f32_16x16x32_bf16 v[40:43], v[176:179], v[192:195], v[40:43]\n"
    "v_mfma_f32_16x16x32_bf16 v[40:43], v[184:187], v[196:199], v[40:43]\n"
    "ds_read_b128 v[208:211], v242 offset:16384\n"
    "v_mfma_f32_16x16x32_bf16 v[76:79], v[188:191], v[196:199], v[76:79]\n"
    "v_mfma_f32_16x16x32_bf16 v[76:79], v[180:183], v[192:195], v[76:79]\n"
    "ds_read_b128 v[212:215], v243 offset:16384\n"
    "s_waitcnt lgkmcnt(2)\n"
    "v_mfma_f32_16x16x32_bf16 v[44:47], v[176:179], v[200:203], v[44:47]\n"
    "v_mfma_f32_16x16x32_bf16 v[44:47], v[184:187], v[204:207], v[44:47]\n"
    "v_mfma_f32_16x16x32_bf16 v[80:83], v[188:191], v[204:207], v[80:83]\n"
    "v_mfma_f32_16x16x32_bf16 v[80:83], v[180:183], v[200:203], v[80:83]\n"
    "s_waitcnt lgkmcnt(0)\n"
    "s_barrier\n"
    "v_add_u32 v232, 0x1e800, v228\n"
    "v_add_u32 v233, 0x1e800, v229\n"
    "v_add_u32 v234, 0x1e800, v230\n"
    "v_add_u32 v235, 0x1e800, v231\n"
    "v_add_u32 v242, 0x1e800, v240\n"
    "v_add_u32 v243, 0x1e800, v241\n"
    "ds_read_b64_tr_b16 v[112:113], v232\n"
    "ds_read_b64_tr_b16 v[114:115], v233\n"
    "ds_read_b128 v[192:195], v242\n"
    "ds_read_b64_tr_b16 v[120:121], v232 offset:8192\n"
    "ds_read_b64_tr_b16 v[122:123], v233 offset:8192\n"
    "ds_read_b128 v[196:199], v243\n"
    "ds_read_b64_tr_b16 v[124:125], v234 offset:8192\n"
    "ds_read_b64_tr_b16 v[126:127], v235 offset:8192\n"
    "ds_read_b64_tr_b16 v[116:117], v234\n"
    "ds_read_b64_tr_b16 v[118:119], v235\n"
    "ds_read_b128 v[200:203], v242 offset:2048\n"
    "ds_read_b128 v[204:207], v243 offset:2048\n"
    "v_mfma_f32_16x16x32_bf16 v[48:51], v[176:179], v[208:211], v[48:51]\n"
    "v_mfma_f32_16x16x32_bf16 v[48:51], v[184:187], v[212:215], v[48:51]\n"
    "v_mfma_f32_16x16x32_bf16 v[84:87], v[188:191], v[212:215], v[84:87]\n"
    "v_mfma_f32_16x16x32_bf16 v[84:87], v[180:183], v[208:211], v[84:87]\n"
    "s_waitcnt lgkmcnt(2)\n"
    "v_mfma_f32_16x16x32_bf16 v[16:19], v[112:115], v[192:195], v[16:19]\n"
    "v_mfma_f32_16x16x32_bf16 v[16:19], v[120:123], v[196:199], v[16:19]\n"
    "ds_read_b128 v[208:211], v242 offset:4096\n"
    "v_mfma_f32_16x16x32_bf16 v[52:55], v[124:127], v[196:199], v[52:55]\n"
    "v_mfma_f32_16x16x32_bf16 v[52:55], v[116:119], v[192:195], v[52:55]\n"
    "ds_read_b128 v[212:215], v243 offset:4096\n"
    "s_waitcnt lgkmcnt(2)\n"
    "v_mfma_f32_16x16x32_bf16 v[20:23], v[112:115], v[200:203], v[20:23]\n"
    "v_mfma_f32_16x16x32_bf16 v[20:23], v[120:123], v[204:207], v[20:23]\n"
    "ds_read_b128 v[192:195], v242 offset:6144\n"
    "v_mfma_f32_16x16x32_bf16 v[56:59], v[124:127], v[204:207], v[56:59]\n"
    "v_mfma_f32_16x16x32_bf16 v[56:59], v[116:119], v[200:203], v[56:59]\n"
    "ds_read_b128 v[196:199], v243 offset:6144\n"
    "s_waitcnt lgkmcnt(2)\n"
    "v_mfma_f32_16x16x32_bf16 v[24:27], v[112:115], v[208:211], v[24:27]\n"
    "v_mfma_f32_16x16x32_bf16 v[24:27], v[120:123], v[212:215], v[24:27]\n"
    "ds_read_b128 v[200:203], v242 offset:8192\n"
    "v_mfma_f32_16x16x32_bf16 v[60:63], v[124:127], v[212:215], v[60:63]\n"
    "v_mfma_f32_16x16x32_bf16 v[60:63], v[116:119], v[208:211], v[60:63]\n"
    "ds_read_b128 v[204:207], v243 offset:8192\n"
    "s_waitcnt lgkmcnt(2)\n"
    "v_mfma_f32_16x16x32_bf16 v[28:31], v[112:115], v[192:195], v[28:31]\n"
    "v_mfma_f32_16x16x32_bf16 v[28:31], v[120:123], v[196:199], v[28:31]\n"
    "ds_read_b128 v[208:211], v242 offset:10240\n"
    "v_mfma_f32_16x16x32_bf16 v[64:67], v[124:127], v[196:199], v[64:67]\n"
    "v_mfma_f32_16x16x32_bf16 v[64:67], v[116:119], v[192:195], v[64:67]\n"
    "ds_read_b128 v[212:215], v243 offset:10240\n"
    "s_waitcnt lgkmcnt(2)\n"
    "v_mfma_f32_16x16x32_bf16 v[32:35], v[112:115], v[200:203], v[32:35]\n"
    "v_mfma_f32_16x16x32_bf16 v[32:35], v[120:123], v[204:207], v[32:35]\n"
    "ds_read_b128 v[192:195], v242 offset:12288\n"
    "v_mfma_f32_16x16x32_bf16 v[68:71], v[124:127], v[204:207], v[68:71]\n"
    "v_mfma_f32_16x16x32_bf16 v[68:71], v[116:119], v[200:203], v[68:71]\n"
    "ds_read_b128 v[196:199], v243 offset:12288\n"
    "s_waitcnt lgkmcnt(2)\n"
    "v_mfma_f32_16x16x32_bf16 v[36:39], v[112:115], v[208:211], v[36:39]\n"
    "v_mfma_f32_16x16x32_bf16 v[36:39], v[120:123], v[212:215], v[36:39]\n"
    "ds_read_b128 v[200:203], v242 offset:14336\n"
    "v_mfma_f32_16x16x32_bf16 v[72:75], v[124:127], v[212:215], v[72:75]\n"
    "v_mfma_f32_16x16x32_bf16 v[72:75], v[116:119], v[208:211], v[72:75]\n"
    "ds_read_b128 v[204:207], v243 offset:14336\n"
    "s_waitcnt lgkmcnt(2)\n"
    "v_mfma_f32_16x16x32_bf16 v[40:43], v[112:115], v[192:195], v[40:43]\n"
    "v_mfma_f32_16x16x32_bf16 v[40:43], v[120:123], v[196:199], v[40:43]\n"
    "ds_read_b128 v[208:211], v242 offset:16384\n"
    "v_mfma_f32_16x16x32_bf16 v[76:79], v[124:127], v[196:199], v[76:79]\n"
    "v_mfma_f32_16x16x32_bf16 v[76:79], v[116:119], v[192:195], v[76:79]\n"
    "ds_read_b128 v[212:215], v243 offset:16384\n"
    "s_waitcnt lgkmcnt(2)\n"
    "v_mfma_f32_16x16x32_bf16 v[44:47], v[112:115], v[200:203], v[44:47]\n"
    "v_mfma_f32_16x16x32_bf16 v[44:47], v[120:123], v[204:207], v[44:47]\n"
    "v_mfma_f32_16x16x32_bf16 v[80:83], v[124:127], v[204:207], v[80:83]\n"
    "v_mfma_f32_16x16x32_bf16 v[80:83], v[116:119], v[200:203], v[80:83]\n"
    "s_waitcnt lgkmcnt(0)\n"
    "s_barrier\n"
    "v_add_u32 v232, 0xc000, v228\n"
    "v_add_u32 v233, 0xc000, v229\n"
    "v_add_u32 v234, 0xc000, v230\n"
    "v_add_u32 v235, 0xc000, v231\n"
    "v_add_u32 v242, 0xc000, v240\n"
    "v_add_u32 v243, 0xc000, v241\n"
    "ds_read_b64_tr_b16 v[176:177], v232\n"
    "ds_read_b64_tr_b16 v[178:179], v233\n"
    "ds_read_b128 v[192:195], v242\n"
    "ds_read_b64_tr_b16 v[184:185], v232 offset:8192\n"
    "ds_read_b64_tr_b16 v[186:187], v233 offset:8192\n"
    "ds_read_b128 v[196:199], v243\n"
    "ds_read_b64_tr_b16 v[188:189], v234 offset:8192\n"
    "ds_read_b64_tr_b16 v[190:191], v235 offset:8192\n"
    "ds_read_b64_tr_b16 v[180:181], v234\n"
    "ds_read_b64_tr_b16 v[182:183], v235\n"
    "ds_read_b128 v[200:203], v242 offset:2048\n"
    "ds_read_b128 v[204:207], v243 offset:2048\n"
    "v_mfma_f32_16x16x32_bf16 v[48:51], v[112:115], v[208:211], v[48:51]\n"
    "v_mfma_f32_16x16x32_bf16 v[48:51], v[120:123], v[212:215], v[48:51]\n"
    "v_mfma_f32_16x16x32_bf16 v[84:87], v[124:127], v[212:215], v[84:87]\n"
    "v_mfma_f32_16x16x32_bf16 v[84:87], v[116:119], v[208:211], v[84:87]\n"
    "s_waitcnt lgkmcnt(2)\n"
    "v_mfma_f32_16x16x32_bf16 v[16:19], v[176:179], v[192:195], v[16:19]\n"
    "v_mfma_f32_16x16x32_bf16 v[16:19], v[184:187], v[196:199], v[16:19]\n"
    "ds_read_b128 v[208:211], v242 offset:4096\n"
    "v_mfma_f32_16x16x32_bf16 v[52:55], v[188:191], v[196:199], v[52:55]\n"
    "v_mfma_f32_16x16x32_bf16 v[52:55], v[180:183], v[192:195], v[52:55]\n"
    "ds_read_b128 v[212:215], v243 offset:4096\n"
    "s_waitcnt lgkmcnt(2)\n"
    "v_mfma_f32_16x16x32_bf16 v[20:23], v[176:179], v[200:203], v[20:23]\n"
    "v_mfma_f32_16x16x32_bf16 v[20:23], v[184:187], v[204:207], v[20:23]\n"
    "ds_read_b128 v[192:195], v242 offset:6144\n"
    "v_mfma_f32_16x16x32_bf16 v[56:59], v[188:191], v[204:207], v[56:59]\n"
    "v_mfma_f32_16x16x32_bf16 v[56:59], v[180:183], v[200:203], v[56:59]\n"
    "ds_read_b128 v[196:199], v243 offset:6144\n"
    "s_waitcnt lgkmcnt(2)\n"
    "v_mfma_f32_16x16x32_bf16 v[24:27], v[176:179], v[208:211], v[24:27]\n"
    "v_mfma_f32_16x16x32_bf16 v[24:27], v[184:187], v[212:215], v[24:27]\n"
    "ds_read_b128 v[200:203], v242 offset:8192\n"
    "v_mfma_f32_16x16x32_bf16 v[60:63], v[188:191], v[212:215], v[60:63]\n"
    "v_mfma_f32_16x16x32_bf16 v[60:63], v[180:183], v[208:211], v[60:63]\n"
    "ds_read_b128 v[204:207], v243 offset:8192\n"
    "s_waitcnt lgkmcnt(2)\n"
    "v_mfma_f32_16x16x32_bf16 v[28:31], v[176:179], v[192:195], v[28:31]\n"
    "v_mfma_f32_16x16x32_bf16 v[28:31], v[184:187], v[196:199], v[28:31]\n"
    "ds_read_b128 v[208:211], v242 offset:10240\n"
    "v_mfma_f32_16x16x32_bf16 v[64:67], v[188:191], v[196:199], v[64:67]\n"
    "v_mfma_f32_16x16x32_bf16 v[64:67], v[180:183], v[192:195], v[64:67]\n"
    "ds_read_b128 v[212:215], v243 offset:10240\n"
    "s_waitcnt lgkmcnt(2)\n"
    "v_mfma_f32_16x16x32_bf16 v[32:35], v[176:179], v[200:203], v[32:35]\n"
    "v_mfma_f32_16x16x32_bf16 v[32:35], v[184:187], v[204:207], v[32:35]\n"
    "ds_read_b128 v[192:195], v242 offset:12288\n"
    "v_mfma_f32_16x16x32_bf16 v[68:71], v[188:191], v[204:207], v[68:71]\n"
    "v_mfma_f32_16x16x32_bf16 v[68:71], v[180:183], v[200:203], v[68:71]\n"
    "ds_read_b128 v[196:199], v243 offset:12288\n"
    "s_waitcnt lgkmcnt(2)\n"
    "v_mfma_f32_16x16x32_bf16 v[36:39], v[176:179], v[208:211], v[36:39]\n"
    "v_mfma_f32_16x16x32_bf16 v[36:39], v[184:187], v[212:215], v[36:39]\n"
    "ds_read_b128 v[200:203], v242 offset:14336\n"
    "v_mfma_f32_16x16x32_bf16 v[72:75], v[188:191], v[212:215], v[72:75]\n"
    "v_mfma_f32_16x16x32_bf16 v[72:75], v[180:183], v[208:211], v[72:75]\n"
    "ds_read_b128 v[204:207], v243 offset:14336\n"
    "s_waitcnt lgkmcnt(2)\n"
    "v_mfma_f32_16x16x32_bf16 v[40:43], v[176:179], v[192:195], v[40:43]\n"
    "v_mfma_f32_16x16x32_bf16 v[40:43], v[184:187], v[196:199], v[40:43]\n"
    "ds_read_b128 v[208:211], v242 offset:16384\n"
    "v_mfma_f32_16x16x32_bf16 v[76:79], v[188:191], v[196:199], v[76:79]\n"
    "v_mfma_f32_16x16x32_bf16 v[76:79], v[180:183], v[192:195], v[76:79]\n"
    "ds_read_b128 v[212:215], v243 offset:16384\n"
    "s_waitcnt lgkmcnt(2)\n"
    "v_mfma_f32_16x16x32_bf16 v[44:47], v[176:179], v[200:203], v[44:47]\n"
    "v_mfma_f32_16x16x32_bf16 v[44:47], v[184:187], v[204:207], v[44:47]\n"
    "v_mfma_f32_16x16x32_bf16 v[80:83], v[188:191], v[204:207], v[80:83]\n"
    "v_mfma_f32_16x16x32_bf16 v[80:83], v[180:183], v[200:203], v[80:83]\n"
    "s_waitcnt lgkmcnt(0)\n"
    "s_barrier\n"
    "v_add_u32 v232, 0x16000, v228\n"
    "v_add_u32 v233, 0x16000, v229\n"
    "v_add_u32 v234, 0x16000, v230\n"
    "v_add_u32 v235, 0x16000, v231\n"
    "v_add_u32 v242, 0x16000, v240\n"
    "v_add_u32 v243, 0x16000, v241\n"
    "ds_read_b64_tr_b16 v[112:113], v232\n"
    "ds_read_b64_tr_b16 v[114:115], v233\n"
    "ds_read_b128 v[192:195], v242\n"
    "ds_read_b64_tr_b16 v[120:121], v232 offset:8192\n"
    "ds_read_b64_tr_b16 v[122:123], v233 offset:8192\n"
    "ds_read_b128 v[196:199], v243\n"
    "ds_read_b64_tr_b16 v[124:125], v234 offset:8192\n"
    "ds_read_b64_tr_b16 v[126:127], v235 offset:8192\n"
    "ds_read_b64_tr_b16 v[116:117], v234\n"
    "ds_read_b64_tr_b16 v[118:119], v235\n"
    "ds_read_b128 v[200:203], v242 offset:2048\n"
    "ds_read_b128 v[204:207], v243 offset:2048\n"
    "v_mfma_f32_16x16x32_bf16 v[48:51], v[176:179], v[208:211], v[48:51]\n"
    "v_mfma_f32_16x16x32_bf16 v[48:51], v[184:187], v[212:215], v[48:51]\n"
    "v_mfma_f32_16x16x32_bf16 v[84:87], v[188:191], v[212:215], v[84:87]\n"
    "v_mfma_f32_16x16x32_bf16 v[84:87], v[180:183], v[208:211], v[84:87]\n"
    "s_waitcnt lgkmcnt(2)\n"
    "v_mfma_f32_16x16x32_bf16 v[16:19], v[112:115], v[192:195], v[16:19]\n"
    "v_mfma_f32_16x16x32_bf16 v[16:19], v[120:123], v[196:199], v[16:19]\n"
    "ds_read_b128 v[208:211], v242 offset:4096\n"
    "v_mfma_f32_16x16x32_bf16 v[52:55], v[124:127], v[196:199], v[52:55]\n"
    "v_mfma_f32_16x16x32_bf16 v[52:55], v[116:119], v[192:195], v[52:55]\n"
    "ds_read_b128 v[212:215], v243 offset:4096\n"
    "s_add_u32 m0, s73, 0xc000\n"
    "s_add_u32 s64, s73, 0x2000\n"
    "buffer_load_dwordx4 v7, s[20:23], s64 offen nt lds\n"
    "s_waitcnt lgkmcnt(2)\n"
    "v_mfma_f32_16x16x32_bf16 v[20:23], v[112:115], v[200:203], v[20:23]\n"
    "v_mfma_f32_16x16x32_bf16 v[20:23], v[120:123], v[204:207], v[20:23]\n"
    "ds_read_b128 v[192:195], v242 offset:6144\n"
    "v_mfma_f32_16x16x32_bf16 v[56:59], v[124:127], v[204:207], v[56:59]\n"
    "v_mfma_f32_16x16x32_bf16 v[56:59], v[116:119], v[200:203], v[56:59]\n"
    "ds_read_b128 v[196:199], v243 offset:6144\n"
    "buffer_load_dwordx4 v8, s[20:23], s64 offen offset:1024 nt lds\n"
    "s_waitcnt lgkmcnt(2)\n"
    "v_mfma_f32_16x16x32_bf16 v[24:27], v[112:115], v[208:211], v[24:27]\n"
    "v_mfma_f32_16x16x32_bf16 v[24:27], v[120:123], v[212:215], v[24:27]\n"
    "ds_read_b128 v[200:203], v242 offset:8192\n"
    "v_mfma_f32_16x16x32_bf16 v[60:63], v[124:127], v[212:215], v[60:63]\n"
    "v_mfma_f32_16x16x32_bf16 v[60:63], v[116:119], v[208:211], v[60:63]\n"
    "ds_read_b128 v[204:207], v243 offset:8192\n"
    "s_add_u32 s64, s73, 0x4000\n"
    "buffer_load_dwordx4 v[216:219], v7, s[20:23], s64 offen nt\n"
    "s_waitcnt lgkmcnt(2)\n"
    "v_mfma_f32_16x16x32_bf16 v[28:31], v[112:115], v[192:195], v[28:31]\n"
    "v_mfma_f32_16x16x32_bf16 v[28:31], v[120:123], v[196:199], v[28:31]\n"
    "ds_read_b128 v[208:211], v242 offset:10240\n"
    "v_mfma_f32_16x16x32_bf16 v[64:67], v[124:127], v[196:199], v[64:67]\n"
    "v_mfma_f32_16x16x32_bf16 v[64:67], v[116:119], v[192:195], v[64:67]\n"
    "ds_read_b128 v[212:215], v243 offset:10240\n"
    "buffer_load_dwordx4 v[220:223], v8, s[20:23], s64 offen offset:1024 nt\n"
    "s_waitcnt lgkmcnt(2)\n"
    "v_mfma_f32_16x16x32_bf16 v[32:35], v[112:115], v[200:203], v[32:35]\n"
    "v_mfma_f32_16x16x32_bf16 v[32:35], v[120:123], v[204:207], v[32:35]\n"
    "ds_read_b128 v[192:195], v242 offset:12288\n"
    "v_mfma_f32_16x16x32_bf16 v[68:71], v[124:127], v[204:207], v[68:71]\n"
    "v_mfma_f32_16x16x32_bf16 v[68:71], v[116:119], v[200:203], v[68:71]\n"
    "ds_read_b128 v[196:199], v243 offset:12288\n"
    "s_waitcnt lgkmcnt(2)\n"
    "v_mfma_f32_16x16x32_bf16 v[36:39], v[112:115], v[208:211], v[36:39]\n"
    "v_mfma_f32_16x16x32_bf16 v[36:39], v[120:123], v[212:215], v[36:39]\n"
    "ds_read_b128 v[200:203], v242 offset:14336\n"
    "v_mfma_f32_16x16x32_bf16 v[72:75], v[124:127], v[212:215], v[72:75]\n"
    "v_mfma_f32_16x16x32_bf16 v[72:75], v[116:119], v[208:211], v[72:75]\n"
    "ds_read_b128 v[204:207], v243 offset:14336\n"
    "s_waitcnt lgkmcnt(2)\n"
    "v_mfma_f32_16x16x32_bf16 v[40:43], v[112:115], v[192:195], v[40:43]\n"
    "v_mfma_f32_16x16x32_bf16 v[40:43], v[120:123], v[196:199], v[40:43]\n"
    "ds_read_b128 v[208:211], v242 offset:16384\n"
    "v_mfma_f32_16x16x32_bf16 v[76:79], v[124:127], v[196:199], v[76:79]\n"
    "v_mfma_f32_16x16x32_bf16 v[76:79], v[116:119], v[192:195], v[76:79]\n"
    "ds_read_b128 v[212:215], v243 offset:16384\n"
    "s_waitcnt lgkmcnt(2)\n"
    "v_mfma_f32_16x16x32_bf16 v[44:47], v[112:115], v[200:203], v[44:47]\n"
    "v_mfma_f32_16x16x32_bf16 v[44:47], v[120:123], v[204:207], v[44:47]\n"
    "v_mfma_f32_16x16x32_bf16 v[80:83], v[124:127], v[204:207], v[80:83]\n"
    "v_mfma_f32_16x16x32_bf16 v[80:83], v[116:119], v[200:203], v[80:83]\n"
    "s_waitcnt lgkmcnt(0)\n"
    "v_mfma_f32_16x16x32_bf16 v[48:51], v[112:115], v[208:211], v[48:51]\n"
    "v_mfma_f32_16x16x32_bf16 v[48:51], v[120:123], v[212:215], v[48:51]\n"
    "v_mfma_f32_16x16x32_bf16 v[84:87], v[124:127], v[212:215], v[84:87]\n"
    "v_mfma_f32_16x16x32_bf16 v[84:87], v[116:119], v[208:211], v[84:87]\n"
    "v_pk_mul_f32 v[16:17], v[16:17], s[94:95]\n"
    "v_pk_mul_f32 v[18:19], v[18:19], s[94:95]\n"
    "v_cvt_pk_bf16_f32 v16, v16, v17\n"
    "v_cvt_pk_bf16_f32 v17, v18, v19\n"
    "ds_write_b64 v6, v[16:17]\n"
    "v_pk_mul_f32 v[52:53], v[52:53], s[94:95]\n"
    "v_pk_mul_f32 v[54:55], v[54:55], s[94:95]\n"
    "v_cvt_pk_bf16_f32 v52, v52, v53\n"
    "v_cvt_pk_bf16_f32 v53, v54, v55\n"
    "ds_write_b64 v97, v[52:53]\n"
    "v_pk_mul_f32 v[20:21], v[20:21], s[94:95]\n"
    "v_pk_mul_f32 v[22:23], v[22:23], s[94:95]\n"
    "v_cvt_pk_bf16_f32 v20, v20, v21\n"
    "v_cvt_pk_bf16_f32 v21, v22, v23\n"
    "ds_write_b64 v6, v[20:21] offset:4096\n"
    "v_pk_mul_f32 v[56:57], v[56:57], s[94:95]\n"
    "v_pk_mul_f32 v[58:59], v[58:59], s[94:95]\n"
    "v_cvt_pk_bf16_f32 v56, v56, v57\n"
    "v_cvt_pk_bf16_f32 v57, v58, v59\n"
    "ds_write_b64 v97, v[56:57] offset:4096\n"
    "v_pk_mul_f32 v[24:25], v[24:25], s[94:95]\n"
    "v_pk_mul_f32 v[26:27], v[26:27], s[94:95]\n"
    "v_cvt_pk_bf16_f32 v24, v24, v25\n"
    "v_cvt_pk_bf16_f32 v25, v26, v27\n"
    "ds_write_b64 v6, v[24:25] offset:8192\n"
    "v_pk_mul_f32 v[60:61], v[60:61], s[94:95]\n"
    "v_pk_mul_f32 v[62:63], v[62:63], s[94:95]\n"
    "v_cvt_pk_bf16_f32 v60, v60, v61\n"
    "v_cvt_pk_bf16_f32 v61, v62, v63\n"
    "ds_write_b64 v97, v[60:61] offset:8192\n"
    "v_pk_mul_f32 v[28:29], v[28:29], s[94:95]\n"
    "v_pk_mul_f32 v[30:31], v[30:31], s[94:95]\n"
    "v_cvt_pk_bf16_f32 v28, v28, v29\n"
    "v_cvt_pk_bf16_f32 v29, v30, v31\n"
    "ds_write_b64 v6, v[28:29] offset:12288\n"
    "v_pk_mul_f32 v[64:65], v[64:65], s[94:95]\n"
    "v_pk_mul_f32 v[66:67], v[66:67], s[94:95]\n"
    "v_cvt_pk_bf16_f32 v64, v64, v65\n"
    "v_cvt_pk_bf16_f32 v65, v66, v67\n"
    "ds_write_b64 v97, v[64:65] offset:12288\n"
    "v_pk_mul_f32 v[32:33], v[32:33], s[94:95]\n"
    "v_pk_mul_f32 v[34:35], v[34:35], s[94:95]\n"
    "v_cvt_pk_bf16_f32 v32, v32, v33\n"
    "v_cvt_pk_bf16_f32 v33, v34, v35\n"
    "ds_write_b64 v6, v[32:33] offset:16384\n"
    "v_pk_mul_f32 v[68:69], v[68:69], s[94:95]\n"
    "v_pk_mul_f32 v[70:71], v[70:71], s[94:95]\n"
    "v_cvt_pk_bf16_f32 v68, v68, v69\n"
    "v_cvt_pk_bf16_f32 v69, v70, v71\n"
    "ds_write_b64 v97, v[68:69] offset:16384\n"
    "v_pk_mul_f32 v[36:37], v[36:37], s[94:95]\n"
    "v_pk_mul_f32 v[38:39], v[38:39], s[94:95]\n"
    "v_cvt_pk_bf16_f32 v36, v36, v37\n"
    "v_cvt_pk_bf16_f32 v37, v38, v39\n"
    "ds_write_b64 v6, v[36:37] offset:20480\n"
    "v_pk_mul_f32 v[72:73], v[72:73], s[94:95]\n"
    "v_pk_mul_f32 v[74:75], v[74:75], s[94:95]\n"
    "v_cvt_pk_bf16_f32 v72, v72, v73\n"
    "v_cvt_pk_bf16_f32 v73, v74, v75\n"
    "ds_write_b64 v97, v[72:73] offset:20480\n"
    "v_pk_mul_f32 v[40:41], v[40:41], s[94:95]\n"
    "v_pk_mul_f32 v[42:43], v[42:43], s[94:95]\n"
    "v_cvt_pk_bf16_f32 v40, v40, v41\n"
    "v_cvt_pk_bf16_f32 v41, v42, v43\n"
    "ds_write_b64 v6, v[40:41] offset:24576\n"
    "v_pk_mul_f32 v[76:77], v[76:77], s[94:95]\n"
    "v_pk_mul_f32 v[78:79], v[78:79], s[94:95]\n"
    "v_cvt_pk_bf16_f32 v76, v76, v77\n"
    "v_cvt_pk_bf16_f32 v77, v78, v79\n"
    "ds_write_b64 v97, v[76:77] offset:24576\n"
    "v_pk_mul_f32 v[44:45], v[44:45], s[94:95]\n"
    "v_pk_mul_f32 v[46:47], v[46:47], s[94:95]\n"
    "v_cvt_pk_bf16_f32 v44, v44, v45\n"
    "v_cvt_pk_bf16_f32 v45, v46, v47\n"
    "ds_write_b64 v6, v[44:45] offset:28672\n"
    "v_pk_mul_f32 v[80:81], v[80:81], s[94:95]\n"
    "v_pk_mul_f32 v[82:83], v[82:83], s[94:95]\n"
    "v_cvt_pk_bf16_f32 v80, v80, v81\n"
    "v_cvt_pk_bf16_f32 v81, v82, v83\n"
    "s_waitcnt lgkmcnt(13)\n"
    "ds_write_b64 v97, v[80:81] offset:28672\n"
    "v_pk_mul_f32 v[48:49], v[48:49], s[94:95]\n"
    "v_pk_mul_f32 v[50:51], v[50:51], s[94:95]\n"
    "v_cvt_pk_bf16_f32 v48, v48, v49\n"
    "v_cvt_pk_bf16_f32 v49, v50, v51\n"
    "ds_write_b64 v6, v[48:49] offset:32768\n"
    "v_pk_mul_f32 v[84:85], v[84:85], s[94:95]\n"
    "v_pk_mul_f32 v[86:87], v[86:87], s[94:95]\n"
    "v_cvt_pk_bf16_f32 v84, v84, v85\n"
    "v_cvt_pk_bf16_f32 v85, v86, v87\n"
    "s_waitcnt lgkmcnt(13)\n"
    "ds_write_b64 v97, v[84:85] offset:32768\n"
    "v_mov_b32 v176, 0\n"
    "v_mov_b32 v177, 0\n"
    "v_mov_b32 v178, 0\n"
    "v_mov_b32 v179, 0\n"
    "s_nop 1\n"
    "v_mfma_f32_32x32x16_bf16 v[112:127], v[176:179], v[176:179], 0\n"
    "v_mfma_f32_32x32x16_bf16 v[128:143], v[176:179], v[176:179], 0\n"
    "s_mul_i32 s65, s13, 5\n"
    "s_lshr_b32 s65, s65, 2\n"
    "s_lshl_b32 s96, s65, 7\n"
    "s_lshl_b32 s97, s65, 19\n"
    "v_add_u32 v88, 0x100, v88\n"
    "v_add_u32 v89, 0x100, v89\n"
    "v_add_u32 v90, 0x100, v90\n"
    "v_add_u32 v91, 0x100, v91\n"
    "s_mov_b32 s74, 0x1e800\n"
    "s_mov_b32 s75, 0x16000\n"
    "s_waitcnt lgkmcnt(0)\n"
    "s_barrier\n"
    "ds_read_b128 v[144:147], v12\n"
    "ds_read_b128 v[148:151], v12 offset:512\n"
    "ds_read_b128 v[152:155], v12 offset:1024\n"
    "ds_read_b128 v[156:159], v12 offset:1536\n"
    "s_cmp_ge_u32 s12, 2\n"
    "s_cbranch_scc1 .Lkt01_47\n"
    ".Lkt00_46:\n"
    "s_setprio 1\n"
    "v_mov_b32 v192, 0\n"
    "v_mov_b32 v193, 0\n"
    "v_mov_b32 v194, 0\n"
    "v_mov_b32 v195, 0\n"
    "s_nop 1\n"
    "v_mfma_f32_32x32x16_bf16 v[16:31], v[192:195], v[192:195], 0\n"
    "v_mfma_f32_32x32x16_bf16 v[32:47], v[192:195], v[192:195], 0\n"
    "v_mfma_f32_32x32x16_bf16 v[48:63], v[192:195], v[192:195], 0\n"
    "v_mfma_f32_32x32x16_bf16 v[64:79], v[192:195], v[192:195], 0\n"
    "v_mfma_f32_16x16x32_bf16 v[80:83], v[192:195], v[192:195], 0\n"
    "v_mfma_f32_16x16x32_bf16 v[84:87], v[192:195], v[192:195], 0\n"
    "s_cmp_eq_u32 s88, 1\n"
    "s_cselect_b32 s85, s53, s52\n"
    "s_add_u32 s62, s98, 0x16000\n"
    "s_add_u32 m0, s62, 0x4800\n"
    "s_nop 0\n"
    "buffer_load_dwordx4 v88, s[28:31], s97 offen lds\n"
    "v_add_u32 v248, s85, v236\n"
    "v_add_u32 v249, s85, v237\n"
    "ds_read_b128 v[176:179], v248\n"
    "ds_read_b128 v[180:183], v249\n"
    "ds_read_b128 v[184:187], v248 offset:512\n"
    "ds_read_b128 v[188:191], v249 offset:512\n"
    "buffer_load_dwordx4 v89, s[28:31], s97 offen offset:1024 lds\n"
    "s_waitcnt lgkmcnt(3)\n"
    "v_mfma_f32_16x16x32_bf16 v[160:163], v[176:179], v[144:147], v[172:175]\n"
    "ds_read_b128 v[176:179], v248 offset:1024\n"
    "buffer_load_dwordx4 v90, s[28:31], s97 offen offset:2048 lds\n"
    "s_waitcnt lgkmcnt(3)\n"
    "v_mfma_f32_16x16x32_bf16 v[164:167], v[180:183], v[144:147], v[172:175]\n"
    "ds_read_b128 v[180:183], v249 offset:1024\n"
    "buffer_load_dwordx4 v91, s[28:31], s97 offen offset:3072 lds\n"
    "s_mov_b32 m0, s62\n"
    "s_add_u32 s62, s72, 0x16000\n"
    "buffer_load_dwordx4 v92, s[24:27], s96 offen lds\n"
    "s_waitcnt lgkmcnt(3)\n"
    "v_mfma_f32_16x16x32_bf16 v[160:163], v[184:187], v[148:151], v[160:163]\n"
    "ds_read_b128 v[184:187], v248 offset:1536\n"
    "buffer_load_dwordx4 v93, s[24:27], s96 offen offset:1024 lds\n"
    "s_waitcnt lgkmcnt(3)\n"
    "v_mfma_f32_16x16x32_bf16 v[164:167], v[188:191], v[148:151], v[164:167]\n"
    "ds_read_b128 v[188:191], v249 offset:1536\n"
    "buffer_load_dwordx4 v94, s[24:27], s96 offen offset:2048 lds\n"
    "s_waitcnt lgkmcnt(3)\n"
    "v_mfma_f32_16x16x32_bf16 v[160:163], v[176:179], v[152:155], v[160:163]\n"
    "buffer_load_dwordx4 v95, s[24:27], s96 offen offset:3072 lds\n"
    "s_waitcnt lgkmcnt(2)\n"
    "v_mfma_f32_16x16x32_bf16 v[164:167], v[180:183], v[152:155], v[164:167]\n"
    "s_mov_b32 m0, s62\n"
    "s_cmp_ge_u32 s12, 2\n"
    "s_cbranch_scc1 .Lkx5_77\n"
    "buffer_load_dwordx4 v96, s[24:27], s96 offen lds\n"
    ".Lkx5_77:\n"
    "s_add_u32 s96, s96, 0x80\n"
    "s_cmp_lg_u32 s96, 0x500\n"
    "s_cselect_b32 s96, s96, 0\n"
    "s_lshl_b32 s97, s96, 12\n"
    "s_waitcnt lgkmcnt(1)\n"
    "v_mfma_f32_16x16x32_bf16 v[160:163], v[184:187], v[156:159], v[160:163]\n"
    "s_add_u32 m0, s73, s57\n"
    "s_add_u32 s64, s86, 0xa000\n"
    "buffer_load_dwordx4 v2, s[16:19], s64 offen nt lds\n"
    "s_waitcnt lgkmcnt(0)\n"
    "v_mfma_f32_16x16x32_bf16 v[164:167], v[188:191], v[156:159], v[164:167]\n"
    "buffer_load_dwordx4 v3, s[16:19], s64 offen offset:1024 nt lds\n"
    "s_add_u32 m0, s73, s58\n"
    "s_add_u32 s64, s86, s87\n"
    "buffer_load_dwordx4 v9, s[48:51], s64 offen nt lds\n"
    "v_max3_f32 v15, v160, v161, v162\n"
    "s_nop 2\n"
    "v_max3_f32 v244, v164, v165, v166\n"
    "buffer_load_dwordx4 v10, s[48:51], s64 offen offset:1024 nt lds\n"
    "s_add_u32 m0, s73, s59\n"
    "s_add_u32 s64, s86, 0x6000\n"
    "buffer_load_dwordx4 v7, s[20:23], s64 offen nt lds\n"
    "v_max3_f32 v15, v15, v163, v167\n"
    "v_max_f32 v15, v15, v244\n"
    "v_cmp_nge_f32 vcc, s70, v15\n"
    "s_nop 0\n"
    "s_cbranch_vccnz .Lkxrare_78\n"
    ".Lkxret_79:\n"
    "buffer_load_dwordx4 v8, s[20:23], s64 offen offset:1024 nt lds\n"
    "v_exp_f32 v160, v160\n"
    "v_exp_f32 v161, v161\n"
    "v_add_u32 v224, s56, v225\n"
    "s_waitcnt vmcnt(14)\n"
    "ds_write_b128 v224, v[216:219]\n"
    "ds_write_b128 v224, v[220:223] offset:1024\n"
    "v_exp_f32 v162, v162\n"
    "v_exp_f32 v163, v163\n"
    "v_add_f32 v14, v14, v160\n"
    "v_cvt_pk_bf16_f32 v168, v160, v161\n"
    "v_add_f32 v14, v14, v161\n"
    "s_add_u32 s64, s86, 0x8000\n"
    "buffer_load_dwordx4 v[216:219], v7, s[20:23], s64 offen nt\n"
    "buffer_load_dwordx4 v[220:223], v8, s[20:23], s64 offen offset:1024 nt\n"
    "s_add_u32 s67, s52, 0x8000\n"
    "s_sub_u32 s56, s52, 0xe000\n"
    "s_cselect_b32 s56, s67, s56\n"
    "s_add_u32 s67, s53, 0x8000\n"
    "s_sub_u32 s57, s53, 0xe000\n"
    "s_cselect_b32 s57, s67, s57\n"
    "s_add_u32 s67, s54, 0x8000\n"
    "s_sub_u32 s58, s54, 0xe000\n"
    "s_cselect_b32 s58, s67, s58\n"
    "s_add_u32 s67, s55, 0x8000\n"
    "s_sub_u32 s59, s55, 0xe000\n"
    "s_cselect_b32 s59, s67, s59\n"
    "s_add_u32 s15, s15, 1\n"
    "s_add_u32 s86, s86, 0x4000\n"
    "s_waitcnt vmcnt(8) lgkmcnt(0)\n"
    "s_setprio 0\n"
    "s_barrier\n"
    "s_branch .Lkq1_49\n"
    ".Lkt01_47:\n"
    "s_setprio 1\n"
    "v_mov_b32 v192, 0\n"
    "v_mov_b32 v193, 0\n"
    "v_mov_b32 v194, 0\n"
    "v_mov_b32 v195, 0\n"
    "s_nop 1\n"
    "v_mfma_f32_32x32x16_bf16 v[16:31], v[192:195], v[192:195], 0\n"
    "v_mfma_f32_32x32x16_bf16 v[32:47], v[192:195], v[192:195], 0\n"
    "v_mfma_f32_32x32x16_bf16 v[48:63], v[192:195], v[192:195], 0\n"
    "v_mfma_f32_32x32x16_bf16 v[64:79], v[192:195], v[192:195], 0\n"
    "v_mfma_f32_16x16x32_bf16 v[80:83], v[192:195], v[192:195], 0\n"
    "v_mfma_f32_16x16x32_bf16 v[84:87], v[192:195], v[192:195], 0\n"
    "s_add_u32 s62, s98, 0x16000\n"
    "s_add_u32 m0, s62, 0x4800\n"
    "s_nop 0\n"
    "buffer_load_dwordx4 v88, s[28:31], s97 offen lds\n"
    "buffer_load_dwordx4 v89, s[28:31], s97 offen offset:1024 lds\n"
    "buffer_load_dwordx4 v90, s[28:31], s97 offen offset:2048 lds\n"
    "buffer_load_dwordx4 v91, s[28:31], s97 offen offset:3072 lds\n"
    "s_mov_b32 m0, s62\n"
    "s_add_u32 s62, s72, 0x16000\n"
    "buffer_load_dwordx4 v92, s[24:27], s96 offen lds\n"
    "buffer_load_dwordx4 v93, s[24:27], s96 offen offset:1024 lds\n"
    "buffer_load_dwordx4 v94, s[24:27], s96 offen offset:2048 lds\n"
    "buffer_load_dwordx4 v95, s[24:27], s96 offen offset:3072 lds\n"
    "s_mov_b32 m0, s62\n"
    "s_cmp_ge_u32 s12, 2\n"
    "s_cbranch_scc1 .Lkx5_80\n"
    "buffer_load_dwordx4 v96, s[24:27], s96 offen lds\n"
    ".Lkx5_80:\n"
    "s_waitcnt lgkmcnt(0)\n"
    "s_cmp_lg_u32 s12, 3\n"
    "s_cbranch_scc1 .Lkn3_81\n"
    "v_add_u32 v248, s58, v236\n"
    "v_add_u32 v249, s58, v237\n"
    "ds_read_b128 v[176:179], v248\n"
    "ds_read_b128 v[180:183], v249\n"
    "ds_read_b128 v[184:187], v248 offset:512\n"
    "ds_read_b128 v[188:191], v249 offset:512\n"
    "s_waitcnt lgkmcnt(3)\n"
    "v_mfma_f32_16x16x32_bf16 v[160:163], v[176:179], v[144:147], v[172:175]\n"
    "ds_read_b128 v[176:179], v248 offset:1024\n"
    "s_waitcnt lgkmcnt(3)\n"
    "v_mfma_f32_16x16x32_bf16 v[164:167], v[180:183], v[144:147], v[172:175]\n"
    "ds_read_b128 v[180:183], v249 offset:1024\n"
    "s_waitcnt lgkmcnt(3)\n"
    "v_mfma_f32_16x16x32_bf16 v[160:163], v[184:187], v[148:151], v[160:163]\n"
    "ds_read_b128 v[184:187], v248 offset:1536\n"
    "s_waitcnt lgkmcnt(3)\n"
    "v_mfma_f32_16x16x32_bf16 v[164:167], v[188:191], v[148:151], v[164:167]\n"
    "ds_read_b128 v[188:191], v249 offset:1536\n"
    "s_waitcnt lgkmcnt(3)\n"
    "v_mfma_f32_16x16x32_bf16 v[160:163], v[176:179], v[152:155], v[160:163]\n"
    "s_waitcnt lgkmcnt(2)\n"
    "v_mfma_f32_16x16x32_bf16 v[164:167], v[180:183], v[152:155], v[164:167]\n"
    "s_waitcnt lgkmcnt(1)\n"
    "v_mfma_f32_16x16x32_bf16 v[160:163], v[184:187], v[156:159], v[160:163]\n"
    "s_waitcnt lgkmcnt(0)\n"
    "v_mfma_f32_16x16x32_bf16 v[164:167], v[188:191], v[156:159], v[164:167]\n"
    "s_nop 5\n"
    "v_max3_f32 v15, v160, v161, v162\n"
    "s_nop 0\n"
    "v_max3_f32 v244, v164, v165, v166\n"
    "v_max3_f32 v15, v15, v163, v167\n"
    "v_max_f32 v15, v15, v244\n"
    "v_cmp_nge_f32 vcc, s70, v15\n"
    "s_nop 0\n"
    "s_cbranch_vccnz .Lkxrare_82\n"
    ".Lkxret_83:\n"
    "v_exp_f32 v160, v160\n"
    "v_exp_f32 v161, v161\n"
    "v_exp_f32 v162, v162\n"
    "v_exp_f32 v163, v163\n"
    "v_add_f32 v14, v14, v160\n"
    "v_cvt_pk_bf16_f32 v168, v160, v161\n"
    "v_add_f32 v14, v14, v161\n"
    ".Lkn3_81:\n"
    "s_cmp_eq_u32 s88, 1\n"
    "s_cselect_b32 s85, s59, s58\n"
    "v_add_u32 v248, s85, v0\n"
    "v_xor_b32 v249, 32, v248\n"
    "ds_read_b64_tr_b16 v[176:177], v248\n"
    "ds_read_b64_tr_b16 v[178:179], v248 offset:256\n"
    "ds_read_b64_tr_b16 v[180:181], v249\n"
    "ds_read_b64_tr_b16 v[182:183], v249 offset:256\n"
    "ds_read_b64_tr_b16 v[184:185], v248 offset:512\n"
    "ds_read_b64_tr_b16 v[186:187], v248 offset:768\n"
    "ds_read_b64_tr_b16 v[188:189], v249 offset:512\n"
    "ds_read_b64_tr_b16 v[190:191], v249 offset:768\n"
    "s_add_u32 s96, s96, 0x80\n"
    "s_cmp_lg_u32 s96, 0x500\n"
    "s_cselect_b32 s96, s96, 0\n"
    "s_lshl_b32 s97, s96, 12\n"
    "v_exp_f32 v164, v164\n"
    "v_exp_f32 v165, v165\n"
    "v_add_f32 v14, v14, v162\n"
    "v_cvt_pk_bf16_f32 v169, v162, v163\n"
    "v_add_f32 v14, v14, v163\n"
    "s_add_u32 m0, s73, s53\n"
    "s_add_u32 s64, s86, 0xa000\n"
    "buffer_load_dwordx4 v2, s[16:19], s64 offen nt lds\n"
    "v_exp_f32 v166, v166\n"
    "v_exp_f32 v167, v167\n"
    "v_add_f32 v14, v14, v164\n"
    "v_cvt_pk_bf16_f32 v170, v164, v165\n"
    "v_add_f32 v14, v14, v165\n"
    "v_add_f32 v14, v14, v166\n"
    "v_cvt_pk_bf16_f32 v171, v166, v167\n"
    "v_add_f32 v14, v14, v167\n"
    "buffer_load_dwordx4 v3, s[16:19], s64 offen offset:1024 nt lds\n"
    "s_waitcnt lgkmcnt(6)\n"
    "v_mfma_f32_16x16x32_bf16 v[112:115], v[176:179], v[168:171], v[112:115]\n"
    "ds_read_b64_tr_b16 v[176:177], v248 offset:1024\n"
    "ds_read_b64_tr_b16 v[178:179], v248 offset:1280\n"
    "s_add_u32 m0, s73, s54\n"
    "s_add_u32 s64, s86, s87\n"
    "buffer_load_dwordx4 v9, s[48:51], s64 offen nt lds\n"
    "s_waitcnt lgkmcnt(6)\n"
    "v_mfma_f32_16x16x32_bf16 v[116:119], v[180:183], v[168:171], v[116:119]\n"
    "ds_read_b64_tr_b16 v[180:181], v249 offset:1024\n"
    "ds_read_b64_tr_b16 v[182:183], v249 offset:1280\n"
    "buffer_load_dwordx4 v10, s[48:51], s64 offen offset:1024 nt lds\n"
    "s_waitcnt lgkmcnt(6)\n"
    "v_mfma_f32_16x16x32_bf16 v[120:123], v[184:187], v[168:171], v[120:123]\n"
    "ds_read_b64_tr_b16 v[184:185], v248 offset:1536\n"
    "ds_read_b64_tr_b16 v[186:187], v248 offset:1792\n"
    "s_waitcnt lgkmcnt(6)\n"
    "v_mfma_f32_16x16x32_bf16 v[124:127], v[188:191], v[168:171], v[124:127]\n"
    "ds_read_b64_tr_b16 v[188:189], v249 offset:1536\n"
    "ds_read_b64_tr_b16 v[190:191], v249 offset:1792\n"
    "s_add_u32 m0, s73, s55\n"
    "s_add_u32 s64, s86, 0x6000\n"
    "buffer_load_dwordx4 v7, s[20:23], s64 offen nt lds\n"
    "s_waitcnt lgkmcnt(6)\n"
    "v_mfma_f32_16x16x32_bf16 v[128:131], v[176:179], v[168:171], v[128:131]\n"
    "buffer_load_dwordx4 v8, s[20:23], s64 offen offset:1024 nt lds\n"
    "s_waitcnt lgkmcnt(4)\n"
    "v_mfma_f32_16x16x32_bf16 v[132:135], v[180:183], v[168:171], v[132:135]\n"
    "s_waitcnt lgkmcnt(2)\n"
    "v_mfma_f32_16x16x32_bf16 v[136:139], v[184:187], v[168:171], v[136:139]\n"
    "v_add_u32 v224, s52, v225\n"
    "s_waitcnt vmcnt(14)\n"
    "ds_write_b128 v224, v[216:219]\n"
    "ds_write_b128 v224, v[220:223] offset:1024\n"
    "s_waitcnt lgkmcnt(2)\n"
    "v_mfma_f32_16x16x32_bf16 v[140:143], v[188:191], v[168:171], v[140:143]\n"
    "s_add_u32 s64, s86, 0x8000\n"
    "buffer_load_dwordx4 v[216:219], v7, s[20:23], s64 offen nt\n"
    "s_add_u32 s76, s76, 1\n"
    "s_cmp_eq_u32 s76, 8\n"
    "s_cbranch_scc1 .Lkdump_84\n"
    ".Lkdret_85:\n"
    "buffer_load_dwordx4 v[220:223], v8, s[20:23], s64 offen offset:1024 nt\n"
    "s_add_u32 s67, s56, 0x8000\n"
    "s_sub_u32 s52, s56, 0xe000\n"
    "s_cselect_b32 s52, s67, s52\n"
    "s_add_u32 s67, s57, 0x8000\n"
    "s_sub_u32 s53, s57, 0xe000\n"
    "s_cselect_b32 s53, s67, s53\n"
    "s_add_u32 s67, s58, 0x8000\n"
    "s_sub_u32 s54, s58, 0xe000\n"
    "s_cselect_b32 s54, s67, s54\n"
    "s_add_u32 s67, s59, 0x8000\n"
    "s_sub_u32 s55, s59, 0xe000\n"
    "s_cselect_b32 s55, s67, s55\n"
    "s_add_u32 s15, s15, 1\n"
    "s_add_u32 s86, s86, 0x4000\n"
    "s_waitcnt vmcnt(8) lgkmcnt(0)\n"
    "s_setprio 0\n"
    "s_barrier\n"
    "s_branch .Lkq0_48\n"
    ".Lkq0_48:\n"
    "v_add_u32 v232, s75, v228\n"
    "v_add_u32 v233, s75, v229\n"
    "v_add_u32 v234, s75, v230\n"
    "v_add_u32 v235, s75, v231\n"
    "v_add_u32 v242, s75, v240\n"
    "v_add_u32 v243, s75, v241\n"
    "ds_read_b64_tr_b16 v[176:177], v232\n"
    "ds_read_b64_tr_b16 v[178:179], v233\n"
    "ds_read_b128 v[192:195], v242\n"
    "ds_read_b64_tr_b16 v[184:185], v232 offset:8192\n"
    "ds_read_b64_tr_b16 v[186:187], v233 offset:8192\n"
    "ds_read_b128 v[196:199], v243\n"
    "ds_read_b64_tr_b16 v[188:189], v234 offset:8192\n"
    "ds_read_b64_tr_b16 v[190:191], v235 offset:8192\n"
    "ds_read_b64_tr_b16 v[180:181], v234\n"
    "ds_read_b64_tr_b16 v[182:183], v235\n"
    "ds_read_b128 v[200:203], v242 offset:2048\n"
    "ds_read_b128 v[204:207], v243 offset:2048\n"
    "s_cmp_eq_u32 s88, 1\n"
    "s_cselect_b32 s85, s53, s52\n"
    "v_add_u32 v248, s85, v236\n"
    "v_add_u32 v249, s85, v237\n"
    "ds_read_b128 v[100:103], v248\n"
    "ds_read_b128 v[104:107], v249\n"
    "ds_read_b128 v[108:111], v248 offset:512\n"
    "s_add_u32 s62, s98, s74\n"
    "s_add_u32 m0, s62, 0x4800\n"
    "s_nop 0\n"
    "buffer_load_dwordx4 v88, s[28:31], s97 offen lds\n"
    "buffer_load_dwordx4 v89, s[28:31], s97 offen offset:1024 lds\n"
    "s_setprio 1\n"
    "s_waitcnt lgkmcnt(5)\n"
    "v_mfma_f32_16x16x32_bf16 v[16:19], v[176:179], v[192:195], v[16:19]\n"
    "ds_read_b128 v[208:211], v242 offset:4096\n"
    "buffer_load_dwordx4 v90, s[28:31], s97 offen offset:2048 lds\n"
    "v_mfma_f32_16x16x32_bf16 v[16:19], v[184:187], v[196:199], v[16:19]\n"
    "ds_read_b128 v[212:215], v243 offset:4096\n"
    "buffer_load_dwordx4 v91, s[28:31], s97 offen offset:3072 lds\n"
    "v_mfma_f32_16x16x32_bf16 v[52:55], v[188:191], v[196:199], v[52:55]\n"
    "v_mfma_f32_16x16x32_bf16 v[52:55], v[180:183], v[192:195], v[52:55]\n"
    "ds_read_b128 v[192:195], v242 offset:6144\n"
    "s_waitcnt lgkmcnt(5)\n"
    "v_mfma_f32_16x16x32_bf16 v[20:23], v[176:179], v[200:203], v[20:23]\n"
    "v_mfma_f32_16x16x32_bf16 v[20:23], v[184:187], v[204:207], v[20:23]\n"
    "v_mfma_f32_16x16x32_bf16 v[160:163], v[100:103], v[144:147], v[172:175]\n"
    "ds_read_b128 v[100:103], v249 offset:512\n"
    "s_mov_b32 m0, s62\n"
    "s_add_u32 s62, s72, s74\n"
    "buffer_load_dwordx4 v92, s[24:27], s96 offen lds\n"
    "v_mfma_f32_16x16x32_bf16 v[56:59], v[188:191], v[204:207], v[56:59]\n"
    "ds_read_b128 v[196:199], v243 offset:6144\n"
    "v_mfma_f32_16x16x32_bf16 v[56:59], v[180:183], v[200:203], v[56:59]\n"
    "s_waitcnt lgkmcnt(6)\n"
    "v_mfma_f32_16x16x32_bf16 v[164:167], v[104:107], v[144:147], v[172:175]\n"
    "ds_read_b128 v[104:107], v248 offset:1024\n"
    "buffer_load_dwordx4 v93, s[24:27], s96 offen offset:1024 lds\n"
    "s_waitcnt lgkmcnt(4)\n"
    "v_mfma_f32_16x16x32_bf16 v[24:27], v[176:179], v[208:211], v[24:27]\n"
    "ds_read_b128 v[200:203], v242 offset:8192\n"
    "v_mfma_f32_16x16x32_bf16 v[24:27], v[184:187], v[212:215], v[24:27]\n"
    "v_mfma_f32_16x16x32_bf16 v[160:163], v[108:111], v[148:151], v[160:163]\n"
    "ds_read_b128 v[108:111], v249 offset:1024\n"
    "buffer_load_dwordx4 v94, s[24:27], s96 offen offset:2048 lds\n"
    "buffer_load_dwordx4 v95, s[24:27], s96 offen offset:3072 lds\n"
    "v_mfma_f32_16x16x32_bf16 v[60:63], v[188:191], v[212:215], v[60:63]\n"
    "ds_read_b128 v[204:207], v243 offset:8192\n"
    "s_mov_b32 m0, s62\n"
    "v_mfma_f32_16x16x32_bf16 v[60:63], v[180:183], v[208:211], v[60:63]\n"
    "s_cmp_ge_u32 s12, 2\n"
    "s_cbranch_scc1 .Lkx5_86\n"
    "buffer_load_dwordx4 v96, s[24:27], s96 offen lds\n"
    ".Lkx5_86:\n"
    "s_waitcnt lgkmcnt(4)\n"
    "v_mfma_f32_16x16x32_bf16 v[28:31], v[176:179], v[192:195], v[28:31]\n"
    "ds_read_b128 v[208:211], v242 offset:10240\n"
    "v_mfma_f32_16x16x32_bf16 v[28:31], v[184:187], v[196:199], v[28:31]\n"
    "v_mfma_f32_16x16x32_bf16 v[164:167], v[100:103], v[148:151], v[164:167]\n"
    "ds_read_b128 v[100:103], v248 offset:1536\n"
    "s_add_u32 s96, s96, 0x80\n"
    "s_cmp_lg_u32 s96, 0x500\n"
    "s_cselect_b32 s96, s96, 0\n"
    "s_lshl_b32 s97, s96, 12\n"
    "v_mfma_f32_16x16x32_bf16 v[64:67], v[188:191], v[196:199], v[64:67]\n"
    "ds_read_b128 v[212:215], v243 offset:10240\n"
    "v_mfma_f32_16x16x32_bf16 v[64:67], v[180:183], v[192:195], v[64:67]\n"
    "s_waitcnt lgkmcnt(6)\n"
    "v_mfma_f32_16x16x32_bf16 v[160:163], v[104:107], v[152:155], v[160:163]\n"
    "ds_read_b128 v[104:107], v249 offset:1536\n"
    "s_add_u32 m0, s73, s57\n"
    "s_add_u32 s64, s86, 0xa000\n"
    "buffer_load_dwordx4 v2, s[16:19], s64 offen nt lds\n"
    "s_waitcnt lgkmcnt(4)\n"
    "v_mfma_f32_16x16x32_bf16 v[32:35], v[176:179], v[200:203], v[32:35]\n"
    "ds_read_b128 v[192:195], v242 offset:12288\n"
    "v_mfma_f32_16x16x32_bf16 v[32:35], v[184:187], v[204:207], v[32:35]\n"
    "buffer_load_dwordx4 v3, s[16:19], s64 offen offset:1024 nt lds\n"
    "v_mfma_f32_16x16x32_bf16 v[164:167], v[108:111], v[152:155], v[164:167]\n"
    "ds_read_b128 v[196:199], v243 offset:12288\n"
    "s_add_u32 m0, s73, s58\n"
    "v_mfma_f32_16x16x32_bf16 v[68:71], v[188:191], v[204:207], v[68:71]\n"
    "s_add_u32 s64, s86, s87\n"
    "buffer_load_dwordx4 v9, s[48:51], s64 offen nt lds\n"
    "v_mfma_f32_16x16x32_bf16 v[68:71], v[180:183], v[200:203], v[68:71]\n"
    "buffer_load_dwordx4 v10, s[48:51], s64 offen offset:1024 nt lds\n"
    "s_waitcnt lgkmcnt(3)\n"
    "v_mfma_f32_16x16x32_bf16 v[36:39], v[176:179], v[208:211], v[36:39]\n"
    "ds_read_b128 v[200:203], v242 offset:14336\n"
    "v_mfma_f32_16x16x32_bf16 v[36:39], v[184:187], v[212:215], v[36:39]\n"
    "s_add_u32 m0, s73, s59\n"
    "s_add_u32 s64, s86, 0x6000\n"
    "v_mfma_f32_16x16x32_bf16 v[160:163], v[100:103], v[156:159], v[160:163]\n"
    "buffer_load_dwordx4 v7, s[20:23], s64 offen nt lds\n"
    "v_mfma_f32_16x16x32_bf16 v[72:75], v[188:191], v[212:215], v[72:75]\n"
    "ds_read_b128 v[204:207], v243 offset:14336\n"
    "v_mfma_f32_16x16x32_bf16 v[72:75], v[180:183], v[208:211], v[72:75]\n"
    "buffer_load_dwordx4 v8, s[20:23], s64 offen offset:1024 nt lds\n"
    "s_waitcnt lgkmcnt(4)\n"
    "v_mfma_f32_16x16x32_bf16 v[164:167], v[104:107], v[156:159], v[164:167]\n"
    "ds_read_b128 v[208:211], v242 offset:16384\n"
    "v_add_u32 v224, s56, v225\n"
    "s_waitcnt lgkmcnt(3)\n"
    "v_mfma_f32_16x16x32_bf16 v[40:43], v[176:179], v[192:195], v[40:43]\n"
    "v_mfma_f32_16x16x32_bf16 v[40:43], v[184:187], v[196:199], v[40:43]\n"
    "s_waitcnt vmcnt(14)\n"
    "ds_write_b128 v224, v[216:219]\n"
    "ds_write_b128 v224, v[220:223] offset:1024\n"
    "v_mfma_f32_16x16x32_bf16 v[76:79], v[188:191], v[196:199], v[76:79]\n"
    "ds_read_b128 v[212:215], v243 offset:16384\n"
    "s_add_u32 s64, s86, 0x8000\n"
    "v_mfma_f32_16x16x32_bf16 v[76:79], v[180:183], v[192:195], v[76:79]\n"
    "buffer_load_dwordx4 v[216:219], v7, s[20:23], s64 offen nt\n"
    "buffer_load_dwordx4 v[220:223], v8, s[20:23], s64 offen offset:1024 nt\n"
    "s_waitcnt lgkmcnt(4)\n"
    "v_mfma_f32_16x16x32_bf16 v[44:47], v[176:179], v[200:203], v[44:47]\n"
    "v_max3_f32 v15, v160, v161, v162\n"
    "v_max3_f32 v244, v164, v165, v166\n"
    "v_mfma_f32_16x16x32_bf16 v[44:47], v[184:187], v[204:207], v[44:47]\n"
    "s_add_u32 s67, s52, 0x8000\n"
    "s_sub_u32 s56, s52, 0xe000\n"
    "s_cselect_b32 s56, s67, s56\n"
    "v_mfma_f32_16x16x32_bf16 v[80:83], v[188:191], v[204:207], v[80:83]\n"
    "v_max3_f32 v15, v15, v163, v167\n"
    "v_max_f32 v15, v15, v244\n"
    "v_mfma_f32_16x16x32_bf16 v[80:83], v[180:183], v[200:203], v[80:83]\n"
    "v_cmp_nge_f32 vcc, s70, v15\n"
    "s_nop 0\n"
    "s_cbranch_vccnz .Lkxrare_87\n"
    ".Lkxret_88:\n"
    "s_add_u32 s67, s53, 0x8000\n"
    "s_sub_u32 s57, s53, 0xe000\n"
    "s_cselect_b32 s57, s67, s57\n"
    "s_add_u32 s67, s54, 0x8000\n"
    "s_sub_u32 s58, s54, 0xe000\n"
    "s_cselect_b32 s58, s67, s58\n"
    "s_waitcnt lgkmcnt(0)\n"
    "v_mfma_f32_16x16x32_bf16 v[48:51], v[176:179], v[208:211], v[48:51]\n"
    "v_exp_f32 v160, v160\n"
    "v_exp_f32 v161, v161\n"
    "v_mfma_f32_16x16x32_bf16 v[48:51], v[184:187], v[212:215], v[48:51]\n"
    "s_add_u32 s67, s55, 0x8000\n"
    "s_sub_u32 s59, s55, 0xe000\n"
    "s_cselect_b32 s59, s67, s59\n"
    "v_mfma_f32_16x16x32_bf16 v[84:87], v[188:191], v[212:215], v[84:87]\n"
    "v_exp_f32 v162, v162\n"
    "v_exp_f32 v163, v163\n"
    "v_mfma_f32_16x16x32_bf16 v[84:87], v[180:183], v[208:211], v[84:87]\n"
    "v_add_f32 v14, v14, v160\n"
    "v_cvt_pk_bf16_f32 v168, v160, v161\n"
    "v_add_f32 v14, v14, v161\n"
    "s_add_u32 s15, s15, 1\n"
    "s_add_u32 s86, s86, 0x4000\n"
    "s_xor_b32 s74, s74, 0x8800\n"
    "s_xor_b32 s75, s75, 0x8800\n"
    "s_waitcnt vmcnt(8) lgkmcnt(0)\n"
    "s_cmp_lt_u32 s15, 10\n"
    "s_setprio 0\n"
    "s_barrier\n"
    "s_cbranch_scc1 .Lkq1_49\n"
    "s_mov_b32 s96, 0x7ff00000\n"
    "s_mov_b32 s97, 0x7ff00000\n"
    "s_cmp_eq_u32 s15, 10\n"
    "s_cbranch_scc1 .Lkq1_49\n"
    "s_branch .Lks11_1_62\n"
    ".Lkq1_49:\n"
    "v_add_u32 v232, s75, v228\n"
    "v_add_u32 v233, s75, v229\n"
    "v_add_u32 v234, s75, v230\n"
    "v_add_u32 v235, s75, v231\n"
    "v_add_u32 v242, s75, v240\n"
    "v_add_u32 v243, s75, v241\n"
    "ds_read_b64_tr_b16 v[176:177], v232\n"
    "ds_read_b64_tr_b16 v[178:179], v233\n"
    "ds_read_b128 v[192:195], v242\n"
    "ds_read_b64_tr_b16 v[184:185], v232 offset:8192\n"
    "ds_read_b64_tr_b16 v[186:187], v233 offset:8192\n"
    "ds_read_b128 v[196:199], v243\n"
    "ds_read_b64_tr_b16 v[188:189], v234 offset:8192\n"
    "ds_read_b64_tr_b16 v[190:191], v235 offset:8192\n"
    "ds_read_b64_tr_b16 v[180:181], v234\n"
    "ds_read_b64_tr_b16 v[182:183], v235\n"
    "ds_read_b128 v[200:203], v242 offset:2048\n"
    "ds_read_b128 v[204:207], v243 offset:2048\n"
    "s_cmp_eq_u32 s88, 1\n"
    "s_cselect_b32 s85, s59, s58\n"
    "v_add_u32 v248, s85, v0\n"
    "v_xor_b32 v249, 32, v248\n"
    "ds_read_b64_tr_b16 v[100:101], v248\n"
    "ds_read_b64_tr_b16 v[102:103], v248 offset:256\n"
    "ds_read_b64_tr_b16 v[104:105], v249\n"
    "s_waitcnt lgkmcnt(13)\n"
    "ds_read_b64_tr_b16 v[106:107], v249 offset:256\n"
    "ds_read_b64_tr_b16 v[108:109], v248 offset:512\n"
    "s_waitcnt lgkmcnt(13)\n"
    "ds_read_b64_tr_b16 v[110:111], v248 offset:768\n"
    "s_add_u32 s62, s98, s74\n"
    "s_add_u32 m0, s62, 0x4800\n"
    "s_nop 0\n"
    "buffer_load_dwordx4 v88, s[28:31], s97 offen lds\n"
    "buffer_load_dwordx4 v89, s[28:31], s97 offen offset:1024 lds\n"
    "s_setprio 1\n"
    "v_mfma_f32_16x16x32_bf16 v[16:19], v[176:179], v[192:195], v[16:19]\n"
    "ds_read_b128 v[208:211], v242 offset:4096\n"
    "buffer_load_dwordx4 v90, s[28:31], s97 offen offset:2048 lds\n"
    "s_waitcnt lgkmcnt(8)\n"
    "v_mfma_f32_16x16x32_bf16 v[16:19], v[184:187], v[196:199], v[16:19]\n"
    "ds_read_b128 v[212:215], v243 offset:4096\n"
    "buffer_load_dwordx4 v91, s[28:31], s97 offen offset:3072 lds\n"
    "v_mfma_f32_16x16x32_bf16 v[52:55], v[188:191], v[196:199], v[52:55]\n"
    "v_mfma_f32_16x16x32_bf16 v[52:55], v[180:183], v[192:195], v[52:55]\n"
    "ds_read_b128 v[192:195], v242 offset:6144\n"
    "v_exp_f32 v164, v164\n"
    "v_mfma_f32_16x16x32_bf16 v[20:23], v[176:179], v[200:203], v[20:23]\n"
    "v_exp_f32 v165, v165\n"
    "v_add_f32 v14, v14, v162\n"
    "s_waitcnt lgkmcnt(2)\n"
    "v_mfma_f32_16x16x32_bf16 v[20:23], v[184:187], v[204:207], v[20:23]\n"
    "v_cvt_pk_bf16_f32 v169, v162, v163\n"
    "v_add_f32 v14, v14, v163\n"
    "s_mov_b32 m0, s62\n"
    "s_add_u32 s62, s72, s74\n"
    "buffer_load_dwordx4 v92, s[24:27], s96 offen lds\n"
    "v_mfma_f32_16x16x32_bf16 v[56:59], v[188:191], v[204:207], v[56:59]\n"
    "ds_read_b128 v[196:199], v243 offset:6144\n"
    "v_exp_f32 v166, v166\n"
    "v_mfma_f32_16x16x32_bf16 v[56:59], v[180:183], v[200:203], v[56:59]\n"
    "v_exp_f32 v167, v167\n"
    "v_add_f32 v14, v14, v164\n"
    "v_cvt_pk_bf16_f32 v170, v164, v165\n"
    "v_add_f32 v14, v14, v165\n"
    "buffer_load_dwordx4 v93, s[24:27], s96 offen offset:1024 lds\n"
    "v_mfma_f32_16x16x32_bf16 v[24:27], v[176:179], v[208:211], v[24:27]\n"
    "ds_read_b128 v[200:203], v242 offset:8192\n"
    "buffer_load_dwordx4 v94, s[24:27], s96 offen offset:2048 lds\n"
    "s_waitcnt lgkmcnt(2)\n"
    "v_mfma_f32_16x16x32_bf16 v[24:27], v[184:187], v[212:215], v[24:27]\n"
    "buffer_load_dwordx4 v95, s[24:27], s96 offen offset:3072 lds\n"
    "v_mfma_f32_16x16x32_bf16 v[60:63], v[188:191], v[212:215], v[60:63]\n"
    "ds_read_b128 v[204:207], v243 offset:8192\n"
    "v_add_f32 v14, v14, v166\n"
    "v_mfma_f32_16x16x32_bf16 v[60:63], v[180:183], v[208:211], v[60:63]\n"
    "v_cvt_pk_bf16_f32 v171, v166, v167\n"
    "v_add_f32 v14, v14, v167\n"
    "s_mov_b32 m0, s62\n"
    "s_cmp_ge_u32 s12, 2\n"
    "s_cbranch_scc1 .Lkx5_89\n"
    "buffer_load_dwordx4 v96, s[24:27], s96 offen lds\n"
    ".Lkx5_89:\n"
    "v_mfma_f32_16x16x32_bf16 v[28:31], v[176:179], v[192:195], v[28:31]\n"
    "ds_read_b128 v[208:211], v242 offset:10240\n"
    "s_waitcnt lgkmcnt(3)\n"
    "v_mfma_f32_16x16x32_bf16 v[28:31], v[184:187], v[196:199], v[28:31]\n"
    "v_mfma_f32_16x16x32_bf16 v[112:115], v[100:103], v[168:171], v[112:115]\n"
    "ds_read_b64_tr_b16 v[100:101], v249 offset:512\n"
    "ds_read_b64_tr_b16 v[102:103], v249 offset:768\n"
    "s_add_u32 s96, s96, 0x80\n"
    "s_cmp_lg_u32 s96, 0x500\n"
    "s_cselect_b32 s96, s96, 0\n"
    "s_lshl_b32 s97, s96, 12\n"
    "v_mfma_f32_16x16x32_bf16 v[64:67], v[188:191], v[196:199], v[64:67]\n"
    "ds_read_b128 v[212:215], v243 offset:10240\n"
    "s_add_u32 m0, s73, s53\n"
    "v_mfma_f32_16x16x32_bf16 v[64:67], v[180:183], v[192:195], v[64:67]\n"
    "s_add_u32 s64, s86, 0xa000\n"
    "buffer_load_dwordx4 v2, s[16:19], s64 offen nt lds\n"
    "s_waitcnt lgkmcnt(4)\n"
    "v_mfma_f32_16x16x32_bf16 v[32:35], v[176:179], v[200:203], v[32:35]\n"
    "ds_read_b128 v[192:195], v242 offset:12288\n"
    "v_mfma_f32_16x16x32_bf16 v[32:35], v[184:187], v[204:207], v[32:35]\n"
    "v_mfma_f32_16x16x32_bf16 v[116:119], v[104:107], v[168:171], v[116:119]\n"
    "ds_read_b64_tr_b16 v[104:105], v248 offset:1024\n"
    "ds_read_b64_tr_b16 v[106:107], v248 offset:1280\n"
    "buffer_load_dwordx4 v3, s[16:19], s64 offen offset:1024 nt lds\n"
    "v_mfma_f32_16x16x32_bf16 v[68:71], v[188:191], v[204:207], v[68:71]\n"
    "ds_read_b128 v[196:199], v243 offset:12288\n"
    "v_mfma_f32_16x16x32_bf16 v[68:71], v[180:183], v[200:203], v[68:71]\n"
    "v_mfma_f32_16x16x32_bf16 v[120:123], v[108:111], v[168:171], v[120:123]\n"
    "ds_read_b64_tr_b16 v[108:109], v249 offset:1024\n"
    "ds_read_b64_tr_b16 v[110:111], v249 offset:1280\n"
    "s_add_u32 m0, s73, s54\n"
    "s_add_u32 s64, s86, s87\n"
    "buffer_load_dwordx4 v9, s[48:51], s64 offen nt lds\n"
    "buffer_load_dwordx4 v10, s[48:51], s64 offen offset:1024 nt lds\n"
    "s_waitcnt lgkmcnt(6)\n"
    "v_mfma_f32_16x16x32_bf16 v[36:39], v[176:179], v[208:211], v[36:39]\n"
    "ds_read_b128 v[200:203], v242 offset:14336\n"
    "v_mfma_f32_16x16x32_bf16 v[36:39], v[184:187], v[212:215], v[36:39]\n"
    "v_mfma_f32_16x16x32_bf16 v[124:127], v[100:103], v[168:171], v[124:127]\n"
    "ds_read_b64_tr_b16 v[100:101], v248 offset:1536\n"
    "ds_read_b64_tr_b16 v[102:103], v248 offset:1792\n"
    "s_add_u32 m0, s73, s55\n"
    "s_add_u32 s64, s86, 0x6000\n"
    "buffer_load_dwordx4 v7, s[20:23], s64 offen nt lds\n"
    "v_mfma_f32_16x16x32_bf16 v[72:75], v[188:191], v[212:215], v[72:75]\n"
    "ds_read_b128 v[204:207], v243 offset:14336\n"
    "buffer_load_dwordx4 v8, s[20:23], s64 offen offset:1024 nt lds\n"
    "v_mfma_f32_16x16x32_bf16 v[72:75], v[180:183], v[208:211], v[72:75]\n"
    "ds_read_b128 v[208:211], v242 offset:16384\n"
    "s_waitcnt lgkmcnt(7)\n"
    "v_mfma_f32_16x16x32_bf16 v[40:43], v[176:179], v[192:195], v[40:43]\n"
    "v_mfma_f32_16x16x32_bf16 v[40:43], v[184:187], v[196:199], v[40:43]\n"
    "v_mfma_f32_16x16x32_bf16 v[128:131], v[104:107], v[168:171], v[128:131]\n"
    "ds_read_b64_tr_b16 v[104:105], v249 offset:1536\n"
    "ds_read_b64_tr_b16 v[106:107], v249 offset:1792\n"
    "v_add_u32 v224, s52, v225\n"
    "s_waitcnt vmcnt(14)\n"
    "ds_write_b128 v224, v[216:219]\n"
    "ds_write_b128 v224, v[220:223] offset:1024\n"
    "v_mfma_f32_16x16x32_bf16 v[76:79], v[188:191], v[196:199], v[76:79]\n"
    "ds_read_b128 v[212:215], v243 offset:16384\n"
    "v_mfma_f32_16x16x32_bf16 v[76:79], v[180:183], v[192:195], v[76:79]\n"
    "s_add_u32 s64, s86, 0x8000\n"
    "buffer_load_dwordx4 v[216:219], v7, s[20:23], s64 offen nt\n"
    "s_waitcnt lgkmcnt(10)\n"
    "v_mfma_f32_16x16x32_bf16 v[132:135], v[108:111], v[168:171], v[132:135]\n"
    "buffer_load_dwordx4 v[220:223], v8, s[20:23], s64 offen offset:1024 nt\n"
    "s_waitcnt lgkmcnt(6)\n"
    "v_mfma_f32_16x16x32_bf16 v[44:47], v[176:179], v[200:203], v[44:47]\n"
    "s_add_u32 s67, s56, 0x8000\n"
    "s_sub_u32 s52, s56, 0xe000\n"
    "v_mfma_f32_16x16x32_bf16 v[44:47], v[184:187], v[204:207], v[44:47]\n"
    "s_cselect_b32 s52, s67, s52\n"
    "v_mfma_f32_16x16x32_bf16 v[80:83], v[188:191], v[204:207], v[80:83]\n"
    "s_add_u32 s67, s57, 0x8000\n"
    "s_sub_u32 s53, s57, 0xe000\n"
    "v_mfma_f32_16x16x32_bf16 v[80:83], v[180:183], v[200:203], v[80:83]\n"
    "s_cselect_b32 s53, s67, s53\n"
    "s_add_u32 s67, s58, 0x8000\n"
    "v_mfma_f32_16x16x32_bf16 v[136:139], v[100:103], v[168:171], v[136:139]\n"
    "s_sub_u32 s54, s58, 0xe000\n"
    "s_cselect_b32 s54, s67, s54\n"
    "s_waitcnt lgkmcnt(0)\n"
    "v_mfma_f32_16x16x32_bf16 v[48:51], v[176:179], v[208:211], v[48:51]\n"
    "s_add_u32 s67, s59, 0x8000\n"
    "s_sub_u32 s55, s59, 0xe000\n"
    "v_mfma_f32_16x16x32_bf16 v[48:51], v[184:187], v[212:215], v[48:51]\n"
    "s_cselect_b32 s55, s67, s55\n"
    "v_mfma_f32_16x16x32_bf16 v[140:143], v[104:107], v[168:171], v[140:143]\n"
    "s_add_u32 s76, s76, 1\n"
    "s_cmp_eq_u32 s76, 8\n"
    "v_mfma_f32_16x16x32_bf16 v[84:87], v[188:191], v[212:215], v[84:87]\n"
    "v_mfma_f32_16x16x32_bf16 v[84:87], v[180:183], v[208:211], v[84:87]\n"
    "s_cbranch_scc1 .Lkdump_90\n"
    ".Lkdret_91:\n"
    "s_add_u32 s15, s15, 1\n"
    "s_add_u32 s86, s86, 0x4000\n"
    "s_xor_b32 s74, s74, 0x8800\n"
    "s_xor_b32 s75, s75, 0x8800\n"
    "s_waitcnt vmcnt(8) lgkmcnt(0)\n"
    "s_cmp_lt_u32 s15, 10\n"
    "s_setprio 0\n"
    "s_barrier\n"
    "s_cbranch_scc1 .Lkq0_48\n"
    "s_mov_b32 s96, 0x7ff00000\n"
    "s_mov_b32 s97, 0x7ff00000\n"
    "s_cmp_eq_u32 s15, 10\n"
    "s_cbranch_scc1 .Lkq0_48\n"
    "s_branch .Lks11_0_61\n"
    ".Lks11_0_61:\n"
    "s_cmp_eq_u32 s88, 1\n"
    "s_cselect_b32 s85, s53, s52\n"
    "v_add_u32 v248, s85, v236\n"
    "v_add_u32 v249, s85, v237\n"
    "ds_read_b128 v[176:179], v248\n"
    "ds_read_b128 v[180:183], v249\n"
    "ds_read_b128 v[184:187], v248 offset:512\n"
    "ds_read_b128 v[188:191], v249 offset:512\n"
    "s_setprio 0\n"
    "s_add_u32 m0, s73, s57\n"
    "s_add_u32 s64, s86, 0xa000\n"
    "buffer_load_dwordx4 v2, s[16:19], s64 offen nt lds\n"
    "s_waitcnt lgkmcnt(3)\n"
    "v_mfma_f32_16x16x32_bf16 v[160:163], v[176:179], v[144:147], v[172:175]\n"
    "ds_read_b128 v[176:179], v248 offset:1024\n"
    "s_waitcnt lgkmcnt(3)\n"
    "v_mfma_f32_16x16x32_bf16 v[164:167], v[180:183], v[144:147], v[172:175]\n"
    "ds_read_b128 v[180:183], v249 offset:1024\n"
    "buffer_load_dwordx4 v3, s[16:19], s64 offen offset:1024 nt lds\n"
    "v_mul_f32 v16, s94, v16\n"
    "v_mul_f32 v17, s94, v17\n"
    "v_mul_f32 v18, s94, v18\n"
    "v_mul_f32 v19, s94, v19\n"
    "v_cvt_pk_bf16_f32 v16, v16, v17\n"
    "v_cvt_pk_bf16_f32 v17, v18, v19\n"
    "ds_write_b64 v6, v[16:17]\n"
    "s_waitcnt lgkmcnt(4)\n"
    "v_mfma_f32_16x16x32_bf16 v[160:163], v[184:187], v[148:151], v[160:163]\n"
    "ds_read_b128 v[184:187], v248 offset:1536\n"
    "s_add_u32 m0, s73, s58\n"
    "s_add_u32 s64, s86, s87\n"
    "buffer_load_dwordx4 v9, s[48:51], s64 offen nt lds\n"
    "s_waitcnt lgkmcnt(4)\n"
    "v_mfma_f32_16x16x32_bf16 v[164:167], v[188:191], v[148:151], v[164:167]\n"
    "ds_read_b128 v[188:191], v249 offset:1536\n"
    "v_mul_f32 v52, s94, v52\n"
    "v_mul_f32 v53, s94, v53\n"
    "v_mul_f32 v54, s94, v54\n"
    "v_mul_f32 v55, s94, v55\n"
    "v_cvt_pk_bf16_f32 v52, v52, v53\n"
    "v_cvt_pk_bf16_f32 v53, v54, v55\n"
    "ds_write_b64 v97, v[52:53]\n"
    "s_waitcnt lgkmcnt(5)\n"
    "v_mfma_f32_16x16x32_bf16 v[160:163], v[176:179], v[152:155], v[160:163]\n"
    "buffer_load_dwordx4 v10, s[48:51], s64 offen offset:1024 nt lds\n"
    "s_waitcnt lgkmcnt(4)\n"
    "v_mfma_f32_16x16x32_bf16 v[164:167], v[180:183], v[152:155], v[164:167]\n"
    "v_mul_f32 v20, s94, v20\n"
    "v_mul_f32 v21, s94, v21\n"
    "v_mul_f32 v22, s94, v22\n"
    "v_mul_f32 v23, s94, v23\n"
    "v_cvt_pk_bf16_f32 v20, v20, v21\n"
    "v_cvt_pk_bf16_f32 v21, v22, v23\n"
    "ds_write_b64 v6, v[20:21] offset:4096\n"
    "s_waitcnt lgkmcnt(3)\n"
    "v_mfma_f32_16x16x32_bf16 v[160:163], v[184:187], v[156:159], v[160:163]\n"
    "s_add_u32 m0, s73, s59\n"
    "s_add_u32 s64, s86, 0x6000\n"
    "buffer_load_dwordx4 v7, s[20:23], s64 offen nt lds\n"
    "s_waitcnt lgkmcnt(2)\n"
    "v_mfma_f32_16x16x32_bf16 v[164:167], v[188:191], v[156:159], v[164:167]\n"
    "buffer_load_dwordx4 v8, s[20:23], s64 offen offset:1024 nt lds\n"
    "v_mul_f32 v56, s94, v56\n"
    "v_mul_f32 v57, s94, v57\n"
    "v_mul_f32 v58, s94, v58\n"
    "v_mul_f32 v59, s94, v59\n"
    "v_cvt_pk_bf16_f32 v56, v56, v57\n"
    "v_cvt_pk_bf16_f32 v57, v58, v59\n"
    "ds_write_b64 v97, v[56:57] offset:4096\n"
    "v_max3_f32 v15, v160, v161, v162\n"
    "v_max3_f32 v244, v164, v165, v166\n"
    "v_add_u32 v224, s56, v225\n"
    "s_waitcnt vmcnt(6)\n"
    "ds_write_b128 v224, v[216:219]\n"
    "ds_write_b128 v224, v[220:223] offset:1024\n"
    "v_max3_f32 v15, v15, v163, v167\n"
    "v_max_f32 v15, v15, v244\n"
    "v_cmp_nge_f32 vcc, s70, v15\n"
    "s_nop 0\n"
    "s_cbranch_vccnz .Lkxrare_92\n"
    ".Lkxret_93:\n"
    "v_mul_f32 v24, s94, v24\n"
    "v_mul_f32 v25, s94, v25\n"
    "v_mul_f32 v26, s94, v26\n"
    "v_mul_f32 v27, s94, v27\n"
    "v_cvt_pk_bf16_f32 v24, v24, v25\n"
    "v_cvt_pk_bf16_f32 v25, v26, v27\n"
    "ds_write_b64 v6, v[24:25] offset:8192\n"
    "v_exp_f32 v160, v160\n"
    "v_exp_f32 v161, v161\n"
    "s_add_u32 s64, s86, 0x8000\n"
    "buffer_load_dwordx4 v[216:219], v7, s[20:23], s64 offen nt\n"
    "v_exp_f32 v162, v162\n"
    "v_exp_f32 v163, v163\n"
    "v_add_f32 v14, v14, v160\n"
    "v_cvt_pk_bf16_f32 v168, v160, v161\n"
    "v_add_f32 v14, v14, v161\n"
    "buffer_load_dwordx4 v[220:223], v8, s[20:23], s64 offen offset:1024 nt\n"
    "v_mul_f32 v60, s94, v60\n"
    "v_mul_f32 v61, s94, v61\n"
    "v_mul_f32 v62, s94, v62\n"
    "v_mul_f32 v63, s94, v63\n"
    "v_cvt_pk_bf16_f32 v60, v60, v61\n"
    "v_cvt_pk_bf16_f32 v61, v62, v63\n"
    "ds_write_b64 v97, v[60:61] offset:8192\n"
    "s_add_u32 s67, s52, 0x8000\n"
    "s_sub_u32 s56, s52, 0xe000\n"
    "s_cselect_b32 s56, s67, s56\n"
    "s_add_u32 s67, s53, 0x8000\n"
    "s_sub_u32 s57, s53, 0xe000\n"
    "s_cselect_b32 s57, s67, s57\n"
    "s_add_u32 s67, s54, 0x8000\n"
    "s_sub_u32 s58, s54, 0xe000\n"
    "s_cselect_b32 s58, s67, s58\n"
    "s_add_u32 s67, s55, 0x8000\n"
    "s_sub_u32 s59, s55, 0xe000\n"
    "s_cselect_b32 s59, s67, s59\n"
    "s_add_u32 s15, s15, 1\n"
    "s_add_u32 s86, s86, 0x4000\n"
    "s_waitcnt vmcnt(8) lgkmcnt(0)\n"
    "s_setprio 0\n"
    "s_barrier\n"
    "s_branch .Lks12_1_64\n"
    ".Lks11_1_62:\n"
    "s_cmp_eq_u32 s88, 1\n"
    "s_cselect_b32 s85, s59, s58\n"
    "v_add_u32 v248, s85, v0\n"
    "v_xor_b32 v249, 32, v248\n"
    "ds_read_b64_tr_b16 v[176:177], v248\n"
    "ds_read_b64_tr_b16 v[178:179], v248 offset:256\n"
    "ds_read_b64_tr_b16 v[180:181], v249\n"
    "ds_read_b64_tr_b16 v[182:183], v249 offset:256\n"
    "ds_read_b64_tr_b16 v[184:185], v248 offset:512\n"
    "ds_read_b64_tr_b16 v[186:187], v248 offset:768\n"
    "ds_read_b64_tr_b16 v[188:189], v249 offset:512\n"
    "ds_read_b64_tr_b16 v[190:191], v249 offset:768\n"
    "s_setprio 0\n"
    "s_add_u32 m0, s73, s53\n"
    "s_add_u32 s64, s86, 0xa000\n"
    "buffer_load_dwordx4 v2, s[16:19], s64 offen nt lds\n"
    "v_exp_f32 v164, v164\n"
    "v_exp_f32 v165, v165\n"
    "v_add_f32 v14, v14, v162\n"
    "v_cvt_pk_bf16_f32 v169, v162, v163\n"
    "v_add_f32 v14, v14, v163\n"
    "v_exp_f32 v166, v166\n"
    "v_exp_f32 v167, v167\n"
    "v_add_f32 v14, v14, v164\n"
    "v_cvt_pk_bf16_f32 v170, v164, v165\n"
    "v_add_f32 v14, v14, v165\n"
    "buffer_load_dwordx4 v3, s[16:19], s64 offen offset:1024 nt lds\n"
    "v_mul_f32 v16, s94, v16\n"
    "v_mul_f32 v17, s94, v17\n"
    "v_mul_f32 v18, s94, v18\n"
    "v_mul_f32 v19, s94, v19\n"
    "v_cvt_pk_bf16_f32 v16, v16, v17\n"
    "v_cvt_pk_bf16_f32 v17, v18, v19\n"
    "ds_write_b64 v6, v[16:17]\n"
    "v_add_f32 v14, v14, v166\n"
    "v_cvt_pk_bf16_f32 v171, v166, v167\n"
    "v_add_f32 v14, v14, v167\n"
    "s_add_u32 m0, s73, s54\n"
    "s_add_u32 s64, s86, s87\n"
    "buffer_load_dwordx4 v9, s[48:51], s64 offen nt lds\n"
    "s_waitcnt lgkmcnt(7)\n"
    "v_mfma_f32_16x16x32_bf16 v[112:115], v[176:179], v[168:171], v[112:115]\n"
    "ds_read_b64_tr_b16 v[176:177], v248 offset:1024\n"
    "ds_read_b64_tr_b16 v[178:179], v248 offset:1280\n"
    "v_mul_f32 v52, s94, v52\n"
    "v_mul_f32 v53, s94, v53\n"
    "v_mul_f32 v54, s94, v54\n"
    "v_mul_f32 v55, s94, v55\n"
    "v_cvt_pk_bf16_f32 v52, v52, v53\n"
    "v_cvt_pk_bf16_f32 v53, v54, v55\n"
    "ds_write_b64 v97, v[52:53]\n"
    "s_waitcnt lgkmcnt(8)\n"
    "v_mfma_f32_16x16x32_bf16 v[116:119], v[180:183], v[168:171], v[116:119]\n"
    "ds_read_b64_tr_b16 v[180:181], v249 offset:1024\n"
    "ds_read_b64_tr_b16 v[182:183], v249 offset:1280\n"
    "buffer_load_dwordx4 v10, s[48:51], s64 offen offset:1024 nt lds\n"
    "s_waitcnt lgkmcnt(8)\n"
    "v_mfma_f32_16x16x32_bf16 v[120:123], v[184:187], v[168:171], v[120:123]\n"
    "ds_read_b64_tr_b16 v[184:185], v248 offset:1536\n"
    "ds_read_b64_tr_b16 v[186:187], v248 offset:1792\n"
    "s_add_u32 m0, s73, s55\n"
    "s_add_u32 s64, s86, 0x6000\n"
    "buffer_load_dwordx4 v7, s[20:23], s64 offen nt lds\n"
    "v_mul_f32 v20, s94, v20\n"
    "v_mul_f32 v21, s94, v21\n"
    "v_mul_f32 v22, s94, v22\n"
    "v_mul_f32 v23, s94, v23\n"
    "v_cvt_pk_bf16_f32 v20, v20, v21\n"
    "v_cvt_pk_bf16_f32 v21, v22, v23\n"
    "ds_write_b64 v6, v[20:21] offset:4096\n"
    "s_waitcnt lgkmcnt(9)\n"
    "v_mfma_f32_16x16x32_bf16 v[124:127], v[188:191], v[168:171], v[124:127]\n"
    "ds_read_b64_tr_b16 v[188:189], v249 offset:1536\n"
    "ds_read_b64_tr_b16 v[190:191], v249 offset:1792\n"
    "s_waitcnt lgkmcnt(8)\n"
    "v_mfma_f32_16x16x32_bf16 v[128:131], v[176:179], v[168:171], v[128:131]\n"
    "buffer_load_dwordx4 v8, s[20:23], s64 offen offset:1024 nt lds\n"
    "v_mul_f32 v56, s94, v56\n"
    "v_mul_f32 v57, s94, v57\n"
    "v_mul_f32 v58, s94, v58\n"
    "v_mul_f32 v59, s94, v59\n"
    "v_cvt_pk_bf16_f32 v56, v56, v57\n"
    "v_cvt_pk_bf16_f32 v57, v58, v59\n"
    "ds_write_b64 v97, v[56:57] offset:4096\n"
    "s_waitcnt lgkmcnt(6)\n"
    "v_mfma_f32_16x16x32_bf16 v[132:135], v[180:183], v[168:171], v[132:135]\n"
    "v_add_u32 v224, s52, v225\n"
    "s_waitcnt vmcnt(6)\n"
    "ds_write_b128 v224, v[216:219]\n"
    "ds_write_b128 v224, v[220:223] offset:1024\n"
    "s_waitcnt lgkmcnt(6)\n"
    "v_mfma_f32_16x16x32_bf16 v[136:139], v[184:187], v[168:171], v[136:139]\n"
    "v_mul_f32 v24, s94, v24\n"
    "v_mul_f32 v25, s94, v25\n"
    "v_mul_f32 v26, s94, v26\n"
    "v_mul_f32 v27, s94, v27\n"
    "v_cvt_pk_bf16_f32 v24, v24, v25\n"
    "v_cvt_pk_bf16_f32 v25, v26, v27\n"
    "ds_write_b64 v6, v[24:25] offset:8192\n"
    "s_waitcnt lgkmcnt(4)\n"
    "v_mfma_f32_16x16x32_bf16 v[140:143], v[188:191], v[168:171], v[140:143]\n"
    "s_add_u32 s64, s86, 0x8000\n"
    "buffer_load_dwordx4 v[216:219], v7, s[20:23], s64 offen nt\n"
    "s_add_u32 s76, s76, 1\n"
    "s_cmp_eq_u32 s76, 8\n"
    "s_cbranch_scc1 .Lkdump_94\n"
    ".Lkdret_95:\n"
    "buffer_load_dwordx4 v[220:223], v8, s[20:23], s64 offen offset:1024 nt\n"
    "v_mul_f32 v60, s94, v60\n"
    "v_mul_f32 v61, s94, v61\n"
    "v_mul_f32 v62, s94, v62\n"
    "v_mul_f32 v63, s94, v63\n"
    "v_cvt_pk_bf16_f32 v60, v60, v61\n"
    "v_cvt_pk_bf16_f32 v61, v62, v63\n"
    "ds_write_b64 v97, v[60:61] offset:8192\n"
    "s_add_u32 s67, s56, 0x8000\n"
    "s_sub_u32 s52, s56, 0xe000\n"
    "s_cselect_b32 s52, s67, s52\n"
    "s_add_u32 s67, s57, 0x8000\n"
    "s_sub_u32 s53, s57, 0xe000\n"
    "s_cselect_b32 s53, s67, s53\n"
    "s_add_u32 s67, s58, 0x8000\n"
    "s_sub_u32 s54, s58, 0xe000\n"
    "s_cselect_b32 s54, s67, s54\n"
    "s_add_u32 s67, s59, 0x8000\n"
    "s_sub_u32 s55, s59, 0xe000\n"
    "s_cselect_b32 s55, s67, s55\n"
    "s_add_u32 s15, s15, 1\n"
    "s_add_u32 s86, s86, 0x4000\n"
    "s_waitcnt vmcnt(8) lgkmcnt(0)\n"
    "s_setprio 0\n"
    "s_barrier\n"
    "s_branch .Lks12_0_63\n"
    ".Lks12_0_63:\n"
    "s_cmp_eq_u32 s88, 1\n"
    "s_cselect_b32 s85, s53, s52\n"
    "v_add_u32 v248, s85, v236\n"
    "v_add_u32 v249, s85, v237\n"
    "ds_read_b128 v[176:179], v248\n"
    "ds_read_b128 v[180:183], v249\n"
    "ds_read_b128 v[184:187], v248 offset:512\n"
    "ds_read_b128 v[188:191], v249 offset:512\n"
    "s_setprio 0\n"
    "s_add_u32 m0, s73, s57\n"
    "s_add_u32 s64, s86, 0xa000\n"
    "buffer_load_dwordx4 v2, s[16:19], s64 offen nt lds\n"
    "s_waitcnt lgkmcnt(3)\n"
    "v_mfma_f32_16x16x32_bf16 v[160:163], v[176:179], v[144:147], v[172:175]\n"
    "ds_read_b128 v[176:179], v248 offset:1024\n"
    "s_waitcnt lgkmcnt(3)\n"
    "v_mfma_f32_16x16x32_bf16 v[164:167], v[180:183], v[144:147], v[172:175]\n"
    "ds_read_b128 v[180:183], v249 offset:1024\n"
    "buffer_load_dwordx4 v3, s[16:19], s64 offen offset:1024 nt lds\n"
    "v_mul_f32 v28, s94, v28\n"
    "v_mul_f32 v29, s94, v29\n"
    "v_mul_f32 v30, s94, v30\n"
    "v_mul_f32 v31, s94, v31\n"
    "v_cvt_pk_bf16_f32 v28, v28, v29\n"
    "v_cvt_pk_bf16_f32 v29, v30, v31\n"
    "ds_write_b64 v6, v[28:29] offset:12288\n"
    "s_waitcnt lgkmcnt(4)\n"
    "v_mfma_f32_16x16x32_bf16 v[160:163], v[184:187], v[148:151], v[160:163]\n"
    "ds_read_b128 v[184:187], v248 offset:1536\n"
    "s_add_u32 m0, s73, s58\n"
    "s_add_u32 s64, s86, s87\n"
    "buffer_load_dwordx4 v9, s[48:51], s64 offen nt lds\n"
    "s_waitcnt lgkmcnt(4)\n"
    "v_mfma_f32_16x16x32_bf16 v[164:167], v[188:191], v[148:151], v[164:167]\n"
    "ds_read_b128 v[188:191], v249 offset:1536\n"
    "v_mul_f32 v64, s94, v64\n"
    "v_mul_f32 v65, s94, v65\n"
    "v_mul_f32 v66, s94, v66\n"
    "v_mul_f32 v67, s94, v67\n"
    "v_cvt_pk_bf16_f32 v64, v64, v65\n"
    "v_cvt_pk_bf16_f32 v65, v66, v67\n"
    "ds_write_b64 v97, v[64:65] offset:12288\n"
    "s_waitcnt lgkmcnt(5)\n"
    "v_mfma_f32_16x16x32_bf16 v[160:163], v[176:179], v[152:155], v[160:163]\n"
    "buffer_load_dwordx4 v10, s[48:51], s64 offen offset:1024 nt lds\n"
    "s_waitcnt lgkmcnt(4)\n"
    "v_mfma_f32_16x16x32_bf16 v[164:167], v[180:183], v[152:155], v[164:167]\n"
    "v_mul_f32 v32, s94, v32\n"
    "v_mul_f32 v33, s94, v33\n"
    "v_mul_f32 v34, s94, v34\n"
    "v_mul_f32 v35, s94, v35\n"
    "v_cvt_pk_bf16_f32 v32, v32, v33\n"
    "v_cvt_pk_bf16_f32 v33, v34, v35\n"
    "ds_write_b64 v6, v[32:33] offset:16384\n"
    "s_waitcnt lgkmcnt(3)\n"
    "v_mfma_f32_16x16x32_bf16 v[160:163], v[184:187], v[156:159], v[160:163]\n"
    "s_add_u32 m0, s73, s59\n"
    "s_add_u32 s64, s86, 0x6000\n"
    "buffer_load_dwordx4 v7, s[20:23], s64 offen nt lds\n"
    "s_waitcnt lgkmcnt(2)\n"
    "v_mfma_f32_16x16x32_bf16 v[164:167], v[188:191], v[156:159], v[164:167]\n"
    "buffer_load_dwordx4 v8, s[20:23], s64 offen offset:1024 nt lds\n"
    "v_mul_f32 v68, s94, v68\n"
    "v_mul_f32 v69, s94, v69\n"
    "v_mul_f32 v70, s94, v70\n"
    "v_mul_f32 v71, s94, v71\n"
    "v_cvt_pk_bf16_f32 v68, v68, v69\n"
    "v_cvt_pk_bf16_f32 v69, v70, v71\n"
    "ds_write_b64 v97, v[68:69] offset:16384\n"
    "v_max3_f32 v15, v160, v161, v162\n"
    "v_max3_f32 v244, v164, v165, v166\n"
    "v_add_u32 v224, s56, v225\n"
    "s_waitcnt vmcnt(6)\n"
    "ds_write_b128 v224, v[216:219]\n"
    "ds_write_b128 v224, v[220:223] offset:1024\n"
    "v_max3_f32 v15, v15, v163, v167\n"
    "v_max_f32 v15, v15, v244\n"
    "v_cmp_nge_f32 vcc, s70, v15\n"
    "s_nop 0\n"
    "s_cbranch_vccnz .Lkxrare_96\n"
    ".Lkxret_97:\n"
    "v_mul_f32 v36, s94, v36\n"
    "v_mul_f32 v37, s94, v37\n"
    "v_mul_f32 v38, s94, v38\n"
    "v_mul_f32 v39, s94, v39\n"
    "v_cvt_pk_bf16_f32 v36, v36, v37\n"
    "v_cvt_pk_bf16_f32 v37, v38, v39\n"
    "ds_write_b64 v6, v[36:37] offset:20480\n"
    "v_exp_f32 v160, v160\n"
    "v_exp_f32 v161, v161\n"
    "s_add_u32 s64, s86, 0x8000\n"
    "buffer_load_dwordx4 v[216:219], v7, s[20:23], s64 offen nt\n"
    "v_exp_f32 v162, v162\n"
    "v_exp_f32 v163, v163\n"
    "v_add_f32 v14, v14, v160\n"
    "v_cvt_pk_bf16_f32 v168, v160, v161\n"
    "v_add_f32 v14, v14, v161\n"
    "buffer_load_dwordx4 v[220:223], v8, s[20:23], s64 offen offset:1024 nt\n"
    "v_mul_f32 v72, s94, v72\n"
    "v_mul_f32 v73, s94, v73\n"
    "v_mul_f32 v74, s94, v74\n"
    "v_mul_f32 v75, s94, v75\n"
    "v_cvt_pk_bf16_f32 v72, v72, v73\n"
    "v_cvt_pk_bf16_f32 v73, v74, v75\n"
    "ds_write_b64 v97, v[72:73] offset:20480\n"
    "s_add_u32 s67, s52, 0x8000\n"
    "s_sub_u32 s56, s52, 0xe000\n"
    "s_cselect_b32 s56, s67, s56\n"
    "s_add_u32 s67, s53, 0x8000\n"
    "s_sub_u32 s57, s53, 0xe000\n"
    "s_cselect_b32 s57, s67, s57\n"
    "s_add_u32 s67, s54, 0x8000\n"
    "s_sub_u32 s58, s54, 0xe000\n"
    "s_cselect_b32 s58, s67, s58\n"
    "s_add_u32 s67, s55, 0x8000\n"
    "s_sub_u32 s59, s55, 0xe000\n"
    "s_cselect_b32 s59, s67, s59\n"
    "s_add_u32 s15, s15, 1\n"
    "s_add_u32 s86, s86, 0x4000\n"
    "s_waitcnt vmcnt(8) lgkmcnt(0)\n"
    "s_setprio 0\n"
    "s_barrier\n"
    "s_branch .Lks13_1_66\n"
    ".Lks12_1_64:\n"
    "s_cmp_eq_u32 s88, 1\n"
    "s_cselect_b32 s85, s59, s58\n"
    "v_add_u32 v248, s85, v0\n"
    "v_xor_b32 v249, 32, v248\n"
    "ds_read_b64_tr_b16 v[176:177], v248\n"
    "ds_read_b64_tr_b16 v[178:179], v248 offset:256\n"
    "ds_read_b64_tr_b16 v[180:181], v249\n"
    "ds_read_b64_tr_b16 v[182:183], v249 offset:256\n"
    "ds_read_b64_tr_b16 v[184:185], v248 offset:512\n"
    "ds_read_b64_tr_b16 v[186:187], v248 offset:768\n"
    "ds_read_b64_tr_b16 v[188:189], v249 offset:512\n"
    "ds_read_b64_tr_b16 v[190:191], v249 offset:768\n"
    "s_setprio 0\n"
    "s_add_u32 m0, s73, s53\n"
    "s_add_u32 s64, s86, 0xa000\n"
    "buffer_load_dwordx4 v2, s[16:19], s64 offen nt lds\n"
    "v_exp_f32 v164, v164\n"
    "v_exp_f32 v165, v165\n"
    "v_add_f32 v14, v14, v162\n"
    "v_cvt_pk_bf16_f32 v169, v162, v163\n"
    "v_add_f32 v14, v14, v163\n"
    "v_exp_f32 v166, v166\n"
    "v_exp_f32 v167, v167\n"
    "v_add_f32 v14, v14, v164\n"
    "v_cvt_pk_bf16_f32 v170, v164, v165\n"
    "v_add_f32 v14, v14, v165\n"
    "buffer_load_dwordx4 v3, s[16:19], s64 offen offset:1024 nt lds\n"
    "v_mul_f32 v28, s94, v28\n"
    "v_mul_f32 v29, s94, v29\n"
    "v_mul_f32 v30, s94, v30\n"
    "v_mul_f32 v31, s94, v31\n"
    "v_cvt_pk_bf16_f32 v28, v28, v29\n"
    "v_cvt_pk_bf16_f32 v29, v30, v31\n"
    "ds_write_b64 v6, v[28:29] offset:12288\n"
    "v_add_f32 v14, v14, v166\n"
    "v_cvt_pk_bf16_f32 v171, v166, v167\n"
    "v_add_f32 v14, v14, v167\n"
    "s_add_u32 m0, s73, s54\n"
    "s_add_u32 s64, s86, s87\n"
    "buffer_load_dwordx4 v9, s[48:51], s64 offen nt lds\n"
    "s_waitcnt lgkmcnt(7)\n"
    "v_mfma_f32_16x16x32_bf16 v[112:115], v[176:179], v[168:171], v[112:115]\n"
    "ds_read_b64_tr_b16 v[176:177], v248 offset:1024\n"
    "ds_read_b64_tr_b16 v[178:179], v248 offset:1280\n"
    "v_mul_f32 v64, s94, v64\n"
    "v_mul_f32 v65, s94, v65\n"
    "v_mul_f32 v66, s94, v66\n"
    "v_mul_f32 v67, s94, v67\n"
    "v_cvt_pk_bf16_f32 v64, v64, v65\n"
    "v_cvt_pk_bf16_f32 v65, v66, v67\n"
    "ds_write_b64 v97, v[64:65] offset:12288\n"
    "s_waitcnt lgkmcnt(8)\n"
    "v_mfma_f32_16x16x32_bf16 v[116:119], v[180:183], v[168:171], v[116:119]\n"
    "ds_read_b64_tr_b16 v[180:181], v249 offset:1024\n"
    "ds_read_b64_tr_b16 v[182:183], v249 offset:1280\n"
    "buffer_load_dwordx4 v10, s[48:51], s64 offen offset:1024 nt lds\n"
    "s_waitcnt lgkmcnt(8)\n"
    "v_mfma_f32_16x16x32_bf16 v[120:123], v[184:187], v[168:171], v[120:123]\n"
    "ds_read_b64_tr_b16 v[184:185], v248 offset:1536\n"
    "ds_read_b64_tr_b16 v[186:187], v248 offset:1792\n"
    "s_add_u32 m0, s73, s55\n"
    "s_add_u32 s64, s86, 0x6000\n"
    "buffer_load_dwordx4 v7, s[20:23], s64 offen nt lds\n"
    "v_mul_f32 v32, s94, v32\n"
    "v_mul_f32 v33, s94, v33\n"
    "v_mul_f32 v34, s94, v34\n"
    "v_mul_f32 v35, s94, v35\n"
    "v_cvt_pk_bf16_f32 v32, v32, v33\n"
    "v_cvt_pk_bf16_f32 v33, v34, v35\n"
    "ds_write_b64 v6, v[32:33] offset:16384\n"
    "s_waitcnt lgkmcnt(9)\n"
    "v_mfma_f32_16x16x32_bf16 v[124:127], v[188:191], v[168:171], v[124:127]\n"
    "ds_read_b64_tr_b16 v[188:189], v249 offset:1536\n"
    "ds_read_b64_tr_b16 v[190:191], v249 offset:1792\n"
    "s_waitcnt lgkmcnt(8)\n"
    "v_mfma_f32_16x16x32_bf16 v[128:131], v[176:179], v[168:171], v[128:131]\n"
    "buffer_load_dwordx4 v8, s[20:23], s64 offen offset:1024 nt lds\n"
    "v_mul_f32 v68, s94, v68\n"
    "v_mul_f32 v69, s94, v69\n"
    "v_mul_f32 v70, s94, v70\n"
    "v_mul_f32 v71, s94, v71\n"
    "v_cvt_pk_bf16_f32 v68, v68, v69\n"
    "v_cvt_pk_bf16_f32 v69, v70, v71\n"
    "ds_write_b64 v97, v[68:69] offset:16384\n"
    "s_waitcnt lgkmcnt(6)\n"
    "v_mfma_f32_16x16x32_bf16 v[132:135], v[180:183], v[168:171], v[132:135]\n"
    "v_add_u32 v224, s52, v225\n"
    "s_waitcnt vmcnt(6)\n"
    "ds_write_b128 v224, v[216:219]\n"
    "ds_write_b128 v224, v[220:223] offset:1024\n"
    "s_waitcnt lgkmcnt(6)\n"
    "v_mfma_f32_16x16x32_bf16 v[136:139], v[184:187], v[168:171], v[136:139]\n"
    "v_mul_f32 v36, s94, v36\n"
    "v_mul_f32 v37, s94, v37\n"
    "v_mul_f32 v38, s94, v38\n"
    "v_mul_f32 v39, s94, v39\n"
    "v_cvt_pk_bf16_f32 v36, v36, v37\n"
    "v_cvt_pk_bf16_f32 v37, v38, v39\n"
    "ds_write_b64 v6, v[36:37] offset:20480\n"
    "s_waitcnt lgkmcnt(4)\n"
    "v_mfma_f32_16x16x32_bf16 v[140:143], v[188:191], v[168:171], v[140:143]\n"
    "s_add_u32 s64, s86, 0x8000\n"
    "buffer_load_dwordx4 v[216:219], v7, s[20:23], s64 offen nt\n"
    "s_add_u32 s76, s76, 1\n"
    "s_cmp_eq_u32 s76, 8\n"
    "s_cbranch_scc1 .Lkdump_98\n"
    ".Lkdret_99:\n"
    "buffer_load_dwordx4 v[220:223], v8, s[20:23], s64 offen offset:1024 nt\n"
    "v_mul_f32 v72, s94, v72\n"
    "v_mul_f32 v73, s94, v73\n"
    "v_mul_f32 v74, s94, v74\n"
    "v_mul_f32 v75, s94, v75\n"
    "v_cvt_pk_bf16_f32 v72, v72, v73\n"
    "v_cvt_pk_bf16_f32 v73, v74, v75\n"
    "ds_write_b64 v97, v[72:73] offset:20480\n"
    "s_add_u32 s67, s56, 0x8000\n"
    "s_sub_u32 s52, s56, 0xe000\n"
    "s_cselect_b32 s52, s67, s52\n"
    "s_add_u32 s67, s57, 0x8000\n"
    "s_sub_u32 s53, s57, 0xe000\n"
    "s_cselect_b32 s53, s67, s53\n"
    "s_add_u32 s67, s58, 0x8000\n"
    "s_sub_u32 s54, s58, 0xe000\n"
    "s_cselect_b32 s54, s67, s54\n"
    "s_add_u32 s67, s59, 0x8000\n"
    "s_sub_u32 s55, s59, 0xe000\n"
    "s_cselect_b32 s55, s67, s55\n"
    "s_add_u32 s15, s15, 1\n"
    "s_add_u32 s86, s86, 0x4000\n"
    "s_waitcnt vmcnt(8) lgkmcnt(0)\n"
    "s_setprio 0\n"
    "s_barrier\n"
    "s_branch .Lks13_0_65\n"
    ".Lks13_0_65:\n"
    "s_cmp_eq_u32 s88, 1\n"
    "s_cselect_b32 s85, s53, s52\n"
    "v_add_u32 v248, s85, v236\n"
    "v_add_u32 v249, s85, v237\n"
    "ds_read_b128 v[176:179], v248\n"
    "ds_read_b128 v[180:183], v249\n"
    "ds_read_b128 v[184:187], v248 offset:512\n"
    "ds_read_b128 v[188:191], v249 offset:512\n"
    "s_setprio 0\n"
    "s_add_u32 m0, s73, s57\n"
    "s_add_u32 s64, s86, 0xa000\n"
    "buffer_load_dwordx4 v2, s[16:19], s64 offen nt lds\n"
    "s_waitcnt lgkmcnt(3)\n"
    "v_mfma_f32_16x16x32_bf16 v[160:163], v[176:179], v[144:147], v[172:175]\n"
    "ds_read_b128 v[176:179], v248 offset:1024\n"
    "s_waitcnt lgkmcnt(3)\n"
    "v_mfma_f32_16x16x32_bf16 v[164:167], v[180:183], v[144:147], v[172:175]\n"
    "ds_read_b128 v[180:183], v249 offset:1024\n"
    "buffer_load_dwordx4 v3, s[16:19], s64 offen offset:1024 nt lds\n"
    "v_mul_f32 v40, s94, v40\n"
    "v_mul_f32 v41, s94, v41\n"
    "v_mul_f32 v42, s94, v42\n"
    "v_mul_f32 v43, s94, v43\n"
    "v_cvt_pk_bf16_f32 v40, v40, v41\n"
    "v_cvt_pk_bf16_f32 v41, v42, v43\n"
    "ds_write_b64 v6, v[40:41] offset:24576\n"
    "s_waitcnt lgkmcnt(4)\n"
    "v_mfma_f32_16x16x32_bf16 v[160:163], v[184:187], v[148:151], v[160:163]\n"
    "ds_read_b128 v[184:187], v248 offset:1536\n"
    "s_add_u32 m0, s73, s58\n"
    "s_add_u32 s64, s86, s87\n"
    "buffer_load_dwordx4 v9, s[48:51], s64 offen nt lds\n"
    "s_waitcnt lgkmcnt(4)\n"
    "v_mfma_f32_16x16x32_bf16 v[164:167], v[188:191], v[148:151], v[164:167]\n"
    "ds_read_b128 v[188:191], v249 offset:1536\n"
    "v_mul_f32 v76, s94, v76\n"
    "v_mul_f32 v77, s94, v77\n"
    "v_mul_f32 v78, s94, v78\n"
    "v_mul_f32 v79, s94, v79\n"
    "v_cvt_pk_bf16_f32 v76, v76, v77\n"
    "v_cvt_pk_bf16_f32 v77, v78, v79\n"
    "ds_write_b64 v97, v[76:77] offset:24576\n"
    "s_waitcnt lgkmcnt(5)\n"
    "v_mfma_f32_16x16x32_bf16 v[160:163], v[176:179], v[152:155], v[160:163]\n"
    "buffer_load_dwordx4 v10, s[48:51], s64 offen offset:1024 nt lds\n"
    "s_waitcnt lgkmcnt(4)\n"
    "v_mfma_f32_16x16x32_bf16 v[164:167], v[180:183], v[152:155], v[164:167]\n"
    "v_mul_f32 v44, s94, v44\n"
    "v_mul_f32 v45, s94, v45\n"
    "v_mul_f32 v46, s94, v46\n"
    "v_mul_f32 v47, s94, v47\n"
    "v_cvt_pk_bf16_f32 v44, v44, v45\n"
    "v_cvt_pk_bf16_f32 v45, v46, v47\n"
    "ds_write_b64 v6, v[44:45] offset:28672\n"
    "s_waitcnt lgkmcnt(3)\n"
    "v_mfma_f32_16x16x32_bf16 v[160:163], v[184:187], v[156:159], v[160:163]\n"
    "s_add_u32 m0, s73, s59\n"
    "s_add_u32 s64, s86, 0x6000\n"
    "buffer_load_dwordx4 v7, s[20:23], s64 offen nt lds\n"
    "s_waitcnt lgkmcnt(2)\n"
    "v_mfma_f32_16x16x32_bf16 v[164:167], v[188:191], v[156:159], v[164:167]\n"
    "buffer_load_dwordx4 v8, s[20:23], s64 offen offset:1024 nt lds\n"
    "v_mul_f32 v80, s94, v80\n"
    "v_mul_f32 v81, s94, v81\n"
    "v_mul_f32 v82, s94, v82\n"
    "v_mul_f32 v83, s94, v83\n"
    "v_cvt_pk_bf16_f32 v80, v80, v81\n"
    "v_cvt_pk_bf16_f32 v81, v82, v83\n"
    "ds_write_b64 v97, v[80:81] offset:28672\n"
    "v_max3_f32 v15, v160, v161, v162\n"
    "v_max3_f32 v244, v164, v165, v166\n"
    "v_add_u32 v224, s56, v225\n"
    "s_waitcnt vmcnt(6)\n"
    "ds_write_b128 v224, v[216:219]\n"
    "ds_write_b128 v224, v[220:223] offset:1024\n"
    "v_max3_f32 v15, v15, v163, v167\n"
    "v_max_f32 v15, v15, v244\n"
    "v_cmp_nge_f32 vcc, s70, v15\n"
    "s_nop 0\n"
    "s_cbranch_vccnz .Lkxrare_100\n"
    ".Lkxret_101:\n"
    "v_mul_f32 v48, s94, v48\n"
    "v_mul_f32 v49, s94, v49\n"
    "v_mul_f32 v50, s94, v50\n"
    "v_mul_f32 v51, s94, v51\n"
    "v_cvt_pk_bf16_f32 v48, v48, v49\n"
    "v_cvt_pk_bf16_f32 v49, v50, v51\n"
    "ds_write_b64 v6, v[48:49] offset:32768\n"
    "v_exp_f32 v160, v160\n"
    "v_exp_f32 v161, v161\n"
    "s_add_u32 s64, s86, 0x8000\n"
    "buffer_load_dwordx4 v[216:219], v7, s[20:23], s64 offen nt\n"
    "v_exp_f32 v162, v162\n"
    "v_exp_f32 v163, v163\n"
    "v_add_f32 v14, v14, v160\n"
    "v_cvt_pk_bf16_f32 v168, v160, v161\n"
    "v_add_f32 v14, v14, v161\n"
    "buffer_load_dwordx4 v[220:223], v8, s[20:23], s64 offen offset:1024 nt\n"
    "v_mul_f32 v84, s94, v84\n"
    "v_mul_f32 v85, s94, v85\n"
    "v_mul_f32 v86, s94, v86\n"
    "v_mul_f32 v87, s94, v87\n"
    "v_cvt_pk_bf16_f32 v84, v84, v85\n"
    "v_cvt_pk_bf16_f32 v85, v86, v87\n"
    "ds_write_b64 v97, v[84:85] offset:32768\n"
    "s_add_u32 s67, s52, 0x8000\n"
    "s_sub_u32 s56, s52, 0xe000\n"
    "s_cselect_b32 s56, s67, s56\n"
    "s_add_u32 s67, s53, 0x8000\n"
    "s_sub_u32 s57, s53, 0xe000\n"
    "s_cselect_b32 s57, s67, s57\n"
    "s_add_u32 s67, s54, 0x8000\n"
    "s_sub_u32 s58, s54, 0xe000\n"
    "s_cselect_b32 s58, s67, s58\n"
    "s_add_u32 s67, s55, 0x8000\n"
    "s_sub_u32 s59, s55, 0xe000\n"
    "s_cselect_b32 s59, s67, s59\n"
    "s_add_u32 s15, s15, 1\n"
    "s_add_u32 s86, s86, 0x4000\n"
    "s_waitcnt vmcnt(8) lgkmcnt(0)\n"
    "s_setprio 0\n"
    "s_barrier\n"
    "s_branch .Lks14_1_68\n"
    ".Lks13_1_66:\n"
    "s_cmp_eq_u32 s88, 1\n"
    "s_cselect_b32 s85, s59, s58\n"
    "v_add_u32 v248, s85, v0\n"
    "v_xor_b32 v249, 32, v248\n"
    "ds_read_b64_tr_b16 v[176:177], v248\n"
    "ds_read_b64_tr_b16 v[178:179], v248 offset:256\n"
    "ds_read_b64_tr_b16 v[180:181], v249\n"
    "ds_read_b64_tr_b16 v[182:183], v249 offset:256\n"
    "ds_read_b64_tr_b16 v[184:185], v248 offset:512\n"
    "ds_read_b64_tr_b16 v[186:187], v248 offset:768\n"
    "ds_read_b64_tr_b16 v[188:189], v249 offset:512\n"
    "ds_read_b64_tr_b16 v[190:191], v249 offset:768\n"
    "s_setprio 0\n"
    "s_add_u32 m0, s73, s53\n"
    "s_add_u32 s64, s86, 0xa000\n"
    "buffer_load_dwordx4 v2, s[16:19], s64 offen nt lds\n"
    "v_exp_f32 v164, v164\n"
    "v_exp_f32 v165, v165\n"
    "v_add_f32 v14, v14, v162\n"
    "v_cvt_pk_bf16_f32 v169, v162, v163\n"
    "v_add_f32 v14, v14, v163\n"
    "v_exp_f32 v166, v166\n"
    "v_exp_f32 v167, v167\n"
    "v_add_f32 v14, v14, v164\n"
    "v_cvt_pk_bf16_f32 v170, v164, v165\n"
    "v_add_f32 v14, v14, v165\n"
    "buffer_load_dwordx4 v3, s[16:19], s64 offen offset:1024 nt lds\n"
    "v_mul_f32 v40, s94, v40\n"
    "v_mul_f32 v41, s94, v41\n"
    "v_mul_f32 v42, s94, v42\n"
    "v_mul_f32 v43, s94, v43\n"
    "v_cvt_pk_bf16_f32 v40, v40, v41\n"
    "v_cvt_pk_bf16_f32 v41, v42, v43\n"
    "ds_write_b64 v6, v[40:41] offset:24576\n"
    "v_add_f32 v14, v14, v166\n"
    "v_cvt_pk_bf16_f32 v171, v166, v167\n"
    "v_add_f32 v14, v14, v167\n"
    "s_add_u32 m0, s73, s54\n"
    "s_add_u32 s64, s86, s87\n"
    "buffer_load_dwordx4 v9, s[48:51], s64 offen nt lds\n"
    "s_waitcnt lgkmcnt(7)\n"
    "v_mfma_f32_16x16x32_bf16 v[112:115], v[176:179], v[168:171], v[112:115]\n"
    "ds_read_b64_tr_b16 v[176:177], v248 offset:1024\n"
    "ds_read_b64_tr_b16 v[178:179], v248 offset:1280\n"
    "v_mul_f32 v76, s94, v76\n"
    "v_mul_f32 v77, s94, v77\n"
    "v_mul_f32 v78, s94, v78\n"
    "v_mul_f32 v79, s94, v79\n"
    "v_cvt_pk_bf16_f32 v76, v76, v77\n"
    "v_cvt_pk_bf16_f32 v77, v78, v79\n"
    "ds_write_b64 v97, v[76:77] offset:24576\n"
    "s_waitcnt lgkmcnt(8)\n"
    "v_mfma_f32_16x16x32_bf16 v[116:119], v[180:183], v[168:171], v[116:119]\n"
    "ds_read_b64_tr_b16 v[180:181], v249 offset:1024\n"
    "ds_read_b64_tr_b16 v[182:183], v249 offset:1280\n"
    "buffer_load_dwordx4 v10, s[48:51], s64 offen offset:1024 nt lds\n"
    "s_waitcnt lgkmcnt(8)\n"
    "v_mfma_f32_16x16x32_bf16 v[120:123], v[184:187], v[168:171], v[120:123]\n"
    "ds_read_b64_tr_b16 v[184:185], v248 offset:1536\n"
    "ds_read_b64_tr_b16 v[186:187], v248 offset:1792\n"
    "s_add_u32 m0, s73, s55\n"
    "s_add_u32 s64, s86, 0x6000\n"
    "buffer_load_dwordx4 v7, s[20:23], s64 offen nt lds\n"
    "v_mul_f32 v44, s94, v44\n"
    "v_mul_f32 v45, s94, v45\n"
    "v_mul_f32 v46, s94, v46\n"
    "v_mul_f32 v47, s94, v47\n"
    "v_cvt_pk_bf16_f32 v44, v44, v45\n"
    "v_cvt_pk_bf16_f32 v45, v46, v47\n"
    "ds_write_b64 v6, v[44:45] offset:28672\n"
    "s_waitcnt lgkmcnt(9)\n"
    "v_mfma_f32_16x16x32_bf16 v[124:127], v[188:191], v[168:171], v[124:127]\n"
    "ds_read_b64_tr_b16 v[188:189], v249 offset:1536\n"
    "ds_read_b64_tr_b16 v[190:191], v249 offset:1792\n"
    "s_waitcnt lgkmcnt(8)\n"
    "v_mfma_f32_16x16x32_bf16 v[128:131], v[176:179], v[168:171], v[128:131]\n"
    "buffer_load_dwordx4 v8, s[20:23], s64 offen offset:1024 nt lds\n"
    "v_mul_f32 v80, s94, v80\n"
    "v_mul_f32 v81, s94, v81\n"
    "v_mul_f32 v82, s94, v82\n"
    "v_mul_f32 v83, s94, v83\n"
    "v_cvt_pk_bf16_f32 v80, v80, v81\n"
    "v_cvt_pk_bf16_f32 v81, v82, v83\n"
    "ds_write_b64 v97, v[80:81] offset:28672\n"
    "s_waitcnt lgkmcnt(6)\n"
    "v_mfma_f32_16x16x32_bf16 v[132:135], v[180:183], v[168:171], v[132:135]\n"
    "v_add_u32 v224, s52, v225\n"
    "s_waitcnt vmcnt(6)\n"
    "ds_write_b128 v224, v[216:219]\n"
    "ds_write_b128 v224, v[220:223] offset:1024\n"
    "s_waitcnt lgkmcnt(6)\n"
    "v_mfma_f32_16x16x32_bf16 v[136:139], v[184:187], v[168:171], v[136:139]\n"
    "v_mul_f32 v48, s94, v48\n"
    "v_mul_f32 v49, s94, v49\n"
    "v_mul_f32 v50, s94, v50\n"
    "v_mul_f32 v51, s94, v51\n"
    "v_cvt_pk_bf16_f32 v48, v48, v49\n"
    "v_cvt_pk_bf16_f32 v49, v50, v51\n"
    "ds_write_b64 v6, v[48:49] offset:32768\n"
    "s_waitcnt lgkmcnt(4)\n"
    "v_mfma_f32_16x16x32_bf16 v[140:143], v[188:191], v[168:171], v[140:143]\n"
    "s_add_u32 s64, s86, 0x8000\n"
    "buffer_load_dwordx4 v[216:219], v7, s[20:23], s64 offen nt\n"
    "s_add_u32 s76, s76, 1\n"
    "s_cmp_eq_u32 s76, 8\n"
    "s_cbranch_scc1 .Lkdump_102\n"
    ".Lkdret_103:\n"
    "buffer_load_dwordx4 v[220:223], v8, s[20:23], s64 offen offset:1024 nt\n"
    "v_mul_f32 v84, s94, v84\n"
    "v_mul_f32 v85, s94, v85\n"
    "v_mul_f32 v86, s94, v86\n"
    "v_mul_f32 v87, s94, v87\n"
    "v_cvt_pk_bf16_f32 v84, v84, v85\n"
    "v_cvt_pk_bf16_f32 v85, v86, v87\n"
    "ds_write_b64 v97, v[84:85] offset:32768\n"
    "s_add_u32 s67, s56, 0x8000\n"
    "s_sub_u32 s52, s56, 0xe000\n"
    "s_cselect_b32 s52, s67, s52\n"
    "s_add_u32 s67, s57, 0x8000\n"
    "s_sub_u32 s53, s57, 0xe000\n"
    "s_cselect_b32 s53, s67, s53\n"
    "s_add_u32 s67, s58, 0x8000\n"
    "s_sub_u32 s54, s58, 0xe000\n"
    "s_cselect_b32 s54, s67, s54\n"
    "s_add_u32 s67, s59, 0x8000\n"
    "s_sub_u32 s55, s59, 0xe000\n"
    "s_cselect_b32 s55, s67, s55\n"
    "s_add_u32 s15, s15, 1\n"
    "s_add_u32 s86, s86, 0x4000\n"
    "s_waitcnt vmcnt(8) lgkmcnt(0)\n"
    "s_setprio 0\n"
    "s_barrier\n"
    "s_branch .Lks14_0_67\n"
    ".Lks14_0_67:\n"
    "s_cmp_eq_u32 s88, 1\n"
    "s_cselect_b32 s85, s53, s52\n"
    "v_add_u32 v248, s85, v236\n"
    "v_add_u32 v249, s85, v237\n"
    "ds_read_b128 v[176:179], v248\n"
    "ds_read_b128 v[180:183], v249\n"
    "ds_read_b128 v[184:187], v248 offset:512\n"
    "ds_read_b128 v[188:191], v249 offset:512\n"
    "s_setprio 0\n"
    "s_add_u32 m0, s73, s57\n"
    "s_add_u32 s64, s86, 0xa000\n"
    "buffer_load_dwordx4 v2, s[16:19], s64 offen nt lds\n"
    "s_waitcnt lgkmcnt(3)\n"
    "v_mfma_f32_16x16x32_bf16 v[160:163], v[176:179], v[144:147], v[172:175]\n"
    "ds_read_b128 v[176:179], v248 offset:1024\n"
    "s_waitcnt lgkmcnt(3)\n"
    "v_mfma_f32_16x16x32_bf16 v[164:167], v[180:183], v[144:147], v[172:175]\n"
    "ds_read_b128 v[180:183], v249 offset:1024\n"
    "buffer_load_dwordx4 v3, s[16:19], s64 offen offset:1024 nt lds\n"
    "s_waitcnt lgkmcnt(3)\n"
    "v_mfma_f32_16x16x32_bf16 v[160:163], v[184:187], v[148:151], v[160:163]\n"
    "ds_read_b128 v[184:187], v248 offset:1536\n"
    "s_add_u32 m0, s73, s58\n"
    "s_add_u32 s64, s86, s87\n"
    "buffer_load_dwordx4 v9, s[48:51], s64 offen nt lds\n"
    "s_waitcnt lgkmcnt(3)\n"
    "v_mfma_f32_16x16x32_bf16 v[164:167], v[188:191], v[148:151], v[164:167]\n"
    "ds_read_b128 v[188:191], v249 offset:1536\n"
    "s_waitcnt lgkmcnt(3)\n"
    "v_mfma_f32_16x16x32_bf16 v[160:163], v[176:179], v[152:155], v[160:163]\n"
    "buffer_load_dwordx4 v10, s[48:51], s64 offen offset:1024 nt lds\n"
    "s_waitcnt lgkmcnt(2)\n"
    "v_mfma_f32_16x16x32_bf16 v[164:167], v[180:183], v[152:155], v[164:167]\n"
    "s_waitcnt lgkmcnt(1)\n"
    "v_mfma_f32_16x16x32_bf16 v[160:163], v[184:187], v[156:159], v[160:163]\n"
    "s_add_u32 m0, s73, s59\n"
    "s_add_u32 s64, s86, 0x6000\n"
    "buffer_load_dwordx4 v7, s[20:23], s64 offen nt lds\n"
    "s_waitcnt lgkmcnt(0)\n"
    "v_mfma_f32_16x16x32_bf16 v[164:167], v[188:191], v[156:159], v[164:167]\n"
    "buffer_load_dwordx4 v8, s[20:23], s64 offen offset:1024 nt lds\n"
    "s_nop 1\n"
    "v_max3_f32 v15, v160, v161, v162\n"
    "s_nop 3\n"
    "v_max3_f32 v244, v164, v165, v166\n"
    "v_add_u32 v224, s56, v225\n"
    "s_waitcnt vmcnt(6)\n"
    "ds_write_b128 v224, v[216:219]\n"
    "ds_write_b128 v224, v[220:223] offset:1024\n"
    "v_max3_f32 v15, v15, v163, v167\n"
    "v_max_f32 v15, v15, v244\n"
    "v_cmp_nge_f32 vcc, s70, v15\n"
    "s_nop 0\n"
    "s_cbranch_vccnz .Lkxrare_104\n"
    ".Lkxret_105:\n"
    "v_exp_f32 v160, v160\n"
    "v_exp_f32 v161, v161\n"
    "s_add_u32 s64, s86, 0x8000\n"
    "buffer_load_dwordx4 v[216:219], v7, s[20:23], s64 offen nt\n"
    "v_exp_f32 v162, v162\n"
    "v_exp_f32 v163, v163\n"
    "v_add_f32 v14, v14, v160\n"
    "v_cvt_pk_bf16_f32 v168, v160, v161\n"
    "v_add_f32 v14, v14, v161\n"
    "buffer_load_dwordx4 v[220:223], v8, s[20:23], s64 offen offset:1024 nt\n"
    "s_add_u32 s67, s52, 0x8000\n"
    "s_sub_u32 s56, s52, 0xe000\n"
    "s_cselect_b32 s56, s67, s56\n"
    "s_add_u32 s67, s53, 0x8000\n"
    "s_sub_u32 s57, s53, 0xe000\n"
    "s_cselect_b32 s57, s67, s57\n"
    "s_add_u32 s67, s54, 0x8000\n"
    "s_sub_u32 s58, s54, 0xe000\n"
    "s_cselect_b32 s58, s67, s58\n"
    "s_add_u32 s67, s55, 0x8000\n"
    "s_sub_u32 s59, s55, 0xe000\n"
    "s_cselect_b32 s59, s67, s59\n"
    "s_add_u32 s15, s15, 1\n"
    "s_add_u32 s86, s86, 0x4000\n"
    "s_waitcnt vmcnt(8) lgkmcnt(0)\n"
    "s_setprio 0\n"
    "s_barrier\n"
    "s_branch .Lks15_1_70\n"
    ".Lks14_1_68:\n"
    "s_cmp_eq_u32 s88, 1\n"
    "s_cselect_b32 s85, s59, s58\n"
    "v_add_u32 v248, s85, v0\n"
    "v_xor_b32 v249, 32, v248\n"
    "ds_read_b64_tr_b16 v[176:177], v248\n"
    "ds_read_b64_tr_b16 v[178:179], v248 offset:256\n"
    "ds_read_b64_tr_b16 v[180:181], v249\n"
    "ds_read_b64_tr_b16 v[182:183], v249 offset:256\n"
    "ds_read_b64_tr_b16 v[184:185], v248 offset:512\n"
    "ds_read_b64_tr_b16 v[186:187], v248 offset:768\n"
    "ds_read_b64_tr_b16 v[188:189], v249 offset:512\n"
    "ds_read_b64_tr_b16 v[190:191], v249 offset:768\n"
    "s_setprio 0\n"
    "s_add_u32 m0, s73, s53\n"
    "s_add_u32 s64, s86, 0xa000\n"
    "buffer_load_dwordx4 v2, s[16:19], s64 offen nt lds\n"
    "v_exp_f32 v164, v164\n"
    "v_exp_f32 v165, v165\n"
    "v_add_f32 v14, v14, v162\n"
    "v_cvt_pk_bf16_f32 v169, v162, v163\n"
    "v_add_f32 v14, v14, v163\n"
    "v_exp_f32 v166, v166\n"
    "v_exp_f32 v167, v167\n"
    "v_add_f32 v14, v14, v164\n"
    "v_cvt_pk_bf16_f32 v170, v164, v165\n"
    "v_add_f32 v14, v14, v165\n"
    "buffer_load_dwordx4 v3, s[16:19], s64 offen offset:1024 nt lds\n"
    "v_add_f32 v14, v14, v166\n"
    "v_cvt_pk_bf16_f32 v171, v166, v167\n"
    "v_add_f32 v14, v14, v167\n"
    "s_add_u32 m0, s73, s54\n"
    "s_add_u32 s64, s86, s87\n"
    "buffer_load_dwordx4 v9, s[48:51], s64 offen nt lds\n"
    "s_waitcnt lgkmcnt(6)\n"
    "v_mfma_f32_16x16x32_bf16 v[112:115], v[176:179], v[168:171], v[112:115]\n"
    "ds_read_b64_tr_b16 v[176:177], v248 offset:1024\n"
    "ds_read_b64_tr_b16 v[178:179], v248 offset:1280\n"
    "s_waitcnt lgkmcnt(6)\n"
    "v_mfma_f32_16x16x32_bf16 v[116:119], v[180:183], v[168:171], v[116:119]\n"
    "ds_read_b64_tr_b16 v[180:181], v249 offset:1024\n"
    "ds_read_b64_tr_b16 v[182:183], v249 offset:1280\n"
    "buffer_load_dwordx4 v10, s[48:51], s64 offen offset:1024 nt lds\n"
    "s_waitcnt lgkmcnt(6)\n"
    "v_mfma_f32_16x16x32_bf16 v[120:123], v[184:187], v[168:171], v[120:123]\n"
    "ds_read_b64_tr_b16 v[184:185], v248 offset:1536\n"
    "ds_read_b64_tr_b16 v[186:187], v248 offset:1792\n"
    "s_add_u32 m0, s73, s55\n"
    "s_add_u32 s64, s86, 0x6000\n"
    "buffer_load_dwordx4 v7, s[20:23], s64 offen nt lds\n"
    "s_waitcnt lgkmcnt(6)\n"
    "v_mfma_f32_16x16x32_bf16 v[124:127], v[188:191], v[168:171], v[124:127]\n"
    "ds_read_b64_tr_b16 v[188:189], v249 offset:1536\n"
    "ds_read_b64_tr_b16 v[190:191], v249 offset:1792\n"
    "s_waitcnt lgkmcnt(6)\n"
    "v_mfma_f32_16x16x32_bf16 v[128:131], v[176:179], v[168:171], v[128:131]\n"
    "buffer_load_dwordx4 v8, s[20:23], s64 offen offset:1024 nt lds\n"
    "s_waitcnt lgkmcnt(4)\n"
    "v_mfma_f32_16x16x32_bf16 v[132:135], v[180:183], v[168:171], v[132:135]\n"
    "v_add_u32 v224, s52, v225\n"
    "s_waitcnt vmcnt(6)\n"
    "ds_write_b128 v224, v[216:219]\n"
    "ds_write_b128 v224, v[220:223] offset:1024\n"
    "s_waitcnt lgkmcnt(4)\n"
    "v_mfma_f32_16x16x32_bf16 v[136:139], v[184:187], v[168:171], v[136:139]\n"
    "s_waitcnt lgkmcnt(2)\n"
    "v_mfma_f32_16x16x32_bf16 v[140:143], v[188:191], v[168:171], v[140:143]\n"
    "s_add_u32 s64, s86, 0x8000\n"
    "buffer_load_dwordx4 v[216:219], v7, s[20:23], s64 offen nt\n"
    "s_add_u32 s76, s76, 1\n"
    "s_cmp_eq_u32 s76, 8\n"
    "s_cbranch_scc1 .Lkdump_106\n"
    ".Lkdret_107:\n"
    "buffer_load_dwordx4 v[220:223], v8, s[20:23], s64 offen offset:1024 nt\n"
    "s_add_u32 s67, s56, 0x8000\n"
    "s_sub_u32 s52, s56, 0xe000\n"
    "s_cselect_b32 s52, s67, s52\n"
    "s_add_u32 s67, s57, 0x8000\n"
    "s_sub_u32 s53, s57, 0xe000\n"
    "s_cselect_b32 s53, s67, s53\n"
    "s_add_u32 s67, s58, 0x8000\n"
    "s_sub_u32 s54, s58, 0xe000\n"
    "s_cselect_b32 s54, s67, s54\n"
    "s_add_u32 s67, s59, 0x8000\n"
    "s_sub_u32 s55, s59, 0xe000\n"
    "s_cselect_b32 s55, s67, s55\n"
    "s_add_u32 s15, s15, 1\n"
    "s_add_u32 s86, s86, 0x4000\n"
    "s_waitcnt vmcnt(8) lgkmcnt(0)\n"
    "s_setprio 0\n"
    "s_barrier\n"
    "s_branch .Lks15_0_69\n"
    ".Lks15_0_69:\n"
    "s_cmp_eq_u32 s88, 1\n"
    "s_cselect_b32 s85, s53, s52\n"
    "v_add_u32 v248, s85, v236\n"
    "v_add_u32 v249, s85, v237\n"
    "ds_read_b128 v[176:179], v248\n"
    "ds_read_b128 v[180:183], v249\n"
    "ds_read_b128 v[184:187], v248 offset:512\n"
    "ds_read_b128 v[188:191], v249 offset:512\n"
    "s_setprio 0\n"
    "s_add_u32 m0, s73, s57\n"
    "s_add_u32 s64, s86, 0xa000\n"
    "buffer_load_dwordx4 v2, s[16:19], s64 offen nt lds\n"
    "s_waitcnt lgkmcnt(3)\n"
    "v_mfma_f32_16x16x32_bf16 v[160:163], v[176:179], v[144:147], v[172:175]\n"
    "ds_read_b128 v[176:179], v248 offset:1024\n"
    "s_waitcnt lgkmcnt(3)\n"
    "v_mfma_f32_16x16x32_bf16 v[164:167], v[180:183], v[144:147], v[172:175]\n"
    "ds_read_b128 v[180:183], v249 offset:1024\n"
    "buffer_load_dwordx4 v3, s[16:19], s64 offen offset:1024 nt lds\n"
    "s_waitcnt lgkmcnt(3)\n"
    "v_mfma_f32_16x16x32_bf16 v[160:163], v[184:187], v[148:151], v[160:163]\n"
    "ds_read_b128 v[184:187], v248 offset:1536\n"
    "s_add_u32 m0, s73, s58\n"
    "s_add_u32 s64, s86, s87\n"
    "buffer_load_dwordx4 v9, s[48:51], s64 offen nt lds\n"
    "s_waitcnt lgkmcnt(3)\n"
    "v_mfma_f32_16x16x32_bf16 v[164:167], v[188:191], v[148:151], v[164:167]\n"
    "ds_read_b128 v[188:191], v249 offset:1536\n"
    "s_waitcnt lgkmcnt(3)\n"
    "v_mfma_f32_16x16x32_bf16 v[160:163], v[176:179], v[152:155], v[160:163]\n"
    "buffer_load_dwordx4 v10, s[48:51], s64 offen offset:1024 nt lds\n"
    "s_waitcnt lgkmcnt(2)\n"
    "v_mfma_f32_16x16x32_bf16 v[164:167], v[180:183], v[152:155], v[164:167]\n"
    "s_waitcnt lgkmcnt(1)\n"
    "v_mfma_f32_16x16x32_bf16 v[160:163], v[184:187], v[156:159], v[160:163]\n"
    "s_add_u32 m0, s73, s59\n"
    "s_add_u32 s64, s86, 0x6000\n"
    "buffer_load_dwordx4 v7, s[20:23], s64 offen nt lds\n"
    "s_waitcnt lgkmcnt(0)\n"
    "v_mfma_f32_16x16x32_bf16 v[164:167], v[188:191], v[156:159], v[164:167]\n"
    "buffer_load_dwordx4 v8, s[20:23], s64 offen offset:1024 nt lds\n"
    "s_nop 1\n"
    "v_max3_f32 v15, v160, v161, v162\n"
    "s_nop 3\n"
    "v_max3_f32 v244, v164, v165, v166\n"
    "v_add_u32 v224, s56, v225\n"
    "s_waitcnt vmcnt(6)\n"
    "ds_write_b128 v224, v[216:219]\n"
    "ds_write_b128 v224, v[220:223] offset:1024\n"
    "v_max3_f32 v15, v15, v163, v167\n"
    "v_max_f32 v15, v15, v244\n"
    "v_cmp_nge_f32 vcc, s70, v15\n"
    "s_nop 0\n"
    "s_cbranch_vccnz .Lkxrare_108\n"
    ".Lkxret_109:\n"
    "v_exp_f32 v160, v160\n"
    "v_exp_f32 v161, v161\n"
    "s_add_u32 s64, s86, 0x8000\n"
    "buffer_load_dwordx4 v[216:219], v7, s[20:23], s64 offen nt\n"
    "v_exp_f32 v162, v162\n"
    "v_exp_f32 v163, v163\n"
    "v_add_f32 v14, v14, v160\n"
    "v_cvt_pk_bf16_f32 v168, v160, v161\n"
    "v_add_f32 v14, v14, v161\n"
    "buffer_load_dwordx4 v[220:223], v8, s[20:23], s64 offen offset:1024 nt\n"
    "s_add_u32 s67, s52, 0x8000\n"
    "s_sub_u32 s56, s52, 0xe000\n"
    "s_cselect_b32 s56, s67, s56\n"
    "s_add_u32 s67, s53, 0x8000\n"
    "s_sub_u32 s57, s53, 0xe000\n"
    "s_cselect_b32 s57, s67, s57\n"
    "s_add_u32 s67, s54, 0x8000\n"
    "s_sub_u32 s58, s54, 0xe000\n"
    "s_cselect_b32 s58, s67, s58\n"
    "s_add_u32 s67, s55, 0x8000\n"
    "s_sub_u32 s59, s55, 0xe000\n"
    "s_cselect_b32 s59, s67, s59\n"
    "s_add_u32 s15, s15, 1\n"
    "s_add_u32 s86, s86, 0x4000\n"
    "s_waitcnt vmcnt(8) lgkmcnt(0)\n"
    "s_setprio 0\n"
    "s_barrier\n"
    "s_branch .Lks16_1_72\n"
    ".Lks15_1_70:\n"
    "s_cmp_eq_u32 s88, 1\n"
    "s_cselect_b32 s85, s59, s58\n"
    "v_add_u32 v248, s85, v0\n"
    "v_xor_b32 v249, 32, v248\n"
    "ds_read_b64_tr_b16 v[176:177], v248\n"
    "ds_read_b64_tr_b16 v[178:179], v248 offset:256\n"
    "ds_read_b64_tr_b16 v[180:181], v249\n"
    "ds_read_b64_tr_b16 v[182:183], v249 offset:256\n"
    "ds_read_b64_tr_b16 v[184:185], v248 offset:512\n"
    "ds_read_b64_tr_b16 v[186:187], v248 offset:768\n"
    "ds_read_b64_tr_b16 v[188:189], v249 offset:512\n"
    "ds_read_b64_tr_b16 v[190:191], v249 offset:768\n"
    "s_setprio 0\n"
    "s_add_u32 m0, s73, s53\n"
    "s_add_u32 s64, s86, 0xa000\n"
    "buffer_load_dwordx4 v2, s[16:19], s64 offen nt lds\n"
    "v_exp_f32 v164, v164\n"
    "v_exp_f32 v165, v165\n"
    "v_add_f32 v14, v14, v162\n"
    "v_cvt_pk_bf16_f32 v169, v162, v163\n"
    "v_add_f32 v14, v14, v163\n"
    "v_exp_f32 v166, v166\n"
    "v_exp_f32 v167, v167\n"
    "v_add_f32 v14, v14, v164\n"
    "v_cvt_pk_bf16_f32 v170, v164, v165\n"
    "v_add_f32 v14, v14, v165\n"
    "buffer_load_dwordx4 v3, s[16:19], s64 offen offset:1024 nt lds\n"
    "v_add_f32 v14, v14, v166\n"
    "v_cvt_pk_bf16_f32 v171, v166, v167\n"
    "v_add_f32 v14, v14, v167\n"
    "s_add_u32 m0, s73, s54\n"
    "s_add_u32 s64, s86, s87\n"
    "buffer_load_dwordx4 v9, s[48:51], s64 offen nt lds\n"
    "s_waitcnt lgkmcnt(6)\n"
    "v_mfma_f32_16x16x32_bf16 v[112:115], v[176:179], v[168:171], v[112:115]\n"
    "ds_read_b64_tr_b16 v[176:177], v248 offset:1024\n"
    "ds_read_b64_tr_b16 v[178:179], v248 offset:1280\n"
    "s_waitcnt lgkmcnt(6)\n"
    "v_mfma_f32_16x16x32_bf16 v[116:119], v[180:183], v[168:171], v[116:119]\n"
    "ds_read_b64_tr_b16 v[180:181], v249 offset:1024\n"
    "ds_read_b64_tr_b16 v[182:183], v249 offset:1280\n"
    "buffer_load_dwordx4 v10, s[48:51], s64 offen offset:1024 nt lds\n"
    "s_waitcnt lgkmcnt(6)\n"
    "v_mfma_f32_16x16x32_bf16 v[120:123], v[184:187], v[168:171], v[120:123]\n"
    "ds_read_b64_tr_b16 v[184:185], v248 offset:1536\n"
    "ds_read_b64_tr_b16 v[186:187], v248 offset:1792\n"
    "s_add_u32 m0, s73, s55\n"
    "s_add_u32 s64, s86, 0x6000\n"
    "buffer_load_dwordx4 v7, s[20:23], s64 offen nt lds\n"
    "s_waitcnt lgkmcnt(6)\n"
    "v_mfma_f32_16x16x32_bf16 v[124:127], v[188:191], v[168:171], v[124:127]\n"
    "ds_read_b64_tr_b16 v[188:189], v249 offset:1536\n"
    "ds_read_b64_tr_b16 v[190:191], v249 offset:1792\n"
    "s_waitcnt lgkmcnt(6)\n"
    "v_mfma_f32_16x16x32_bf16 v[128:131], v[176:179], v[168:171], v[128:131]\n"
    "buffer_load_dwordx4 v8, s[20:23], s64 offen offset:1024 nt lds\n"
    "s_waitcnt lgkmcnt(4)\n"
    "v_mfma_f32_16x16x32_bf16 v[132:135], v[180:183], v[168:171], v[132:135]\n"
    "v_add_u32 v224, s52, v225\n"
    "s_waitcnt vmcnt(6)\n"
    "ds_write_b128 v224, v[216:219]\n"
    "ds_write_b128 v224, v[220:223] offset:1024\n"
    "s_waitcnt lgkmcnt(4)\n"
    "v_mfma_f32_16x16x32_bf16 v[136:139], v[184:187], v[168:171], v[136:139]\n"
    "s_waitcnt lgkmcnt(2)\n"
    "v_mfma_f32_16x16x32_bf16 v[140:143], v[188:191], v[168:171], v[140:143]\n"
    "s_add_u32 s64, s86, 0x8000\n"
    "buffer_load_dwordx4 v[216:219], v7, s[20:23], s64 offen nt\n"
    "s_add_u32 s76, s76, 1\n"
    "s_cmp_eq_u32 s76, 8\n"
    "s_cbranch_scc1 .Lkdump_110\n"
    ".Lkdret_111:\n"
    "buffer_load_dwordx4 v[220:223], v8, s[20:23], s64 offen offset:1024 nt\n"
    "s_add_u32 s67, s56, 0x8000\n"
    "s_sub_u32 s52, s56, 0xe000\n"
    "s_cselect_b32 s52, s67, s52\n"
    "s_add_u32 s67, s57, 0x8000\n"
    "s_sub_u32 s53, s57, 0xe000\n"
    "s_cselect_b32 s53, s67, s53\n"
    "s_add_u32 s67, s58, 0x8000\n"
    "s_sub_u32 s54, s58, 0xe000\n"
    "s_cselect_b32 s54, s67, s54\n"
    "s_add_u32 s67, s59, 0x8000\n"
    "s_sub_u32 s55, s59, 0xe000\n"
    "s_cselect_b32 s55, s67, s55\n"
    "s_add_u32 s15, s15, 1\n"
    "s_add_u32 s86, s86, 0x4000\n"
    "s_waitcnt vmcnt(8) lgkmcnt(0)\n"
    "s_setprio 0\n"
    "s_barrier\n"
    "s_branch .Lks16_0_71\n"
    ".Lks16_0_71:\n"
    "s_cmp_eq_u32 s88, 1\n"
    "s_cselect_b32 s85, s53, s52\n"
    "v_add_u32 v248, s85, v236\n"
    "v_add_u32 v249, s85, v237\n"
    "ds_read_b128 v[176:179], v248\n"
    "ds_read_b128 v[180:183], v249\n"
    "ds_read_b128 v[184:187], v248 offset:512\n"
    "ds_read_b128 v[188:191], v249 offset:512\n"
    "s_setprio 0\n"
    "s_add_u32 m0, s73, s57\n"
    "s_add_u32 s64, s86, 0xa000\n"
    "buffer_load_dwordx4 v2, s[16:19], s64 offen nt lds\n"
    "s_waitcnt lgkmcnt(3)\n"
    "v_mfma_f32_16x16x32_bf16 v[160:163], v[176:179], v[144:147], v[172:175]\n"
    "ds_read_b128 v[176:179], v248 offset:1024\n"
    "s_waitcnt lgkmcnt(3)\n"
    "v_mfma_f32_16x16x32_bf16 v[164:167], v[180:183], v[144:147], v[172:175]\n"
    "ds_read_b128 v[180:183], v249 offset:1024\n"
    "buffer_load_dwordx4 v3, s[16:19], s64 offen offset:1024 nt lds\n"
    "s_waitcnt lgkmcnt(3)\n"
    "v_mfma_f32_16x16x32_bf16 v[160:163], v[184:187], v[148:151], v[160:163]\n"
    "ds_read_b128 v[184:187], v248 offset:1536\n"
    "s_add_u32 m0, s73, s58\n"
    "s_add_u32 s64, s86, s87\n"
    "buffer_load_dwordx4 v9, s[48:51], s64 offen nt lds\n"
    "s_waitcnt lgkmcnt(3)\n"
    "v_mfma_f32_16x16x32_bf16 v[164:167], v[188:191], v[148:151], v[164:167]\n"
    "ds_read_b128 v[188:191], v249 offset:1536\n"
    "s_waitcnt lgkmcnt(3)\n"
    "v_mfma_f32_16x16x32_bf16 v[160:163], v[176:179], v[152:155], v[160:163]\n"
    "buffer_load_dwordx4 v10, s[48:51], s64 offen offset:1024 nt lds\n"
    "s_waitcnt lgkmcnt(2)\n"
    "v_mfma_f32_16x16x32_bf16 v[164:167], v[180:183], v[152:155], v[164:167]\n"
    "s_waitcnt lgkmcnt(1)\n"
    "v_mfma_f32_16x16x32_bf16 v[160:163], v[184:187], v[156:159], v[160:163]\n"
    "s_add_u32 m0, s73, s59\n"
    "s_add_u32 s64, s86, 0x6000\n"
    "buffer_load_dwordx4 v7, s[20:23], s64 offen nt lds\n"
    "s_waitcnt lgkmcnt(0)\n"
    "v_mfma_f32_16x16x32_bf16 v[164:167], v[188:191], v[156:159], v[164:167]\n"
    "buffer_load_dwordx4 v8, s[20:23], s64 offen offset:1024 nt lds\n"
    "s_nop 1\n"
    "v_max3_f32 v15, v160, v161, v162\n"
    "s_nop 3\n"
    "v_max3_f32 v244, v164, v165, v166\n"
    "v_add_u32 v224, s56, v225\n"
    "s_waitcnt vmcnt(6)\n"
    "ds_write_b128 v224, v[216:219]\n"
    "ds_write_b128 v224, v[220:223] offset:1024\n"
    "v_max3_f32 v15, v15, v163, v167\n"
    "v_max_f32 v15, v15, v244\n"
    "v_cmp_nge_f32 vcc, s70, v15\n"
    "s_nop 0\n"
    "s_cbranch_vccnz .Lkxrare_112\n"
    ".Lkxret_113:\n"
    "v_exp_f32 v160, v160\n"
    "v_exp_f32 v161, v161\n"
    "s_add_u32 s64, s86, 0x8000\n"
    "buffer_load_dwordx4 v[216:219], v7, s[20:23], s64 offen nt\n"
    "v_exp_f32 v162, v162\n"
    "v_exp_f32 v163, v163\n"
    "v_add_f32 v14, v14, v160\n"
    "v_cvt_pk_bf16_f32 v168, v160, v161\n"
    "v_add_f32 v14, v14, v161\n"
    "buffer_load_dwordx4 v[220:223], v8, s[20:23], s64 offen offset:1024 nt\n"
    "s_setprio 1\n"
    "s_waitcnt lgkmcnt(0)\n"
    "s_barrier\n"
    "v_and_b32 v244, 15, v1\n"
    "v_lshlrev_b32 v244, 3, v244\n"
    "v_add_u32 v244, 0x1e000, v244\n"
    "ds_read_b64 v[176:177], v244\n"
    "ds_read_b64 v[178:179], v244 offset:512\n"
    "ds_read_b64 v[180:181], v244 offset:1024\n"
    "ds_read_b64 v[182:183], v244 offset:1536\n"
    "s_add_u32 s60, s73, 0x16000\n"
    "v_add_u32 v245, s60, v11\n"
    "ds_read_b128 v[192:195], v245\n"
    "ds_read_b128 v[196:199], v245 offset:1024\n"
    "ds_read_b128 v[200:203], v245 offset:8192\n"
    "ds_read_b128 v[204:207], v245 offset:9216\n"
    "ds_read_b128 v[208:211], v245 offset:16384\n"
    "ds_read_b128 v[212:215], v245 offset:17408\n"
    "ds_read_b128 v[228:231], v245 offset:24576\n"
    "ds_read_b128 v[240:243], v245 offset:25600\n"
    "s_waitcnt lgkmcnt(10)\n"
    "v_min_f32 v244, v176, v178\n"
    "s_waitcnt lgkmcnt(8)\n"
    "v_min_f32 v245, v180, v182\n"
    "v_min_f32 v244, v244, v245\n"
    "v_sub_f32 v176, v244, v176\n"
    "v_sub_f32 v178, v244, v178\n"
    "v_sub_f32 v180, v244, v180\n"
    "v_sub_f32 v182, v244, v182\n"
    "v_exp_f32 v176, v176\n"
    "v_exp_f32 v178, v178\n"
    "v_exp_f32 v180, v180\n"
    "v_exp_f32 v182, v182\n"
    "v_mul_f32 v244, v176, v177\n"
    "v_fmac_f32 v244, v178, v179\n"
    "v_fmac_f32 v244, v180, v181\n"
    "v_fmac_f32 v244, v182, v183\n"
    "v_rcp_f32 v244, v244\n"
    "s_nop 0\n"
    "v_mul_f32 v176, v176, v244\n"
    "v_mul_f32 v178, v178, v244\n"
    "v_mul_f32 v180, v180, v244\n"
    "v_mul_f32 v182, v182, v244\n"
    "s_waitcnt lgkmcnt(7)\n"
    "v_mul_f32 v184, v176, v192\n"
    "v_mul_f32 v185, v176, v193\n"
    "v_mul_f32 v186, v176, v194\n"
    "v_mul_f32 v187, v176, v195\n"
    "s_waitcnt lgkmcnt(6)\n"
    "v_mul_f32 v188, v176, v196\n"
    "v_mul_f32 v189, v176, v197\n"
    "v_mul_f32 v190, v176, v198\n"
    "v_mul_f32 v191, v176, v199\n"
    "s_waitcnt lgkmcnt(5)\n"
    "v_fmac_f32 v184, v178, v200\n"
    "v_fmac_f32 v185, v178, v201\n"
    "v_fmac_f32 v186, v178, v202\n"
    "v_fmac_f32 v187, v178, v203\n"
    "s_waitcnt lgkmcnt(4)\n"
    "v_fmac_f32 v188, v178, v204\n"
    "v_fmac_f32 v189, v178, v205\n"
    "v_fmac_f32 v190, v178, v206\n"
    "v_fmac_f32 v191, v178, v207\n"
    "s_waitcnt lgkmcnt(3)\n"
    "v_fmac_f32 v184, v180, v208\n"
    "v_fmac_f32 v185, v180, v209\n"
    "v_fmac_f32 v186, v180, v210\n"
    "v_fmac_f32 v187, v180, v211\n"
    "s_waitcnt lgkmcnt(2)\n"
    "v_fmac_f32 v188, v180, v212\n"
    "v_fmac_f32 v189, v180, v213\n"
    "v_fmac_f32 v190, v180, v214\n"
    "v_fmac_f32 v191, v180, v215\n"
    "s_waitcnt lgkmcnt(1)\n"
    "v_fmac_f32 v184, v182, v228\n"
    "v_fmac_f32 v185, v182, v229\n"
    "v_fmac_f32 v186, v182, v230\n"
    "v_fmac_f32 v187, v182, v231\n"
    "s_waitcnt lgkmcnt(0)\n"
    "v_fmac_f32 v188, v182, v240\n"
    "v_fmac_f32 v189, v182, v241\n"
    "v_fmac_f32 v190, v182, v242\n"
    "v_fmac_f32 v191, v182, v243\n"
    "v_cvt_pk_bf16_f32 v184, v184, v185\n"
    "v_cvt_pk_bf16_f32 v185, v186, v187\n"
    "v_cvt_pk_bf16_f32 v186, v188, v189\n"
    "v_cvt_pk_bf16_f32 v187, v190, v191\n"
    "buffer_store_dwordx4 v[184:187], v11, s[36:39], s84 offen sc1\n"
    "s_add_u32 s84, s84, 0x1000\n"
    "s_add_u32 s67, s52, 0x8000\n"
    "s_sub_u32 s56, s52, 0xe000\n"
    "s_cselect_b32 s56, s67, s56\n"
    "s_add_u32 s67, s53, 0x8000\n"
    "s_sub_u32 s57, s53, 0xe000\n"
    "s_cselect_b32 s57, s67, s57\n"
    "s_add_u32 s67, s54, 0x8000\n"
    "s_sub_u32 s58, s54, 0xe000\n"
    "s_cselect_b32 s58, s67, s58\n"
    "s_add_u32 s67, s55, 0x8000\n"
    "s_sub_u32 s59, s55, 0xe000\n"
    "s_cselect_b32 s59, s67, s59\n"
    "s_add_u32 s15, s15, 1\n"
    "s_add_u32 s86, s86, 0x4000\n"
    "s_waitcnt vmcnt(8) lgkmcnt(0)\n"
    "s_setprio 0\n"
    "s_barrier\n"
    "s_branch .Lks17_1_74\n"
    ".Lks16_1_72:\n"
    "s_cmp_eq_u32 s88, 1\n"
    "s_cselect_b32 s85, s59, s58\n"
    "v_add_u32 v248, s85, v0\n"
    "v_xor_b32 v249, 32, v248\n"
    "ds_read_b64_tr_b16 v[176:177], v248\n"
    "ds_read_b64_tr_b16 v[178:179], v248 offset:256\n"
    "ds_read_b64_tr_b16 v[180:181], v249\n"
    "ds_read_b64_tr_b16 v[182:183], v249 offset:256\n"
    "ds_read_b64_tr_b16 v[184:185], v248 offset:512\n"
    "ds_read_b64_tr_b16 v[186:187], v248 offset:768\n"
    "ds_read_b64_tr_b16 v[188:189], v249 offset:512\n"
    "ds_read_b64_tr_b16 v[190:191], v249 offset:768\n"
    "s_setprio 0\n"
    "s_add_u32 m0, s73, s53\n"
    "s_add_u32 s64, s86, 0xa000\n"
    "buffer_load_dwordx4 v2, s[16:19], s64 offen nt lds\n"
    "v_exp_f32 v164, v164\n"
    "v_exp_f32 v165, v165\n"
    "v_add_f32 v14, v14, v162\n"
    "v_cvt_pk_bf16_f32 v169, v162, v163\n"
    "v_add_f32 v14, v14, v163\n"
    "v_exp_f32 v166, v166\n"
    "v_exp_f32 v167, v167\n"
    "v_add_f32 v14, v14, v164\n"
    "v_cvt_pk_bf16_f32 v170, v164, v165\n"
    "v_add_f32 v14, v14, v165\n"
    "buffer_load_dwordx4 v3, s[16:19], s64 offen offset:1024 nt lds\n"
    "v_add_f32 v14, v14, v166\n"
    "v_cvt_pk_bf16_f32 v171, v166, v167\n"
    "v_add_f32 v14, v14, v167\n"
    "s_add_u32 m0, s73, s54\n"
    "s_add_u32 s64, s86, s87\n"
    "buffer_load_dwordx4 v9, s[48:51], s64 offen nt lds\n"
    "s_waitcnt lgkmcnt(6)\n"
    "v_mfma_f32_16x16x32_bf16 v[112:115], v[176:179], v[168:171], v[112:115]\n"
    "ds_read_b64_tr_b16 v[176:177], v248 offset:1024\n"
    "ds_read_b64_tr_b16 v[178:179], v248 offset:1280\n"
    "s_waitcnt lgkmcnt(6)\n"
    "v_mfma_f32_16x16x32_bf16 v[116:119], v[180:183], v[168:171], v[116:119]\n"
    "ds_read_b64_tr_b16 v[180:181], v249 offset:1024\n"
    "ds_read_b64_tr_b16 v[182:183], v249 offset:1280\n"
    "buffer_load_dwordx4 v10, s[48:51], s64 offen offset:1024 nt lds\n"
    "s_waitcnt lgkmcnt(6)\n"
    "v_mfma_f32_16x16x32_bf16 v[120:123], v[184:187], v[168:171], v[120:123]\n"
    "ds_read_b64_tr_b16 v[184:185], v248 offset:1536\n"
    "ds_read_b64_tr_b16 v[186:187], v248 offset:1792\n"
    "s_add_u32 m0, s73, s55\n"
    "s_add_u32 s64, s86, 0x6000\n"
    "buffer_load_dwordx4 v7, s[20:23], s64 offen nt lds\n"
    "s_waitcnt lgkmcnt(6)\n"
    "v_mfma_f32_16x16x32_bf16 v[124:127], v[188:191], v[168:171], v[124:127]\n"
    "ds_read_b64_tr_b16 v[188:189], v249 offset:1536\n"
    "ds_read_b64_tr_b16 v[190:191], v249 offset:1792\n"
    "s_waitcnt lgkmcnt(6)\n"
    "v_mfma_f32_16x16x32_bf16 v[128:131], v[176:179], v[168:171], v[128:131]\n"
    "buffer_load_dwordx4 v8, s[20:23], s64 offen offset:1024 nt lds\n"
    "s_waitcnt lgkmcnt(4)\n"
    "v_mfma_f32_16x16x32_bf16 v[132:135], v[180:183], v[168:171], v[132:135]\n"
    "v_add_u32 v224, s52, v225\n"
    "s_waitcnt vmcnt(6)\n"
    "ds_write_b128 v224, v[216:219]\n"
    "ds_write_b128 v224, v[220:223] offset:1024\n"
    "s_waitcnt lgkmcnt(4)\n"
    "v_mfma_f32_16x16x32_bf16 v[136:139], v[184:187], v[168:171], v[136:139]\n"
    "s_waitcnt lgkmcnt(2)\n"
    "v_mfma_f32_16x16x32_bf16 v[140:143], v[188:191], v[168:171], v[140:143]\n"
    "s_add_u32 s64, s86, 0x8000\n"
    "buffer_load_dwordx4 v[216:219], v7, s[20:23], s64 offen nt\n"
    "s_add_u32 s76, s76, 1\n"
    "s_cmp_eq_u32 s76, 8\n"
    "s_cbranch_scc1 .Lkdump_114\n"
    ".Lkdret_115:\n"
    "buffer_load_dwordx4 v[220:223], v8, s[20:23], s64 offen offset:1024 nt\n"
    "s_setprio 1\n"
    "s_waitcnt lgkmcnt(0)\n"
    "s_barrier\n"
    "v_and_b32 v244, 15, v1\n"
    "v_lshlrev_b32 v244, 3, v244\n"
    "v_add_u32 v244, 0x1e000, v244\n"
    "ds_read_b64 v[176:177], v244\n"
    "ds_read_b64 v[178:179], v244 offset:512\n"
    "ds_read_b64 v[180:181], v244 offset:1024\n"
    "ds_read_b64 v[182:183], v244 offset:1536\n"
    "s_add_u32 s60, s73, 0x16000\n"
    "v_add_u32 v245, s60, v11\n"
    "ds_read_b128 v[192:195], v245\n"
    "ds_read_b128 v[196:199], v245 offset:1024\n"
    "ds_read_b128 v[200:203], v245 offset:8192\n"
    "ds_read_b128 v[204:207], v245 offset:9216\n"
    "ds_read_b128 v[208:211], v245 offset:16384\n"
    "ds_read_b128 v[212:215], v245 offset:17408\n"
    "ds_read_b128 v[228:231], v245 offset:24576\n"
    "ds_read_b128 v[240:243], v245 offset:25600\n"
    "s_waitcnt lgkmcnt(10)\n"
    "v_min_f32 v244, v176, v178\n"
    "s_waitcnt lgkmcnt(8)\n"
    "v_min_f32 v245, v180, v182\n"
    "v_min_f32 v244, v244, v245\n"
    "v_sub_f32 v176, v244, v176\n"
    "v_sub_f32 v178, v244, v178\n"
    "v_sub_f32 v180, v244, v180\n"
    "v_sub_f32 v182, v244, v182\n"
    "v_exp_f32 v176, v176\n"
    "v_exp_f32 v178, v178\n"
    "v_exp_f32 v180, v180\n"
    "v_exp_f32 v182, v182\n"
    "v_mul_f32 v244, v176, v177\n"
    "v_fmac_f32 v244, v178, v179\n"
    "v_fmac_f32 v244, v180, v181\n"
    "v_fmac_f32 v244, v182, v183\n"
    "v_rcp_f32 v244, v244\n"
    "s_nop 0\n"
    "v_mul_f32 v176, v176, v244\n"
    "v_mul_f32 v178, v178, v244\n"
    "v_mul_f32 v180, v180, v244\n"
    "v_mul_f32 v182, v182, v244\n"
    "s_waitcnt lgkmcnt(7)\n"
    "v_mul_f32 v184, v176, v192\n"
    "v_mul_f32 v185, v176, v193\n"
    "v_mul_f32 v186, v176, v194\n"
    "v_mul_f32 v187, v176, v195\n"
    "s_waitcnt lgkmcnt(6)\n"
    "v_mul_f32 v188, v176, v196\n"
    "v_mul_f32 v189, v176, v197\n"
    "v_mul_f32 v190, v176, v198\n"
    "v_mul_f32 v191, v176, v199\n"
    "s_waitcnt lgkmcnt(5)\n"
    "v_fmac_f32 v184, v178, v200\n"
    "v_fmac_f32 v185, v178, v201\n"
    "v_fmac_f32 v186, v178, v202\n"
    "v_fmac_f32 v187, v178, v203\n"
    "s_waitcnt lgkmcnt(4)\n"
    "v_fmac_f32 v188, v178, v204\n"
    "v_fmac_f32 v189, v178, v205\n"
    "v_fmac_f32 v190, v178, v206\n"
    "v_fmac_f32 v191, v178, v207\n"
    "s_waitcnt lgkmcnt(3)\n"
    "v_fmac_f32 v184, v180, v208\n"
    "v_fmac_f32 v185, v180, v209\n"
    "v_fmac_f32 v186, v180, v210\n"
    "v_fmac_f32 v187, v180, v211\n"
    "s_waitcnt lgkmcnt(2)\n"
    "v_fmac_f32 v188, v180, v212\n"
    "v_fmac_f32 v189, v180, v213\n"
    "v_fmac_f32 v190, v180, v214\n"
    "v_fmac_f32 v191, v180, v215\n"
    "s_waitcnt lgkmcnt(1)\n"
    "v_fmac_f32 v184, v182, v228\n"
    "v_fmac_f32 v185, v182, v229\n"
    "v_fmac_f32 v186, v182, v230\n"
    "v_fmac_f32 v187, v182, v231\n"
    "s_waitcnt lgkmcnt(0)\n"
    "v_fmac_f32 v188, v182, v240\n"
    "v_fmac_f32 v189, v182, v241\n"
    "v_fmac_f32 v190, v182, v242\n"
    "v_fmac_f32 v191, v182, v243\n"
    "v_cvt_pk_bf16_f32 v184, v184, v185\n"
    "v_cvt_pk_bf16_f32 v185, v186, v187\n"
    "v_cvt_pk_bf16_f32 v186, v188, v189\n"
    "v_cvt_pk_bf16_f32 v187, v190, v191\n"
    "buffer_store_dwordx4 v[184:187], v11, s[36:39], s84 offen sc1\n"
    "s_add_u32 s84, s84, 0x1000\n"
    "s_add_u32 s67, s56, 0x8000\n"
    "s_sub_u32 s52, s56, 0xe000\n"
    "s_cselect_b32 s52, s67, s52\n"
    "s_add_u32 s67, s57, 0x8000\n"
    "s_sub_u32 s53, s57, 0xe000\n"
    "s_cselect_b32 s53, s67, s53\n"
    "s_add_u32 s67, s58, 0x8000\n"
    "s_sub_u32 s54, s58, 0xe000\n"
    "s_cselect_b32 s54, s67, s54\n"
    "s_add_u32 s67, s59, 0x8000\n"
    "s_sub_u32 s55, s59, 0xe000\n"
    "s_cselect_b32 s55, s67, s55\n"
    "s_add_u32 s15, s15, 1\n"
    "s_add_u32 s86, s86, 0x4000\n"
    "s_waitcnt vmcnt(8) lgkmcnt(0)\n"
    "s_setprio 0\n"
    "s_barrier\n"
    "s_branch .Lks17_0_73\n"
    ".Lks17_0_73:\n"
    "s_cmp_eq_u32 s88, 1\n"
    "s_cselect_b32 s85, s53, s52\n"
    "v_add_u32 v248, s85, v236\n"
    "v_add_u32 v249, s85, v237\n"
    "ds_read_b128 v[176:179], v248\n"
    "ds_read_b128 v[180:183], v249\n"
    "ds_read_b128 v[184:187], v248 offset:512\n"
    "ds_read_b128 v[188:191], v249 offset:512\n"
    "s_setprio 0\n"
    "s_add_u32 m0, s73, s57\n"
    "s_add_u32 s64, s86, 0xa000\n"
    "buffer_load_dwordx4 v2, s[16:19], s64 offen nt lds\n"
    "s_waitcnt lgkmcnt(3)\n"
    "v_mfma_f32_16x16x32_bf16 v[160:163], v[176:179], v[144:147], v[172:175]\n"
    "ds_read_b128 v[176:179], v248 offset:1024\n"
    "s_waitcnt lgkmcnt(3)\n"
    "v_mfma_f32_16x16x32_bf16 v[164:167], v[180:183], v[144:147], v[172:175]\n"
    "ds_read_b128 v[180:183], v249 offset:1024\n"
    "buffer_load_dwordx4 v3, s[16:19], s64 offen offset:1024 nt lds\n"
    "s_waitcnt lgkmcnt(3)\n"
    "v_mfma_f32_16x16x32_bf16 v[160:163], v[184:187], v[148:151], v[160:163]\n"
    "ds_read_b128 v[184:187], v248 offset:1536\n"
    "s_add_u32 m0, s73, s58\n"
    "s_add_u32 s64, s86, s87\n"
    "buffer_load_dwordx4 v9, s[48:51], s64 offen nt lds\n"
    "v_mov_b32 v192, 0\n"
    "v_mov_b32 v193, 0\n"
    "v_mov_b32 v194, 0\n"
    "v_mov_b32 v195, 0\n"
    "s_nop 1\n"
    "v_mfma_f32_32x32x16_bf16 v[16:31], v[192:195], v[192:195], 0\n"
    "v_mfma_f32_32x32x16_bf16 v[32:47], v[192:195], v[192:195], 0\n"
    "v_mfma_f32_32x32x16_bf16 v[48:63], v[192:195], v[192:195], 0\n"
    "v_mfma_f32_32x32x16_bf16 v[64:79], v[192:195], v[192:195], 0\n"
    "v_mfma_f32_32x32x16_bf16 v[80:95], v[192:195], v[192:195], 0\n"
    "v_mfma_f32_32x32x16_bf16 v[96:111], v[192:195], v[192:195], 0\n"
    "s_waitcnt lgkmcnt(3)\n"
    "v_mfma_f32_16x16x32_bf16 v[164:167], v[188:191], v[148:151], v[164:167]\n"
    "ds_read_b128 v[188:191], v249 offset:1536\n"
    "s_waitcnt lgkmcnt(3)\n"
    "v_mfma_f32_16x16x32_bf16 v[160:163], v[176:179], v[152:155], v[160:163]\n"
    "buffer_load_dwordx4 v10, s[48:51], s64 offen offset:1024 nt lds\n"
    "s_mov_b32 s74, 0x1e800\n"
    "s_lshl_b32 s60, s90, 7\n"
    "s_add_u32 s60, s60, s12\n"
    "s_mul_i32 s75, s60, 0x2800\n"
    "v_add_u32 v6, 0x20000, v11\n"
    "v_add_u32 v12, 0x40000, v11\n"
    "s_mov_b32 s83, s82\n"
    "s_add_u32 s60, s99, s91\n"
    "s_mul_i32 s98, s60, 6\n"
    "s_waitcnt lgkmcnt(2)\n"
    "v_mfma_f32_16x16x32_bf16 v[164:167], v[180:183], v[152:155], v[164:167]\n"
    "s_waitcnt lgkmcnt(1)\n"
    "v_mfma_f32_16x16x32_bf16 v[160:163], v[184:187], v[156:159], v[160:163]\n"
    "s_add_u32 m0, s73, s59\n"
    "s_add_u32 s64, s86, 0x6000\n"
    "buffer_load_dwordx4 v7, s[20:23], s64 offen nt lds\n"
    "s_add_u32 s60, s98, s79\n"
    "s_add_u32 s60, s60, 0\n"
    "s_and_b32 s61, s60, 12\n"
    "s_and_b32 s60, s60, 3\n"
    "s_waitcnt lgkmcnt(0)\n"
    "v_mfma_f32_16x16x32_bf16 v[164:167], v[188:191], v[156:159], v[164:167]\n"
    "buffer_load_dwordx4 v8, s[20:23], s64 offen offset:1024 nt lds\n"
    "s_lshl1_add_u32 s60, s61, s60\n"
    "s_mul_i32 s61, s60, 0xa000\n"
    "s_lshl_b32 s60, s60, 10\n"
    "s_add_u32 s96, s60, s81\n"
    "v_max3_f32 v15, v160, v161, v162\n"
    "s_nop 1\n"
    "v_max3_f32 v244, v164, v165, v166\n"
    "v_add_u32 v224, s56, v225\n"
    "s_waitcnt vmcnt(6)\n"
    "ds_write_b128 v224, v[216:219]\n"
    "ds_write_b128 v224, v[220:223] offset:1024\n"
    "v_max3_f32 v15, v15, v163, v167\n"
    "v_max_f32 v15, v15, v244\n"
    "v_cmp_nge_f32 vcc, s70, v15\n"
    "s_nop 0\n"
    "s_cbranch_vccnz .Lkxrare_116\n"
    ".Lkxret_117:\n"
    "s_add_u32 s97, s61, s75\n"
    "s_cmp_lt_u32 s79, 16\n"
    "s_cselect_b32 s96, s96, 0x7ff00000\n"
    "s_cselect_b32 s97, s97, 0x7ff00000\n"
    "v_exp_f32 v160, v160\n"
    "v_exp_f32 v161, v161\n"
    "s_add_u32 s64, s86, 0x8000\n"
    "buffer_load_dwordx4 v[216:219], v7, s[20:23], s64 offen nt\n"
    "v_exp_f32 v162, v162\n"
    "v_exp_f32 v163, v163\n"
    "v_add_f32 v14, v14, v160\n"
    "v_cvt_pk_bf16_f32 v168, v160, v161\n"
    "v_add_f32 v14, v14, v161\n"
    "buffer_load_dwordx4 v[220:223], v8, s[20:23], s64 offen offset:1024 nt\n"
    "s_add_u32 s60, s80, 0\n"
    "s_and_b32 s60, s60, 3\n"
    "s_mul_i32 s60, s60, 0x50000\n"
    "s_add_u32 s60, s60, 0x28000\n"
    "s_add_u32 s28, s60, s75\n"
    "s_add_u32 s60, s80, 1\n"
    "s_and_b32 s60, s60, 3\n"
    "s_mul_i32 s60, s60, 0x50000\n"
    "s_add_u32 s60, s60, 0x28000\n"
    "s_add_u32 s29, s60, s75\n"
    "s_add_u32 s60, s80, 2\n"
    "s_and_b32 s60, s60, 3\n"
    "s_mul_i32 s60, s60, 0x50000\n"
    "s_add_u32 s60, s60, 0x28000\n"
    "s_add_u32 s30, s60, s75\n"
    "s_add_u32 s60, s80, 3\n"
    "s_and_b32 s60, s60, 3\n"
    "s_mul_i32 s60, s60, 0x50000\n"
    "s_add_u32 s60, s60, 0x28000\n"
    "s_add_u32 s31, s60, s75\n"
    "s_add_u32 s67, s52, 0x8000\n"
    "s_sub_u32 s56, s52, 0xe000\n"
    "s_cselect_b32 s56, s67, s56\n"
    "s_add_u32 s67, s53, 0x8000\n"
    "s_sub_u32 s57, s53, 0xe000\n"
    "s_cselect_b32 s57, s67, s57\n"
    "s_add_u32 s67, s54, 0x8000\n"
    "s_sub_u32 s58, s54, 0xe000\n"
    "s_cselect_b32 s58, s67, s58\n"
    "s_add_u32 s67, s55, 0x8000\n"
    "s_sub_u32 s59, s55, 0xe000\n"
    "s_cselect_b32 s59, s67, s59\n"
    "s_add_u32 s15, s15, 1\n"
    "s_add_u32 s86, s86, 0x4000\n"
    "s_waitcnt vmcnt(8) lgkmcnt(0)\n"
    "s_setprio 0\n"
    "s_barrier\n"
    "s_branch .Lks18_1_76\n"
    ".Lks17_1_74:\n"
    "s_cmp_eq_u32 s88, 1\n"
    "s_cselect_b32 s85, s59, s58\n"
    "v_add_u32 v248, s85, v0\n"
    "v_xor_b32 v249, 32, v248\n"
    "ds_read_b64_tr_b16 v[176:177], v248\n"
    "ds_read_b64_tr_b16 v[178:179], v248 offset:256\n"
    "ds_read_b64_tr_b16 v[180:181], v249\n"
    "ds_read_b64_tr_b16 v[182:183], v249 offset:256\n"
    "ds_read_b64_tr_b16 v[184:185], v248 offset:512\n"
    "ds_read_b64_tr_b16 v[186:187], v248 offset:768\n"
    "ds_read_b64_tr_b16 v[188:189], v249 offset:512\n"
    "ds_read_b64_tr_b16 v[190:191], v249 offset:768\n"
    "s_setprio 0\n"
    "s_add_u32 m0, s73, s53\n"
    "s_add_u32 s64, s86, 0xa000\n"
    "buffer_load_dwordx4 v2, s[16:19], s64 offen nt lds\n"
    "v_exp_f32 v164, v164\n"
    "v_exp_f32 v165, v165\n"
    "v_add_f32 v14, v14, v162\n"
    "v_cvt_pk_bf16_f32 v169, v162, v163\n"
    "v_add_f32 v14, v14, v163\n"
    "v_exp_f32 v166, v166\n"
    "v_exp_f32 v167, v167\n"
    "v_add_f32 v14, v14, v164\n"
    "v_cvt_pk_bf16_f32 v170, v164, v165\n"
    "v_add_f32 v14, v14, v165\n"
    "buffer_load_dwordx4 v3, s[16:19], s64 offen offset:1024 nt lds\n"
    "v_mov_b32 v192, 0\n"
    "v_mov_b32 v193, 0\n"
    "v_mov_b32 v194, 0\n"
    "v_mov_b32 v195, 0\n"
    "s_nop 1\n"
    "v_mfma_f32_32x32x16_bf16 v[16:31], v[192:195], v[192:195], 0\n"
    "v_mfma_f32_32x32x16_bf16 v[32:47], v[192:195], v[192:195], 0\n"
    "v_mfma_f32_32x32x16_bf16 v[48:63], v[192:195], v[192:195], 0\n"
    "v_mfma_f32_32x32x16_bf16 v[64:79], v[192:195], v[192:195], 0\n"
    "v_mfma_f32_32x32x16_bf16 v[80:95], v[192:195], v[192:195], 0\n"
    "v_mfma_f32_32x32x16_bf16 v[96:111], v[192:195], v[192:195], 0\n"
    "v_add_f32 v14, v14, v166\n"
    "v_cvt_pk_bf16_f32 v171, v166, v167\n"
    "v_add_f32 v14, v14, v167\n"
    "s_add_u32 m0, s73, s54\n"
    "s_add_u32 s64, s86, s87\n"
    "buffer_load_dwordx4 v9, s[48:51], s64 offen nt lds\n"
    "s_waitcnt lgkmcnt(6)\n"
    "v_mfma_f32_16x16x32_bf16 v[112:115], v[176:179], v[168:171], v[112:115]\n"
    "ds_read_b64_tr_b16 v[176:177], v248 offset:1024\n"
    "ds_read_b64_tr_b16 v[178:179], v248 offset:1280\n"
    "s_mov_b32 s74, 0x1e800\n"
    "s_lshl_b32 s60, s90, 7\n"
    "s_add_u32 s60, s60, s12\n"
    "s_mul_i32 s75, s60, 0x2800\n"
    "v_add_u32 v6, 0x20000, v11\n"
    "v_add_u32 v12, 0x40000, v11\n"
    "s_mov_b32 s83, s82\n"
    "s_add_u32 s60, s99, s91\n"
    "s_mul_i32 s98, s60, 6\n"
    "s_waitcnt lgkmcnt(6)\n"
    "v_mfma_f32_16x16x32_bf16 v[116:119], v[180:183], v[168:171], v[116:119]\n"
    "ds_read_b64_tr_b16 v[180:181], v249 offset:1024\n"
    "ds_read_b64_tr_b16 v[182:183], v249 offset:1280\n"
    "buffer_load_dwordx4 v10, s[48:51], s64 offen offset:1024 nt lds\n"
    "s_waitcnt lgkmcnt(6)\n"
    "v_mfma_f32_16x16x32_bf16 v[120:123], v[184:187], v[168:171], v[120:123]\n"
    "ds_read_b64_tr_b16 v[184:185], v248 offset:1536\n"
    "ds_read_b64_tr_b16 v[186:187], v248 offset:1792\n"
    "s_add_u32 m0, s73, s55\n"
    "s_add_u32 s64, s86, 0x6000\n"
    "buffer_load_dwordx4 v7, s[20:23], s64 offen nt lds\n"
    "s_add_u32 s60, s98, s79\n"
    "s_add_u32 s60, s60, 0\n"
    "s_and_b32 s61, s60, 12\n"
    "s_and_b32 s60, s60, 3\n"
    "s_waitcnt lgkmcnt(6)\n"
    "v_mfma_f32_16x16x32_bf16 v[124:127], v[188:191], v[168:171], v[124:127]\n"
    "ds_read_b64_tr_b16 v[188:189], v249 offset:1536\n"
    "ds_read_b64_tr_b16 v[190:191], v249 offset:1792\n"
    "s_waitcnt lgkmcnt(6)\n"
    "v_mfma_f32_16x16x32_bf16 v[128:131], v[176:179], v[168:171], v[128:131]\n"
    "buffer_load_dwordx4 v8, s[20:23], s64 offen offset:1024 nt lds\n"
    "s_lshl1_add_u32 s60, s61, s60\n"
    "s_mul_i32 s61, s60, 0xa000\n"
    "s_lshl_b32 s60, s60, 10\n"
    "s_add_u32 s96, s60, s81\n"
    "s_waitcnt lgkmcnt(4)\n"
    "v_mfma_f32_16x16x32_bf16 v[132:135], v[180:183], v[168:171], v[132:135]\n"
    "v_add_u32 v224, s52, v225\n"
    "s_waitcnt vmcnt(6)\n"
    "ds_write_b128 v224, v[216:219]\n"
    "ds_write_b128 v224, v[220:223] offset:1024\n"
    "s_waitcnt lgkmcnt(4)\n"
    "v_mfma_f32_16x16x32_bf16 v[136:139], v[184:187], v[168:171], v[136:139]\n"
    "s_add_u32 s97, s61, s75\n"
    "s_cmp_lt_u32 s79, 16\n"
    "s_cselect_b32 s96, s96, 0x7ff00000\n"
    "s_cselect_b32 s97, s97, 0x7ff00000\n"
    "s_waitcnt lgkmcnt(2)\n"
    "v_mfma_f32_16x16x32_bf16 v[140:143], v[188:191], v[168:171], v[140:143]\n"
    "s_add_u32 s64, s86, 0x8000\n"
    "buffer_load_dwordx4 v[216:219], v7, s[20:23], s64 offen nt\n"
    "s_add_u32 s76, s76, 1\n"
    "s_cmp_eq_u32 s76, 8\n"
    "s_cbranch_scc1 .Lkdump_118\n"
    ".Lkdret_119:\n"
    "buffer_load_dwordx4 v[220:223], v8, s[20:23], s64 offen offset:1024 nt\n"
    "s_add_u32 s60, s80, 0\n"
    "s_and_b32 s60, s60, 3\n"
    "s_mul_i32 s60, s60, 0x50000\n"
    "s_add_u32 s60, s60, 0x28000\n"
    "s_add_u32 s28, s60, s75\n"
    "s_add_u32 s60, s80, 1\n"
    "s_and_b32 s60, s60, 3\n"
    "s_mul_i32 s60, s60, 0x50000\n"
    "s_add_u32 s60, s60, 0x28000\n"
    "s_add_u32 s29, s60, s75\n"
    "s_add_u32 s60, s80, 2\n"
    "s_and_b32 s60, s60, 3\n"
    "s_mul_i32 s60, s60, 0x50000\n"
    "s_add_u32 s60, s60, 0x28000\n"
    "s_add_u32 s30, s60, s75\n"
    "s_add_u32 s60, s80, 3\n"
    "s_and_b32 s60, s60, 3\n"
    "s_mul_i32 s60, s60, 0x50000\n"
    "s_add_u32 s60, s60, 0x28000\n"
    "s_add_u32 s31, s60, s75\n"
    "s_add_u32 s67, s56, 0x8000\n"
    "s_sub_u32 s52, s56, 0xe000\n"
    "s_cselect_b32 s52, s67, s52\n"
    "s_add_u32 s67, s57, 0x8000\n"
    "s_sub_u32 s53, s57, 0xe000\n"
    "s_cselect_b32 s53, s67, s53\n"
    "s_add_u32 s67, s58, 0x8000\n"
    "s_sub_u32 s54, s58, 0xe000\n"
    "s_cselect_b32 s54, s67, s54\n"
    "s_add_u32 s67, s59, 0x8000\n"
    "s_sub_u32 s55, s59, 0xe000\n"
    "s_cselect_b32 s55, s67, s55\n"
    "s_add_u32 s15, s15, 1\n"
    "s_add_u32 s86, s86, 0x4000\n"
    "s_waitcnt vmcnt(8) lgkmcnt(0)\n"
    "s_setprio 0\n"
    "s_barrier\n"
    "s_branch .Lks18_0_75\n"
    ".Lks18_0_75:\n"
    "s_cmp_eq_u32 s88, 1\n"
    "s_cselect_b32 s85, s53, s52\n"
    "v_add_u32 v248, s85, v236\n"
    "v_add_u32 v249, s85, v237\n"
    "ds_read_b128 v[176:179], v248\n"
    "ds_read_b128 v[180:183], v249\n"
    "ds_read_b128 v[184:187], v248 offset:512\n"
    "ds_read_b128 v[188:191], v249 offset:512\n"
    "s_setprio 0\n"
    "s_add_u32 m0, s73, 0x1e800\n"
    "s_add_u32 s61, s97, 0\n"
    "buffer_load_dwordx4 v4, s[32:35], s61 offen lds\n"
    "s_waitcnt lgkmcnt(3)\n"
    "v_mfma_f32_16x16x32_bf16 v[160:163], v[176:179], v[144:147], v[172:175]\n"
    "ds_read_b128 v[176:179], v248 offset:1024\n"
    "buffer_load_dwordx4 v5, s[32:35], s61 offen offset:1024 lds\n"
    "s_waitcnt lgkmcnt(3)\n"
    "v_mfma_f32_16x16x32_bf16 v[164:167], v[180:183], v[144:147], v[172:175]\n"
    "ds_read_b128 v[180:183], v249 offset:1024\n"
    "s_add_u32 m0, s73, 0x20800\n"
    "s_add_u32 s61, s97, 0xa000\n"
    "buffer_load_dwordx4 v4, s[32:35], s61 offen lds\n"
    "s_waitcnt lgkmcnt(3)\n"
    "v_mfma_f32_16x16x32_bf16 v[160:163], v[184:187], v[148:151], v[160:163]\n"
    "ds_read_b128 v[184:187], v248 offset:1536\n"
    "buffer_load_dwordx4 v5, s[32:35], s61 offen offset:1024 lds\n"
    "s_waitcnt lgkmcnt(3)\n"
    "v_mfma_f32_16x16x32_bf16 v[164:167], v[188:191], v[148:151], v[164:167]\n"
    "ds_read_b128 v[188:191], v249 offset:1536\n"
    "s_add_u32 m0, s73, s57\n"
    "s_add_u32 s64, s86, 0xa000\n"
    "buffer_load_dwordx4 v2, s[16:19], s64 offen nt lds\n"
    "s_waitcnt lgkmcnt(3)\n"
    "v_mfma_f32_16x16x32_bf16 v[160:163], v[176:179], v[152:155], v[160:163]\n"
    "buffer_load_dwordx4 v3, s[16:19], s64 offen offset:1024 nt lds\n"
    "s_waitcnt lgkmcnt(2)\n"
    "v_mfma_f32_16x16x32_bf16 v[164:167], v[180:183], v[152:155], v[164:167]\n"
    "s_waitcnt lgkmcnt(1)\n"
    "v_mfma_f32_16x16x32_bf16 v[160:163], v[184:187], v[156:159], v[160:163]\n"
    "s_add_u32 m0, s73, s58\n"
    "s_add_u32 s64, s86, s87\n"
    "buffer_load_dwordx4 v9, s[48:51], s64 offen nt lds\n"
    "s_waitcnt lgkmcnt(0)\n"
    "v_mfma_f32_16x16x32_bf16 v[164:167], v[188:191], v[156:159], v[164:167]\n"
    "buffer_load_dwordx4 v10, s[48:51], s64 offen offset:1024 nt lds\n"
    "s_add_u32 m0, s73, s59\n"
    "s_add_u32 s64, s86, 0x6000\n"
    "buffer_load_dwordx4 v7, s[20:23], s64 offen nt lds\n"
    "v_max3_f32 v15, v160, v161, v162\n"
    "s_nop 2\n"
    "v_max3_f32 v244, v164, v165, v166\n"
    "buffer_load_dwordx4 v8, s[20:23], s64 offen offset:1024 nt lds\n"
    "v_max3_f32 v15, v15, v163, v167\n"
    "v_max_f32 v15, v15, v244\n"
    "v_cmp_nge_f32 vcc, s70, v15\n"
    "s_nop 0\n"
    "s_cbranch_vccnz .Lkxrare_120\n"
    ".Lkxret_121:\n"
    "v_add_u32 v224, s56, v225\n"
    "s_waitcnt vmcnt(10)\n"
    "ds_write_b128 v224, v[216:219]\n"
    "ds_write_b128 v224, v[220:223] offset:1024\n"
    "v_exp_f32 v160, v160\n"
    "v_exp_f32 v161, v161\n"
    "s_add_u32 s64, s86, 0x8000\n"
    "buffer_load_dwordx4 v[216:219], v7, s[20:23], s64 offen nt\n"
    "v_exp_f32 v162, v162\n"
    "v_exp_f32 v163, v163\n"
    "v_add_f32 v14, v14, v160\n"
    "v_cvt_pk_bf16_f32 v168, v160, v161\n"
    "v_add_f32 v14, v14, v161\n"
    "buffer_load_dwordx4 v[220:223], v8, s[20:23], s64 offen offset:1024 nt\n"
    "s_cmp_gt_u32 s82, s12\n"
    "s_cbranch_scc0 .Lknp_122\n"
    "s_and_b32 s60, s15, 1\n"
    "s_xor_b32 s60, s60, 1\n"
    "s_lshl2_add_u32 s60, s60, 0x27800\n"
    "v_mov_b32 v244, s60\n"
    "v_mov_b32 v245, 0\n"
    "s_mov_b64 exec, 1\n"
    "s_nop 0\n"
    "ds_write_b32 v244, v245\n"
    "s_mov_b64 exec, -1\n"
    "s_mov_b32 s63, 0x1000\n"
    "buffer_load_dwordx2 v[250:251], v13, s[44:47], s63 offen sc1\n"
    ".Lknp_122:\n"
    "s_add_u32 s67, s52, 0x8000\n"
    "s_sub_u32 s56, s52, 0xe000\n"
    "s_cselect_b32 s56, s67, s56\n"
    "s_add_u32 s67, s53, 0x8000\n"
    "s_sub_u32 s57, s53, 0xe000\n"
    "s_cselect_b32 s57, s67, s57\n"
    "s_add_u32 s67, s54, 0x8000\n"
    "s_sub_u32 s58, s54, 0xe000\n"
    "s_cselect_b32 s58, s67, s58\n"
    "s_add_u32 s67, s55, 0x8000\n"
    "s_sub_u32 s59, s55, 0xe000\n"
    "s_cselect_b32 s59, s67, s59\n"
    "s_add_u32 s15, s15, 1\n"
    "s_add_u32 s86, s86, 0x4000\n"
    "s_waitcnt vmcnt(12) lgkmcnt(0)\n"
    "s_setprio 0\n"
    "s_barrier\n"
    "s_branch .Lkpl1_51\n"
    ".Lks18_1_76:\n"
    "s_cmp_eq_u32 s88, 1\n"
    "s_cselect_b32 s85, s59, s58\n"
    "v_add_u32 v248, s85, v0\n"
    "v_xor_b32 v249, 32, v248\n"
    "ds_read_b64_tr_b16 v[176:177], v248\n"
    "ds_read_b64_tr_b16 v[178:179], v248 offset:256\n"
    "ds_read_b64_tr_b16 v[180:181], v249\n"
    "ds_read_b64_tr_b16 v[182:183], v249 offset:256\n"
    "ds_read_b64_tr_b16 v[184:185], v248 offset:512\n"
    "ds_read_b64_tr_b16 v[186:187], v248 offset:768\n"
    "ds_read_b64_tr_b16 v[188:189], v249 offset:512\n"
    "ds_read_b64_tr_b16 v[190:191], v249 offset:768\n"
    "s_setprio 0\n"
    "s_add_u32 m0, s73, 0x1e800\n"
    "s_add_u32 s61, s97, 0\n"
    "buffer_load_dwordx4 v4, s[32:35], s61 offen lds\n"
    "v_exp_f32 v164, v164\n"
    "v_exp_f32 v165, v165\n"
    "v_add_f32 v14, v14, v162\n"
    "v_cvt_pk_bf16_f32 v169, v162, v163\n"
    "v_add_f32 v14, v14, v163\n"
    "buffer_load_dwordx4 v5, s[32:35], s61 offen offset:1024 lds\n"
    "v_exp_f32 v166, v166\n"
    "v_exp_f32 v167, v167\n"
    "v_add_f32 v14, v14, v164\n"
    "v_cvt_pk_bf16_f32 v170, v164, v165\n"
    "v_add_f32 v14, v14, v165\n"
    "s_add_u32 m0, s73, 0x20800\n"
    "s_add_u32 s61, s97, 0xa000\n"
    "buffer_load_dwordx4 v4, s[32:35], s61 offen lds\n"
    "v_add_f32 v14, v14, v166\n"
    "v_cvt_pk_bf16_f32 v171, v166, v167\n"
    "v_add_f32 v14, v14, v167\n"
    "buffer_load_dwordx4 v5, s[32:35], s61 offen offset:1024 lds\n"
    "s_waitcnt lgkmcnt(6)\n"
    "v_mfma_f32_16x16x32_bf16 v[112:115], v[176:179], v[168:171], v[112:115]\n"
    "ds_read_b64_tr_b16 v[176:177], v248 offset:1024\n"
    "ds_read_b64_tr_b16 v[178:179], v248 offset:1280\n"
    "s_add_u32 m0, s73, s53\n"
    "s_add_u32 s64, s86, 0xa000\n"
    "buffer_load_dwordx4 v2, s[16:19], s64 offen nt lds\n"
    "s_waitcnt lgkmcnt(6)\n"
    "v_mfma_f32_16x16x32_bf16 v[116:119], v[180:183], v[168:171], v[116:119]\n"
    "ds_read_b64_tr_b16 v[180:181], v249 offset:1024\n"
    "ds_read_b64_tr_b16 v[182:183], v249 offset:1280\n"
    "buffer_load_dwordx4 v3, s[16:19], s64 offen offset:1024 nt lds\n"
    "s_waitcnt lgkmcnt(6)\n"
    "v_mfma_f32_16x16x32_bf16 v[120:123], v[184:187], v[168:171], v[120:123]\n"
    "ds_read_b64_tr_b16 v[184:185], v248 offset:1536\n"
    "ds_read_b64_tr_b16 v[186:187], v248 offset:1792\n"
    "s_add_u32 m0, s73, s54\n"
    "s_add_u32 s64, s86, s87\n"
    "buffer_load_dwordx4 v9, s[48:51], s64 offen nt lds\n"
    "s_waitcnt lgkmcnt(6)\n"
    "v_mfma_f32_16x16x32_bf16 v[124:127], v[188:191], v[168:171], v[124:127]\n"
    "ds_read_b64_tr_b16 v[188:189], v249 offset:1536\n"
    "ds_read_b64_tr_b16 v[190:191], v249 offset:1792\n"
    "buffer_load_dwordx4 v10, s[48:51], s64 offen offset:1024 nt lds\n"
    "s_waitcnt lgkmcnt(6)\n"
    "v_mfma_f32_16x16x32_bf16 v[128:131], v[176:179], v[168:171], v[128:131]\n"
    "s_add_u32 m0, s73, s55\n"
    "s_add_u32 s64, s86, 0x6000\n"
    "buffer_load_dwordx4 v7, s[20:23], s64 offen nt lds\n"
    "s_waitcnt lgkmcnt(4)\n"
    "v_mfma_f32_16x16x32_bf16 v[132:135], v[180:183], v[168:171], v[132:135]\n"
    "buffer_load_dwordx4 v8, s[20:23], s64 offen offset:1024 nt lds\n"
    "s_waitcnt lgkmcnt(2)\n"
    "v_mfma_f32_16x16x32_bf16 v[136:139], v[184:187], v[168:171], v[136:139]\n"
    "v_add_u32 v224, s52, v225\n"
    "s_waitcnt vmcnt(10)\n"
    "ds_write_b128 v224, v[216:219]\n"
    "ds_write_b128 v224, v[220:223] offset:1024\n"
    "s_waitcnt lgkmcnt(2)\n"
    "v_mfma_f32_16x16x32_bf16 v[140:143], v[188:191], v[168:171], v[140:143]\n"
    "s_add_u32 s64, s86, 0x8000\n"
    "buffer_load_dwordx4 v[216:219], v7, s[20:23], s64 offen nt\n"
    "s_add_u32 s76, s76, 1\n"
    "s_cmp_eq_u32 s76, 8\n"
    "s_cbranch_scc1 .Lkdump_123\n"
    ".Lkdret_124:\n"
    "buffer_load_dwordx4 v[220:223], v8, s[20:23], s64 offen offset:1024 nt\n"
    "s_cmp_gt_u32 s82, s12\n"
    "s_cbranch_scc0 .Lknp_125\n"
    "s_and_b32 s60, s15, 1\n"
    "s_xor_b32 s60, s60, 1\n"
    "s_lshl2_add_u32 s60, s60, 0x27800\n"
    "v_mov_b32 v244, s60\n"
    "v_mov_b32 v245, 0\n"
    "s_mov_b64 exec, 1\n"
    "s_nop 0\n"
    "ds_write_b32 v244, v245\n"
    "s_mov_b64 exec, -1\n"
    "s_mov_b32 s63, 0x1000\n"
    "buffer_load_dwordx2 v[250:251], v13, s[44:47], s63 offen sc1\n"
    ".Lknp_125:\n"
    "s_add_u32 s67, s56, 0x8000\n"
    "s_sub_u32 s52, s56, 0xe000\n"
    "s_cselect_b32 s52, s67, s52\n"
    "s_add_u32 s67, s57, 0x8000\n"
    "s_sub_u32 s53, s57, 0xe000\n"
    "s_cselect_b32 s53, s67, s53\n"
    "s_add_u32 s67, s58, 0x8000\n"
    "s_sub_u32 s54, s58, 0xe000\n"
    "s_cselect_b32 s54, s67, s54\n"
    "s_add_u32 s67, s59, 0x8000\n"
    "s_sub_u32 s55, s59, 0xe000\n"
    "s_cselect_b32 s55, s67, s55\n"
    "s_add_u32 s15, s15, 1\n"
    "s_add_u32 s86, s86, 0x4000\n"
    "s_waitcnt vmcnt(12) lgkmcnt(0)\n"
    "s_setprio 0\n"
    "s_barrier\n"
    "s_branch .Lkpl0_50\n"
    ".Lkd0_54:\n"
    "s_cmp_lg_u32 s15, 29\n"
    "s_cbranch_scc1 .Lkn29_126\n"
    "s_mov_b32 s48, s20\n"
    "s_mov_b32 s49, s21\n"
    "v_mov_b32 v9, v7\n"
    "v_mov_b32 v10, v8\n"
    "s_mov_b32 s87, 0xa000\n"
    ".Lkn29_126:\n"
    "s_cmp_eq_u32 s15, 30\n"
    "s_cbranch_scc1 .Lkp300_56\n"
    "s_cmp_lt_u32 s79, 16\n"
    "s_cselect_b32 s60, s77, 0\n"
    "s_cmp_eq_u32 s60, 1\n"
    "s_cbranch_scc1 .Lko0_52\n"
    "s_cmp_lg_u32 s77, 1\n"
    "s_cbranch_scc1 .Lkpl0_50\n"
    "s_mov_b32 s77, 2\n"
    "s_cmp_eq_u32 s88, 1\n"
    "s_cselect_b32 s85, s53, s52\n"
    "s_add_u32 m0, s73, 0x1e800\n"
    "s_add_u32 s61, s28, 0x1e000\n"
    "buffer_load_dwordx4 v4, s[32:35], s61 offen lds\n"
    "v_add_u32 v248, s85, v236\n"
    "v_add_u32 v249, s85, v237\n"
    "ds_read_b128 v[176:179], v248\n"
    "ds_read_b128 v[180:183], v249\n"
    "ds_read_b128 v[184:187], v248 offset:512\n"
    "ds_read_b128 v[188:191], v249 offset:512\n"
    "s_setprio 0\n"
    "buffer_load_dwordx4 v5, s[32:35], s61 offen offset:1024 lds\n"
    "s_waitcnt lgkmcnt(3)\n"
    "v_mfma_f32_16x16x32_bf16 v[160:163], v[176:179], v[144:147], v[172:175]\n"
    "ds_read_b128 v[176:179], v248 offset:1024\n"
    "s_add_u32 m0, s73, 0x20800\n"
    "s_add_u32 s61, s29, 0\n"
    "buffer_load_dwordx4 v4, s[32:35], s61 offen lds\n"
    "s_waitcnt lgkmcnt(3)\n"
    "v_mfma_f32_16x16x32_bf16 v[164:167], v[180:183], v[144:147], v[172:175]\n"
    "ds_read_b128 v[180:183], v249 offset:1024\n"
    "buffer_load_dwordx4 v5, s[32:35], s61 offen offset:1024 lds\n"
    "s_waitcnt lgkmcnt(3)\n"
    "v_mfma_f32_16x16x32_bf16 v[160:163], v[184:187], v[148:151], v[160:163]\n"
    "ds_read_b128 v[184:187], v248 offset:1536\n"
    "s_add_u32 m0, s73, 0x22800\n"
    "s_add_u32 s61, s29, 0xa000\n"
    "buffer_load_dwordx4 v4, s[32:35], s61 offen lds\n"
    "s_waitcnt lgkmcnt(3)\n"
    "v_mfma_f32_16x16x32_bf16 v[164:167], v[188:191], v[148:151], v[164:167]\n"
    "ds_read_b128 v[188:191], v249 offset:1536\n"
    "buffer_load_dwordx4 v5, s[32:35], s61 offen offset:1024 lds\n"
    "s_waitcnt lgkmcnt(3)\n"
    "v_mfma_f32_16x16x32_bf16 v[160:163], v[176:179], v[152:155], v[160:163]\n"
    "s_add_u32 m0, s73, 0x24800\n"
    "s_add_u32 s61, s29, 0x14000\n"
    "buffer_load_dwordx4 v4, s[32:35], s61 offen lds\n"
    "s_waitcnt lgkmcnt(2)\n"
    "v_mfma_f32_16x16x32_bf16 v[164:167], v[180:183], v[152:155], v[164:167]\n"
    "buffer_load_dwordx4 v5, s[32:35], s61 offen offset:1024 lds\n"
    "s_add_u32 m0, s73, s57\n"
    "s_add_u32 s64, s86, 0xa000\n"
    "buffer_load_dwordx4 v2, s[16:19], s64 offen nt lds\n"
    "s_waitcnt lgkmcnt(1)\n"
    "v_mfma_f32_16x16x32_bf16 v[160:163], v[184:187], v[156:159], v[160:163]\n"
    "buffer_load_dwordx4 v3, s[16:19], s64 offen offset:1024 nt lds\n"
    "s_waitcnt lgkmcnt(0)\n"
    "v_mfma_f32_16x16x32_bf16 v[164:167], v[188:191], v[156:159], v[164:167]\n"
    "s_add_u32 m0, s73, s58\n"
    "s_add_u32 s64, s86, s87\n"
    "buffer_load_dwordx4 v9, s[48:51], s64 offen nt lds\n"
    "buffer_load_dwordx4 v10, s[48:51], s64 offen offset:1024 nt lds\n"
    "s_nop 0\n"
    "v_max3_f32 v15, v160, v161, v162\n"
    "s_nop 1\n"
    "v_max3_f32 v244, v164, v165, v166\n"
    "s_add_u32 m0, s73, s59\n"
    "s_add_u32 s64, s86, 0x6000\n"
    "buffer_load_dwordx4 v7, s[20:23], s64 offen nt lds\n"
    "v_max3_f32 v15, v15, v163, v167\n"
    "v_max_f32 v15, v15, v244\n"
    "v_cmp_nge_f32 vcc, s70, v15\n"
    "s_nop 0\n"
    "s_cbranch_vccnz .Lkxrare_127\n"
    ".Lkxret_128:\n"
    "buffer_load_dwordx4 v8, s[20:23], s64 offen offset:1024 nt lds\n"
    "v_exp_f32 v160, v160\n"
    "v_exp_f32 v161, v161\n"
    "v_add_u32 v224, s56, v225\n"
    "s_waitcnt vmcnt(14)\n"
    "ds_write_b128 v224, v[216:219]\n"
    "ds_write_b128 v224, v[220:223] offset:1024\n"
    "v_exp_f32 v162, v162\n"
    "v_exp_f32 v163, v163\n"
    "v_add_f32 v14, v14, v160\n"
    "v_cvt_pk_bf16_f32 v168, v160, v161\n"
    "v_add_f32 v14, v14, v161\n"
    "s_add_u32 s64, s86, 0x8000\n"
    "buffer_load_dwordx4 v[216:219], v7, s[20:23], s64 offen nt\n"
    "buffer_load_dwordx4 v[220:223], v8, s[20:23], s64 offen offset:1024 nt\n"
    "s_add_u32 s67, s52, 0x8000\n"
    "s_sub_u32 s56, s52, 0xe000\n"
    "s_cselect_b32 s56, s67, s56\n"
    "s_add_u32 s67, s53, 0x8000\n"
    "s_sub_u32 s57, s53, 0xe000\n"
    "s_cselect_b32 s57, s67, s57\n"
    "s_add_u32 s67, s54, 0x8000\n"
    "s_sub_u32 s58, s54, 0xe000\n"
    "s_cselect_b32 s58, s67, s58\n"
    "s_add_u32 s67, s55, 0x8000\n"
    "s_sub_u32 s59, s55, 0xe000\n"
    "s_cselect_b32 s59, s67, s59\n"
    "s_add_u32 s15, s15, 1\n"
    "s_add_u32 s86, s86, 0x4000\n"
    "s_waitcnt vmcnt(16) lgkmcnt(0)\n"
    "s_setprio 0\n"
    "s_barrier\n"
    "s_branch .Lkd1_55\n"
    ".Lkpl0_50:\n"
    "v_mov_b32 v244, 0x27800\n"
    "ds_read_b64 v[246:247], v244\n"
    "s_cmp_eq_u32 s88, 1\n"
    "s_cselect_b32 s85, s53, s52\n"
    "v_add_u32 v248, s85, v236\n"
    "v_add_u32 v249, s85, v237\n"
    "ds_read_b128 v[176:179], v248\n"
    "ds_read_b128 v[180:183], v249\n"
    "ds_read_b128 v[184:187], v248 offset:512\n"
    "ds_read_b128 v[188:191], v249 offset:512\n"
    "s_setprio 0\n"
    "s_cmp_eq_u32 s83, 1\n"
    "s_cbranch_scc1 .Lkss_129\n"
    ".Lkssr_130:\n"
    "s_waitcnt lgkmcnt(3)\n"
    "v_mfma_f32_16x16x32_bf16 v[160:163], v[176:179], v[144:147], v[172:175]\n"
    "ds_read_b128 v[176:179], v248 offset:1024\n"
    "s_add_u32 m0, s73, s57\n"
    "s_add_u32 s64, s86, 0xa000\n"
    "buffer_load_dwordx4 v2, s[16:19], s64 offen nt lds\n"
    "s_waitcnt lgkmcnt(3)\n"
    "v_mfma_f32_16x16x32_bf16 v[164:167], v[180:183], v[144:147], v[172:175]\n"
    "ds_read_b128 v[180:183], v249 offset:1024\n"
    "s_waitcnt lgkmcnt(3)\n"
    "v_mfma_f32_16x16x32_bf16 v[160:163], v[184:187], v[148:151], v[160:163]\n"
    "ds_read_b128 v[184:187], v248 offset:1536\n"
    "buffer_load_dwordx4 v3, s[16:19], s64 offen offset:1024 nt lds\n"
    "s_waitcnt lgkmcnt(3)\n"
    "v_mfma_f32_16x16x32_bf16 v[164:167], v[188:191], v[148:151], v[164:167]\n"
    "ds_read_b128 v[188:191], v249 offset:1536\n"
    "s_add_u32 m0, s73, s58\n"
    "s_add_u32 s64, s86, s87\n"
    "buffer_load_dwordx4 v9, s[48:51], s64 offen nt lds\n"
    "s_waitcnt lgkmcnt(3)\n"
    "v_mfma_f32_16x16x32_bf16 v[160:163], v[176:179], v[152:155], v[160:163]\n"
    "s_waitcnt lgkmcnt(2)\n"
    "v_mfma_f32_16x16x32_bf16 v[164:167], v[180:183], v[152:155], v[164:167]\n"
    "buffer_load_dwordx4 v10, s[48:51], s64 offen offset:1024 nt lds\n"
    "s_waitcnt lgkmcnt(1)\n"
    "v_mfma_f32_16x16x32_bf16 v[160:163], v[184:187], v[156:159], v[160:163]\n"
    "s_add_u32 m0, s73, s59\n"
    "s_add_u32 s64, s86, 0x6000\n"
    "buffer_load_dwordx4 v7, s[20:23], s64 offen nt lds\n"
    "s_waitcnt lgkmcnt(0)\n"
    "v_mfma_f32_16x16x32_bf16 v[164:167], v[188:191], v[156:159], v[164:167]\n"
    "buffer_load_dwordx4 v8, s[20:23], s64 offen offset:1024 nt lds\n"
    "s_nop 1\n"
    "v_max3_f32 v15, v160, v161, v162\n"
    "s_nop 3\n"
    "v_max3_f32 v244, v164, v165, v166\n"
    "v_add_u32 v224, s56, v225\n"
    "s_waitcnt vmcnt(6)\n"
    "ds_write_b128 v224, v[216:219]\n"
    "ds_write_b128 v224, v[220:223] offset:1024\n"
    "v_max3_f32 v15, v15, v163, v167\n"
    "v_max_f32 v15, v15, v244\n"
    "v_cmp_nge_f32 vcc, s70, v15\n"
    "s_nop 0\n"
    "s_cbranch_vccnz .Lkxrare_133\n"
    ".Lkxret_134:\n"
    "v_exp_f32 v160, v160\n"
    "v_exp_f32 v161, v161\n"
    "s_add_u32 s64, s86, 0x8000\n"
    "buffer_load_dwordx4 v[216:219], v7, s[20:23], s64 offen nt\n"
    "v_exp_f32 v162, v162\n"
    "v_exp_f32 v163, v163\n"
    "v_add_f32 v14, v14, v160\n"
    "v_cvt_pk_bf16_f32 v168, v160, v161\n"
    "v_add_f32 v14, v14, v161\n"
    "buffer_load_dwordx4 v[220:223], v8, s[20:23], s64 offen offset:1024 nt\n"
    "s_cmp_lg_u32 s83, 0\n"
    "s_cbranch_scc1 .Lkse_131\n"
    ".Lkser_132:\n"
    "s_add_u32 s67, s52, 0x8000\n"
    "s_sub_u32 s56, s52, 0xe000\n"
    "s_cselect_b32 s56, s67, s56\n"
    "s_add_u32 s67, s53, 0x8000\n"
    "s_sub_u32 s57, s53, 0xe000\n"
    "s_cselect_b32 s57, s67, s57\n"
    "s_add_u32 s67, s54, 0x8000\n"
    "s_sub_u32 s58, s54, 0xe000\n"
    "s_cselect_b32 s58, s67, s58\n"
    "s_add_u32 s67, s55, 0x8000\n"
    "s_sub_u32 s59, s55, 0xe000\n"
    "s_cselect_b32 s59, s67, s59\n"
    "s_add_u32 s15, s15, 1\n"
    "s_add_u32 s86, s86, 0x4000\n"
    "s_waitcnt vmcnt(8) lgkmcnt(0)\n"
    "s_cmp_lt_u32 s15, 29\n"
    "s_setprio 0\n"
    "s_barrier\n"
    "s_cbranch_scc1 .Lkpl1_51\n"
    "s_branch .Lkd1_55\n"
    ".Lkd1_55:\n"
    "s_cmp_lg_u32 s15, 29\n"
    "s_cbranch_scc1 .Lkn29_135\n"
    "s_mov_b32 s48, s20\n"
    "s_mov_b32 s49, s21\n"
    "v_mov_b32 v9, v7\n"
    "v_mov_b32 v10, v8\n"
    "s_mov_b32 s87, 0xa000\n"
    ".Lkn29_135:\n"
    "s_cmp_eq_u32 s15, 30\n"
    "s_cbranch_scc1 .Lkp301_57\n"
    "s_cmp_lt_u32 s79, 16\n"
    "s_cselect_b32 s60, s77, 0\n"
    "s_cmp_eq_u32 s60, 1\n"
    "s_cbranch_scc1 .Lko1_53\n"
    "s_cmp_lg_u32 s77, 1\n"
    "s_cbranch_scc1 .Lkpl1_51\n"
    "s_mov_b32 s77, 2\n"
    "s_cmp_eq_u32 s88, 1\n"
    "s_cselect_b32 s85, s59, s58\n"
    "s_add_u32 m0, s73, 0x1e800\n"
    "s_add_u32 s61, s28, 0x1e000\n"
    "buffer_load_dwordx4 v4, s[32:35], s61 offen lds\n"
    "v_add_u32 v248, s85, v0\n"
    "v_xor_b32 v249, 32, v248\n"
    "ds_read_b64_tr_b16 v[176:177], v248\n"
    "ds_read_b64_tr_b16 v[178:179], v248 offset:256\n"
    "ds_read_b64_tr_b16 v[180:181], v249\n"
    "ds_read_b64_tr_b16 v[182:183], v249 offset:256\n"
    "ds_read_b64_tr_b16 v[184:185], v248 offset:512\n"
    "ds_read_b64_tr_b16 v[186:187], v248 offset:768\n"
    "ds_read_b64_tr_b16 v[188:189], v249 offset:512\n"
    "ds_read_b64_tr_b16 v[190:191], v249 offset:768\n"
    "s_setprio 0\n"
    "buffer_load_dwordx4 v5, s[32:35], s61 offen offset:1024 lds\n"
    "v_exp_f32 v164, v164\n"
    "v_exp_f32 v165, v165\n"
    "v_add_f32 v14, v14, v162\n"
    "v_cvt_pk_bf16_f32 v169, v162, v163\n"
    "v_add_f32 v14, v14, v163\n"
    "s_add_u32 m0, s73, 0x20800\n"
    "s_add_u32 s61, s29, 0\n"
    "buffer_load_dwordx4 v4, s[32:35], s61 offen lds\n"
    "v_exp_f32 v166, v166\n"
    "v_exp_f32 v167, v167\n"
    "v_add_f32 v14, v14, v164\n"
    "v_cvt_pk_bf16_f32 v170, v164, v165\n"
    "v_add_f32 v14, v14, v165\n"
    "buffer_load_dwordx4 v5, s[32:35], s61 offen offset:1024 lds\n"
    "v_add_f32 v14, v14, v166\n"
    "v_cvt_pk_bf16_f32 v171, v166, v167\n"
    "v_add_f32 v14, v14, v167\n"
    "s_add_u32 m0, s73, 0x22800\n"
    "s_add_u32 s61, s29, 0xa000\n"
    "buffer_load_dwordx4 v4, s[32:35], s61 offen lds\n"
    "buffer_load_dwordx4 v5, s[32:35], s61 offen offset:1024 lds\n"
    "s_waitcnt lgkmcnt(6)\n"
    "v_mfma_f32_16x16x32_bf16 v[112:115], v[176:179], v[168:171], v[112:115]\n"
    "ds_read_b64_tr_b16 v[176:177], v248 offset:1024\n"
    "ds_read_b64_tr_b16 v[178:179], v248 offset:1280\n"
    "s_add_u32 m0, s73, 0x24800\n"
    "s_add_u32 s61, s29, 0x14000\n"
    "buffer_load_dwordx4 v4, s[32:35], s61 offen lds\n"
    "s_waitcnt lgkmcnt(6)\n"
    "v_mfma_f32_16x16x32_bf16 v[116:119], v[180:183], v[168:171], v[116:119]\n"
    "ds_read_b64_tr_b16 v[180:181], v249 offset:1024\n"
    "ds_read_b64_tr_b16 v[182:183], v249 offset:1280\n"
    "buffer_load_dwordx4 v5, s[32:35], s61 offen offset:1024 lds\n"
    "s_waitcnt lgkmcnt(6)\n"
    "v_mfma_f32_16x16x32_bf16 v[120:123], v[184:187], v[168:171], v[120:123]\n"
    "ds_read_b64_tr_b16 v[184:185], v248 offset:1536\n"
    "ds_read_b64_tr_b16 v[186:187], v248 offset:1792\n"
    "s_add_u32 m0, s73, s53\n"
    "s_add_u32 s64, s86, 0xa000\n"
    "buffer_load_dwordx4 v2, s[16:19], s64 offen nt lds\n"
    "s_waitcnt lgkmcnt(6)\n"
    "v_mfma_f32_16x16x32_bf16 v[124:127], v[188:191], v[168:171], v[124:127]\n"
    "ds_read_b64_tr_b16 v[188:189], v249 offset:1536\n"
    "ds_read_b64_tr_b16 v[190:191], v249 offset:1792\n"
    "buffer_load_dwordx4 v3, s[16:19], s64 offen offset:1024 nt lds\n"
    "s_waitcnt lgkmcnt(6)\n"
    "v_mfma_f32_16x16x32_bf16 v[128:131], v[176:179], v[168:171], v[128:131]\n"
    "s_add_u32 m0, s73, s54\n"
    "s_add_u32 s64, s86, s87\n"
    "buffer_load_dwordx4 v9, s[48:51], s64 offen nt lds\n"
    "buffer_load_dwordx4 v10, s[48:51], s64 offen offset:1024 nt lds\n"
    "s_waitcnt lgkmcnt(4)\n"
    "v_mfma_f32_16x16x32_bf16 v[132:135], v[180:183], v[168:171], v[132:135]\n"
    "s_add_u32 m0, s73, s55\n"
    "s_add_u32 s64, s86, 0x6000\n"
    "buffer_load_dwordx4 v7, s[20:23], s64 offen nt lds\n"
    "s_waitcnt lgkmcnt(2)\n"
    "v_mfma_f32_16x16x32_bf16 v[136:139], v[184:187], v[168:171], v[136:139]\n"
    "buffer_load_dwordx4 v8, s[20:23], s64 offen offset:1024 nt lds\n"
    "s_waitcnt lgkmcnt(0)\n"
    "v_mfma_f32_16x16x32_bf16 v[140:143], v[188:191], v[168:171], v[140:143]\n"
    "v_add_u32 v224, s52, v225\n"
    "s_waitcnt vmcnt(14)\n"
    "ds_write_b128 v224, v[216:219]\n"
    "ds_write_b128 v224, v[220:223] offset:1024\n"
    "s_add_u32 s76, s76, 1\n"
    "s_cmp_eq_u32 s76, 8\n"
    "s_cbranch_scc1 .Lkdump_136\n"
    ".Lkdret_137:\n"
    "s_add_u32 s64, s86, 0x8000\n"
    "buffer_load_dwordx4 v[216:219], v7, s[20:23], s64 offen nt\n"
    "buffer_load_dwordx4 v[220:223], v8, s[20:23], s64 offen offset:1024 nt\n"
    "s_add_u32 s67, s56, 0x8000\n"
    "s_sub_u32 s52, s56, 0xe000\n"
    "s_cselect_b32 s52, s67, s52\n"
    "s_add_u32 s67, s57, 0x8000\n"
    "s_sub_u32 s53, s57, 0xe000\n"
    "s_cselect_b32 s53, s67, s53\n"
    "s_add_u32 s67, s58, 0x8000\n"
    "s_sub_u32 s54, s58, 0xe000\n"
    "s_cselect_b32 s54, s67, s54\n"
    "s_add_u32 s67, s59, 0x8000\n"
    "s_sub_u32 s55, s59, 0xe000\n"
    "s_cselect_b32 s55, s67, s55\n"
    "s_add_u32 s15, s15, 1\n"
    "s_add_u32 s86, s86, 0x4000\n"
    "s_waitcnt vmcnt(16) lgkmcnt(0)\n"
    "s_setprio 0\n"
    "s_barrier\n"
    "s_branch .Lkd0_54\n"
    ".Lkpl1_51:\n"
    "v_mov_b32 v244, 0x27800\n"
    "ds_read_b64 v[246:247], v244\n"
    "s_cmp_eq_u32 s88, 1\n"
    "s_cselect_b32 s85, s59, s58\n"
    "v_add_u32 v248, s85, v0\n"
    "v_xor_b32 v249, 32, v248\n"
    "ds_read_b64_tr_b16 v[176:177], v248\n"
    "ds_read_b64_tr_b16 v[178:179], v248 offset:256\n"
    "ds_read_b64_tr_b16 v[180:181], v249\n"
    "ds_read_b64_tr_b16 v[182:183], v249 offset:256\n"
    "ds_read_b64_tr_b16 v[184:185], v248 offset:512\n"
    "ds_read_b64_tr_b16 v[186:187], v248 offset:768\n"
    "ds_read_b64_tr_b16 v[188:189], v249 offset:512\n"
    "ds_read_b64_tr_b16 v[190:191], v249 offset:768\n"
    "s_setprio 0\n"
    "s_cmp_eq_u32 s83, 1\n"
    "s_cbranch_scc1 .Lkss_138\n"
    ".Lkssr_139:\n"
    "v_exp_f32 v164, v164\n"
    "v_exp_f32 v165, v165\n"
    "v_add_f32 v14, v14, v162\n"
    "v_cvt_pk_bf16_f32 v169, v162, v163\n"
    "v_add_f32 v14, v14, v163\n"
    "s_add_u32 m0, s73, s53\n"
    "s_add_u32 s64, s86, 0xa000\n"
    "buffer_load_dwordx4 v2, s[16:19], s64 offen nt lds\n"
    "v_exp_f32 v166, v166\n"
    "v_exp_f32 v167, v167\n"
    "v_add_f32 v14, v14, v164\n"
    "v_cvt_pk_bf16_f32 v170, v164, v165\n"
    "v_add_f32 v14, v14, v165\n"
    "buffer_load_dwordx4 v3, s[16:19], s64 offen offset:1024 nt lds\n"
    "v_add_f32 v14, v14, v166\n"
    "v_cvt_pk_bf16_f32 v171, v166, v167\n"
    "v_add_f32 v14, v14, v167\n"
    "s_waitcnt lgkmcnt(6)\n"
    "v_mfma_f32_16x16x32_bf16 v[112:115], v[176:179], v[168:171], v[112:115]\n"
    "ds_read_b64_tr_b16 v[176:177], v248 offset:1024\n"
    "ds_read_b64_tr_b16 v[178:179], v248 offset:1280\n"
    "s_add_u32 m0, s73, s54\n"
    "s_add_u32 s64, s86, s87\n"
    "buffer_load_dwordx4 v9, s[48:51], s64 offen nt lds\n"
    "s_waitcnt lgkmcnt(6)\n"
    "v_mfma_f32_16x16x32_bf16 v[116:119], v[180:183], v[168:171], v[116:119]\n"
    "ds_read_b64_tr_b16 v[180:181], v249 offset:1024\n"
    "ds_read_b64_tr_b16 v[182:183], v249 offset:1280\n"
    "buffer_load_dwordx4 v10, s[48:51], s64 offen offset:1024 nt lds\n"
    "s_waitcnt lgkmcnt(6)\n"
    "v_mfma_f32_16x16x32_bf16 v[120:123], v[184:187], v[168:171], v[120:123]\n"
    "ds_read_b64_tr_b16 v[184:185], v248 offset:1536\n"
    "ds_read_b64_tr_b16 v[186:187], v248 offset:1792\n"
    "s_waitcnt lgkmcnt(6)\n"
    "v_mfma_f32_16x16x32_bf16 v[124:127], v[188:191], v[168:171], v[124:127]\n"
    "ds_read_b64_tr_b16 v[188:189], v249 offset:1536\n"
    "ds_read_b64_tr_b16 v[190:191], v249 offset:1792\n"
    "s_add_u32 m0, s73, s55\n"
    "s_add_u32 s64, s86, 0x6000\n"
    "buffer_load_dwordx4 v7, s[20:23], s64 offen nt lds\n"
    "s_waitcnt lgkmcnt(6)\n"
    "v_mfma_f32_16x16x32_bf16 v[128:131], v[176:179], v[168:171], v[128:131]\n"
    "buffer_load_dwordx4 v8, s[20:23], s64 offen offset:1024 nt lds\n"
    "s_waitcnt lgkmcnt(4)\n"
    "v_mfma_f32_16x16x32_bf16 v[132:135], v[180:183], v[168:171], v[132:135]\n"
    "s_waitcnt lgkmcnt(2)\n"
    "v_mfma_f32_16x16x32_bf16 v[136:139], v[184:187], v[168:171], v[136:139]\n"
    "v_add_u32 v224, s52, v225\n"
    "s_waitcnt vmcnt(6)\n"
    "ds_write_b128 v224, v[216:219]\n"
    "ds_write_b128 v224, v[220:223] offset:1024\n"
    "s_waitcnt lgkmcnt(2)\n"
    "v_mfma_f32_16x16x32_bf16 v[140:143], v[188:191], v[168:171], v[140:143]\n"
    "s_add_u32 s64, s86, 0x8000\n"
    "buffer_load_dwordx4 v[216:219], v7, s[20:23], s64 offen nt\n"
    "s_add_u32 s76, s76, 1\n"
    "s_cmp_eq_u32 s76, 8\n"
    "s_cbranch_scc1 .Lkdump_142\n"
    ".Lkdret_143:\n"
    "buffer_load_dwordx4 v[220:223], v8, s[20:23], s64 offen offset:1024 nt\n"
    "s_cmp_lg_u32 s83, 0\n"
    "s_cbranch_scc1 .Lkse_140\n"
    ".Lkser_141:\n"
    "s_add_u32 s67, s56, 0x8000\n"
    "s_sub_u32 s52, s56, 0xe000\n"
    "s_cselect_b32 s52, s67, s52\n"
    "s_add_u32 s67, s57, 0x8000\n"
    "s_sub_u32 s53, s57, 0xe000\n"
    "s_cselect_b32 s53, s67, s53\n"
    "s_add_u32 s67, s58, 0x8000\n"
    "s_sub_u32 s54, s58, 0xe000\n"
    "s_cselect_b32 s54, s67, s54\n"
    "s_add_u32 s67, s59, 0x8000\n"
    "s_sub_u32 s55, s59, 0xe000\n"
    "s_cselect_b32 s55, s67, s55\n"
    "s_add_u32 s15, s15, 1\n"
    "s_add_u32 s86, s86, 0x4000\n"
    "s_waitcnt vmcnt(8) lgkmcnt(0)\n"
    "s_cmp_lt_u32 s15, 29\n"
    "s_setprio 0\n"
    "s_barrier\n"
    "s_cbranch_scc1 .Lkpl0_50\n"
    "s_branch .Lkd0_54\n"
    ".Lko0_52:\n"
    "v_add_u32 v234, s74, v238\n"
    "v_add_u32 v235, s74, v239\n"
    "ds_read_b64_tr_b16 v[176:177], v234\n"
    "ds_read_b64_tr_b16 v[178:179], v234 offset:4096\n"
    "ds_read_b64_tr_b16 v[180:181], v235\n"
    "ds_read_b64_tr_b16 v[182:183], v235 offset:4096\n"
    "ds_read_b64_tr_b16 v[184:185], v234 offset:512\n"
    "ds_read_b64_tr_b16 v[186:187], v234 offset:4608\n"
    "s_cmp_eq_u32 s88, 1\n"
    "s_cselect_b32 s85, s53, s52\n"
    "v_add_u32 v248, s85, v236\n"
    "v_add_u32 v249, s85, v237\n"
    "ds_read_b128 v[228:231], v248\n"
    "ds_read_b128 v[240:243], v249\n"
    "s_waitcnt vmcnt(11)\n"
    "s_setprio 1\n"
    "s_waitcnt lgkmcnt(4)\n"
    "v_mfma_f32_16x16x32_bf16 v[16:19], v[176:179], v[192:195], v[16:19]\n"
    "ds_read_b64_tr_b16 v[188:189], v235 offset:512\n"
    "ds_read_b64_tr_b16 v[190:191], v235 offset:4608\n"
    "v_mfma_f32_16x16x32_bf16 v[48:51], v[176:179], v[196:199], v[48:51]\n"
    "s_add_u32 s60, s98, s79\n"
    "s_add_u32 s60, s60, 2\n"
    "v_mfma_f32_16x16x32_bf16 v[80:83], v[176:179], v[200:203], v[80:83]\n"
    "s_and_b32 s61, s60, 12\n"
    "s_and_b32 s60, s60, 3\n"
    "v_mfma_f32_16x16x32_bf16 v[20:23], v[180:183], v[192:195], v[20:23]\n"
    "ds_read_b64_tr_b16 v[176:177], v234 offset:1024\n"
    "ds_read_b64_tr_b16 v[178:179], v234 offset:5120\n"
    "v_mfma_f32_16x16x32_bf16 v[52:55], v[180:183], v[196:199], v[52:55]\n"
    "s_lshl1_add_u32 s60, s61, s60\n"
    "s_mul_i32 s61, s60, 0xa000\n"
    "v_mfma_f32_16x16x32_bf16 v[84:87], v[180:183], v[200:203], v[84:87]\n"
    "s_lshl_b32 s60, s60, 10\n"
    "s_add_u32 s96, s60, s81\n"
    "s_waitcnt lgkmcnt(5)\n"
    "v_mfma_f32_16x16x32_bf16 v[24:27], v[184:187], v[192:195], v[24:27]\n"
    "ds_read_b64_tr_b16 v[180:181], v235 offset:1024\n"
    "ds_read_b64_tr_b16 v[182:183], v235 offset:5120\n"
    "v_mfma_f32_16x16x32_bf16 v[56:59], v[184:187], v[196:199], v[56:59]\n"
    "v_mfma_f32_16x16x32_bf16 v[88:91], v[184:187], v[200:203], v[88:91]\n"
    "v_mfma_f32_16x16x32_bf16 v[160:163], v[228:231], v[144:147], v[172:175]\n"
    "ds_read_b128 v[228:231], v248 offset:512\n"
    "s_add_u32 s97, s61, s75\n"
    "s_cmp_lt_u32 s79, 14\n"
    "s_cselect_b32 s96, s96, 0x7ff00000\n"
    "s_cselect_b32 s97, s97, 0x7ff00000\n"
    "s_waitcnt lgkmcnt(5)\n"
    "v_mfma_f32_16x16x32_bf16 v[28:31], v[188:191], v[192:195], v[28:31]\n"
    "ds_read_b64_tr_b16 v[184:185], v234 offset:1536\n"
    "ds_read_b64_tr_b16 v[186:187], v234 offset:5632\n"
    "v_mfma_f32_16x16x32_bf16 v[60:63], v[188:191], v[196:199], v[60:63]\n"
    "v_mfma_f32_16x16x32_bf16 v[92:95], v[188:191], v[200:203], v[92:95]\n"
    "v_mfma_f32_16x16x32_bf16 v[164:167], v[240:243], v[144:147], v[172:175]\n"
    "ds_read_b128 v[240:243], v249 offset:512\n"
    "s_xor_b32 s63, s74, 0x3c000\n"
    "s_add_u32 m0, s73, s63\n"
    "s_add_u32 s61, s97, 0\n"
    "buffer_load_dwordx4 v4, s[32:35], s61 offen lds\n"
    "s_waitcnt lgkmcnt(3)\n"
    "v_mfma_f32_16x16x32_bf16 v[32:35], v[176:179], v[192:195], v[32:35]\n"
    "ds_read_b64_tr_b16 v[188:189], v235 offset:1536\n"
    "ds_read_b64_tr_b16 v[190:191], v235 offset:5632\n"
    "v_mfma_f32_16x16x32_bf16 v[64:67], v[176:179], v[196:199], v[64:67]\n"
    "v_mfma_f32_16x16x32_bf16 v[96:99], v[176:179], v[200:203], v[96:99]\n"
    "v_mfma_f32_16x16x32_bf16 v[160:163], v[228:231], v[148:151], v[160:163]\n"
    "ds_read_b128 v[228:231], v248 offset:1024\n"
    "buffer_load_dwordx4 v5, s[32:35], s61 offen offset:1024 lds\n"
    "v_mfma_f32_16x16x32_bf16 v[36:39], v[180:183], v[192:195], v[36:39]\n"
    "ds_read_b64_tr_b16 v[176:177], v234 offset:8192\n"
    "ds_read_b64_tr_b16 v[178:179], v234 offset:12288\n"
    "v_mfma_f32_16x16x32_bf16 v[68:71], v[180:183], v[196:199], v[68:71]\n"
    "v_mfma_f32_16x16x32_bf16 v[100:103], v[180:183], v[200:203], v[100:103]\n"
    "s_waitcnt lgkmcnt(5)\n"
    "v_mfma_f32_16x16x32_bf16 v[164:167], v[240:243], v[148:151], v[164:167]\n"
    "ds_read_b128 v[240:243], v249 offset:1024\n"
    "s_add_u32 s63, s63, 0x2000\n"
    "s_add_u32 m0, s73, s63\n"
    "s_add_u32 s61, s97, 0xa000\n"
    "buffer_load_dwordx4 v4, s[32:35], s61 offen lds\n"
    "buffer_load_dwordx4 v5, s[32:35], s61 offen offset:1024 lds\n"
    "v_mfma_f32_16x16x32_bf16 v[40:43], v[184:187], v[192:195], v[40:43]\n"
    "ds_read_b64_tr_b16 v[180:181], v235 offset:8192\n"
    "ds_read_b64_tr_b16 v[182:183], v235 offset:12288\n"
    "v_mfma_f32_16x16x32_bf16 v[72:75], v[184:187], v[196:199], v[72:75]\n"
    "v_mfma_f32_16x16x32_bf16 v[104:107], v[184:187], v[200:203], v[104:107]\n"
    "s_waitcnt lgkmcnt(5)\n"
    "v_mfma_f32_16x16x32_bf16 v[160:163], v[228:231], v[152:155], v[160:163]\n"
    "ds_read_b128 v[228:231], v248 offset:1536\n"
    "v_mfma_f32_16x16x32_bf16 v[44:47], v[188:191], v[192:195], v[44:47]\n"
    "ds_read_b64_tr_b16 v[184:185], v234 offset:8704\n"
    "ds_read_b64_tr_b16 v[186:187], v234 offset:12800\n"
    "v_mfma_f32_16x16x32_bf16 v[76:79], v[188:191], v[196:199], v[76:79]\n"
    "v_mfma_f32_16x16x32_bf16 v[108:111], v[188:191], v[200:203], v[108:111]\n"
    "s_waitcnt lgkmcnt(5)\n"
    "v_mfma_f32_16x16x32_bf16 v[164:167], v[240:243], v[152:155], v[164:167]\n"
    "ds_read_b128 v[240:243], v249 offset:1536\n"
    "s_waitcnt vmcnt(4)\n"
    "buffer_load_dwordx4 v[192:195], v11, s[36:39], s96 offen sc1\n"
    "v_mfma_f32_16x16x32_bf16 v[16:19], v[176:179], v[204:207], v[16:19]\n"
    "ds_read_b64_tr_b16 v[188:189], v235 offset:8704\n"
    "ds_read_b64_tr_b16 v[190:191], v235 offset:12800\n"
    "v_mfma_f32_16x16x32_bf16 v[48:51], v[176:179], v[208:211], v[48:51]\n"
    "buffer_load_dwordx4 v[196:199], v6, s[36:39], s96 offen sc1\n"
    "buffer_load_dwordx4 v[200:203], v12, s[36:39], s96 offen sc1\n"
    "v_mfma_f32_16x16x32_bf16 v[80:83], v[176:179], v[212:215], v[80:83]\n"
    "ds_read_b64_tr_b16 v[176:177], v234 offset:9216\n"
    "ds_read_b64_tr_b16 v[178:179], v234 offset:13312\n"
    "s_waitcnt lgkmcnt(7)\n"
    "v_mfma_f32_16x16x32_bf16 v[20:23], v[180:183], v[204:207], v[20:23]\n"
    "s_add_u32 m0, s73, s57\n"
    "s_add_u32 s64, s86, 0xa000\n"
    "v_mfma_f32_16x16x32_bf16 v[52:55], v[180:183], v[208:211], v[52:55]\n"
    "buffer_load_dwordx4 v2, s[16:19], s64 offen nt lds\n"
    "v_mfma_f32_16x16x32_bf16 v[84:87], v[180:183], v[212:215], v[84:87]\n"
    "ds_read_b64_tr_b16 v[180:181], v235 offset:9216\n"
    "ds_read_b64_tr_b16 v[182:183], v235 offset:13312\n"
    "v_mfma_f32_16x16x32_bf16 v[160:163], v[228:231], v[156:159], v[160:163]\n"
    "buffer_load_dwordx4 v3, s[16:19], s64 offen offset:1024 nt lds\n"
    "s_add_u32 m0, s73, s58\n"
    "s_waitcnt lgkmcnt(6)\n"
    "v_mfma_f32_16x16x32_bf16 v[24:27], v[184:187], v[204:207], v[24:27]\n"
    "s_add_u32 s64, s86, s87\n"
    "buffer_load_dwordx4 v9, s[48:51], s64 offen nt lds\n"
    "v_mfma_f32_16x16x32_bf16 v[56:59], v[184:187], v[208:211], v[56:59]\n"
    "v_mfma_f32_16x16x32_bf16 v[88:91], v[184:187], v[212:215], v[88:91]\n"
    "ds_read_b64_tr_b16 v[184:185], v234 offset:9728\n"
    "ds_read_b64_tr_b16 v[186:187], v234 offset:13824\n"
    "v_mfma_f32_16x16x32_bf16 v[164:167], v[240:243], v[156:159], v[164:167]\n"
    "buffer_load_dwordx4 v10, s[48:51], s64 offen offset:1024 nt lds\n"
    "s_waitcnt lgkmcnt(4)\n"
    "v_mfma_f32_16x16x32_bf16 v[28:31], v[188:191], v[204:207], v[28:31]\n"
    "v_mfma_f32_16x16x32_bf16 v[60:63], v[188:191], v[208:211], v[60:63]\n"
    "v_mfma_f32_16x16x32_bf16 v[92:95], v[188:191], v[212:215], v[92:95]\n"
    "ds_read_b64_tr_b16 v[188:189], v235 offset:9728\n"
    "ds_read_b64_tr_b16 v[190:191], v235 offset:13824\n"
    "v_mfma_f32_16x16x32_bf16 v[32:35], v[176:179], v[204:207], v[32:35]\n"
    "v_max3_f32 v15, v160, v161, v162\n"
    "v_max3_f32 v244, v164, v165, v166\n"
    "v_mfma_f32_16x16x32_bf16 v[64:67], v[176:179], v[208:211], v[64:67]\n"
    "s_add_u32 m0, s73, s59\n"
    "s_add_u32 s64, s86, 0x6000\n"
    "v_mfma_f32_16x16x32_bf16 v[96:99], v[176:179], v[212:215], v[96:99]\n"
    "buffer_load_dwordx4 v7, s[20:23], s64 offen nt lds\n"
    "buffer_load_dwordx4 v8, s[20:23], s64 offen offset:1024 nt lds\n"
    "s_waitcnt lgkmcnt(2)\n"
    "v_mfma_f32_16x16x32_bf16 v[36:39], v[180:183], v[204:207], v[36:39]\n"
    "v_max3_f32 v15, v15, v163, v167\n"
    "v_max_f32 v15, v15, v244\n"
    "v_mfma_f32_16x16x32_bf16 v[68:71], v[180:183], v[208:211], v[68:71]\n"
    "v_cmp_nge_f32 vcc, s70, v15\n"
    "v_mfma_f32_16x16x32_bf16 v[100:103], v[180:183], v[212:215], v[100:103]\n"
    "s_cbranch_vccnz .Lkxrare_144\n"
    ".Lkxret_145:\n"
    "v_add_u32 v224, s56, v225\n"
    "ds_write_b128 v224, v[216:219]\n"
    "ds_write_b128 v224, v[220:223] offset:1024\n"
    "v_mfma_f32_16x16x32_bf16 v[40:43], v[184:187], v[204:207], v[40:43]\n"
    "v_exp_f32 v160, v160\n"
    "v_exp_f32 v161, v161\n"
    "v_mfma_f32_16x16x32_bf16 v[72:75], v[184:187], v[208:211], v[72:75]\n"
    "s_add_u32 s64, s86, 0x8000\n"
    "buffer_load_dwordx4 v[216:219], v7, s[20:23], s64 offen nt\n"
    "v_mfma_f32_16x16x32_bf16 v[104:107], v[184:187], v[212:215], v[104:107]\n"
    "buffer_load_dwordx4 v[220:223], v8, s[20:23], s64 offen offset:1024 nt\n"
    "s_waitcnt lgkmcnt(2)\n"
    "v_mfma_f32_16x16x32_bf16 v[44:47], v[188:191], v[204:207], v[44:47]\n"
    "v_exp_f32 v162, v162\n"
    "v_exp_f32 v163, v163\n"
    "v_mfma_f32_16x16x32_bf16 v[76:79], v[188:191], v[208:211], v[76:79]\n"
    "v_add_f32 v14, v14, v160\n"
    "v_cvt_pk_bf16_f32 v168, v160, v161\n"
    "v_mfma_f32_16x16x32_bf16 v[108:111], v[188:191], v[212:215], v[108:111]\n"
    "v_add_f32 v14, v14, v161\n"
    "s_add_u32 s67, s52, 0x8000\n"
    "s_sub_u32 s56, s52, 0xe000\n"
    "s_cselect_b32 s56, s67, s56\n"
    "s_add_u32 s67, s53, 0x8000\n"
    "s_sub_u32 s57, s53, 0xe000\n"
    "s_cselect_b32 s57, s67, s57\n"
    "s_add_u32 s67, s54, 0x8000\n"
    "s_sub_u32 s58, s54, 0xe000\n"
    "s_cselect_b32 s58, s67, s58\n"
    "s_add_u32 s67, s55, 0x8000\n"
    "s_sub_u32 s59, s55, 0xe000\n"
    "s_cselect_b32 s59, s67, s59\n"
    "s_add_u32 s15, s15, 1\n"
    "s_add_u32 s86, s86, 0x4000\n"
    "buffer_load_dwordx4 v[204:207], v11, s[36:39], s96 offen offset:1024 sc1\n"
    "buffer_load_dwordx4 v[208:211], v6, s[36:39], s96 offen offset:1024 sc1\n"
    "buffer_load_dwordx4 v[212:215], v12, s[36:39], s96 offen offset:1024 sc1\n"
    "s_add_u32 s79, s79, 2\n"
    "s_xor_b32 s74, s74, 0x3c000\n"
    "s_waitcnt vmcnt(14) lgkmcnt(0)\n"
    "s_cmp_lt_u32 s79, 16\n"
    "s_cselect_b32 s60, s15, 0x63\n"
    "s_cmp_lt_u32 s60, 29\n"
    "s_setprio 0\n"
    "s_barrier\n"
    "s_cbranch_scc1 .Lko1_53\n"
    "s_branch .Lkd1_55\n"
    ".Lko1_53:\n"
    "v_add_u32 v234, s74, v238\n"
    "v_add_u32 v235, s74, v239\n"
    "ds_read_b64_tr_b16 v[176:177], v234\n"
    "ds_read_b64_tr_b16 v[178:179], v234 offset:4096\n"
    "ds_read_b64_tr_b16 v[180:181], v235\n"
    "ds_read_b64_tr_b16 v[182:183], v235 offset:4096\n"
    "ds_read_b64_tr_b16 v[184:185], v234 offset:512\n"
    "ds_read_b64_tr_b16 v[186:187], v234 offset:4608\n"
    "s_cmp_eq_u32 s88, 1\n"
    "s_cselect_b32 s85, s59, s58\n"
    "v_add_u32 v248, s85, v0\n"
    "v_xor_b32 v249, 32, v248\n"
    "ds_read_b64_tr_b16 v[228:229], v248\n"
    "ds_read_b64_tr_b16 v[230:231], v248 offset:256\n"
    "ds_read_b64_tr_b16 v[240:241], v249\n"
    "ds_read_b64_tr_b16 v[242:243], v249 offset:256\n"
    "s_waitcnt vmcnt(11)\n"
    "s_setprio 1\n"
    "s_waitcnt lgkmcnt(6)\n"
    "v_mfma_f32_16x16x32_bf16 v[16:19], v[176:179], v[192:195], v[16:19]\n"
    "ds_read_b64_tr_b16 v[188:189], v235 offset:512\n"
    "ds_read_b64_tr_b16 v[190:191], v235 offset:4608\n"
    "v_mfma_f32_16x16x32_bf16 v[48:51], v[176:179], v[196:199], v[48:51]\n"
    "s_add_u32 s60, s98, s79\n"
    "s_add_u32 s60, s60, 2\n"
    "v_mfma_f32_16x16x32_bf16 v[80:83], v[176:179], v[200:203], v[80:83]\n"
    "s_and_b32 s61, s60, 12\n"
    "s_and_b32 s60, s60, 3\n"
    "v_mfma_f32_16x16x32_bf16 v[20:23], v[180:183], v[192:195], v[20:23]\n"
    "ds_read_b64_tr_b16 v[176:177], v234 offset:1024\n"
    "ds_read_b64_tr_b16 v[178:179], v234 offset:5120\n"
    "v_mfma_f32_16x16x32_bf16 v[52:55], v[180:183], v[196:199], v[52:55]\n"
    "s_lshl1_add_u32 s60, s61, s60\n"
    "s_mul_i32 s61, s60, 0xa000\n"
    "v_mfma_f32_16x16x32_bf16 v[84:87], v[180:183], v[200:203], v[84:87]\n"
    "s_lshl_b32 s60, s60, 10\n"
    "s_add_u32 s96, s60, s81\n"
    "s_waitcnt lgkmcnt(2)\n"
    "v_mfma_f32_16x16x32_bf16 v[24:27], v[184:187], v[192:195], v[24:27]\n"
    "ds_read_b64_tr_b16 v[180:181], v235 offset:1024\n"
    "ds_read_b64_tr_b16 v[182:183], v235 offset:5120\n"
    "v_mfma_f32_16x16x32_bf16 v[56:59], v[184:187], v[196:199], v[56:59]\n"
    "v_exp_f32 v164, v164\n"
    "v_exp_f32 v165, v165\n"
    "v_mfma_f32_16x16x32_bf16 v[88:91], v[184:187], v[200:203], v[88:91]\n"
    "v_add_f32 v14, v14, v162\n"
    "v_cvt_pk_bf16_f32 v169, v162, v163\n"
    "v_add_f32 v14, v14, v163\n"
    "s_add_u32 s97, s61, s75\n"
    "s_cmp_lt_u32 s79, 14\n"
    "s_cselect_b32 s96, s96, 0x7ff00000\n"
    "s_cselect_b32 s97, s97, 0x7ff00000\n"
    "v_mfma_f32_16x16x32_bf16 v[28:31], v[188:191], v[192:195], v[28:31]\n"
    "ds_read_b64_tr_b16 v[184:185], v234 offset:1536\n"
    "ds_read_b64_tr_b16 v[186:187], v234 offset:5632\n"
    "v_mfma_f32_16x16x32_bf16 v[60:63], v[188:191], v[196:199], v[60:63]\n"
    "v_exp_f32 v166, v166\n"
    "v_exp_f32 v167, v167\n"
    "v_mfma_f32_16x16x32_bf16 v[92:95], v[188:191], v[200:203], v[92:95]\n"
    "v_add_f32 v14, v14, v164\n"
    "v_cvt_pk_bf16_f32 v170, v164, v165\n"
    "v_add_f32 v14, v14, v165\n"
    "s_xor_b32 s63, s74, 0x3c000\n"
    "s_add_u32 m0, s73, s63\n"
    "s_add_u32 s61, s97, 0\n"
    "buffer_load_dwordx4 v4, s[32:35], s61 offen lds\n"
    "s_waitcnt lgkmcnt(2)\n"
    "v_mfma_f32_16x16x32_bf16 v[32:35], v[176:179], v[192:195], v[32:35]\n"
    "ds_read_b64_tr_b16 v[188:189], v235 offset:1536\n"
    "ds_read_b64_tr_b16 v[190:191], v235 offset:5632\n"
    "v_mfma_f32_16x16x32_bf16 v[64:67], v[176:179], v[196:199], v[64:67]\n"
    "v_add_f32 v14, v14, v166\n"
    "v_cvt_pk_bf16_f32 v171, v166, v167\n"
    "v_mfma_f32_16x16x32_bf16 v[96:99], v[176:179], v[200:203], v[96:99]\n"
    "v_add_f32 v14, v14, v167\n"
    "buffer_load_dwordx4 v5, s[32:35], s61 offen offset:1024 lds\n"
    "v_mfma_f32_16x16x32_bf16 v[36:39], v[180:183], v[192:195], v[36:39]\n"
    "ds_read_b64_tr_b16 v[176:177], v234 offset:8192\n"
    "ds_read_b64_tr_b16 v[178:179], v234 offset:12288\n"
    "v_mfma_f32_16x16x32_bf16 v[68:71], v[180:183], v[196:199], v[68:71]\n"
    "v_mfma_f32_16x16x32_bf16 v[100:103], v[180:183], v[200:203], v[100:103]\n"
    "v_mfma_f32_16x16x32_bf16 v[112:115], v[228:231], v[168:171], v[112:115]\n"
    "ds_read_b64_tr_b16 v[228:229], v248 offset:512\n"
    "ds_read_b64_tr_b16 v[230:231], v248 offset:768\n"
    "s_add_u32 s63, s63, 0x2000\n"
    "s_add_u32 m0, s73, s63\n"
    "s_add_u32 s61, s97, 0xa000\n"
    "buffer_load_dwordx4 v4, s[32:35], s61 offen lds\n"
    "buffer_load_dwordx4 v5, s[32:35], s61 offen offset:1024 lds\n"
    "s_waitcnt lgkmcnt(4)\n"
    "v_mfma_f32_16x16x32_bf16 v[40:43], v[184:187], v[192:195], v[40:43]\n"
    "ds_read_b64_tr_b16 v[180:181], v235 offset:8192\n"
    "ds_read_b64_tr_b16 v[182:183], v235 offset:12288\n"
    "v_mfma_f32_16x16x32_bf16 v[72:75], v[184:187], v[196:199], v[72:75]\n"
    "v_mfma_f32_16x16x32_bf16 v[104:107], v[184:187], v[200:203], v[104:107]\n"
    "ds_read_b64_tr_b16 v[184:185], v234 offset:8704\n"
    "ds_read_b64_tr_b16 v[186:187], v234 offset:12800\n"
    "v_mfma_f32_16x16x32_bf16 v[44:47], v[188:191], v[192:195], v[44:47]\n"
    "v_mfma_f32_16x16x32_bf16 v[76:79], v[188:191], v[196:199], v[76:79]\n"
    "v_mfma_f32_16x16x32_bf16 v[108:111], v[188:191], v[200:203], v[108:111]\n"
    "v_mfma_f32_16x16x32_bf16 v[116:119], v[240:243], v[168:171], v[116:119]\n"
    "ds_read_b64_tr_b16 v[240:241], v249 offset:512\n"
    "ds_read_b64_tr_b16 v[242:243], v249 offset:768\n"
    "s_waitcnt vmcnt(4)\n"
    "buffer_load_dwordx4 v[192:195], v11, s[36:39], s96 offen sc1\n"
    "s_waitcnt lgkmcnt(6)\n"
    "v_mfma_f32_16x16x32_bf16 v[16:19], v[176:179], v[204:207], v[16:19]\n"
    "ds_read_b64_tr_b16 v[188:189], v235 offset:8704\n"
    "ds_read_b64_tr_b16 v[190:191], v235 offset:12800\n"
    "v_mfma_f32_16x16x32_bf16 v[48:51], v[176:179], v[208:211], v[48:51]\n"
    "v_mfma_f32_16x16x32_bf16 v[80:83], v[176:179], v[212:215], v[80:83]\n"
    "v_mfma_f32_16x16x32_bf16 v[120:123], v[228:231], v[168:171], v[120:123]\n"
    "ds_read_b64_tr_b16 v[228:229], v248 offset:1024\n"
    "ds_read_b64_tr_b16 v[230:231], v248 offset:1280\n"
    "buffer_load_dwordx4 v[196:199], v6, s[36:39], s96 offen sc1\n"
    "buffer_load_dwordx4 v[200:203], v12, s[36:39], s96 offen sc1\n"
    "s_waitcnt lgkmcnt(4)\n"
    "v_mfma_f32_16x16x32_bf16 v[20:23], v[180:183], v[204:207], v[20:23]\n"
    "ds_read_b64_tr_b16 v[176:177], v234 offset:9216\n"
    "ds_read_b64_tr_b16 v[178:179], v234 offset:13312\n"
    "v_mfma_f32_16x16x32_bf16 v[52:55], v[180:183], v[208:211], v[52:55]\n"
    "v_mfma_f32_16x16x32_bf16 v[84:87], v[180:183], v[212:215], v[84:87]\n"
    "v_mfma_f32_16x16x32_bf16 v[124:127], v[240:243], v[168:171], v[124:127]\n"
    "ds_read_b64_tr_b16 v[240:241], v249 offset:1024\n"
    "ds_read_b64_tr_b16 v[242:243], v249 offset:1280\n"
    "s_add_u32 m0, s73, s53\n"
    "s_add_u32 s64, s86, 0xa000\n"
    "buffer_load_dwordx4 v2, s[16:19], s64 offen nt lds\n"
    "v_mfma_f32_16x16x32_bf16 v[24:27], v[184:187], v[204:207], v[24:27]\n"
    "ds_read_b64_tr_b16 v[180:181], v235 offset:9216\n"
    "ds_read_b64_tr_b16 v[182:183], v235 offset:13312\n"
    "v_mfma_f32_16x16x32_bf16 v[56:59], v[184:187], v[208:211], v[56:59]\n"
    "v_mfma_f32_16x16x32_bf16 v[88:91], v[184:187], v[212:215], v[88:91]\n"
    "s_waitcnt lgkmcnt(6)\n"
    "v_mfma_f32_16x16x32_bf16 v[128:131], v[228:231], v[168:171], v[128:131]\n"
    "ds_read_b64_tr_b16 v[228:229], v248 offset:1536\n"
    "ds_read_b64_tr_b16 v[230:231], v248 offset:1792\n"
    "buffer_load_dwordx4 v3, s[16:19], s64 offen offset:1024 nt lds\n"
    "s_add_u32 m0, s73, s54\n"
    "s_add_u32 s64, s86, s87\n"
    "buffer_load_dwordx4 v9, s[48:51], s64 offen nt lds\n"
    "v_mfma_f32_16x16x32_bf16 v[28:31], v[188:191], v[204:207], v[28:31]\n"
    "ds_read_b64_tr_b16 v[184:185], v234 offset:9728\n"
    "ds_read_b64_tr_b16 v[186:187], v234 offset:13824\n"
    "v_mfma_f32_16x16x32_bf16 v[60:63], v[188:191], v[208:211], v[60:63]\n"
    "buffer_load_dwordx4 v10, s[48:51], s64 offen offset:1024 nt lds\n"
    "v_mfma_f32_16x16x32_bf16 v[92:95], v[188:191], v[212:215], v[92:95]\n"
    "ds_read_b64_tr_b16 v[188:189], v235 offset:9728\n"
    "ds_read_b64_tr_b16 v[190:191], v235 offset:13824\n"
    "s_waitcnt lgkmcnt(8)\n"
    "v_mfma_f32_16x16x32_bf16 v[32:35], v[176:179], v[204:207], v[32:35]\n"
    "v_mfma_f32_16x16x32_bf16 v[64:67], v[176:179], v[208:211], v[64:67]\n"
    "v_mfma_f32_16x16x32_bf16 v[96:99], v[176:179], v[212:215], v[96:99]\n"
    "v_mfma_f32_16x16x32_bf16 v[132:135], v[240:243], v[168:171], v[132:135]\n"
    "ds_read_b64_tr_b16 v[240:241], v249 offset:1536\n"
    "ds_read_b64_tr_b16 v[242:243], v249 offset:1792\n"
    "s_add_u32 m0, s73, s55\n"
    "s_add_u32 s64, s86, 0x6000\n"
    "buffer_load_dwordx4 v7, s[20:23], s64 offen nt lds\n"
    "buffer_load_dwordx4 v8, s[20:23], s64 offen offset:1024 nt lds\n"
    "s_waitcnt lgkmcnt(6)\n"
    "v_mfma_f32_16x16x32_bf16 v[36:39], v[180:183], v[204:207], v[36:39]\n"
    "v_add_u32 v224, s52, v225\n"
    "v_mfma_f32_16x16x32_bf16 v[68:71], v[180:183], v[208:211], v[68:71]\n"
    "v_mfma_f32_16x16x32_bf16 v[100:103], v[180:183], v[212:215], v[100:103]\n"
    "v_mfma_f32_16x16x32_bf16 v[136:139], v[228:231], v[168:171], v[136:139]\n"
    "ds_write_b128 v224, v[216:219]\n"
    "ds_write_b128 v224, v[220:223] offset:1024\n"
    "s_waitcnt lgkmcnt(2)\n"
    "v_mfma_f32_16x16x32_bf16 v[40:43], v[184:187], v[204:207], v[40:43]\n"
    "s_add_u32 s64, s86, 0x8000\n"
    "buffer_load_dwordx4 v[216:219], v7, s[20:23], s64 offen nt\n"
    "v_mfma_f32_16x16x32_bf16 v[72:75], v[184:187], v[208:211], v[72:75]\n"
    "buffer_load_dwordx4 v[220:223], v8, s[20:23], s64 offen offset:1024 nt\n"
    "v_mfma_f32_16x16x32_bf16 v[104:107], v[184:187], v[212:215], v[104:107]\n"
    "s_add_u32 s76, s76, 1\n"
    "s_cmp_eq_u32 s76, 8\n"
    "v_mfma_f32_16x16x32_bf16 v[140:143], v[240:243], v[168:171], v[140:143]\n"
    "v_mfma_f32_16x16x32_bf16 v[44:47], v[188:191], v[204:207], v[44:47]\n"
    "v_mfma_f32_16x16x32_bf16 v[76:79], v[188:191], v[208:211], v[76:79]\n"
    "v_mfma_f32_16x16x32_bf16 v[108:111], v[188:191], v[212:215], v[108:111]\n"
    "s_cbranch_scc1 .Lkdump_146\n"
    ".Lkdret_147:\n"
    "s_add_u32 s67, s56, 0x8000\n"
    "s_sub_u32 s52, s56, 0xe000\n"
    "s_cselect_b32 s52, s67, s52\n"
    "s_add_u32 s67, s57, 0x8000\n"
    "s_sub_u32 s53, s57, 0xe000\n"
    "s_cselect_b32 s53, s67, s53\n"
    "s_add_u32 s67, s58, 0x8000\n"
    "s_sub_u32 s54, s58, 0xe000\n"
    "s_cselect_b32 s54, s67, s54\n"
    "s_add_u32 s67, s59, 0x8000\n"
    "s_sub_u32 s55, s59, 0xe000\n"
    "s_cselect_b32 s55, s67, s55\n"
    "s_add_u32 s15, s15, 1\n"
    "s_add_u32 s86, s86, 0x4000\n"
    "buffer_load_dwordx4 v[204:207], v11, s[36:39], s96 offen offset:1024 sc1\n"
    "buffer_load_dwordx4 v[208:211], v6, s[36:39], s96 offen offset:1024 sc1\n"
    "buffer_load_dwordx4 v[212:215], v12, s[36:39], s96 offen offset:1024 sc1\n"
    "s_add_u32 s79, s79, 2\n"
    "s_xor_b32 s74, s74, 0x3c000\n"
    "s_waitcnt vmcnt(14) lgkmcnt(0)\n"
    "s_cmp_lt_u32 s79, 16\n"
    "s_cselect_b32 s60, s15, 0x63\n"
    "s_cmp_lt_u32 s60, 29\n"
    "s_setprio 0\n"
    "s_barrier\n"
    "s_cbranch_scc1 .Lko0_52\n"
    "s_branch .Lkd0_54\n"
    ".Lkp300_56:\n"
    "s_cmp_eq_u32 s88, 1\n"
    "s_cselect_b32 s85, s53, s52\n"
    "v_add_u32 v248, s85, v236\n"
    "v_add_u32 v249, s85, v237\n"
    "ds_read_b128 v[176:179], v248\n"
    "ds_read_b128 v[180:183], v249\n"
    "ds_read_b128 v[184:187], v248 offset:512\n"
    "ds_read_b128 v[188:191], v249 offset:512\n"
    "s_setprio 0\n"
    "s_waitcnt lgkmcnt(3)\n"
    "v_mfma_f32_16x16x32_bf16 v[160:163], v[176:179], v[144:147], v[172:175]\n"
    "ds_read_b128 v[176:179], v248 offset:1024\n"
    "s_add_u32 m0, s73, 0xe000\n"
    "s_add_u32 s61, s28, 0\n"
    "buffer_load_dwordx4 v4, s[32:35], s61 offen lds\n"
    "s_waitcnt lgkmcnt(3)\n"
    "v_mfma_f32_16x16x32_bf16 v[164:167], v[180:183], v[144:147], v[172:175]\n"
    "ds_read_b128 v[180:183], v249 offset:1024\n"
    "s_waitcnt lgkmcnt(3)\n"
    "v_mfma_f32_16x16x32_bf16 v[160:163], v[184:187], v[148:151], v[160:163]\n"
    "ds_read_b128 v[184:187], v248 offset:1536\n"
    "buffer_load_dwordx4 v5, s[32:35], s61 offen offset:1024 lds\n"
    "s_waitcnt lgkmcnt(3)\n"
    "v_mfma_f32_16x16x32_bf16 v[164:167], v[188:191], v[148:151], v[164:167]\n"
    "ds_read_b128 v[188:191], v249 offset:1536\n"
    "s_waitcnt lgkmcnt(3)\n"
    "v_mfma_f32_16x16x32_bf16 v[160:163], v[176:179], v[152:155], v[160:163]\n"
    "s_add_u32 m0, s73, 0x10000\n"
    "s_add_u32 s61, s28, 0xa000\n"
    "buffer_load_dwordx4 v4, s[32:35], s61 offen lds\n"
    "s_waitcnt lgkmcnt(2)\n"
    "v_mfma_f32_16x16x32_bf16 v[164:167], v[180:183], v[152:155], v[164:167]\n"
    "s_waitcnt lgkmcnt(1)\n"
    "v_mfma_f32_16x16x32_bf16 v[160:163], v[184:187], v[156:159], v[160:163]\n"
    "buffer_load_dwordx4 v5, s[32:35], s61 offen offset:1024 lds\n"
    "s_waitcnt lgkmcnt(0)\n"
    "v_mfma_f32_16x16x32_bf16 v[164:167], v[188:191], v[156:159], v[164:167]\n"
    "s_add_u32 m0, s73, 0x12000\n"
    "s_add_u32 s61, s28, 0x14000\n"
    "buffer_load_dwordx4 v4, s[32:35], s61 offen lds\n"
    "s_nop 1\n"
    "v_max3_f32 v15, v160, v161, v162\n"
    "s_nop 1\n"
    "v_max3_f32 v244, v164, v165, v166\n"
    "v_max3_f32 v15, v15, v163, v167\n"
    "v_max_f32 v15, v15, v244\n"
    "v_cmp_nge_f32 vcc, s70, v15\n"
    "s_nop 0\n"
    "s_cbranch_vccnz .Lkxrare_148\n"
    ".Lkxret_149:\n"
    "buffer_load_dwordx4 v5, s[32:35], s61 offen offset:1024 lds\n"
    "v_exp_f32 v160, v160\n"
    "v_exp_f32 v161, v161\n"
    "v_exp_f32 v162, v162\n"
    "v_exp_f32 v163, v163\n"
    "v_add_f32 v14, v14, v160\n"
    "v_cvt_pk_bf16_f32 v168, v160, v161\n"
    "v_add_f32 v14, v14, v161\n"
    "v_add_u32 v224, s56, v225\n"
    "s_waitcnt vmcnt(6)\n"
    "ds_write_b128 v224, v[216:219]\n"
    "ds_write_b128 v224, v[220:223] offset:1024\n"
    "s_cmp_lt_u32 s79, 16\n"
    "s_cbranch_scc1 .Lkns1_150\n"
    "s_cmp_eq_u32 s77, 2\n"
    "s_cbranch_scc1 .Lkns1_150\n"
    "s_add_u32 m0, s73, 0x1e800\n"
    "s_add_u32 s61, s28, 0x1e000\n"
    "buffer_load_dwordx4 v4, s[32:35], s61 offen lds\n"
    "buffer_load_dwordx4 v5, s[32:35], s61 offen offset:1024 lds\n"
    "s_add_u32 m0, s73, 0x20800\n"
    "s_add_u32 s61, s29, 0\n"
    "buffer_load_dwordx4 v4, s[32:35], s61 offen lds\n"
    "buffer_load_dwordx4 v5, s[32:35], s61 offen offset:1024 lds\n"
    "s_add_u32 m0, s73, 0x22800\n"
    "s_add_u32 s61, s29, 0xa000\n"
    "buffer_load_dwordx4 v4, s[32:35], s61 offen lds\n"
    "buffer_load_dwordx4 v5, s[32:35], s61 offen offset:1024 lds\n"
    "s_add_u32 m0, s73, 0x24800\n"
    "s_add_u32 s61, s29, 0x14000\n"
    "buffer_load_dwordx4 v4, s[32:35], s61 offen lds\n"
    "buffer_load_dwordx4 v5, s[32:35], s61 offen offset:1024 lds\n"
    ".Lkns1_150:\n"
    "s_add_u32 s67, s52, 0x8000\n"
    "s_sub_u32 s56, s52, 0xe000\n"
    "s_cselect_b32 s56, s67, s56\n"
    "s_add_u32 s67, s53, 0x8000\n"
    "s_sub_u32 s57, s53, 0xe000\n"
    "s_cselect_b32 s57, s67, s57\n"
    "s_add_u32 s67, s54, 0x8000\n"
    "s_sub_u32 s58, s54, 0xe000\n"
    "s_cselect_b32 s58, s67, s58\n"
    "s_add_u32 s67, s55, 0x8000\n"
    "s_sub_u32 s59, s55, 0xe000\n"
    "s_cselect_b32 s59, s67, s59\n"
    "s_add_u32 s15, s15, 1\n"
    "s_add_u32 s86, s86, 0x4000\n"
    "s_waitcnt lgkmcnt(0)\n"
    "s_setprio 0\n"
    "s_barrier\n"
    "s_branch .Lkp311_59\n"
    ".Lkp301_57:\n"
    "s_cmp_eq_u32 s88, 1\n"
    "s_cselect_b32 s85, s59, s58\n"
    "v_add_u32 v248, s85, v0\n"
    "v_xor_b32 v249, 32, v248\n"
    "ds_read_b64_tr_b16 v[176:177], v248\n"
    "ds_read_b64_tr_b16 v[178:179], v248 offset:256\n"
    "ds_read_b64_tr_b16 v[180:181], v249\n"
    "ds_read_b64_tr_b16 v[182:183], v249 offset:256\n"
    "ds_read_b64_tr_b16 v[184:185], v248 offset:512\n"
    "ds_read_b64_tr_b16 v[186:187], v248 offset:768\n"
    "ds_read_b64_tr_b16 v[188:189], v249 offset:512\n"
    "ds_read_b64_tr_b16 v[190:191], v249 offset:768\n"
    "s_setprio 0\n"
    "s_add_u32 m0, s73, 0xe000\n"
    "s_add_u32 s61, s28, 0\n"
    "buffer_load_dwordx4 v4, s[32:35], s61 offen lds\n"
    "v_exp_f32 v164, v164\n"
    "v_exp_f32 v165, v165\n"
    "v_add_f32 v14, v14, v162\n"
    "v_cvt_pk_bf16_f32 v169, v162, v163\n"
    "v_add_f32 v14, v14, v163\n"
    "v_exp_f32 v166, v166\n"
    "v_exp_f32 v167, v167\n"
    "v_add_f32 v14, v14, v164\n"
    "v_cvt_pk_bf16_f32 v170, v164, v165\n"
    "v_add_f32 v14, v14, v165\n"
    "buffer_load_dwordx4 v5, s[32:35], s61 offen offset:1024 lds\n"
    "v_add_f32 v14, v14, v166\n"
    "v_cvt_pk_bf16_f32 v171, v166, v167\n"
    "v_add_f32 v14, v14, v167\n"
    "s_waitcnt lgkmcnt(6)\n"
    "v_mfma_f32_16x16x32_bf16 v[112:115], v[176:179], v[168:171], v[112:115]\n"
    "ds_read_b64_tr_b16 v[176:177], v248 offset:1024\n"
    "ds_read_b64_tr_b16 v[178:179], v248 offset:1280\n"
    "s_add_u32 m0, s73, 0x10000\n"
    "s_add_u32 s61, s28, 0xa000\n"
    "buffer_load_dwordx4 v4, s[32:35], s61 offen lds\n"
    "s_waitcnt lgkmcnt(6)\n"
    "v_mfma_f32_16x16x32_bf16 v[116:119], v[180:183], v[168:171], v[116:119]\n"
    "ds_read_b64_tr_b16 v[180:181], v249 offset:1024\n"
    "ds_read_b64_tr_b16 v[182:183], v249 offset:1280\n"
    "s_waitcnt lgkmcnt(6)\n"
    "v_mfma_f32_16x16x32_bf16 v[120:123], v[184:187], v[168:171], v[120:123]\n"
    "ds_read_b64_tr_b16 v[184:185], v248 offset:1536\n"
    "ds_read_b64_tr_b16 v[186:187], v248 offset:1792\n"
    "buffer_load_dwordx4 v5, s[32:35], s61 offen offset:1024 lds\n"
    "s_waitcnt lgkmcnt(6)\n"
    "v_mfma_f32_16x16x32_bf16 v[124:127], v[188:191], v[168:171], v[124:127]\n"
    "ds_read_b64_tr_b16 v[188:189], v249 offset:1536\n"
    "ds_read_b64_tr_b16 v[190:191], v249 offset:1792\n"
    "s_waitcnt lgkmcnt(6)\n"
    "v_mfma_f32_16x16x32_bf16 v[128:131], v[176:179], v[168:171], v[128:131]\n"
    "s_add_u32 m0, s73, 0x12000\n"
    "s_add_u32 s61, s28, 0x14000\n"
    "buffer_load_dwordx4 v4, s[32:35], s61 offen lds\n"
    "s_waitcnt lgkmcnt(4)\n"
    "v_mfma_f32_16x16x32_bf16 v[132:135], v[180:183], v[168:171], v[132:135]\n"
    "s_waitcnt lgkmcnt(2)\n"
    "v_mfma_f32_16x16x32_bf16 v[136:139], v[184:187], v[168:171], v[136:139]\n"
    "buffer_load_dwordx4 v5, s[32:35], s61 offen offset:1024 lds\n"
    "s_waitcnt lgkmcnt(0)\n"
    "v_mfma_f32_16x16x32_bf16 v[140:143], v[188:191], v[168:171], v[140:143]\n"
    "s_add_u32 s76, s76, 1\n"
    "s_cmp_eq_u32 s76, 8\n"
    "s_cbranch_scc1 .Lkdump_151\n"
    ".Lkdret_152:\n"
    "v_add_u32 v224, s52, v225\n"
    "s_waitcnt vmcnt(6)\n"
    "ds_write_b128 v224, v[216:219]\n"
    "ds_write_b128 v224, v[220:223] offset:1024\n"
    "s_cmp_lt_u32 s79, 16\n"
    "s_cbranch_scc1 .Lkns1_153\n"
    "s_cmp_eq_u32 s77, 2\n"
    "s_cbranch_scc1 .Lkns1_153\n"
    "s_add_u32 m0, s73, 0x1e800\n"
    "s_add_u32 s61, s28, 0x1e000\n"
    "buffer_load_dwordx4 v4, s[32:35], s61 offen lds\n"
    "buffer_load_dwordx4 v5, s[32:35], s61 offen offset:1024 lds\n"
    "s_add_u32 m0, s73, 0x20800\n"
    "s_add_u32 s61, s29, 0\n"
    "buffer_load_dwordx4 v4, s[32:35], s61 offen lds\n"
    "buffer_load_dwordx4 v5, s[32:35], s61 offen offset:1024 lds\n"
    "s_add_u32 m0, s73, 0x22800\n"
    "s_add_u32 s61, s29, 0xa000\n"
    "buffer_load_dwordx4 v4, s[32:35], s61 offen lds\n"
    "buffer_load_dwordx4 v5, s[32:35], s61 offen offset:1024 lds\n"
    "s_add_u32 m0, s73, 0x24800\n"
    "s_add_u32 s61, s29, 0x14000\n"
    "buffer_load_dwordx4 v4, s[32:35], s61 offen lds\n"
    "buffer_load_dwordx4 v5, s[32:35], s61 offen offset:1024 lds\n"
    ".Lkns1_153:\n"
    "s_add_u32 s67, s56, 0x8000\n"
    "s_sub_u32 s52, s56, 0xe000\n"
    "s_cselect_b32 s52, s67, s52\n"
    "s_add_u32 s67, s57, 0x8000\n"
    "s_sub_u32 s53, s57, 0xe000\n"
    "s_cselect_b32 s53, s67, s53\n"
    "s_add_u32 s67, s58, 0x8000\n"
    "s_sub_u32 s54, s58, 0xe000\n"
    "s_cselect_b32 s54, s67, s54\n"
    "s_add_u32 s67, s59, 0x8000\n"
    "s_sub_u32 s55, s59, 0xe000\n"
    "s_cselect_b32 s55, s67, s55\n"
    "s_add_u32 s15, s15, 1\n"
    "s_add_u32 s86, s86, 0x4000\n"
    "s_waitcnt lgkmcnt(0)\n"
    "s_setprio 0\n"
    "s_barrier\n"
    "s_branch .Lkp310_58\n"
    ".Lkp310_58:\n"
    "s_setprio 0\n"
    "s_cmp_eq_u32 s88, 1\n"
    "s_cbranch_scc1 .Lkw3_154\n"
    "v_add_u32 v248, s52, v236\n"
    "v_add_u32 v249, s52, v237\n"
    "ds_read_b128 v[176:179], v248\n"
    "ds_read_b128 v[180:183], v249\n"
    "ds_read_b128 v[184:187], v248 offset:512\n"
    "ds_read_b128 v[188:191], v249 offset:512\n"
    "s_waitcnt lgkmcnt(3)\n"
    "v_mfma_f32_16x16x32_bf16 v[160:163], v[176:179], v[144:147], v[172:175]\n"
    "ds_read_b128 v[176:179], v248 offset:1024\n"
    "s_waitcnt lgkmcnt(3)\n"
    "v_mfma_f32_16x16x32_bf16 v[164:167], v[180:183], v[144:147], v[172:175]\n"
    "ds_read_b128 v[180:183], v249 offset:1024\n"
    "s_waitcnt lgkmcnt(3)\n"
    "v_mfma_f32_16x16x32_bf16 v[160:163], v[184:187], v[148:151], v[160:163]\n"
    "ds_read_b128 v[184:187], v248 offset:1536\n"
    "s_waitcnt lgkmcnt(3)\n"
    "v_mfma_f32_16x16x32_bf16 v[164:167], v[188:191], v[148:151], v[164:167]\n"
    "ds_read_b128 v[188:191], v249 offset:1536\n"
    "s_waitcnt lgkmcnt(3)\n"
    "v_mfma_f32_16x16x32_bf16 v[160:163], v[176:179], v[152:155], v[160:163]\n"
    "s_waitcnt lgkmcnt(2)\n"
    "v_mfma_f32_16x16x32_bf16 v[164:167], v[180:183], v[152:155], v[164:167]\n"
    "s_waitcnt lgkmcnt(1)\n"
    "v_mfma_f32_16x16x32_bf16 v[160:163], v[184:187], v[156:159], v[160:163]\n"
    "s_waitcnt lgkmcnt(0)\n"
    "v_mfma_f32_16x16x32_bf16 v[164:167], v[188:191], v[156:159], v[164:167]\n"
    "s_nop 5\n"
    "v_max3_f32 v15, v160, v161, v162\n"
    "s_nop 0\n"
    "v_max3_f32 v244, v164, v165, v166\n"
    "v_max3_f32 v15, v15, v163, v167\n"
    "v_max_f32 v15, v15, v244\n"
    "v_cmp_nge_f32 vcc, s70, v15\n"
    "s_nop 0\n"
    "s_cbranch_vccnz .Lkxrare_155\n"
    ".Lkxret_156:\n"
    "v_exp_f32 v160, v160\n"
    "v_exp_f32 v161, v161\n"
    "v_exp_f32 v162, v162\n"
    "v_exp_f32 v163, v163\n"
    "v_add_f32 v14, v14, v160\n"
    "v_cvt_pk_bf16_f32 v168, v160, v161\n"
    "v_add_f32 v14, v14, v161\n"
    "v_add_u32 v248, s53, v0\n"
    "v_xor_b32 v249, 32, v248\n"
    "ds_read_b64_tr_b16 v[176:177], v248\n"
    "ds_read_b64_tr_b16 v[178:179], v248 offset:256\n"
    "ds_read_b64_tr_b16 v[180:181], v249\n"
    "ds_read_b64_tr_b16 v[182:183], v249 offset:256\n"
    "ds_read_b64_tr_b16 v[184:185], v248 offset:512\n"
    "ds_read_b64_tr_b16 v[186:187], v248 offset:768\n"
    "ds_read_b64_tr_b16 v[188:189], v249 offset:512\n"
    "ds_read_b64_tr_b16 v[190:191], v249 offset:768\n"
    "v_exp_f32 v164, v164\n"
    "v_exp_f32 v165, v165\n"
    "v_add_f32 v14, v14, v162\n"
    "v_cvt_pk_bf16_f32 v169, v162, v163\n"
    "v_add_f32 v14, v14, v163\n"
    "v_exp_f32 v166, v166\n"
    "v_exp_f32 v167, v167\n"
    "v_add_f32 v14, v14, v164\n"
    "v_cvt_pk_bf16_f32 v170, v164, v165\n"
    "v_add_f32 v14, v14, v165\n"
    "v_add_f32 v14, v14, v166\n"
    "v_cvt_pk_bf16_f32 v171, v166, v167\n"
    "v_add_f32 v14, v14, v167\n"
    "s_waitcnt lgkmcnt(6)\n"
    "v_mfma_f32_16x16x32_bf16 v[112:115], v[176:179], v[168:171], v[112:115]\n"
    "ds_read_b64_tr_b16 v[176:177], v248 offset:1024\n"
    "ds_read_b64_tr_b16 v[178:179], v248 offset:1280\n"
    "s_waitcnt lgkmcnt(6)\n"
    "v_mfma_f32_16x16x32_bf16 v[116:119], v[180:183], v[168:171], v[116:119]\n"
    "ds_read_b64_tr_b16 v[180:181], v249 offset:1024\n"
    "ds_read_b64_tr_b16 v[182:183], v249 offset:1280\n"
    "s_waitcnt lgkmcnt(6)\n"
    "v_mfma_f32_16x16x32_bf16 v[120:123], v[184:187], v[168:171], v[120:123]\n"
    "ds_read_b64_tr_b16 v[184:185], v248 offset:1536\n"
    "ds_read_b64_tr_b16 v[186:187], v248 offset:1792\n"
    "s_waitcnt lgkmcnt(6)\n"
    "v_mfma_f32_16x16x32_bf16 v[124:127], v[188:191], v[168:171], v[124:127]\n"
    "ds_read_b64_tr_b16 v[188:189], v249 offset:1536\n"
    "ds_read_b64_tr_b16 v[190:191], v249 offset:1792\n"
    "s_waitcnt lgkmcnt(6)\n"
    "v_mfma_f32_16x16x32_bf16 v[128:131], v[176:179], v[168:171], v[128:131]\n"
    "s_waitcnt lgkmcnt(4)\n"
    "v_mfma_f32_16x16x32_bf16 v[132:135], v[180:183], v[168:171], v[132:135]\n"
    "s_waitcnt lgkmcnt(2)\n"
    "v_mfma_f32_16x16x32_bf16 v[136:139], v[184:187], v[168:171], v[136:139]\n"
    "s_waitcnt lgkmcnt(0)\n"
    "v_mfma_f32_16x16x32_bf16 v[140:143], v[188:191], v[168:171], v[140:143]\n"
    "s_add_u32 s76, s76, 1\n"
    "s_cmp_eq_u32 s76, 8\n"
    "s_cbranch_scc1 .Lkdump_157\n"
    ".Lkdret_158:\n"
    "s_branch .Lkjoin_60\n"
    ".Lkw3_154:\n"
    "s_add_u32 m0, s73, 0x14000\n"
    "s_add_u32 s61, s29, 0x1e000\n"
    "buffer_load_dwordx4 v4, s[32:35], s61 offen lds\n"
    "buffer_load_dwordx4 v5, s[32:35], s61 offen offset:1024 lds\n"
    "s_add_u32 m0, s73, 0x13800\n"
    "s_add_u32 s61, s29, 0x1b800\n"
    "buffer_load_dwordx4 v4, s[32:35], s61 offen lds\n"
    "buffer_load_dwordx4 v5, s[32:35], s61 offen offset:1024 lds\n"
    "s_add_u32 m0, s73, 0\n"
    "s_add_u32 s61, s30, 0\n"
    "buffer_load_dwordx4 v4, s[32:35], s61 offen lds\n"
    "buffer_load_dwordx4 v5, s[32:35], s61 offen offset:1024 lds\n"
    "s_add_u32 m0, s73, 0xfffff800\n"
    "s_add_u32 s61, s30, 0xffffd800\n"
    "buffer_load_dwordx4 v4, s[32:35], s61 offen lds\n"
    "buffer_load_dwordx4 v5, s[32:35], s61 offen offset:1024 lds\n"
    "s_add_u32 m0, s73, 0x2000\n"
    "s_add_u32 s61, s30, 0xa000\n"
    "buffer_load_dwordx4 v4, s[32:35], s61 offen lds\n"
    "buffer_load_dwordx4 v5, s[32:35], s61 offen offset:1024 lds\n"
    "s_add_u32 m0, s73, 0x1800\n"
    "s_add_u32 s61, s30, 0x7800\n"
    "buffer_load_dwordx4 v4, s[32:35], s61 offen lds\n"
    "buffer_load_dwordx4 v5, s[32:35], s61 offen offset:1024 lds\n"
    "s_add_u32 m0, s73, 0x4000\n"
    "s_add_u32 s61, s30, 0x14000\n"
    "buffer_load_dwordx4 v4, s[32:35], s61 offen lds\n"
    "buffer_load_dwordx4 v5, s[32:35], s61 offen offset:1024 lds\n"
    "s_add_u32 m0, s73, 0x3800\n"
    "s_add_u32 s61, s30, 0x11800\n"
    "buffer_load_dwordx4 v4, s[32:35], s61 offen lds\n"
    "buffer_load_dwordx4 v5, s[32:35], s61 offen offset:1024 lds\n"
    "s_branch .Lkjoin_60\n"
    ".Lkp311_59:\n"
    "s_setprio 0\n"
    "s_cmp_eq_u32 s88, 1\n"
    "s_cselect_b32 s85, s59, s58\n"
    "v_add_u32 v248, s85, v0\n"
    "v_xor_b32 v249, 32, v248\n"
    "ds_read_b64_tr_b16 v[176:177], v248\n"
    "ds_read_b64_tr_b16 v[178:179], v248 offset:256\n"
    "ds_read_b64_tr_b16 v[180:181], v249\n"
    "ds_read_b64_tr_b16 v[182:183], v249 offset:256\n"
    "ds_read_b64_tr_b16 v[184:185], v248 offset:512\n"
    "ds_read_b64_tr_b16 v[186:187], v248 offset:768\n"
    "ds_read_b64_tr_b16 v[188:189], v249 offset:512\n"
    "ds_read_b64_tr_b16 v[190:191], v249 offset:768\n"
    "v_exp_f32 v164, v164\n"
    "v_exp_f32 v165, v165\n"
    "v_add_f32 v14, v14, v162\n"
    "v_cvt_pk_bf16_f32 v169, v162, v163\n"
    "v_add_f32 v14, v14, v163\n"
    "v_exp_f32 v166, v166\n"
    "v_exp_f32 v167, v167\n"
    "v_add_f32 v14, v14, v164\n"
    "v_cvt_pk_bf16_f32 v170, v164, v165\n"
    "v_add_f32 v14, v14, v165\n"
    "v_add_f32 v14, v14, v166\n"
    "v_cvt_pk_bf16_f32 v171, v166, v167\n"
    "v_add_f32 v14, v14, v167\n"
    "s_waitcnt lgkmcnt(6)\n"
    "v_mfma_f32_16x16x32_bf16 v[112:115], v[176:179], v[168:171], v[112:115]\n"
    "ds_read_b64_tr_b16 v[176:177], v248 offset:1024\n"
    "ds_read_b64_tr_b16 v[178:179], v248 offset:1280\n"
    "s_waitcnt lgkmcnt(6)\n"
    "v_mfma_f32_16x16x32_bf16 v[116:119], v[180:183], v[168:171], v[116:119]\n"
    "ds_read_b64_tr_b16 v[180:181], v249 offset:1024\n"
    "ds_read_b64_tr_b16 v[182:183], v249 offset:1280\n"
    "s_waitcnt lgkmcnt(6)\n"
    "v_mfma_f32_16x16x32_bf16 v[120:123], v[184:187], v[168:171], v[120:123]\n"
    "ds_read_b64_tr_b16 v[184:185], v248 offset:1536\n"
    "ds_read_b64_tr_b16 v[186:187], v248 offset:1792\n"
    "s_waitcnt lgkmcnt(6)\n"
    "v_mfma_f32_16x16x32_bf16 v[124:127], v[188:191], v[168:171], v[124:127]\n"
    "ds_read_b64_tr_b16 v[188:189], v249 offset:1536\n"
    "ds_read_b64_tr_b16 v[190:191], v249 offset:1792\n"
    "s_waitcnt lgkmcnt(6)\n"
    "v_mfma_f32_16x16x32_bf16 v[128:131], v[176:179], v[168:171], v[128:131]\n"
    "s_waitcnt lgkmcnt(4)\n"
    "v_mfma_f32_16x16x32_bf16 v[132:135], v[180:183], v[168:171], v[132:135]\n"
    "s_waitcnt lgkmcnt(2)\n"
    "v_mfma_f32_16x16x32_bf16 v[136:139], v[184:187], v[168:171], v[136:139]\n"
    "s_waitcnt lgkmcnt(0)\n"
    "v_mfma_f32_16x16x32_bf16 v[140:143], v[188:191], v[168:171], v[140:143]\n"
    "s_add_u32 s76, s76, 1\n"
    "s_cmp_eq_u32 s76, 8\n"
    "s_cbranch_scc1 .Lkdump_159\n"
    ".Lkdret_160:\n"
    "s_add_u32 m0, s73, 0x14000\n"
    "s_add_u32 s61, s29, 0x1e000\n"
    "buffer_load_dwordx4 v4, s[32:35], s61 offen lds\n"
    "buffer_load_dwordx4 v5, s[32:35], s61 offen offset:1024 lds\n"
    "s_add_u32 m0, s73, 0\n"
    "s_add_u32 s61, s30, 0\n"
    "buffer_load_dwordx4 v4, s[32:35], s61 offen lds\n"
    "buffer_load_dwordx4 v5, s[32:35], s61 offen offset:1024 lds\n"
    "s_add_u32 m0, s73, 0x2000\n"
    "s_add_u32 s61, s30, 0xa000\n"
    "buffer_load_dwordx4 v4, s[32:35], s61 offen lds\n"
    "buffer_load_dwordx4 v5, s[32:35], s61 offen offset:1024 lds\n"
    "s_add_u32 m0, s73, 0x4000\n"
    "s_add_u32 s61, s30, 0x14000\n"
    "buffer_load_dwordx4 v4, s[32:35], s61 offen lds\n"
    "buffer_load_dwordx4 v5, s[32:35], s61 offen offset:1024 lds\n"
    ".Lkjoin_60:\n"
    "s_waitcnt lgkmcnt(0)\n"
    "s_barrier\n"
    "s_setprio 0\n"
    "v_and_b32 v244, 15, v1\n"
    "v_lshlrev_b32 v244, 3, v244\n"
    "v_add_u32 v244, 0x1e000, v244\n"
    "ds_read_b64 v[176:177], v244\n"
    "ds_read_b64 v[178:179], v244 offset:512\n"
    "ds_read_b64 v[180:181], v244 offset:1024\n"
    "ds_read_b64 v[182:183], v244 offset:1536\n"
    "s_add_u32 s60, s73, 0x16000\n"
    "v_add_u32 v245, s60, v11\n"
    "ds_read_b128 v[192:195], v245\n"
    "ds_read_b128 v[196:199], v245 offset:1024\n"
    "ds_read_b128 v[200:203], v245 offset:8192\n"
    "ds_read_b128 v[204:207], v245 offset:9216\n"
    "ds_read_b128 v[208:211], v245 offset:16384\n"
    "ds_read_b128 v[212:215], v245 offset:17408\n"
    "ds_read_b128 v[228:231], v245 offset:24576\n"
    "ds_read_b128 v[240:243], v245 offset:25600\n"
    "s_waitcnt lgkmcnt(10)\n"
    "v_min_f32 v244, v176, v178\n"
    "s_waitcnt lgkmcnt(8)\n"
    "v_min_f32 v245, v180, v182\n"
    "v_min_f32 v244, v244, v245\n"
    "v_sub_f32 v176, v244, v176\n"
    "v_sub_f32 v178, v244, v178\n"
    "v_sub_f32 v180, v244, v180\n"
    "v_sub_f32 v182, v244, v182\n"
    "v_exp_f32 v176, v176\n"
    "v_exp_f32 v178, v178\n"
    "v_exp_f32 v180, v180\n"
    "v_exp_f32 v182, v182\n"
    "v_mul_f32 v244, v176, v177\n"
    "v_fmac_f32 v244, v178, v179\n"
    "v_fmac_f32 v244, v180, v181\n"
    "v_fmac_f32 v244, v182, v183\n"
    "v_rcp_f32 v244, v244\n"
    "s_nop 0\n"
    "v_mul_f32 v176, v176, v244\n"
    "v_mul_f32 v178, v178, v244\n"
    "v_mul_f32 v180, v180, v244\n"
    "v_mul_f32 v182, v182, v244\n"
    "s_waitcnt lgkmcnt(7)\n"
    "v_mul_f32 v184, v176, v192\n"
    "v_mul_f32 v185, v176, v193\n"
    "v_mul_f32 v186, v176, v194\n"
    "v_mul_f32 v187, v176, v195\n"
    "s_waitcnt lgkmcnt(6)\n"
    "v_mul_f32 v188, v176, v196\n"
    "v_mul_f32 v189, v176, v197\n"
    "v_mul_f32 v190, v176, v198\n"
    "v_mul_f32 v191, v176, v199\n"
    "s_waitcnt lgkmcnt(5)\n"
    "v_fmac_f32 v184, v178, v200\n"
    "v_fmac_f32 v185, v178, v201\n"
    "v_fmac_f32 v186, v178, v202\n"
    "v_fmac_f32 v187, v178, v203\n"
    "s_waitcnt lgkmcnt(4)\n"
    "v_fmac_f32 v188, v178, v204\n"
    "v_fmac_f32 v189, v178, v205\n"
    "v_fmac_f32 v190, v178, v206\n"
    "v_fmac_f32 v191, v178, v207\n"
    "s_waitcnt lgkmcnt(3)\n"
    "v_fmac_f32 v184, v180, v208\n"
    "v_fmac_f32 v185, v180, v209\n"
    "v_fmac_f32 v186, v180, v210\n"
    "v_fmac_f32 v187, v180, v211\n"
    "s_waitcnt lgkmcnt(2)\n"
    "v_fmac_f32 v188, v180, v212\n"
    "v_fmac_f32 v189, v180, v213\n"
    "v_fmac_f32 v190, v180, v214\n"
    "v_fmac_f32 v191, v180, v215\n"
    "s_waitcnt lgkmcnt(1)\n"
    "v_fmac_f32 v184, v182, v228\n"
    "v_fmac_f32 v185, v182, v229\n"
    "v_fmac_f32 v186, v182, v230\n"
    "v_fmac_f32 v187, v182, v231\n"
    "s_waitcnt lgkmcnt(0)\n"
    "v_fmac_f32 v188, v182, v240\n"
    "v_fmac_f32 v189, v182, v241\n"
    "v_fmac_f32 v190, v182, v242\n"
    "v_fmac_f32 v191, v182, v243\n"
    "v_cvt_pk_bf16_f32 v184, v184, v185\n"
    "v_cvt_pk_bf16_f32 v185, v186, v187\n"
    "v_cvt_pk_bf16_f32 v186, v188, v189\n"
    "v_cvt_pk_bf16_f32 v187, v190, v191\n"
    "buffer_store_dwordx4 v[184:187], v11, s[36:39], s84 offen sc1\n"
    "s_add_u32 s84, s84, 0x1000\n"
    "s_waitcnt vmcnt(0) lgkmcnt(0)\n"
    "s_barrier\n"
    "s_cmp_eq_u32 s82, 0\n"
    "s_cbranch_scc1 .Lkna_162\n"
    "s_cmp_lt_u32 s79, 16\n"
    "s_cbranch_scc1 .Lkleft_163\n"
    ".Lkleftr_164:\n"
    "s_lshl2_add_u32 s60, s91, s12\n"
    "s_mul_i32 s60, s60, 43\n"
    "s_lshr_b32 s60, s60, 7\n"
    "s_lshl_b32 s60, s60, 11\n"
    "s_and_b32 s61, s10, 7\n"
    "s_lshl_b32 s61, s61, 6\n"
    "s_add_u32 s60, s60, s61\n"
    "v_and_b32 v13, 3, v1\n"
    "v_lshlrev_b32 v13, 9, v13\n"
    "v_add_u32 v13, s60, v13\n"
    "v_add_u32 v232, 0x10000, v238\n"
    "v_add_u32 v233, 0x20000, v238\n"
    "v_add_u32 v234, 0x10000, v239\n"
    "v_add_u32 v235, 0x20000, v239\n"
    "s_lshr_b32 s60, s10, 3\n"
    "s_lshl_b32 s61, s89, 5\n"
    "s_add_u32 s60, s60, s61\n"
    "s_mul_i32 s60, s60, 0x60000\n"
    "s_add_u32 s60, s60, 0x1210000\n"
    "s_add_u32 s24, s44, s60\n"
    "s_addc_u32 s25, s45, 0\n"
    "s_and_b32 s25, s25, 0xffff\n"
    "s_mov_b32 s26, 0x60000\n"
    "s_mov_b32 s27, 0x20000\n"
    "s_lshl2_add_u32 s62, s90, s12\n"
    "s_mul_i32 s86, s62, 0x1800\n"
    "s_add_u32 s60, s80, 0\n"
    "s_and_b32 s60, s60, 3\n"
    "s_mul_i32 s60, s60, 0x2000\n"
    "s_add_u32 s60, s60, 0x1000\n"
    "s_add_u32 s16, s60, s81\n"
    "s_add_u32 s60, s80, 1\n"
    "s_and_b32 s60, s60, 3\n"
    "s_mul_i32 s60, s60, 0x2000\n"
    "s_add_u32 s60, s60, 0x1000\n"
    "s_add_u32 s17, s60, s81\n"
    "s_add_u32 s60, s80, 2\n"
    "s_and_b32 s60, s60, 3\n"
    "s_mul_i32 s60, s60, 0x2000\n"
    "s_add_u32 s60, s60, 0x1000\n"
    "s_add_u32 s18, s60, s81\n"
    "s_add_u32 s60, s80, 3\n"
    "s_and_b32 s60, s60, 3\n"
    "s_mul_i32 s60, s60, 0x2000\n"
    "s_add_u32 s60, s60, 0x1000\n"
    "s_add_u32 s19, s60, s81\n"
    "s_mov_b32 s83, 0\n"
    ".Lkspin_165:\n"
    "s_mov_b32 s63, 0x5000\n"
    "buffer_load_dwordx2 v[250:251], v13, s[44:47], s63 offen sc1\n"
    "s_waitcnt vmcnt(0)\n"
    "v_cmp_ne_u64 vcc, s[68:69], v[250:251]\n"
    "s_nop 0\n"
    "s_cmp_eq_u64 vcc, 0\n"
    "s_cselect_b32 s78, 1, 0\n"
    "s_cmp_eq_u32 s78, 1\n"
    "s_cbranch_scc1 .Lkok_166\n"
    "s_sleep 1\n"
    "s_add_u32 s83, s83, 1\n"
    "s_cmp_lt_u32 s83, 0x9c40\n"
    "s_cbranch_scc1 .Lkspin_165\n"
    ".Lkok_166:\n"
    "buffer_load_dwordx4 v[112:115], v11, s[36:39], s16 offen sc1\n"
    "buffer_load_dwordx4 v[116:119], v6, s[36:39], s16 offen sc1\n"
    "buffer_load_dwordx4 v[120:123], v12, s[36:39], s16 offen sc1\n"
    "buffer_load_dwordx4 v[124:127], v11, s[36:39], s16 offen offset:1024 sc1\n"
    "buffer_load_dwordx4 v[128:131], v6, s[36:39], s16 offen offset:1024 sc1\n"
    "buffer_load_dwordx4 v[132:135], v12, s[36:39], s16 offen offset:1024 sc1\n"
    ".Lktk_167:\n"
    "ds_read_b64_tr_b16 v[176:177], v238 offset:57344\n"
    "ds_read_b64_tr_b16 v[178:179], v238 offset:61440\n"
    "ds_read_b64_tr_b16 v[180:181], v232\n"
    "ds_read_b64_tr_b16 v[182:183], v232 offset:4096\n"
    "ds_read_b64_tr_b16 v[184:185], v239 offset:57344\n"
    "ds_read_b64_tr_b16 v[186:187], v239 offset:61440\n"
    "ds_read_b64_tr_b16 v[188:189], v234\n"
    "ds_read_b64_tr_b16 v[190:191], v234 offset:4096\n"
    "s_waitcnt vmcnt(5) lgkmcnt(4)\n"
    "v_mfma_f32_16x16x32_bf16 v[16:19], v[176:179], v[112:115], v[16:19]\n"
    "ds_read_b64_tr_b16 v[228:229], v238 offset:57856\n"
    "s_waitcnt vmcnt(2)\n"
    "v_mfma_f32_16x16x32_bf16 v[16:19], v[180:183], v[124:127], v[16:19]\n"
    "ds_read_b64_tr_b16 v[230:231], v238 offset:61952\n"
    "s_waitcnt vmcnt(1)\n"
    "v_mfma_f32_16x16x32_bf16 v[48:51], v[180:183], v[128:131], v[48:51]\n"
    "ds_read_b64_tr_b16 v[240:241], v232 offset:512\n"
    "v_mfma_f32_16x16x32_bf16 v[48:51], v[176:179], v[116:119], v[48:51]\n"
    "ds_read_b64_tr_b16 v[242:243], v232 offset:4608\n"
    "v_mfma_f32_16x16x32_bf16 v[80:83], v[176:179], v[120:123], v[80:83]\n"
    "buffer_load_dwordx4 v[136:139], v11, s[36:39], s16 offen offset:2048 sc1\n"
    "s_waitcnt vmcnt(1)\n"
    "v_mfma_f32_16x16x32_bf16 v[80:83], v[180:183], v[132:135], v[80:83]\n"
    "ds_read_b64_tr_b16 v[176:177], v239 offset:57856\n"
    "s_waitcnt lgkmcnt(5)\n"
    "v_mfma_f32_16x16x32_bf16 v[20:23], v[184:187], v[112:115], v[20:23]\n"
    "ds_read_b64_tr_b16 v[178:179], v239 offset:61952\n"
    "v_mfma_f32_16x16x32_bf16 v[20:23], v[188:191], v[124:127], v[20:23]\n"
    "ds_read_b64_tr_b16 v[180:181], v234 offset:512\n"
    "v_mfma_f32_16x16x32_bf16 v[52:55], v[188:191], v[128:131], v[52:55]\n"
    "ds_read_b64_tr_b16 v[182:183], v234 offset:4608\n"
    "v_mfma_f32_16x16x32_bf16 v[52:55], v[184:187], v[116:119], v[52:55]\n"
    "buffer_load_dwordx4 v[140:143], v6, s[36:39], s16 offen offset:2048 sc1\n"
    "v_mfma_f32_16x16x32_bf16 v[84:87], v[184:187], v[120:123], v[84:87]\n"
    "ds_read_b64_tr_b16 v[184:185], v238 offset:58368\n"
    "v_mfma_f32_16x16x32_bf16 v[84:87], v[188:191], v[132:135], v[84:87]\n"
    "ds_read_b64_tr_b16 v[186:187], v238 offset:62464\n"
    "s_waitcnt lgkmcnt(6)\n"
    "v_mfma_f32_16x16x32_bf16 v[24:27], v[228:231], v[112:115], v[24:27]\n"
    "ds_read_b64_tr_b16 v[188:189], v232 offset:1024\n"
    "v_mfma_f32_16x16x32_bf16 v[24:27], v[240:243], v[124:127], v[24:27]\n"
    "ds_read_b64_tr_b16 v[190:191], v232 offset:5120\n"
    "v_mfma_f32_16x16x32_bf16 v[56:59], v[240:243], v[128:131], v[56:59]\n"
    "buffer_load_dwordx4 v[144:147], v12, s[36:39], s16 offen offset:2048 sc1\n"
    "v_mfma_f32_16x16x32_bf16 v[56:59], v[228:231], v[116:119], v[56:59]\n"
    "buffer_load_dwordx4 v[148:151], v11, s[36:39], s16 offen offset:3072 sc1\n"
    "v_mfma_f32_16x16x32_bf16 v[88:91], v[228:231], v[120:123], v[88:91]\n"
    "ds_read_b64_tr_b16 v[228:229], v239 offset:58368\n"
    "v_mfma_f32_16x16x32_bf16 v[88:91], v[240:243], v[132:135], v[88:91]\n"
    "ds_read_b64_tr_b16 v[230:231], v239 offset:62464\n"
    "s_waitcnt lgkmcnt(6)\n"
    "v_mfma_f32_16x16x32_bf16 v[28:31], v[176:179], v[112:115], v[28:31]\n"
    "ds_read_b64_tr_b16 v[240:241], v234 offset:1024\n"
    "v_mfma_f32_16x16x32_bf16 v[28:31], v[180:183], v[124:127], v[28:31]\n"
    "ds_read_b64_tr_b16 v[242:243], v234 offset:5120\n"
    "v_mfma_f32_16x16x32_bf16 v[60:63], v[180:183], v[128:131], v[60:63]\n"
    "buffer_load_dwordx4 v[152:155], v6, s[36:39], s16 offen offset:3072 sc1\n"
    "v_mfma_f32_16x16x32_bf16 v[60:63], v[176:179], v[116:119], v[60:63]\n"
    "v_mfma_f32_16x16x32_bf16 v[92:95], v[176:179], v[120:123], v[92:95]\n"
    "ds_read_b64_tr_b16 v[176:177], v238 offset:58880\n"
    "v_mfma_f32_16x16x32_bf16 v[92:95], v[180:183], v[132:135], v[92:95]\n"
    "ds_read_b64_tr_b16 v[178:179], v238 offset:62976\n"
    "s_waitcnt lgkmcnt(6)\n"
    "v_mfma_f32_16x16x32_bf16 v[32:35], v[184:187], v[112:115], v[32:35]\n"
    "ds_read_b64_tr_b16 v[180:181], v232 offset:1536\n"
    "v_mfma_f32_16x16x32_bf16 v[32:35], v[188:191], v[124:127], v[32:35]\n"
    "ds_read_b64_tr_b16 v[182:183], v232 offset:5632\n"
    "v_mfma_f32_16x16x32_bf16 v[64:67], v[188:191], v[128:131], v[64:67]\n"
    "buffer_load_dwordx4 v[156:159], v12, s[36:39], s16 offen offset:3072 sc1\n"
    "v_mfma_f32_16x16x32_bf16 v[64:67], v[184:187], v[116:119], v[64:67]\n"
    "v_mfma_f32_16x16x32_bf16 v[96:99], v[184:187], v[120:123], v[96:99]\n"
    "ds_read_b64_tr_b16 v[184:185], v239 offset:58880\n"
    "v_mfma_f32_16x16x32_bf16 v[96:99], v[188:191], v[132:135], v[96:99]\n"
    "ds_read_b64_tr_b16 v[186:187], v239 offset:62976\n"
    "s_waitcnt lgkmcnt(6)\n"
    "v_mfma_f32_16x16x32_bf16 v[36:39], v[228:231], v[112:115], v[36:39]\n"
    "ds_read_b64_tr_b16 v[188:189], v234 offset:1536\n"
    "v_mfma_f32_16x16x32_bf16 v[36:39], v[240:243], v[124:127], v[36:39]\n"
    "ds_read_b64_tr_b16 v[190:191], v234 offset:5632\n"
    "v_mfma_f32_16x16x32_bf16 v[68:71], v[240:243], v[128:131], v[68:71]\n"
    "buffer_load_dwordx4 v[160:163], v11, s[36:39], s17 offen sc1\n"
    "v_mfma_f32_16x16x32_bf16 v[68:71], v[228:231], v[116:119], v[68:71]\n"
    "buffer_load_dwordx4 v[164:167], v6, s[36:39], s17 offen sc1\n"
    "v_mfma_f32_16x16x32_bf16 v[100:103], v[228:231], v[120:123], v[100:103]\n"
    "ds_read_b64_tr_b16 v[228:229], v232 offset:8192\n"
    "v_mfma_f32_16x16x32_bf16 v[100:103], v[240:243], v[132:135], v[100:103]\n"
    "ds_read_b64_tr_b16 v[230:231], v232 offset:12288\n"
    "s_waitcnt lgkmcnt(6)\n"
    "v_mfma_f32_16x16x32_bf16 v[40:43], v[176:179], v[112:115], v[40:43]\n"
    "ds_read_b64_tr_b16 v[240:241], v232 offset:59392\n"
    "v_mfma_f32_16x16x32_bf16 v[40:43], v[180:183], v[124:127], v[40:43]\n"
    "ds_read_b64_tr_b16 v[242:243], v232 offset:63488\n"
    "v_mfma_f32_16x16x32_bf16 v[72:75], v[180:183], v[128:131], v[72:75]\n"
    "buffer_load_dwordx4 v[168:171], v12, s[36:39], s17 offen sc1\n"
    "v_mfma_f32_16x16x32_bf16 v[72:75], v[176:179], v[116:119], v[72:75]\n"
    "v_mfma_f32_16x16x32_bf16 v[104:107], v[176:179], v[120:123], v[104:107]\n"
    "ds_read_b64_tr_b16 v[176:177], v234 offset:8192\n"
    "v_mfma_f32_16x16x32_bf16 v[104:107], v[180:183], v[132:135], v[104:107]\n"
    "ds_read_b64_tr_b16 v[178:179], v234 offset:12288\n"
    "s_waitcnt lgkmcnt(6)\n"
    "v_mfma_f32_16x16x32_bf16 v[44:47], v[184:187], v[112:115], v[44:47]\n"
    "ds_read_b64_tr_b16 v[180:181], v234 offset:59392\n"
    "v_mfma_f32_16x16x32_bf16 v[44:47], v[188:191], v[124:127], v[44:47]\n"
    "ds_read_b64_tr_b16 v[182:183], v234 offset:63488\n"
    "v_mfma_f32_16x16x32_bf16 v[76:79], v[188:191], v[128:131], v[76:79]\n"
    "buffer_load_dwordx4 v[172:175], v11, s[36:39], s17 offen offset:1024 sc1\n"
    "v_mfma_f32_16x16x32_bf16 v[76:79], v[184:187], v[116:119], v[76:79]\n"
    "v_mfma_f32_16x16x32_bf16 v[108:111], v[184:187], v[120:123], v[108:111]\n"
    "ds_read_b64_tr_b16 v[184:185], v232 offset:8704\n"
    "v_mfma_f32_16x16x32_bf16 v[108:111], v[188:191], v[132:135], v[108:111]\n"
    "ds_read_b64_tr_b16 v[186:187], v232 offset:12800\n"
    "s_waitcnt vmcnt(9) lgkmcnt(6)\n"
    "v_mfma_f32_16x16x32_bf16 v[16:19], v[228:231], v[136:139], v[16:19]\n"
    "ds_read_b64_tr_b16 v[188:189], v232 offset:59904\n"
    "s_waitcnt vmcnt(6)\n"
    "v_mfma_f32_16x16x32_bf16 v[16:19], v[240:243], v[148:151], v[16:19]\n"
    "ds_read_b64_tr_b16 v[190:191], v232 offset:64000\n"
    "s_waitcnt vmcnt(5)\n"
    "v_mfma_f32_16x16x32_bf16 v[48:51], v[240:243], v[152:155], v[48:51]\n"
    "buffer_load_dwordx4 v[192:195], v6, s[36:39], s17 offen offset:1024 sc1\n"
    "v_mfma_f32_16x16x32_bf16 v[48:51], v[228:231], v[140:143], v[48:51]\n"
    "buffer_load_dwordx4 v[196:199], v12, s[36:39], s17 offen offset:1024 sc1\n"
    "v_mfma_f32_16x16x32_bf16 v[80:83], v[228:231], v[144:147], v[80:83]\n"
    "ds_read_b64_tr_b16 v[228:229], v234 offset:8704\n"
    "s_waitcnt vmcnt(6)\n"
    "v_mfma_f32_16x16x32_bf16 v[80:83], v[240:243], v[156:159], v[80:83]\n"
    "ds_read_b64_tr_b16 v[230:231], v234 offset:12800\n"
    "s_waitcnt lgkmcnt(6)\n"
    "v_mfma_f32_16x16x32_bf16 v[20:23], v[176:179], v[136:139], v[20:23]\n"
    "ds_read_b64_tr_b16 v[240:241], v234 offset:59904\n"
    "v_mfma_f32_16x16x32_bf16 v[20:23], v[180:183], v[148:151], v[20:23]\n"
    "ds_read_b64_tr_b16 v[242:243], v234 offset:64000\n"
    "v_mfma_f32_16x16x32_bf16 v[52:55], v[180:183], v[152:155], v[52:55]\n"
    "buffer_load_dwordx4 v[200:203], v11, s[36:39], s17 offen offset:2048 sc1\n"
    "v_mfma_f32_16x16x32_bf16 v[52:55], v[176:179], v[140:143], v[52:55]\n"
    "v_mfma_f32_16x16x32_bf16 v[84:87], v[176:179], v[144:147], v[84:87]\n"
    "ds_read_b64_tr_b16 v[176:177], v232 offset:9216\n"
    "v_mfma_f32_16x16x32_bf16 v[84:87], v[180:183], v[156:159], v[84:87]\n"
    "ds_read_b64_tr_b16 v[178:179], v232 offset:13312\n"
    "s_waitcnt lgkmcnt(6)\n"
    "v_mfma_f32_16x16x32_bf16 v[24:27], v[184:187], v[136:139], v[24:27]\n"
    "ds_read_b64_tr_b16 v[180:181], v232 offset:60416\n"
    "v_mfma_f32_16x16x32_bf16 v[24:27], v[188:191], v[148:151], v[24:27]\n"
    "ds_read_b64_tr_b16 v[182:183], v232 offset:64512\n"
    "v_mfma_f32_16x16x32_bf16 v[56:59], v[188:191], v[152:155], v[56:59]\n"
    "buffer_load_dwordx4 v[204:207], v6, s[36:39], s17 offen offset:2048 sc1\n"
    "v_mfma_f32_16x16x32_bf16 v[56:59], v[184:187], v[140:143], v[56:59]\n"
    "v_mfma_f32_16x16x32_bf16 v[88:91], v[184:187], v[144:147], v[88:91]\n"
    "ds_read_b64_tr_b16 v[184:185], v234 offset:9216\n"
    "v_mfma_f32_16x16x32_bf16 v[88:91], v[188:191], v[156:159], v[88:91]\n"
    "ds_read_b64_tr_b16 v[186:187], v234 offset:13312\n"
    "s_waitcnt lgkmcnt(6)\n"
    "v_mfma_f32_16x16x32_bf16 v[28:31], v[228:231], v[136:139], v[28:31]\n"
    "ds_read_b64_tr_b16 v[188:189], v234 offset:60416\n"
    "v_mfma_f32_16x16x32_bf16 v[28:31], v[240:243], v[148:151], v[28:31]\n"
    "ds_read_b64_tr_b16 v[190:191], v234 offset:64512\n"
    "v_mfma_f32_16x16x32_bf16 v[60:63], v[240:243], v[152:155], v[60:63]\n"
    "buffer_load_dwordx4 v[208:211], v12, s[36:39], s17 offen offset:2048 sc1\n"
    "v_mfma_f32_16x16x32_bf16 v[60:63], v[228:231], v[140:143], v[60:63]\n"
    "buffer_load_dwordx4 v[212:215], v11, s[36:39], s17 offen offset:3072 sc1\n"
    "v_mfma_f32_16x16x32_bf16 v[92:95], v[228:231], v[144:147], v[92:95]\n"
    "ds_read_b64_tr_b16 v[228:229], v232 offset:9728\n"
    "v_mfma_f32_16x16x32_bf16 v[92:95], v[240:243], v[156:159], v[92:95]\n"
    "ds_read_b64_tr_b16 v[230:231], v232 offset:13824\n"
    "s_waitcnt lgkmcnt(6)\n"
    "v_mfma_f32_16x16x32_bf16 v[32:35], v[176:179], v[136:139], v[32:35]\n"
    "ds_read_b64_tr_b16 v[240:241], v232 offset:60928\n"
    "v_mfma_f32_16x16x32_bf16 v[32:35], v[180:183], v[148:151], v[32:35]\n"
    "ds_read_b64_tr_b16 v[242:243], v232 offset:65024\n"
    "v_mfma_f32_16x16x32_bf16 v[64:67], v[180:183], v[152:155], v[64:67]\n"
    "buffer_load_dwordx4 v[216:219], v6, s[36:39], s17 offen offset:3072 sc1\n"
    "v_mfma_f32_16x16x32_bf16 v[64:67], v[176:179], v[140:143], v[64:67]\n"
    "v_mfma_f32_16x16x32_bf16 v[96:99], v[176:179], v[144:147], v[96:99]\n"
    "ds_read_b64_tr_b16 v[176:177], v234 offset:9728\n"
    "v_mfma_f32_16x16x32_bf16 v[96:99], v[180:183], v[156:159], v[96:99]\n"
    "ds_read_b64_tr_b16 v[178:179], v234 offset:13824\n"
    "s_waitcnt lgkmcnt(6)\n"
    "v_mfma_f32_16x16x32_bf16 v[36:39], v[184:187], v[136:139], v[36:39]\n"
    "ds_read_b64_tr_b16 v[180:181], v234 offset:60928\n"
    "v_mfma_f32_16x16x32_bf16 v[36:39], v[188:191], v[148:151], v[36:39]\n"
    "ds_read_b64_tr_b16 v[182:183], v234 offset:65024\n"
    "v_mfma_f32_16x16x32_bf16 v[68:71], v[188:191], v[152:155], v[68:71]\n"
    "buffer_load_dwordx4 v[220:223], v12, s[36:39], s17 offen offset:3072 sc1\n"
    "v_mfma_f32_16x16x32_bf16 v[68:71], v[184:187], v[140:143], v[68:71]\n"
    "v_mfma_f32_16x16x32_bf16 v[100:103], v[184:187], v[144:147], v[100:103]\n"
    "ds_read_b64_tr_b16 v[184:185], v233 offset:2048\n"
    "v_mfma_f32_16x16x32_bf16 v[100:103], v[188:191], v[156:159], v[100:103]\n"
    "ds_read_b64_tr_b16 v[186:187], v233 offset:6144\n"
    "s_waitcnt lgkmcnt(6)\n"
    "v_mfma_f32_16x16x32_bf16 v[40:43], v[228:231], v[136:139], v[40:43]\n"
    "ds_read_b64_tr_b16 v[188:189], v233 offset:10240\n"
    "v_mfma_f32_16x16x32_bf16 v[40:43], v[240:243], v[148:151], v[40:43]\n"
    "ds_read_b64_tr_b16 v[190:191], v233 offset:14336\n"
    "v_mfma_f32_16x16x32_bf16 v[72:75], v[240:243], v[152:155], v[72:75]\n"
    "s_add_u32 m0, s73, 0x6000\n"
    "v_mfma_f32_16x16x32_bf16 v[72:75], v[228:231], v[140:143], v[72:75]\n"
    "s_add_u32 s61, s30, 0x1e000\n"
    "v_mfma_f32_16x16x32_bf16 v[104:107], v[228:231], v[144:147], v[104:107]\n"
    "buffer_load_dwordx4 v4, s[32:35], s61 offen lds\n"
    "v_mfma_f32_16x16x32_bf16 v[104:107], v[240:243], v[156:159], v[104:107]\n"
    "buffer_load_dwordx4 v5, s[32:35], s61 offen offset:1024 lds\n"
    "s_waitcnt lgkmcnt(4)\n"
    "v_mfma_f32_16x16x32_bf16 v[44:47], v[176:179], v[136:139], v[44:47]\n"
    "ds_read_b64_tr_b16 v[228:229], v235 offset:2048\n"
    "v_mfma_f32_16x16x32_bf16 v[44:47], v[180:183], v[148:151], v[44:47]\n"
    "ds_read_b64_tr_b16 v[230:231], v235 offset:6144\n"
    "v_mfma_f32_16x16x32_bf16 v[76:79], v[180:183], v[152:155], v[76:79]\n"
    "ds_read_b64_tr_b16 v[240:241], v235 offset:10240\n"
    "v_mfma_f32_16x16x32_bf16 v[76:79], v[176:179], v[140:143], v[76:79]\n"
    "ds_read_b64_tr_b16 v[242:243], v235 offset:14336\n"
    "v_mfma_f32_16x16x32_bf16 v[108:111], v[176:179], v[144:147], v[108:111]\n"
    "s_add_u32 m0, s73, 0x8000\n"
    "v_mfma_f32_16x16x32_bf16 v[108:111], v[180:183], v[156:159], v[108:111]\n"
    "s_add_u32 s61, s31, 0\n"
    "buffer_load_dwordx4 v4, s[32:35], s61 offen lds\n"
    "s_waitcnt vmcnt(14) lgkmcnt(4)\n"
    "v_mfma_f32_16x16x32_bf16 v[16:19], v[184:187], v[160:163], v[16:19]\n"
    "ds_read_b64_tr_b16 v[176:177], v233 offset:2560\n"
    "s_waitcnt vmcnt(11)\n"
    "v_mfma_f32_16x16x32_bf16 v[16:19], v[188:191], v[172:175], v[16:19]\n"
    "ds_read_b64_tr_b16 v[178:179], v233 offset:6656\n"
    "s_waitcnt vmcnt(10)\n"
    "v_mfma_f32_16x16x32_bf16 v[48:51], v[188:191], v[192:195], v[48:51]\n"
    "ds_read_b64_tr_b16 v[180:181], v233 offset:10752\n"
    "v_mfma_f32_16x16x32_bf16 v[48:51], v[184:187], v[164:167], v[48:51]\n"
    "ds_read_b64_tr_b16 v[182:183], v233 offset:14848\n"
    "v_mfma_f32_16x16x32_bf16 v[80:83], v[184:187], v[168:171], v[80:83]\n"
    "buffer_load_dwordx4 v5, s[32:35], s61 offen offset:1024 lds\n"
    "s_waitcnt vmcnt(10)\n"
    "v_mfma_f32_16x16x32_bf16 v[80:83], v[188:191], v[196:199], v[80:83]\n"
    "ds_read_b64_tr_b16 v[184:185], v235 offset:2560\n"
    "s_waitcnt lgkmcnt(5)\n"
    "v_mfma_f32_16x16x32_bf16 v[20:23], v[228:231], v[160:163], v[20:23]\n"
    "ds_read_b64_tr_b16 v[186:187], v235 offset:6656\n"
    "v_mfma_f32_16x16x32_bf16 v[20:23], v[240:243], v[172:175], v[20:23]\n"
    "ds_read_b64_tr_b16 v[188:189], v235 offset:10752\n"
    "v_mfma_f32_16x16x32_bf16 v[52:55], v[240:243], v[192:195], v[52:55]\n"
    "ds_read_b64_tr_b16 v[190:191], v235 offset:14848\n"
    "v_mfma_f32_16x16x32_bf16 v[52:55], v[228:231], v[164:167], v[52:55]\n"
    "s_add_u32 m0, s73, 0xa000\n"
    "v_mfma_f32_16x16x32_bf16 v[84:87], v[228:231], v[168:171], v[84:87]\n"
    "s_add_u32 s61, s31, 0xa000\n"
    "v_mfma_f32_16x16x32_bf16 v[84:87], v[240:243], v[196:199], v[84:87]\n"
    "buffer_load_dwordx4 v4, s[32:35], s61 offen lds\n"
    "buffer_load_dwordx4 v5, s[32:35], s61 offen offset:1024 lds\n"
    "s_waitcnt lgkmcnt(4)\n"
    "v_mfma_f32_16x16x32_bf16 v[24:27], v[176:179], v[160:163], v[24:27]\n"
    "ds_read_b64_tr_b16 v[228:229], v233 offset:3072\n"
    "v_mfma_f32_16x16x32_bf16 v[24:27], v[180:183], v[172:175], v[24:27]\n"
    "ds_read_b64_tr_b16 v[230:231], v233 offset:7168\n"
    "v_mfma_f32_16x16x32_bf16 v[56:59], v[180:183], v[192:195], v[56:59]\n"
    "ds_read_b64_tr_b16 v[240:241], v233 offset:11264\n"
    "v_mfma_f32_16x16x32_bf16 v[56:59], v[176:179], v[164:167], v[56:59]\n"
    "ds_read_b64_tr_b16 v[242:243], v233 offset:15360\n"
    "v_mfma_f32_16x16x32_bf16 v[88:91], v[176:179], v[168:171], v[88:91]\n"
    "s_add_u32 m0, s73, 0xc000\n"
    "v_mfma_f32_16x16x32_bf16 v[88:91], v[180:183], v[196:199], v[88:91]\n"
    "s_add_u32 s61, s31, 0x14000\n"
    "buffer_load_dwordx4 v4, s[32:35], s61 offen lds\n"
    "s_waitcnt lgkmcnt(4)\n"
    "v_mfma_f32_16x16x32_bf16 v[28:31], v[184:187], v[160:163], v[28:31]\n"
    "ds_read_b64_tr_b16 v[176:177], v235 offset:3072\n"
    "v_mfma_f32_16x16x32_bf16 v[28:31], v[188:191], v[172:175], v[28:31]\n"
    "ds_read_b64_tr_b16 v[178:179], v235 offset:7168\n"
    "v_mfma_f32_16x16x32_bf16 v[60:63], v[188:191], v[192:195], v[60:63]\n"
    "ds_read_b64_tr_b16 v[180:181], v235 offset:11264\n"
    "v_mfma_f32_16x16x32_bf16 v[60:63], v[184:187], v[164:167], v[60:63]\n"
    "ds_read_b64_tr_b16 v[182:183], v235 offset:15360\n"
    "v_mfma_f32_16x16x32_bf16 v[92:95], v[184:187], v[168:171], v[92:95]\n"
    "buffer_load_dwordx4 v5, s[32:35], s61 offen offset:1024 lds\n"
    "v_mfma_f32_16x16x32_bf16 v[92:95], v[188:191], v[196:199], v[92:95]\n"
    "ds_read_b64_tr_b16 v[184:185], v233 offset:3584\n"
    "s_waitcnt lgkmcnt(5)\n"
    "v_mfma_f32_16x16x32_bf16 v[32:35], v[228:231], v[160:163], v[32:35]\n"
    "ds_read_b64_tr_b16 v[186:187], v233 offset:7680\n"
    "v_mfma_f32_16x16x32_bf16 v[32:35], v[240:243], v[172:175], v[32:35]\n"
    "ds_read_b64_tr_b16 v[188:189], v233 offset:11776\n"
    "v_mfma_f32_16x16x32_bf16 v[64:67], v[240:243], v[192:195], v[64:67]\n"
    "ds_read_b64_tr_b16 v[190:191], v233 offset:15872\n"
    "v_mfma_f32_16x16x32_bf16 v[64:67], v[228:231], v[164:167], v[64:67]\n"
    "s_add_u32 m0, s73, 0x16000\n"
    "v_mfma_f32_16x16x32_bf16 v[96:99], v[228:231], v[168:171], v[96:99]\n"
    "s_add_u32 s61, s31, 0x1e000\n"
    "v_mfma_f32_16x16x32_bf16 v[96:99], v[240:243], v[196:199], v[96:99]\n"
    "buffer_load_dwordx4 v4, s[32:35], s61 offen lds\n"
    "buffer_load_dwordx4 v5, s[32:35], s61 offen offset:1024 lds\n"
    "s_waitcnt lgkmcnt(4)\n"
    "v_mfma_f32_16x16x32_bf16 v[36:39], v[176:179], v[160:163], v[36:39]\n"
    "ds_read_b64_tr_b16 v[228:229], v235 offset:3584\n"
    "v_mfma_f32_16x16x32_bf16 v[36:39], v[180:183], v[172:175], v[36:39]\n"
    "ds_read_b64_tr_b16 v[230:231], v235 offset:7680\n"
    "v_mfma_f32_16x16x32_bf16 v[68:71], v[180:183], v[192:195], v[68:71]\n"
    "ds_read_b64_tr_b16 v[240:241], v235 offset:11776\n"
    "v_mfma_f32_16x16x32_bf16 v[68:71], v[176:179], v[164:167], v[68:71]\n"
    "ds_read_b64_tr_b16 v[242:243], v235 offset:15872\n"
    "v_mfma_f32_16x16x32_bf16 v[100:103], v[176:179], v[168:171], v[100:103]\n"
    "buffer_load_dwordx4 v[112:115], v11, s[36:39], s18 offen sc1\n"
    "v_mfma_f32_16x16x32_bf16 v[100:103], v[180:183], v[196:199], v[100:103]\n"
    "ds_read_b64_tr_b16 v[176:177], v233 offset:18432\n"
    "s_waitcnt lgkmcnt(5)\n"
    "v_mfma_f32_16x16x32_bf16 v[40:43], v[184:187], v[160:163], v[40:43]\n"
    "ds_read_b64_tr_b16 v[178:179], v233 offset:22528\n"
    "v_mfma_f32_16x16x32_bf16 v[40:43], v[188:191], v[172:175], v[40:43]\n"
    "ds_read_b64_tr_b16 v[180:181], v232 offset:16384\n"
    "v_mfma_f32_16x16x32_bf16 v[72:75], v[188:191], v[192:195], v[72:75]\n"
    "ds_read_b64_tr_b16 v[182:183], v232 offset:20480\n"
    "v_mfma_f32_16x16x32_bf16 v[72:75], v[184:187], v[164:167], v[72:75]\n"
    "buffer_load_dwordx4 v[116:119], v6, s[36:39], s18 offen sc1\n"
    "v_mfma_f32_16x16x32_bf16 v[104:107], v[184:187], v[168:171], v[104:107]\n"
    "ds_read_b64_tr_b16 v[184:185], v235 offset:18432\n"
    "v_mfma_f32_16x16x32_bf16 v[104:107], v[188:191], v[196:199], v[104:107]\n"
    "ds_read_b64_tr_b16 v[186:187], v235 offset:22528\n"
    "s_waitcnt lgkmcnt(6)\n"
    "v_mfma_f32_16x16x32_bf16 v[44:47], v[228:231], v[160:163], v[44:47]\n"
    "ds_read_b64_tr_b16 v[188:189], v234 offset:16384\n"
    "v_mfma_f32_16x16x32_bf16 v[44:47], v[240:243], v[172:175], v[44:47]\n"
    "ds_read_b64_tr_b16 v[190:191], v234 offset:20480\n"
    "v_mfma_f32_16x16x32_bf16 v[76:79], v[240:243], v[192:195], v[76:79]\n"
    "buffer_load_dwordx4 v[120:123], v12, s[36:39], s18 offen sc1\n"
    "v_mfma_f32_16x16x32_bf16 v[76:79], v[228:231], v[164:167], v[76:79]\n"
    "buffer_load_dwordx4 v[124:127], v11, s[36:39], s18 offen offset:1024 sc1\n"
    "v_mfma_f32_16x16x32_bf16 v[108:111], v[228:231], v[168:171], v[108:111]\n"
    "ds_read_b64_tr_b16 v[228:229], v233 offset:18944\n"
    "v_mfma_f32_16x16x32_bf16 v[108:111], v[240:243], v[196:199], v[108:111]\n"
    "ds_read_b64_tr_b16 v[230:231], v233 offset:23040\n"
    "s_waitcnt vmcnt(19) lgkmcnt(6)\n"
    "v_mfma_f32_16x16x32_bf16 v[16:19], v[176:179], v[200:203], v[16:19]\n"
    "ds_read_b64_tr_b16 v[240:241], v232 offset:16896\n"
    "s_waitcnt vmcnt(16)\n"
    "v_mfma_f32_16x16x32_bf16 v[16:19], v[180:183], v[212:215], v[16:19]\n"
    "ds_read_b64_tr_b16 v[242:243], v232 offset:20992\n"
    "s_waitcnt vmcnt(15)\n"
    "v_mfma_f32_16x16x32_bf16 v[48:51], v[180:183], v[216:219], v[48:51]\n"
    "buffer_load_dwordx4 v[128:131], v6, s[36:39], s18 offen offset:1024 sc1\n"
    "v_mfma_f32_16x16x32_bf16 v[48:51], v[176:179], v[204:207], v[48:51]\n"
    "v_mfma_f32_16x16x32_bf16 v[80:83], v[176:179], v[208:211], v[80:83]\n"
    "ds_read_b64_tr_b16 v[176:177], v235 offset:18944\n"
    "s_waitcnt vmcnt(15)\n"
    "v_mfma_f32_16x16x32_bf16 v[80:83], v[180:183], v[220:223], v[80:83]\n"
    "ds_read_b64_tr_b16 v[178:179], v235 offset:23040\n"
    "s_waitcnt lgkmcnt(6)\n"
    "v_mfma_f32_16x16x32_bf16 v[20:23], v[184:187], v[200:203], v[20:23]\n"
    "ds_read_b64_tr_b16 v[180:181], v234 offset:16896\n"
    "v_mfma_f32_16x16x32_bf16 v[20:23], v[188:191], v[212:215], v[20:23]\n"
    "ds_read_b64_tr_b16 v[182:183], v234 offset:20992\n"
    "v_mfma_f32_16x16x32_bf16 v[52:55], v[188:191], v[216:219], v[52:55]\n"
    "buffer_load_dwordx4 v[132:135], v12, s[36:39], s18 offen offset:1024 sc1\n"
    "v_mfma_f32_16x16x32_bf16 v[52:55], v[184:187], v[204:207], v[52:55]\n"
    "v_mfma_f32_16x16x32_bf16 v[84:87], v[184:187], v[208:211], v[84:87]\n"
    "ds_read_b64_tr_b16 v[184:185], v233 offset:19456\n"
    "v_mfma_f32_16x16x32_bf16 v[84:87], v[188:191], v[220:223], v[84:87]\n"
    "ds_read_b64_tr_b16 v[186:187], v233 offset:23552\n"
    "s_waitcnt lgkmcnt(6)\n"
    "v_mfma_f32_16x16x32_bf16 v[24:27], v[228:231], v[200:203], v[24:27]\n"
    "ds_read_b64_tr_b16 v[188:189], v232 offset:17408\n"
    "v_mfma_f32_16x16x32_bf16 v[24:27], v[240:243], v[212:215], v[24:27]\n"
    "ds_read_b64_tr_b16 v[190:191], v232 offset:21504\n"
    "v_mfma_f32_16x16x32_bf16 v[56:59], v[240:243], v[216:219], v[56:59]\n"
    "buffer_load_dwordx4 v[136:139], v11, s[36:39], s18 offen offset:2048 sc1\n"
    "v_mfma_f32_16x16x32_bf16 v[56:59], v[228:231], v[204:207], v[56:59]\n"
    "buffer_load_dwordx4 v[140:143], v6, s[36:39], s18 offen offset:2048 sc1\n"
    "v_mfma_f32_16x16x32_bf16 v[88:91], v[228:231], v[208:211], v[88:91]\n"
    "ds_read_b64_tr_b16 v[228:229], v235 offset:19456\n"
    "v_mfma_f32_16x16x32_bf16 v[88:91], v[240:243], v[220:223], v[88:91]\n"
    "ds_read_b64_tr_b16 v[230:231], v235 offset:23552\n"
    "s_waitcnt lgkmcnt(6)\n"
    "v_mfma_f32_16x16x32_bf16 v[28:31], v[176:179], v[200:203], v[28:31]\n"
    "ds_read_b64_tr_b16 v[240:241], v234 offset:17408\n"
    "v_mfma_f32_16x16x32_bf16 v[28:31], v[180:183], v[212:215], v[28:31]\n"
    "ds_read_b64_tr_b16 v[242:243], v234 offset:21504\n"
    "v_mfma_f32_16x16x32_bf16 v[60:63], v[180:183], v[216:219], v[60:63]\n"
    "buffer_load_dwordx4 v[144:147], v12, s[36:39], s18 offen offset:2048 sc1\n"
    "v_mfma_f32_16x16x32_bf16 v[60:63], v[176:179], v[204:207], v[60:63]\n"
    "v_mfma_f32_16x16x32_bf16 v[92:95], v[176:179], v[208:211], v[92:95]\n"
    "ds_read_b64_tr_b16 v[176:177], v233 offset:19968\n"
    "v_mfma_f32_16x16x32_bf16 v[92:95], v[180:183], v[220:223], v[92:95]\n"
    "ds_read_b64_tr_b16 v[178:179], v233 offset:24064\n"
    "s_waitcnt lgkmcnt(6)\n"
    "v_mfma_f32_16x16x32_bf16 v[32:35], v[184:187], v[200:203], v[32:35]\n"
    "ds_read_b64_tr_b16 v[180:181], v232 offset:17920\n"
    "v_mfma_f32_16x16x32_bf16 v[32:35], v[188:191], v[212:215], v[32:35]\n"
    "ds_read_b64_tr_b16 v[182:183], v232 offset:22016\n"
    "v_mfma_f32_16x16x32_bf16 v[64:67], v[188:191], v[216:219], v[64:67]\n"
    "buffer_load_dwordx4 v[148:151], v11, s[36:39], s18 offen offset:3072 sc1\n"
    "v_mfma_f32_16x16x32_bf16 v[64:67], v[184:187], v[204:207], v[64:67]\n"
    "v_mfma_f32_16x16x32_bf16 v[96:99], v[184:187], v[208:211], v[96:99]\n"
    "ds_read_b64_tr_b16 v[184:185], v235 offset:19968\n"
    "v_mfma_f32_16x16x32_bf16 v[96:99], v[188:191], v[220:223], v[96:99]\n"
    "ds_read_b64_tr_b16 v[186:187], v235 offset:24064\n"
    "s_waitcnt lgkmcnt(6)\n"
    "v_mfma_f32_16x16x32_bf16 v[36:39], v[228:231], v[200:203], v[36:39]\n"
    "ds_read_b64_tr_b16 v[188:189], v234 offset:17920\n"
    "v_mfma_f32_16x16x32_bf16 v[36:39], v[240:243], v[212:215], v[36:39]\n"
    "ds_read_b64_tr_b16 v[190:191], v234 offset:22016\n"
    "v_mfma_f32_16x16x32_bf16 v[68:71], v[240:243], v[216:219], v[68:71]\n"
    "buffer_load_dwordx4 v[152:155], v6, s[36:39], s18 offen offset:3072 sc1\n"
    "v_mfma_f32_16x16x32_bf16 v[68:71], v[228:231], v[204:207], v[68:71]\n"
    "buffer_load_dwordx4 v[156:159], v12, s[36:39], s18 offen offset:3072 sc1\n"
    "v_mfma_f32_16x16x32_bf16 v[100:103], v[228:231], v[208:211], v[100:103]\n"
    "ds_read_b64_tr_b16 v[228:229], v238\n"
    "v_mfma_f32_16x16x32_bf16 v[100:103], v[240:243], v[220:223], v[100:103]\n"
    "ds_read_b64_tr_b16 v[230:231], v238 offset:4096\n"
    "s_waitcnt lgkmcnt(6)\n"
    "v_mfma_f32_16x16x32_bf16 v[40:43], v[176:179], v[200:203], v[40:43]\n"
    "ds_read_b64_tr_b16 v[240:241], v238 offset:8192\n"
    "v_mfma_f32_16x16x32_bf16 v[40:43], v[180:183], v[212:215], v[40:43]\n"
    "ds_read_b64_tr_b16 v[242:243], v238 offset:12288\n"
    "v_mfma_f32_16x16x32_bf16 v[72:75], v[180:183], v[216:219], v[72:75]\n"
    "buffer_load_dwordx4 v[160:163], v11, s[36:39], s19 offen sc1\n"
    "v_mfma_f32_16x16x32_bf16 v[72:75], v[176:179], v[204:207], v[72:75]\n"
    "v_mfma_f32_16x16x32_bf16 v[104:107], v[176:179], v[208:211], v[104:107]\n"
    "ds_read_b64_tr_b16 v[176:177], v239\n"
    "v_mfma_f32_16x16x32_bf16 v[104:107], v[180:183], v[220:223], v[104:107]\n"
    "ds_read_b64_tr_b16 v[178:179], v239 offset:4096\n"
    "s_waitcnt lgkmcnt(6)\n"
    "v_mfma_f32_16x16x32_bf16 v[44:47], v[184:187], v[200:203], v[44:47]\n"
    "ds_read_b64_tr_b16 v[180:181], v239 offset:8192\n"
    "v_mfma_f32_16x16x32_bf16 v[44:47], v[188:191], v[212:215], v[44:47]\n"
    "ds_read_b64_tr_b16 v[182:183], v239 offset:12288\n"
    "v_mfma_f32_16x16x32_bf16 v[76:79], v[188:191], v[216:219], v[76:79]\n"
    "buffer_load_dwordx4 v[164:167], v6, s[36:39], s19 offen sc1\n"
    "v_mfma_f32_16x16x32_bf16 v[76:79], v[184:187], v[204:207], v[76:79]\n"
    "v_mfma_f32_16x16x32_bf16 v[108:111], v[184:187], v[208:211], v[108:111]\n"
    "ds_read_b64_tr_b16 v[184:185], v238 offset:512\n"
    "v_mfma_f32_16x16x32_bf16 v[108:111], v[188:191], v[220:223], v[108:111]\n"
    "ds_read_b64_tr_b16 v[186:187], v238 offset:4608\n"
    "s_waitcnt vmcnt(13) lgkmcnt(6)\n"
    "v_mfma_f32_16x16x32_bf16 v[16:19], v[228:231], v[112:115], v[16:19]\n"
    "ds_read_b64_tr_b16 v[188:189], v238 offset:8704\n"
    "s_waitcnt vmcnt(10)\n"
    "v_mfma_f32_16x16x32_bf16 v[16:19], v[240:243], v[124:127], v[16:19]\n"
    "ds_read_b64_tr_b16 v[190:191], v238 offset:12800\n"
    "s_waitcnt vmcnt(9)\n"
    "v_mfma_f32_16x16x32_bf16 v[48:51], v[240:243], v[128:131], v[48:51]\n"
    "buffer_load_dwordx4 v[168:171], v12, s[36:39], s19 offen sc1\n"
    "v_mfma_f32_16x16x32_bf16 v[48:51], v[228:231], v[116:119], v[48:51]\n"
    "buffer_load_dwordx4 v[172:175], v11, s[36:39], s19 offen offset:1024 sc1\n"
    "v_mfma_f32_16x16x32_bf16 v[80:83], v[228:231], v[120:123], v[80:83]\n"
    "ds_read_b64_tr_b16 v[228:229], v239 offset:512\n"
    "s_waitcnt vmcnt(10)\n"
    "v_mfma_f32_16x16x32_bf16 v[80:83], v[240:243], v[132:135], v[80:83]\n"
    "ds_read_b64_tr_b16 v[230:231], v239 offset:4608\n"
    "s_waitcnt lgkmcnt(6)\n"
    "v_mfma_f32_16x16x32_bf16 v[20:23], v[176:179], v[112:115], v[20:23]\n"
    "ds_read_b64_tr_b16 v[240:241], v239 offset:8704\n"
    "v_mfma_f32_16x16x32_bf16 v[20:23], v[180:183], v[124:127], v[20:23]\n"
    "ds_read_b64_tr_b16 v[242:243], v239 offset:12800\n"
    "v_mfma_f32_16x16x32_bf16 v[52:55], v[180:183], v[128:131], v[52:55]\n"
    "buffer_load_dwordx4 v[192:195], v6, s[36:39], s19 offen offset:1024 sc1\n"
    "v_mfma_f32_16x16x32_bf16 v[52:55], v[176:179], v[116:119], v[52:55]\n"
    "v_mfma_f32_16x16x32_bf16 v[84:87], v[176:179], v[120:123], v[84:87]\n"
    "v_mfma_f32_16x16x32_bf16 v[84:87], v[180:183], v[132:135], v[84:87]\n"
    "s_barrier\n"
    "s_waitcnt lgkmcnt(4)\n"
    "v_mfma_f32_16x16x32_bf16 v[24:27], v[184:187], v[112:115], v[24:27]\n"
    "ds_read_b64_tr_b16 v[176:177], v238 offset:1024\n"
    "v_mfma_f32_16x16x32_bf16 v[24:27], v[188:191], v[124:127], v[24:27]\n"
    "ds_read_b64_tr_b16 v[178:179], v238 offset:5120\n"
    "v_mfma_f32_16x16x32_bf16 v[56:59], v[188:191], v[128:131], v[56:59]\n"
    "ds_read_b64_tr_b16 v[180:181], v238 offset:9216\n"
    "v_mfma_f32_16x16x32_bf16 v[56:59], v[184:187], v[116:119], v[56:59]\n"
    "ds_read_b64_tr_b16 v[182:183], v238 offset:13312\n"
    "v_mfma_f32_16x16x32_bf16 v[88:91], v[184:187], v[120:123], v[88:91]\n"
    "buffer_load_dwordx4 v[196:199], v12, s[36:39], s19 offen offset:1024 sc1\n"
    "v_mfma_f32_16x16x32_bf16 v[88:91], v[188:191], v[132:135], v[88:91]\n"
    "ds_read_b64_tr_b16 v[184:185], v239 offset:1024\n"
    "s_waitcnt lgkmcnt(5)\n"
    "v_mfma_f32_16x16x32_bf16 v[28:31], v[228:231], v[112:115], v[28:31]\n"
    "ds_read_b64_tr_b16 v[186:187], v239 offset:5120\n"
    "v_mfma_f32_16x16x32_bf16 v[28:31], v[240:243], v[124:127], v[28:31]\n"
    "ds_read_b64_tr_b16 v[188:189], v239 offset:9216\n"
    "v_mfma_f32_16x16x32_bf16 v[60:63], v[240:243], v[128:131], v[60:63]\n"
    "ds_read_b64_tr_b16 v[190:191], v239 offset:13312\n"
    "v_mfma_f32_16x16x32_bf16 v[60:63], v[228:231], v[116:119], v[60:63]\n"
    "buffer_load_dwordx4 v[200:203], v11, s[36:39], s19 offen offset:2048 sc1\n"
    "v_mfma_f32_16x16x32_bf16 v[92:95], v[228:231], v[120:123], v[92:95]\n"
    "buffer_load_dwordx4 v[204:207], v6, s[36:39], s19 offen offset:2048 sc1\n"
    "v_mfma_f32_16x16x32_bf16 v[92:95], v[240:243], v[132:135], v[92:95]\n"
    "ds_read_b64_tr_b16 v[228:229], v238 offset:16384\n"
    "s_waitcnt lgkmcnt(5)\n"
    "v_mfma_f32_16x16x32_bf16 v[32:35], v[176:179], v[112:115], v[32:35]\n"
    "ds_read_b64_tr_b16 v[230:231], v238 offset:20480\n"
    "v_mfma_f32_16x16x32_bf16 v[32:35], v[180:183], v[124:127], v[32:35]\n"
    "ds_read_b64_tr_b16 v[240:241], v238 offset:24576\n"
    "v_mfma_f32_16x16x32_bf16 v[64:67], v[180:183], v[128:131], v[64:67]\n"
    "ds_read_b64_tr_b16 v[242:243], v238 offset:28672\n"
    "v_mfma_f32_16x16x32_bf16 v[64:67], v[176:179], v[116:119], v[64:67]\n"
    "buffer_load_dwordx4 v[208:211], v12, s[36:39], s19 offen offset:2048 sc1\n"
    "v_mfma_f32_16x16x32_bf16 v[96:99], v[176:179], v[120:123], v[96:99]\n"
    "ds_read_b64_tr_b16 v[176:177], v239 offset:16384\n"
    "v_mfma_f32_16x16x32_bf16 v[96:99], v[180:183], v[132:135], v[96:99]\n"
    "ds_read_b64_tr_b16 v[178:179], v239 offset:20480\n"
    "s_waitcnt lgkmcnt(6)\n"
    "v_mfma_f32_16x16x32_bf16 v[36:39], v[184:187], v[112:115], v[36:39]\n"
    "ds_read_b64_tr_b16 v[180:181], v239 offset:24576\n"
    "v_mfma_f32_16x16x32_bf16 v[36:39], v[188:191], v[124:127], v[36:39]\n"
    "ds_read_b64_tr_b16 v[182:183], v239 offset:28672\n"
    "v_mfma_f32_16x16x32_bf16 v[68:71], v[188:191], v[128:131], v[68:71]\n"
    "buffer_load_dwordx4 v[212:215], v11, s[36:39], s19 offen offset:3072 sc1\n"
    "v_mfma_f32_16x16x32_bf16 v[68:71], v[184:187], v[116:119], v[68:71]\n"
    "v_mfma_f32_16x16x32_bf16 v[100:103], v[184:187], v[120:123], v[100:103]\n"
    "ds_read_b64_tr_b16 v[184:185], v238 offset:16896\n"
    "v_mfma_f32_16x16x32_bf16 v[100:103], v[188:191], v[132:135], v[100:103]\n"
    "ds_read_b64_tr_b16 v[186:187], v238 offset:20992\n"
    "s_waitcnt vmcnt(15) lgkmcnt(6)\n"
    "v_mfma_f32_16x16x32_bf16 v[16:19], v[228:231], v[136:139], v[16:19]\n"
    "ds_read_b64_tr_b16 v[188:189], v238 offset:25088\n"
    "s_waitcnt vmcnt(12)\n"
    "v_mfma_f32_16x16x32_bf16 v[16:19], v[240:243], v[148:151], v[16:19]\n"
    "ds_read_b64_tr_b16 v[190:191], v238 offset:29184\n"
    "s_waitcnt vmcnt(11)\n"
    "v_mfma_f32_16x16x32_bf16 v[48:51], v[240:243], v[152:155], v[48:51]\n"
    "buffer_load_dwordx4 v[216:219], v6, s[36:39], s19 offen offset:3072 sc1\n"
    "v_mfma_f32_16x16x32_bf16 v[48:51], v[228:231], v[140:143], v[48:51]\n"
    "buffer_load_dwordx4 v[220:223], v12, s[36:39], s19 offen offset:3072 sc1\n"
    "v_mfma_f32_16x16x32_bf16 v[80:83], v[228:231], v[144:147], v[80:83]\n"
    "ds_read_b64_tr_b16 v[228:229], v239 offset:16896\n"
    "s_waitcnt vmcnt(12)\n"
    "v_mfma_f32_16x16x32_bf16 v[80:83], v[240:243], v[156:159], v[80:83]\n"
    "ds_read_b64_tr_b16 v[230:231], v239 offset:20992\n"
    "s_waitcnt lgkmcnt(6)\n"
    "v_mfma_f32_16x16x32_bf16 v[20:23], v[176:179], v[136:139], v[20:23]\n"
    "ds_read_b64_tr_b16 v[240:241], v239 offset:25088\n"
    "v_mfma_f32_16x16x32_bf16 v[20:23], v[180:183], v[148:151], v[20:23]\n"
    "ds_read_b64_tr_b16 v[242:243], v239 offset:29184\n"
    "v_mfma_f32_16x16x32_bf16 v[52:55], v[180:183], v[152:155], v[52:55]\n"
    "v_mfma_f32_16x16x32_bf16 v[52:55], v[176:179], v[140:143], v[52:55]\n"
    "v_mfma_f32_16x16x32_bf16 v[84:87], v[176:179], v[144:147], v[84:87]\n"
    "ds_read_b64_tr_b16 v[176:177], v238 offset:17408\n"
    "v_mfma_f32_16x16x32_bf16 v[84:87], v[180:183], v[156:159], v[84:87]\n"
    "ds_read_b64_tr_b16 v[178:179], v238 offset:21504\n"
    "s_waitcnt lgkmcnt(6)\n"
    "v_mfma_f32_16x16x32_bf16 v[24:27], v[184:187], v[136:139], v[24:27]\n"
    "ds_read_b64_tr_b16 v[180:181], v238 offset:25600\n"
    "v_mfma_f32_16x16x32_bf16 v[24:27], v[188:191], v[148:151], v[24:27]\n"
    "ds_read_b64_tr_b16 v[182:183], v238 offset:29696\n"
    "v_mfma_f32_16x16x32_bf16 v[56:59], v[188:191], v[152:155], v[56:59]\n"
    "v_mfma_f32_16x16x32_bf16 v[56:59], v[184:187], v[140:143], v[56:59]\n"
    "v_mfma_f32_16x16x32_bf16 v[88:91], v[184:187], v[144:147], v[88:91]\n"
    "ds_read_b64_tr_b16 v[184:185], v239 offset:17408\n"
    "v_mfma_f32_16x16x32_bf16 v[88:91], v[188:191], v[156:159], v[88:91]\n"
    "ds_read_b64_tr_b16 v[186:187], v239 offset:21504\n"
    "s_waitcnt lgkmcnt(6)\n"
    "v_mfma_f32_16x16x32_bf16 v[28:31], v[228:231], v[136:139], v[28:31]\n"
    "ds_read_b64_tr_b16 v[188:189], v239 offset:25600\n"
    "v_mfma_f32_16x16x32_bf16 v[28:31], v[240:243], v[148:151], v[28:31]\n"
    "ds_read_b64_tr_b16 v[190:191], v239 offset:29696\n"
    "v_mfma_f32_16x16x32_bf16 v[60:63], v[240:243], v[152:155], v[60:63]\n"
    "v_mfma_f32_16x16x32_bf16 v[60:63], v[228:231], v[140:143], v[60:63]\n"
    "v_mfma_f32_16x16x32_bf16 v[92:95], v[228:231], v[144:147], v[92:95]\n"
    "ds_read_b64_tr_b16 v[228:229], v238 offset:32768\n"
    "v_mfma_f32_16x16x32_bf16 v[92:95], v[240:243], v[156:159], v[92:95]\n"
    "ds_read_b64_tr_b16 v[230:231], v238 offset:36864\n"
    "s_waitcnt lgkmcnt(6)\n"
    "v_mfma_f32_16x16x32_bf16 v[32:35], v[176:179], v[136:139], v[32:35]\n"
    "ds_read_b64_tr_b16 v[240:241], v238 offset:40960\n"
    "v_mfma_f32_16x16x32_bf16 v[32:35], v[180:183], v[148:151], v[32:35]\n"
    "ds_read_b64_tr_b16 v[242:243], v238 offset:45056\n"
    "v_mfma_f32_16x16x32_bf16 v[64:67], v[180:183], v[152:155], v[64:67]\n"
    "v_mfma_f32_16x16x32_bf16 v[64:67], v[176:179], v[140:143], v[64:67]\n"
    "v_mfma_f32_16x16x32_bf16 v[96:99], v[176:179], v[144:147], v[96:99]\n"
    "ds_read_b64_tr_b16 v[176:177], v238 offset:49152\n"
    "v_mfma_f32_16x16x32_bf16 v[96:99], v[180:183], v[156:159], v[96:99]\n"
    "ds_read_b64_tr_b16 v[178:179], v238 offset:53248\n"
    "s_waitcnt lgkmcnt(6)\n"
    "v_mfma_f32_16x16x32_bf16 v[36:39], v[184:187], v[136:139], v[36:39]\n"
    "ds_read_b64_tr_b16 v[180:181], v232 offset:24576\n"
    "v_mfma_f32_16x16x32_bf16 v[36:39], v[188:191], v[148:151], v[36:39]\n"
    "ds_read_b64_tr_b16 v[182:183], v232 offset:28672\n"
    "v_mfma_f32_16x16x32_bf16 v[68:71], v[188:191], v[152:155], v[68:71]\n"
    "v_mfma_f32_16x16x32_bf16 v[68:71], v[184:187], v[140:143], v[68:71]\n"
    "v_mfma_f32_16x16x32_bf16 v[100:103], v[184:187], v[144:147], v[100:103]\n"
    "ds_read_b64_tr_b16 v[184:185], v239 offset:32768\n"
    "v_mfma_f32_16x16x32_bf16 v[100:103], v[188:191], v[156:159], v[100:103]\n"
    "ds_read_b64_tr_b16 v[186:187], v239 offset:36864\n"
    "s_waitcnt vmcnt(11) lgkmcnt(6)\n"
    "v_mfma_f32_16x16x32_bf16 v[16:19], v[228:231], v[160:163], v[16:19]\n"
    "ds_read_b64_tr_b16 v[188:189], v239 offset:40960\n"
    "s_waitcnt vmcnt(8)\n"
    "v_mfma_f32_16x16x32_bf16 v[16:19], v[240:243], v[172:175], v[16:19]\n"
    "ds_read_b64_tr_b16 v[190:191], v239 offset:45056\n"
    "s_waitcnt vmcnt(7)\n"
    "v_mfma_f32_16x16x32_bf16 v[48:51], v[240:243], v[192:195], v[48:51]\n"
    "v_mfma_f32_16x16x32_bf16 v[48:51], v[228:231], v[164:167], v[48:51]\n"
    "v_mfma_f32_16x16x32_bf16 v[80:83], v[228:231], v[168:171], v[80:83]\n"
    "ds_read_b64_tr_b16 v[228:229], v239 offset:49152\n"
    "s_waitcnt vmcnt(6)\n"
    "v_mfma_f32_16x16x32_bf16 v[80:83], v[240:243], v[196:199], v[80:83]\n"
    "ds_read_b64_tr_b16 v[230:231], v239 offset:53248\n"
    "s_waitcnt vmcnt(5) lgkmcnt(6)\n"
    "v_mfma_f32_16x16x32_bf16 v[16:19], v[176:179], v[200:203], v[16:19]\n"
    "ds_read_b64_tr_b16 v[240:241], v234 offset:24576\n"
    "s_waitcnt vmcnt(2)\n"
    "v_mfma_f32_16x16x32_bf16 v[16:19], v[180:183], v[212:215], v[16:19]\n"
    "ds_read_b64_tr_b16 v[242:243], v234 offset:28672\n"
    "s_waitcnt vmcnt(1)\n"
    "v_mfma_f32_16x16x32_bf16 v[48:51], v[180:183], v[216:219], v[48:51]\n"
    "v_mfma_f32_16x16x32_bf16 v[48:51], v[176:179], v[204:207], v[48:51]\n"
    "v_mfma_f32_16x16x32_bf16 v[80:83], v[176:179], v[208:211], v[80:83]\n"
    "ds_read_b64_tr_b16 v[176:177], v238 offset:33280\n"
    "s_waitcnt vmcnt(0)\n"
    "v_mfma_f32_16x16x32_bf16 v[80:83], v[180:183], v[220:223], v[80:83]\n"
    "ds_read_b64_tr_b16 v[178:179], v238 offset:37376\n"
    "s_waitcnt lgkmcnt(6)\n"
    "v_mfma_f32_16x16x32_bf16 v[20:23], v[184:187], v[160:163], v[20:23]\n"
    "ds_read_b64_tr_b16 v[180:181], v238 offset:41472\n"
    "v_mfma_f32_16x16x32_bf16 v[20:23], v[188:191], v[172:175], v[20:23]\n"
    "ds_read_b64_tr_b16 v[182:183], v238 offset:45568\n"
    "v_mfma_f32_16x16x32_bf16 v[52:55], v[188:191], v[192:195], v[52:55]\n"
    "v_mfma_f32_16x16x32_bf16 v[52:55], v[184:187], v[164:167], v[52:55]\n"
    "v_mfma_f32_16x16x32_bf16 v[84:87], v[184:187], v[168:171], v[84:87]\n"
    "ds_read_b64_tr_b16 v[184:185], v238 offset:49664\n"
    "v_mfma_f32_16x16x32_bf16 v[84:87], v[188:191], v[196:199], v[84:87]\n"
    "ds_read_b64_tr_b16 v[186:187], v238 offset:53760\n"
    "s_waitcnt lgkmcnt(6)\n"
    "v_mfma_f32_16x16x32_bf16 v[20:23], v[228:231], v[200:203], v[20:23]\n"
    "ds_read_b64_tr_b16 v[188:189], v232 offset:25088\n"
    "v_mfma_f32_16x16x32_bf16 v[20:23], v[240:243], v[212:215], v[20:23]\n"
    "ds_read_b64_tr_b16 v[190:191], v232 offset:29184\n"
    "v_mfma_f32_16x16x32_bf16 v[52:55], v[240:243], v[216:219], v[52:55]\n"
    "v_mfma_f32_16x16x32_bf16 v[52:55], v[228:231], v[204:207], v[52:55]\n"
    "v_mfma_f32_16x16x32_bf16 v[84:87], v[228:231], v[208:211], v[84:87]\n"
    "ds_read_b64_tr_b16 v[228:229], v239 offset:33280\n"
    "v_mfma_f32_16x16x32_bf16 v[84:87], v[240:243], v[220:223], v[84:87]\n"
    "ds_read_b64_tr_b16 v[230:231], v239 offset:37376\n"
    "s_waitcnt lgkmcnt(6)\n"
    "v_mfma_f32_16x16x32_bf16 v[24:27], v[176:179], v[160:163], v[24:27]\n"
    "ds_read_b64_tr_b16 v[240:241], v239 offset:41472\n"
    "v_mfma_f32_16x16x32_bf16 v[24:27], v[180:183], v[172:175], v[24:27]\n"
    "ds_read_b64_tr_b16 v[242:243], v239 offset:45568\n"
    "v_mfma_f32_16x16x32_bf16 v[56:59], v[180:183], v[192:195], v[56:59]\n"
    "s_xor_b32 s60, s90, 3\n"
    "v_mfma_f32_16x16x32_bf16 v[56:59], v[176:179], v[164:167], v[56:59]\n"
    "s_mul_i32 s60, s60, 0x18000\n"
    "v_mfma_f32_16x16x32_bf16 v[88:91], v[176:179], v[168:171], v[88:91]\n"
    "s_add_u32 s60, s60, s86\n"
    "v_mfma_f32_16x16x32_bf16 v[88:91], v[180:183], v[196:199], v[88:91]\n"
    "s_add_u32 s61, s60, 0x1000\n"
    "s_waitcnt lgkmcnt(4)\n"
    "v_mfma_f32_16x16x32_bf16 v[24:27], v[184:187], v[200:203], v[24:27]\n"
    "ds_read_b64_tr_b16 v[176:177], v239 offset:49664\n"
    "v_mfma_f32_16x16x32_bf16 v[24:27], v[188:191], v[212:215], v[24:27]\n"
    "ds_read_b64_tr_b16 v[178:179], v239 offset:53760\n"
    "v_mfma_f32_16x16x32_bf16 v[56:59], v[188:191], v[216:219], v[56:59]\n"
    "ds_read_b64_tr_b16 v[180:181], v234 offset:25088\n"
    "v_mfma_f32_16x16x32_bf16 v[56:59], v[184:187], v[204:207], v[56:59]\n"
    "ds_read_b64_tr_b16 v[182:183], v234 offset:29184\n"
    "v_mfma_f32_16x16x32_bf16 v[88:91], v[184:187], v[208:211], v[88:91]\n"
    "v_cvt_pk_bf16_f32 v16, v16, v17\n"
    "v_mfma_f32_16x16x32_bf16 v[88:91], v[188:191], v[220:223], v[88:91]\n"
    "v_cvt_pk_bf16_f32 v17, v18, v19\n"
    "v_cvt_pk_bf16_f32 v18, v20, v21\n"
    "v_cvt_pk_bf16_f32 v19, v22, v23\n"
    "buffer_store_dwordx4 v[16:19], v11, s[24:27], s60 offen sc1\n"
    "s_waitcnt lgkmcnt(4)\n"
    "v_mfma_f32_16x16x32_bf16 v[28:31], v[228:231], v[160:163], v[28:31]\n"
    "ds_read_b64_tr_b16 v[184:185], v238 offset:33792\n"
    "v_mfma_f32_16x16x32_bf16 v[28:31], v[240:243], v[172:175], v[28:31]\n"
    "ds_read_b64_tr_b16 v[186:187], v238 offset:37888\n"
    "v_mfma_f32_16x16x32_bf16 v[60:63], v[240:243], v[192:195], v[60:63]\n"
    "ds_read_b64_tr_b16 v[188:189], v238 offset:41984\n"
    "v_mfma_f32_16x16x32_bf16 v[60:63], v[228:231], v[164:167], v[60:63]\n"
    "ds_read_b64_tr_b16 v[190:191], v238 offset:46080\n"
    "v_mfma_f32_16x16x32_bf16 v[92:95], v[228:231], v[168:171], v[92:95]\n"
    "v_cvt_pk_bf16_f32 v48, v48, v49\n"
    "v_mfma_f32_16x16x32_bf16 v[92:95], v[240:243], v[196:199], v[92:95]\n"
    "v_cvt_pk_bf16_f32 v49, v50, v51\n"
    "v_cvt_pk_bf16_f32 v50, v52, v53\n"
    "v_cvt_pk_bf16_f32 v51, v54, v55\n"
    "buffer_store_dwordx4 v[48:51], v11, s[24:27], s60 offen offset:1024 sc1\n"
    "s_waitcnt lgkmcnt(4)\n"
    "v_mfma_f32_16x16x32_bf16 v[28:31], v[176:179], v[200:203], v[28:31]\n"
    "ds_read_b64_tr_b16 v[228:229], v238 offset:50176\n"
    "v_mfma_f32_16x16x32_bf16 v[28:31], v[180:183], v[212:215], v[28:31]\n"
    "ds_read_b64_tr_b16 v[230:231], v238 offset:54272\n"
    "v_mfma_f32_16x16x32_bf16 v[60:63], v[180:183], v[216:219], v[60:63]\n"
    "ds_read_b64_tr_b16 v[240:241], v232 offset:25600\n"
    "v_mfma_f32_16x16x32_bf16 v[60:63], v[176:179], v[204:207], v[60:63]\n"
    "ds_read_b64_tr_b16 v[242:243], v232 offset:29696\n"
    "v_mfma_f32_16x16x32_bf16 v[92:95], v[176:179], v[208:211], v[92:95]\n"
    "v_cvt_pk_bf16_f32 v80, v80, v81\n"
    "v_mfma_f32_16x16x32_bf16 v[92:95], v[180:183], v[220:223], v[92:95]\n"
    "v_cvt_pk_bf16_f32 v81, v82, v83\n"
    "v_cvt_pk_bf16_f32 v82, v84, v85\n"
    "v_cvt_pk_bf16_f32 v83, v86, v87\n"
    "buffer_store_dwordx4 v[80:83], v11, s[24:27], s60 offen offset:2048 sc1\n"
    "s_waitcnt lgkmcnt(4)\n"
    "v_mfma_f32_16x16x32_bf16 v[32:35], v[184:187], v[160:163], v[32:35]\n"
    "ds_read_b64_tr_b16 v[176:177], v239 offset:33792\n"
    "v_mfma_f32_16x16x32_bf16 v[32:35], v[188:191], v[172:175], v[32:35]\n"
    "ds_read_b64_tr_b16 v[178:179], v239 offset:37888\n"
    "v_mfma_f32_16x16x32_bf16 v[64:67], v[188:191], v[192:195], v[64:67]\n"
    "ds_read_b64_tr_b16 v[180:181], v239 offset:41984\n"
    "v_mfma_f32_16x16x32_bf16 v[64:67], v[184:187], v[164:167], v[64:67]\n"
    "ds_read_b64_tr_b16 v[182:183], v239 offset:46080\n"
    "v_mfma_f32_16x16x32_bf16 v[96:99], v[184:187], v[168:171], v[96:99]\n"
    "s_xor_b32 s60, s90, 2\n"
    "v_mfma_f32_16x16x32_bf16 v[96:99], v[188:191], v[196:199], v[96:99]\n"
    "s_mul_i32 s60, s60, 0x18000\n"
    "s_add_u32 s60, s60, s86\n"
    "s_add_u32 s61, s60, 0x1000\n"
    "s_waitcnt lgkmcnt(4)\n"
    "v_mfma_f32_16x16x32_bf16 v[32:35], v[228:231], v[200:203], v[32:35]\n"
    "ds_read_b64_tr_b16 v[184:185], v239 offset:50176\n"
    "v_mfma_f32_16x16x32_bf16 v[32:35], v[240:243], v[212:215], v[32:35]\n"
    "ds_read_b64_tr_b16 v[186:187], v239 offset:54272\n"
    "v_mfma_f32_16x16x32_bf16 v[64:67], v[240:243], v[216:219], v[64:67]\n"
    "ds_read_b64_tr_b16 v[188:189], v234 offset:25600\n"
    "v_mfma_f32_16x16x32_bf16 v[64:67], v[228:231], v[204:207], v[64:67]\n"
    "ds_read_b64_tr_b16 v[190:191], v234 offset:29696\n"
    "v_mfma_f32_16x16x32_bf16 v[96:99], v[228:231], v[208:211], v[96:99]\n"
    "v_cvt_pk_bf16_f32 v24, v24, v25\n"
    "v_mfma_f32_16x16x32_bf16 v[96:99], v[240:243], v[220:223], v[96:99]\n"
    "v_cvt_pk_bf16_f32 v25, v26, v27\n"
    "v_cvt_pk_bf16_f32 v26, v28, v29\n"
    "v_cvt_pk_bf16_f32 v27, v30, v31\n"
    "buffer_store_dwordx4 v[24:27], v11, s[24:27], s60 offen sc1\n"
    "s_waitcnt lgkmcnt(4)\n"
    "v_mfma_f32_16x16x32_bf16 v[36:39], v[176:179], v[160:163], v[36:39]\n"
    "ds_read_b64_tr_b16 v[228:229], v238 offset:1536\n"
    "v_mfma_f32_16x16x32_bf16 v[36:39], v[180:183], v[172:175], v[36:39]\n"
    "ds_read_b64_tr_b16 v[230:231], v238 offset:5632\n"
    "v_mfma_f32_16x16x32_bf16 v[68:71], v[180:183], v[192:195], v[68:71]\n"
    "ds_read_b64_tr_b16 v[240:241], v238 offset:9728\n"
    "v_mfma_f32_16x16x32_bf16 v[68:71], v[176:179], v[164:167], v[68:71]\n"
    "ds_read_b64_tr_b16 v[242:243], v238 offset:13824\n"
    "v_mfma_f32_16x16x32_bf16 v[100:103], v[176:179], v[168:171], v[100:103]\n"
    "v_cvt_pk_bf16_f32 v56, v56, v57\n"
    "v_mfma_f32_16x16x32_bf16 v[100:103], v[180:183], v[196:199], v[100:103]\n"
    "v_cvt_pk_bf16_f32 v57, v58, v59\n"
    "v_cvt_pk_bf16_f32 v58, v60, v61\n"
    "v_cvt_pk_bf16_f32 v59, v62, v63\n"
    "buffer_store_dwordx4 v[56:59], v11, s[24:27], s60 offen offset:1024 sc1\n"
    "s_waitcnt lgkmcnt(4)\n"
    "v_mfma_f32_16x16x32_bf16 v[36:39], v[184:187], v[200:203], v[36:39]\n"
    "ds_read_b64_tr_b16 v[176:177], v238 offset:17920\n"
    "v_mfma_f32_16x16x32_bf16 v[36:39], v[188:191], v[212:215], v[36:39]\n"
    "ds_read_b64_tr_b16 v[178:179], v238 offset:22016\n"
    "v_mfma_f32_16x16x32_bf16 v[68:71], v[188:191], v[216:219], v[68:71]\n"
    "ds_read_b64_tr_b16 v[180:181], v238 offset:26112\n"
    "v_mfma_f32_16x16x32_bf16 v[68:71], v[184:187], v[204:207], v[68:71]\n"
    "ds_read_b64_tr_b16 v[182:183], v238 offset:30208\n"
    "v_mfma_f32_16x16x32_bf16 v[100:103], v[184:187], v[208:211], v[100:103]\n"
    "v_cvt_pk_bf16_f32 v88, v88, v89\n"
    "v_mfma_f32_16x16x32_bf16 v[100:103], v[188:191], v[220:223], v[100:103]\n"
    "v_cvt_pk_bf16_f32 v89, v90, v91\n"
    "v_cvt_pk_bf16_f32 v90, v92, v93\n"
    "v_cvt_pk_bf16_f32 v91, v94, v95\n"
    "buffer_store_dwordx4 v[88:91], v11, s[24:27], s60 offen offset:2048 sc1\n"
    "s_waitcnt lgkmcnt(4)\n"
    "v_mfma_f32_16x16x32_bf16 v[40:43], v[228:231], v[112:115], v[40:43]\n"
    "ds_read_b64_tr_b16 v[184:185], v238 offset:34304\n"
    "v_mfma_f32_16x16x32_bf16 v[40:43], v[240:243], v[124:127], v[40:43]\n"
    "ds_read_b64_tr_b16 v[186:187], v238 offset:38400\n"
    "v_mfma_f32_16x16x32_bf16 v[72:75], v[240:243], v[128:131], v[72:75]\n"
    "ds_read_b64_tr_b16 v[188:189], v238 offset:42496\n"
    "v_mfma_f32_16x16x32_bf16 v[72:75], v[228:231], v[116:119], v[72:75]\n"
    "ds_read_b64_tr_b16 v[190:191], v238 offset:46592\n"
    "v_mfma_f32_16x16x32_bf16 v[104:107], v[228:231], v[120:123], v[104:107]\n"
    "s_xor_b32 s60, s90, 1\n"
    "v_mfma_f32_16x16x32_bf16 v[104:107], v[240:243], v[132:135], v[104:107]\n"
    "s_mul_i32 s60, s60, 0x18000\n"
    "s_add_u32 s60, s60, s86\n"
    "s_add_u32 s61, s60, 0x1000\n"
    "s_waitcnt lgkmcnt(4)\n"
    "v_mfma_f32_16x16x32_bf16 v[40:43], v[176:179], v[136:139], v[40:43]\n"
    "ds_read_b64_tr_b16 v[228:229], v238 offset:50688\n"
    "v_mfma_f32_16x16x32_bf16 v[40:43], v[180:183], v[148:151], v[40:43]\n"
    "ds_read_b64_tr_b16 v[230:231], v238 offset:54784\n"
    "v_mfma_f32_16x16x32_bf16 v[72:75], v[180:183], v[152:155], v[72:75]\n"
    "ds_read_b64_tr_b16 v[240:241], v232 offset:26112\n"
    "v_mfma_f32_16x16x32_bf16 v[72:75], v[176:179], v[140:143], v[72:75]\n"
    "ds_read_b64_tr_b16 v[242:243], v232 offset:30208\n"
    "v_mfma_f32_16x16x32_bf16 v[104:107], v[176:179], v[144:147], v[104:107]\n"
    "v_cvt_pk_bf16_f32 v32, v32, v33\n"
    "v_mfma_f32_16x16x32_bf16 v[104:107], v[180:183], v[156:159], v[104:107]\n"
    "v_cvt_pk_bf16_f32 v33, v34, v35\n"
    "v_cvt_pk_bf16_f32 v34, v36, v37\n"
    "v_cvt_pk_bf16_f32 v35, v38, v39\n"
    "buffer_store_dwordx4 v[32:35], v11, s[24:27], s60 offen sc1\n"
    "s_waitcnt lgkmcnt(4)\n"
    "v_mfma_f32_16x16x32_bf16 v[40:43], v[184:187], v[160:163], v[40:43]\n"
    "ds_read_b64_tr_b16 v[176:177], v239 offset:1536\n"
    "v_mfma_f32_16x16x32_bf16 v[40:43], v[188:191], v[172:175], v[40:43]\n"
    "ds_read_b64_tr_b16 v[178:179], v239 offset:5632\n"
    "v_mfma_f32_16x16x32_bf16 v[72:75], v[188:191], v[192:195], v[72:75]\n"
    "ds_read_b64_tr_b16 v[180:181], v239 offset:9728\n"
    "v_mfma_f32_16x16x32_bf16 v[72:75], v[184:187], v[164:167], v[72:75]\n"
    "ds_read_b64_tr_b16 v[182:183], v239 offset:13824\n"
    "v_mfma_f32_16x16x32_bf16 v[104:107], v[184:187], v[168:171], v[104:107]\n"
    "v_cvt_pk_bf16_f32 v64, v64, v65\n"
    "v_mfma_f32_16x16x32_bf16 v[104:107], v[188:191], v[196:199], v[104:107]\n"
    "v_cvt_pk_bf16_f32 v65, v66, v67\n"
    "v_cvt_pk_bf16_f32 v66, v68, v69\n"
    "v_cvt_pk_bf16_f32 v67, v70, v71\n"
    "buffer_store_dwordx4 v[64:67], v11, s[24:27], s60 offen offset:1024 sc1\n"
    "s_waitcnt lgkmcnt(4)\n"
    "v_mfma_f32_16x16x32_bf16 v[40:43], v[228:231], v[200:203], v[40:43]\n"
    "ds_read_b64_tr_b16 v[184:185], v239 offset:17920\n"
    "v_mfma_f32_16x16x32_bf16 v[40:43], v[240:243], v[212:215], v[40:43]\n"
    "ds_read_b64_tr_b16 v[186:187], v239 offset:22016\n"
    "v_mfma_f32_16x16x32_bf16 v[72:75], v[240:243], v[216:219], v[72:75]\n"
    "ds_read_b64_tr_b16 v[188:189], v239 offset:26112\n"
    "v_mfma_f32_16x16x32_bf16 v[72:75], v[228:231], v[204:207], v[72:75]\n"
    "ds_read_b64_tr_b16 v[190:191], v239 offset:30208\n"
    "v_mfma_f32_16x16x32_bf16 v[104:107], v[228:231], v[208:211], v[104:107]\n"
    "v_cvt_pk_bf16_f32 v96, v96, v97\n"
    "v_mfma_f32_16x16x32_bf16 v[104:107], v[240:243], v[220:223], v[104:107]\n"
    "v_cvt_pk_bf16_f32 v97, v98, v99\n"
    "v_cvt_pk_bf16_f32 v98, v100, v101\n"
    "v_cvt_pk_bf16_f32 v99, v102, v103\n"
    "buffer_store_dwordx4 v[96:99], v11, s[24:27], s60 offen offset:2048 sc1\n"
    "s_waitcnt lgkmcnt(4)\n"
    "v_mfma_f32_16x16x32_bf16 v[44:47], v[176:179], v[112:115], v[44:47]\n"
    "ds_read_b64_tr_b16 v[228:229], v239 offset:34304\n"
    "v_mfma_f32_16x16x32_bf16 v[44:47], v[180:183], v[124:127], v[44:47]\n"
    "ds_read_b64_tr_b16 v[230:231], v239 offset:38400\n"
    "v_mfma_f32_16x16x32_bf16 v[76:79], v[180:183], v[128:131], v[76:79]\n"
    "ds_read_b64_tr_b16 v[240:241], v239 offset:42496\n"
    "v_mfma_f32_16x16x32_bf16 v[76:79], v[176:179], v[116:119], v[76:79]\n"
    "ds_read_b64_tr_b16 v[242:243], v239 offset:46592\n"
    "v_mfma_f32_16x16x32_bf16 v[108:111], v[176:179], v[120:123], v[108:111]\n"
    "ds_read_b64_tr_b16 v[176:177], v239 offset:50688\n"
    "v_mfma_f32_16x16x32_bf16 v[108:111], v[180:183], v[132:135], v[108:111]\n"
    "ds_read_b64_tr_b16 v[178:179], v239 offset:54784\n"
    "s_waitcnt lgkmcnt(6)\n"
    "v_mfma_f32_16x16x32_bf16 v[44:47], v[184:187], v[136:139], v[44:47]\n"
    "ds_read_b64_tr_b16 v[180:181], v234 offset:26112\n"
    "v_mfma_f32_16x16x32_bf16 v[44:47], v[188:191], v[148:151], v[44:47]\n"
    "ds_read_b64_tr_b16 v[182:183], v234 offset:30208\n"
    "v_mfma_f32_16x16x32_bf16 v[76:79], v[188:191], v[152:155], v[76:79]\n"
    "v_mfma_f32_16x16x32_bf16 v[76:79], v[184:187], v[140:143], v[76:79]\n"
    "v_mfma_f32_16x16x32_bf16 v[108:111], v[184:187], v[144:147], v[108:111]\n"
    "v_mfma_f32_16x16x32_bf16 v[108:111], v[188:191], v[156:159], v[108:111]\n"
    "s_waitcnt lgkmcnt(4)\n"
    "v_mfma_f32_16x16x32_bf16 v[44:47], v[228:231], v[160:163], v[44:47]\n"
    "v_mfma_f32_16x16x32_bf16 v[44:47], v[240:243], v[172:175], v[44:47]\n"
    "v_mfma_f32_16x16x32_bf16 v[76:79], v[240:243], v[192:195], v[76:79]\n"
    "v_mfma_f32_16x16x32_bf16 v[76:79], v[228:231], v[164:167], v[76:79]\n"
    "v_mfma_f32_16x16x32_bf16 v[108:111], v[228:231], v[168:171], v[108:111]\n"
    "v_mfma_f32_16x16x32_bf16 v[108:111], v[240:243], v[196:199], v[108:111]\n"
    "s_waitcnt lgkmcnt(0)\n"
    "v_mfma_f32_16x16x32_bf16 v[44:47], v[176:179], v[200:203], v[44:47]\n"
    "v_mfma_f32_16x16x32_bf16 v[44:47], v[180:183], v[212:215], v[44:47]\n"
    "v_mfma_f32_16x16x32_bf16 v[76:79], v[180:183], v[216:219], v[76:79]\n"
    "v_mfma_f32_16x16x32_bf16 v[76:79], v[176:179], v[204:207], v[76:79]\n"
    "v_mfma_f32_16x16x32_bf16 v[108:111], v[176:179], v[208:211], v[108:111]\n"
    "v_mfma_f32_16x16x32_bf16 v[108:111], v[180:183], v[220:223], v[108:111]\n"
    ".Lkte_168:\n"
    "s_lshl2_add_u32 s60, s91, s12\n"
    "s_mul_i32 s60, s60, 0x1e000\n"
    "s_mul_i32 s61, s89, 0x500\n"
    "s_add_u32 s60, s60, s61\n"
    "s_lshl_b32 s61, s99, 8\n"
    "s_add_u32 s60, s60, s61\n"
    "s_lshl_b32 s61, s90, 6\n"
    "s_add_u32 s60, s60, s61\n"
    "v_and_b32 v247, 15, v1\n"
    "v_mul_u32_u24 v247, 0xa00, v247\n"
    "v_lshrrev_b32 v249, 4, v1\n"
    "v_and_b32 v249, 1, v249\n"
    "v_lshlrev_b32 v249, 5, v249\n"
    "v_add_u32 v247, v247, v249\n"
    "v_lshrrev_b32 v249, 5, v1\n"
    "v_lshlrev_b32 v249, 4, v249\n"
    "v_add_u32 v247, v247, v249\n"
    "v_add_u32 v247, s60, v247\n"
    "s_and_b32 s60, s10, 0xfc\n"
    "s_lshl_b32 s60, s60, 6\n"
    "v_and_b32 v13, 3, v1\n"
    "v_lshlrev_b32 v13, 6, v13\n"
    "v_add_u32 v13, s60, v13\n"
    "s_waitcnt vmcnt(0)\n"
    "s_barrier\n"
    "s_cmp_lg_u32 s12, 0\n"
    "s_cbranch_scc1 .Lknf_169\n"
    "v_mov_b32 v244, s68\n"
    "v_mov_b32 v245, s69\n"
    "v_mov_b32 v246, 0\n"
    "s_lshl_b32 s60, s10, 6\n"
    "s_add_u32 s60, s60, 0x9000\n"
    "s_mov_b64 exec, 1\n"
    "s_nop 0\n"
    "buffer_store_dwordx2 v[244:245], v246, s[44:47], s60 offen sc1\n"
    "s_mov_b64 exec, -1\n"
    ".Lknf_169:\n"
    "s_mov_b32 s83, 0\n"
    ".Lkspin_170:\n"
    "s_mov_b32 s63, 0x9000\n"
    "buffer_load_dwordx2 v[250:251], v13, s[44:47], s63 offen sc1\n"
    "s_waitcnt vmcnt(0)\n"
    "v_cmp_ne_u64 vcc, s[68:69], v[250:251]\n"
    "s_nop 0\n"
    "s_cmp_eq_u64 vcc, 0\n"
    "s_cselect_b32 s78, 1, 0\n"
    "s_cmp_eq_u32 s78, 1\n"
    "s_cbranch_scc1 .Lkok_171\n"
    "s_sleep 1\n"
    "s_add_u32 s83, s83, 1\n"
    "s_cmp_lt_u32 s83, 0x9c40\n"
    "s_cbranch_scc1 .Lkspin_170\n"
    ".Lkok_171:\n"
    "s_mul_i32 s62, s90, 0x18000\n"
    "s_mul_i32 s63, s12, 0x1800\n"
    "s_add_u32 s62, s62, s63\n"
    "s_add_u32 s60, s90, 1\n"
    "s_and_b32 s60, s60, 3\n"
    "s_mul_i32 s60, s60, 0x6000\n"
    "s_add_u32 s60, s60, s62\n"
    "s_add_u32 s61, s60, 0x1000\n"
    "buffer_load_dwordx4 v[112:115], v11, s[24:27], s60 offen sc1\n"
    "buffer_load_dwordx4 v[116:119], v11, s[24:27], s60 offen offset:1024 sc1\n"
    "buffer_load_dwordx4 v[120:123], v11, s[24:27], s60 offen offset:2048 sc1\n"
    "s_add_u32 s60, s90, 2\n"
    "s_and_b32 s60, s60, 3\n"
    "s_mul_i32 s60, s60, 0x6000\n"
    "s_add_u32 s60, s60, s62\n"
    "s_add_u32 s61, s60, 0x1000\n"
    "buffer_load_dwordx4 v[136:139], v11, s[24:27], s60 offen sc1\n"
    "buffer_load_dwordx4 v[140:143], v11, s[24:27], s60 offen offset:1024 sc1\n"
    "buffer_load_dwordx4 v[144:147], v11, s[24:27], s60 offen offset:2048 sc1\n"
    "s_add_u32 s60, s90, 3\n"
    "s_and_b32 s60, s60, 3\n"
    "s_mul_i32 s60, s60, 0x6000\n"
    "s_add_u32 s60, s60, s62\n"
    "s_add_u32 s61, s60, 0x1000\n"
    "buffer_load_dwordx4 v[160:163], v11, s[24:27], s60 offen sc1\n"
    "buffer_load_dwordx4 v[164:167], v11, s[24:27], s60 offen offset:1024 sc1\n"
    "buffer_load_dwordx4 v[168:171], v11, s[24:27], s60 offen offset:2048 sc1\n"
    "s_waitcnt vmcnt(8)\n"
    "v_lshlrev_b32 v124, 16, v112\n"
    "v_and_b32 v125, 0xffff0000, v112\n"
    "v_lshlrev_b32 v126, 16, v113\n"
    "v_and_b32 v127, 0xffff0000, v113\n"
    "v_pk_add_f32 v[40:41], v[40:41], v[124:125]\n"
    "v_pk_add_f32 v[42:43], v[42:43], v[126:127]\n"
    "v_lshlrev_b32 v128, 16, v114\n"
    "v_and_b32 v129, 0xffff0000, v114\n"
    "v_lshlrev_b32 v130, 16, v115\n"
    "v_and_b32 v131, 0xffff0000, v115\n"
    "v_pk_add_f32 v[44:45], v[44:45], v[128:129]\n"
    "v_pk_add_f32 v[46:47], v[46:47], v[130:131]\n"
    "s_waitcnt vmcnt(7)\n"
    "v_lshlrev_b32 v124, 16, v116\n"
    "v_and_b32 v125, 0xffff0000, v116\n"
    "v_lshlrev_b32 v126, 16, v117\n"
    "v_and_b32 v127, 0xffff0000, v117\n"
    "v_pk_add_f32 v[72:73], v[72:73], v[124:125]\n"
    "v_pk_add_f32 v[74:75], v[74:75], v[126:127]\n"
    "v_lshlrev_b32 v128, 16, v118\n"
    "v_and_b32 v129, 0xffff0000, v118\n"
    "v_lshlrev_b32 v130, 16, v119\n"
    "v_and_b32 v131, 0xffff0000, v119\n"
    "v_pk_add_f32 v[76:77], v[76:77], v[128:129]\n"
    "v_pk_add_f32 v[78:79], v[78:79], v[130:131]\n"
    "s_waitcnt vmcnt(6)\n"
    "v_lshlrev_b32 v124, 16, v120\n"
    "v_and_b32 v125, 0xffff0000, v120\n"
    "v_lshlrev_b32 v126, 16, v121\n"
    "v_and_b32 v127, 0xffff0000, v121\n"
    "v_pk_add_f32 v[104:105], v[104:105], v[124:125]\n"
    "v_pk_add_f32 v[106:107], v[106:107], v[126:127]\n"
    "v_lshlrev_b32 v128, 16, v122\n"
    "v_and_b32 v129, 0xffff0000, v122\n"
    "v_lshlrev_b32 v130, 16, v123\n"
    "v_and_b32 v131, 0xffff0000, v123\n"
    "v_pk_add_f32 v[108:109], v[108:109], v[128:129]\n"
    "v_pk_add_f32 v[110:111], v[110:111], v[130:131]\n"
    "s_waitcnt vmcnt(5)\n"
    "v_lshlrev_b32 v148, 16, v136\n"
    "v_and_b32 v149, 0xffff0000, v136\n"
    "v_lshlrev_b32 v150, 16, v137\n"
    "v_and_b32 v151, 0xffff0000, v137\n"
    "v_pk_add_f32 v[40:41], v[40:41], v[148:149]\n"
    "v_pk_add_f32 v[42:43], v[42:43], v[150:151]\n"
    "v_lshlrev_b32 v152, 16, v138\n"
    "v_and_b32 v153, 0xffff0000, v138\n"
    "v_lshlrev_b32 v154, 16, v139\n"
    "v_and_b32 v155, 0xffff0000, v139\n"
    "v_pk_add_f32 v[44:45], v[44:45], v[152:153]\n"
    "v_pk_add_f32 v[46:47], v[46:47], v[154:155]\n"
    "s_waitcnt vmcnt(4)\n"
    "v_lshlrev_b32 v148, 16, v140\n"
    "v_and_b32 v149, 0xffff0000, v140\n"
    "v_lshlrev_b32 v150, 16, v141\n"
    "v_and_b32 v151, 0xffff0000, v141\n"
    "v_pk_add_f32 v[72:73], v[72:73], v[148:149]\n"
    "v_pk_add_f32 v[74:75], v[74:75], v[150:151]\n"
    "v_lshlrev_b32 v152, 16, v142\n"
    "v_and_b32 v153, 0xffff0000, v142\n"
    "v_lshlrev_b32 v154, 16, v143\n"
    "v_and_b32 v155, 0xffff0000, v143\n"
    "v_pk_add_f32 v[76:77], v[76:77], v[152:153]\n"
    "v_pk_add_f32 v[78:79], v[78:79], v[154:155]\n"
    "s_waitcnt vmcnt(3)\n"
    "v_lshlrev_b32 v148, 16, v144\n"
    "v_and_b32 v149, 0xffff0000, v144\n"
    "v_lshlrev_b32 v150, 16, v145\n"
    "v_and_b32 v151, 0xffff0000, v145\n"
    "v_pk_add_f32 v[104:105], v[104:105], v[148:149]\n"
    "v_pk_add_f32 v[106:107], v[106:107], v[150:151]\n"
    "v_lshlrev_b32 v152, 16, v146\n"
    "v_and_b32 v153, 0xffff0000, v146\n"
    "v_lshlrev_b32 v154, 16, v147\n"
    "v_and_b32 v155, 0xffff0000, v147\n"
    "v_pk_add_f32 v[108:109], v[108:109], v[152:153]\n"
    "v_pk_add_f32 v[110:111], v[110:111], v[154:155]\n"
    "s_waitcnt vmcnt(2)\n"
    "v_lshlrev_b32 v172, 16, v160\n"
    "v_and_b32 v173, 0xffff0000, v160\n"
    "v_lshlrev_b32 v174, 16, v161\n"
    "v_and_b32 v175, 0xffff0000, v161\n"
    "v_pk_add_f32 v[40:41], v[40:41], v[172:173]\n"
    "v_pk_add_f32 v[42:43], v[42:43], v[174:175]\n"
    "v_lshlrev_b32 v176, 16, v162\n"
    "v_and_b32 v177, 0xffff0000, v162\n"
    "v_lshlrev_b32 v178, 16, v163\n"
    "v_and_b32 v179, 0xffff0000, v163\n"
    "v_pk_add_f32 v[44:45], v[44:45], v[176:177]\n"
    "v_pk_add_f32 v[46:47], v[46:47], v[178:179]\n"
    "v_cvt_pk_bf16_f32 v192, v40, v41\n"
    "v_cvt_pk_bf16_f32 v193, v42, v43\n"
    "v_cvt_pk_bf16_f32 v194, v44, v45\n"
    "v_cvt_pk_bf16_f32 v195, v46, v47\n"
    "s_nop 0\n"
    "v_permlane16_swap_b32 v192, v194\n"
    "v_permlane16_swap_b32 v193, v195\n"
    "s_mov_b32 s60, 0\n"
    "buffer_store_dwordx4 v[192:195], v247, s[40:43], s60 offen sc1\n"
    "s_waitcnt vmcnt(2)\n"
    "v_lshlrev_b32 v172, 16, v164\n"
    "v_and_b32 v173, 0xffff0000, v164\n"
    "v_lshlrev_b32 v174, 16, v165\n"
    "v_and_b32 v175, 0xffff0000, v165\n"
    "v_pk_add_f32 v[72:73], v[72:73], v[172:173]\n"
    "v_pk_add_f32 v[74:75], v[74:75], v[174:175]\n"
    "v_lshlrev_b32 v176, 16, v166\n"
    "v_and_b32 v177, 0xffff0000, v166\n"
    "v_lshlrev_b32 v178, 16, v167\n"
    "v_and_b32 v179, 0xffff0000, v167\n"
    "v_pk_add_f32 v[76:77], v[76:77], v[176:177]\n"
    "v_pk_add_f32 v[78:79], v[78:79], v[178:179]\n"
    "v_cvt_pk_bf16_f32 v196, v72, v73\n"
    "v_cvt_pk_bf16_f32 v197, v74, v75\n"
    "v_cvt_pk_bf16_f32 v198, v76, v77\n"
    "v_cvt_pk_bf16_f32 v199, v78, v79\n"
    "s_nop 0\n"
    "v_permlane16_swap_b32 v196, v198\n"
    "v_permlane16_swap_b32 v197, v199\n"
    "s_mov_b32 s60, 0xa000\n"
    "buffer_store_dwordx4 v[196:199], v247, s[40:43], s60 offen sc1\n"
    "s_waitcnt vmcnt(2)\n"
    "v_lshlrev_b32 v172, 16, v168\n"
    "v_and_b32 v173, 0xffff0000, v168\n"
    "v_lshlrev_b32 v174, 16, v169\n"
    "v_and_b32 v175, 0xffff0000, v169\n"
    "v_pk_add_f32 v[104:105], v[104:105], v[172:173]\n"
    "v_pk_add_f32 v[106:107], v[106:107], v[174:175]\n"
    "v_lshlrev_b32 v176, 16, v170\n"
    "v_and_b32 v177, 0xffff0000, v170\n"
    "v_lshlrev_b32 v178, 16, v171\n"
    "v_and_b32 v179, 0xffff0000, v171\n"
    "v_pk_add_f32 v[108:109], v[108:109], v[176:177]\n"
    "v_pk_add_f32 v[110:111], v[110:111], v[178:179]\n"
    "v_cvt_pk_bf16_f32 v200, v104, v105\n"
    "v_cvt_pk_bf16_f32 v201, v106, v107\n"
    "v_cvt_pk_bf16_f32 v202, v108, v109\n"
    "v_cvt_pk_bf16_f32 v203, v110, v111\n"
    "s_nop 0\n"
    "v_permlane16_swap_b32 v200, v202\n"
    "v_permlane16_swap_b32 v201, v203\n"
    "s_mov_b32 s60, 0x14000\n"
    "buffer_store_dwordx4 v[200:203], v247, s[40:43], s60 offen sc1\n"
    ".Lkend_161:\n"
    "s_endpgm\n"
    ".Lkna_162:\n"
    "s_cmp_lg_u32 s10, 0xff\n"
    "s_cbranch_scc1 .Lkend_161\n"
    "s_cmp_lg_u32 s12, 0\n"
    "s_cbranch_scc1 .Lkend_161\n"
    "v_lshlrev_b32 v244, 6, v1\n"
    "s_mov_b32 s83, 0\n"
    ".Lkspin_172:\n"
    "s_mov_b32 s63, 0x5000\n"
    "buffer_load_dwordx2 v[16:17], v244, s[44:47], s63 offen sc1\n"
    "s_mov_b32 s63, 0x6000\n"
    "buffer_load_dwordx2 v[18:19], v244, s[44:47], s63 offen sc1\n"
    "s_mov_b32 s63, 0x7000\n"
    "buffer_load_dwordx2 v[20:21], v244, s[44:47], s63 offen sc1\n"
    "s_mov_b32 s63, 0x8000\n"
    "buffer_load_dwordx2 v[22:23], v244, s[44:47], s63 offen sc1\n"
    "s_waitcnt vmcnt(0)\n"
    "s_mov_b64 s[62:63], 0\n"
    "v_cmp_ne_u64 vcc, s[68:69], v[16:17]\n"
    "s_nop 0\n"
    "s_or_b64 s[62:63], s[62:63], vcc\n"
    "v_cmp_ne_u64 vcc, s[68:69], v[18:19]\n"
    "s_nop 0\n"
    "s_or_b64 s[62:63], s[62:63], vcc\n"
    "v_cmp_ne_u64 vcc, s[68:69], v[20:21]\n"
    "s_nop 0\n"
    "s_or_b64 s[62:63], s[62:63], vcc\n"
    "v_cmp_ne_u64 vcc, s[68:69], v[22:23]\n"
    "s_nop 0\n"
    "s_or_b64 s[62:63], s[62:63], vcc\n"
    "s_cmp_eq_u64 s[62:63], 0\n"
    "s_cselect_b32 s78, 1, 0\n"
    "s_cmp_eq_u32 s78, 1\n"
    "s_cbranch_scc1 .Lkok_173\n"
    "s_sleep 1\n"
    "s_add_u32 s83, s83, 1\n"
    "s_cmp_lt_u32 s83, 0x9c40\n"
    "s_cbranch_scc1 .Lkspin_172\n"
    ".Lkok_173:\n"
    "v_mov_b32 v244, s68\n"
    "v_mov_b32 v245, s69\n"
    "v_mov_b32 v246, 0\n"
    "s_mov_b64 exec, 1\n"
    "s_nop 0\n"
    "buffer_store_dwordx2 v[244:245], v246, s[44:47], 0 offen sc1\n"
    "s_mov_b64 exec, -1\n"
    "s_branch .Lkend_161\n"
    ".Lkxrare_78:\n"
    "v_mov_b32 v244, v15\n"
    "s_nop 1\n"
    "v_permlane16_swap_b32 v15, v244\n"
    "v_max_f32 v15, v15, v244\n"
    "v_mov_b32 v244, v15\n"
    "s_nop 1\n"
    "v_permlane32_swap_b32 v15, v244\n"
    "v_max_f32 v15, v15, v244\n"
    "v_max_f32 v15, s71, v15\n"
    "v_ceil_f32 v15, v15\n"
    "v_sub_f32 v160, v160, v15\n"
    "v_sub_f32 v164, v164, v15\n"
    "v_sub_f32 v172, v172, v15\n"
    "v_sub_f32 v161, v161, v15\n"
    "v_sub_f32 v165, v165, v15\n"
    "v_sub_f32 v173, v173, v15\n"
    "v_sub_f32 v162, v162, v15\n"
    "v_sub_f32 v166, v166, v15\n"
    "v_sub_f32 v174, v174, v15\n"
    "v_sub_f32 v163, v163, v15\n"
    "v_sub_f32 v167, v167, v15\n"
    "v_sub_f32 v175, v175, v15\n"
    "s_cmp_lg_u32 s71, 0\n"
    "s_mov_b32 s70, 0x41000000\n"
    "s_mov_b32 s71, 0\n"
    "s_cbranch_scc1 .Lkxret_79\n"
    "v_sub_f32 v244, 0, v15\n"
    "v_exp_f32 v244, v244\n"
    "s_nop 0\n"
    "v_mul_f32 v112, v112, v244\n"
    "v_mul_f32 v113, v113, v244\n"
    "v_mul_f32 v114, v114, v244\n"
    "v_mul_f32 v115, v115, v244\n"
    "v_mul_f32 v116, v116, v244\n"
    "v_mul_f32 v117, v117, v244\n"
    "v_mul_f32 v118, v118, v244\n"
    "v_mul_f32 v119, v119, v244\n"
    "v_mul_f32 v120, v120, v244\n"
    "v_mul_f32 v121, v121, v244\n"
    "v_mul_f32 v122, v122, v244\n"
    "v_mul_f32 v123, v123, v244\n"
    "v_mul_f32 v124, v124, v244\n"
    "v_mul_f32 v125, v125, v244\n"
    "v_mul_f32 v126, v126, v244\n"
    "v_mul_f32 v127, v127, v244\n"
    "v_mul_f32 v128, v128, v244\n"
    "v_mul_f32 v129, v129, v244\n"
    "v_mul_f32 v130, v130, v244\n"
    "v_mul_f32 v131, v131, v244\n"
    "v_mul_f32 v132, v132, v244\n"
    "v_mul_f32 v133, v133, v244\n"
    "v_mul_f32 v134, v134, v244\n"
    "v_mul_f32 v135, v135, v244\n"
    "v_mul_f32 v136, v136, v244\n"
    "v_mul_f32 v137, v137, v244\n"
    "v_mul_f32 v138, v138, v244\n"
    "v_mul_f32 v139, v139, v244\n"
    "v_mul_f32 v140, v140, v244\n"
    "v_mul_f32 v141, v141, v244\n"
    "v_mul_f32 v142, v142, v244\n"
    "v_mul_f32 v143, v143, v244\n"
    "v_mul_f32 v14, v14, v244\n"
    "s_branch .Lkxret_79\n"
    ".Lkxrare_82:\n"
    "v_mov_b32 v244, v15\n"
    "s_nop 1\n"
    "v_permlane16_swap_b32 v15, v244\n"
    "v_max_f32 v15, v15, v244\n"
    "v_mov_b32 v244, v15\n"
    "s_nop 1\n"
    "v_permlane32_swap_b32 v15, v244\n"
    "v_max_f32 v15, v15, v244\n"
    "v_max_f32 v15, s71, v15\n"
    "v_ceil_f32 v15, v15\n"
    "v_sub_f32 v160, v160, v15\n"
    "v_sub_f32 v164, v164, v15\n"
    "v_sub_f32 v172, v172, v15\n"
    "v_sub_f32 v161, v161, v15\n"
    "v_sub_f32 v165, v165, v15\n"
    "v_sub_f32 v173, v173, v15\n"
    "v_sub_f32 v162, v162, v15\n"
    "v_sub_f32 v166, v166, v15\n"
    "v_sub_f32 v174, v174, v15\n"
    "v_sub_f32 v163, v163, v15\n"
    "v_sub_f32 v167, v167, v15\n"
    "v_sub_f32 v175, v175, v15\n"
    "s_cmp_lg_u32 s71, 0\n"
    "s_mov_b32 s70, 0x41000000\n"
    "s_mov_b32 s71, 0\n"
    "s_cbranch_scc1 .Lkxret_83\n"
    "v_sub_f32 v244, 0, v15\n"
    "v_exp_f32 v244, v244\n"
    "s_nop 0\n"
    "v_mul_f32 v112, v112, v244\n"
    "v_mul_f32 v113, v113, v244\n"
    "v_mul_f32 v114, v114, v244\n"
    "v_mul_f32 v115, v115, v244\n"
    "v_mul_f32 v116, v116, v244\n"
    "v_mul_f32 v117, v117, v244\n"
    "v_mul_f32 v118, v118, v244\n"
    "v_mul_f32 v119, v119, v244\n"
    "v_mul_f32 v120, v120, v244\n"
    "v_mul_f32 v121, v121, v244\n"
    "v_mul_f32 v122, v122, v244\n"
    "v_mul_f32 v123, v123, v244\n"
    "v_mul_f32 v124, v124, v244\n"
    "v_mul_f32 v125, v125, v244\n"
    "v_mul_f32 v126, v126, v244\n"
    "v_mul_f32 v127, v127, v244\n"
    "v_mul_f32 v128, v128, v244\n"
    "v_mul_f32 v129, v129, v244\n"
    "v_mul_f32 v130, v130, v244\n"
    "v_mul_f32 v131, v131, v244\n"
    "v_mul_f32 v132, v132, v244\n"
    "v_mul_f32 v133, v133, v244\n"
    "v_mul_f32 v134, v134, v244\n"
    "v_mul_f32 v135, v135, v244\n"
    "v_mul_f32 v136, v136, v244\n"
    "v_mul_f32 v137, v137, v244\n"
    "v_mul_f32 v138, v138, v244\n"
    "v_mul_f32 v139, v139, v244\n"
    "v_mul_f32 v140, v140, v244\n"
    "v_mul_f32 v141, v141, v244\n"
    "v_mul_f32 v142, v142, v244\n"
    "v_mul_f32 v143, v143, v244\n"
    "v_mul_f32 v14, v14, v244\n"
    "s_branch .Lkxret_83\n"
    ".Lkdump_84:\n"
    "s_setprio 1\n"
    "v_mov_b32 v244, v14\n"
    "s_nop 1\n"
    "v_permlane16_swap_b32 v14, v244\n"
    "v_add_f32 v14, v14, v244\n"
    "v_mov_b32 v244, v14\n"
    "s_nop 1\n"
    "v_permlane32_swap_b32 v14, v244\n"
    "v_add_f32 v14, v14, v244\n"
    "v_mov_b32 v246, v172\n"
    "v_mov_b32 v247, v14\n"
    "s_lshl_b32 s60, s12, 9\n"
    "s_add_u32 s60, s60, 0x1e000\n"
    "v_lshl_add_u32 v244, v1, 3, s60\n"
    "s_lshl_b32 s60, s12, 13\n"
    "s_add_u32 s60, s60, 0x16000\n"
    "v_add_u32 v245, s60, v11\n"
    "s_waitcnt lgkmcnt(0)\n"
    "ds_write_b64 v244, v[246:247]\n"
    "ds_write_b128 v245, v[112:115]\n"
    "ds_write_b128 v245, v[116:119] offset:1024\n"
    "ds_write_b128 v245, v[120:123] offset:2048\n"
    "ds_write_b128 v245, v[124:127] offset:3072\n"
    "ds_write_b128 v245, v[128:131] offset:4096\n"
    "ds_write_b128 v245, v[132:135] offset:5120\n"
    "ds_write_b128 v245, v[136:139] offset:6144\n"
    "ds_write_b128 v245, v[140:143] offset:7168\n"
    "s_cmp_gt_u32 s15, 20\n"
    "s_cbranch_scc1 .Lkdret_85\n"
    "ds_read_b128 v[144:147], v12\n"
    "ds_read_b128 v[148:151], v12 offset:512\n"
    "ds_read_b128 v[152:155], v12 offset:1024\n"
    "ds_read_b128 v[156:159], v12 offset:1536\n"
    "v_mov_b32 v172, 0\n"
    "v_mov_b32 v173, 0\n"
    "v_mov_b32 v174, 0\n"
    "v_mov_b32 v175, 0\n"
    "v_mov_b32 v14, 0\n"
    "s_mov_b32 s70, 0xff800000\n"
    "s_mov_b32 s71, 0xff800000\n"
    "s_mov_b32 s76, 0\n"
    "s_waitcnt lgkmcnt(0)\n"
    "v_mfma_f32_32x32x16_bf16 v[112:127], v[172:175], v[172:175], 0\n"
    "v_mfma_f32_32x32x16_bf16 v[128:143], v[172:175], v[172:175], 0\n"
    "s_branch .Lkdret_85\n"
    ".Lkxrare_87:\n"
    "v_mov_b32 v244, v15\n"
    "s_nop 1\n"
    "v_permlane16_swap_b32 v15, v244\n"
    "v_max_f32 v15, v15, v244\n"
    "v_mov_b32 v244, v15\n"
    "s_nop 1\n"
    "v_permlane32_swap_b32 v15, v244\n"
    "v_max_f32 v15, v15, v244\n"
    "v_max_f32 v15, s71, v15\n"
    "v_ceil_f32 v15, v15\n"
    "v_sub_f32 v160, v160, v15\n"
    "v_sub_f32 v164, v164, v15\n"
    "v_sub_f32 v172, v172, v15\n"
    "v_sub_f32 v161, v161, v15\n"
    "v_sub_f32 v165, v165, v15\n"
    "v_sub_f32 v173, v173, v15\n"
    "v_sub_f32 v162, v162, v15\n"
    "v_sub_f32 v166, v166, v15\n"
    "v_sub_f32 v174, v174, v15\n"
    "v_sub_f32 v163, v163, v15\n"
    "v_sub_f32 v167, v167, v15\n"
    "v_sub_f32 v175, v175, v15\n"
    "s_cmp_lg_u32 s71, 0\n"
    "s_mov_b32 s70, 0x41000000\n"
    "s_mov_b32 s71, 0\n"
    "s_cbranch_scc1 .Lkxret_88\n"
    "v_sub_f32 v244, 0, v15\n"
    "v_exp_f32 v244, v244\n"
    "s_nop 0\n"
    "v_mul_f32 v112, v112, v244\n"
    "v_mul_f32 v113, v113, v244\n"
    "v_mul_f32 v114, v114, v244\n"
    "v_mul_f32 v115, v115, v244\n"
    "v_mul_f32 v116, v116, v244\n"
    "v_mul_f32 v117, v117, v244\n"
    "v_mul_f32 v118, v118, v244\n"
    "v_mul_f32 v119, v119, v244\n"
    "v_mul_f32 v120, v120, v244\n"
    "v_mul_f32 v121, v121, v244\n"
    "v_mul_f32 v122, v122, v244\n"
    "v_mul_f32 v123, v123, v244\n"
    "v_mul_f32 v124, v124, v244\n"
    "v_mul_f32 v125, v125, v244\n"
    "v_mul_f32 v126, v126, v244\n"
    "v_mul_f32 v127, v127, v244\n"
    "v_mul_f32 v128, v128, v244\n"
    "v_mul_f32 v129, v129, v244\n"
    "v_mul_f32 v130, v130, v244\n"
    "v_mul_f32 v131, v131, v244\n"
    "v_mul_f32 v132, v132, v244\n"
    "v_mul_f32 v133, v133, v244\n"
    "v_mul_f32 v134, v134, v244\n"
    "v_mul_f32 v135, v135, v244\n"
    "v_mul_f32 v136, v136, v244\n"
    "v_mul_f32 v137, v137, v244\n"
    "v_mul_f32 v138, v138, v244\n"
    "v_mul_f32 v139, v139, v244\n"
    "v_mul_f32 v140, v140, v244\n"
    "v_mul_f32 v141, v141, v244\n"
    "v_mul_f32 v142, v142, v244\n"
    "v_mul_f32 v143, v143, v244\n"
    "v_mul_f32 v14, v14, v244\n"
    "s_branch .Lkxret_88\n"
    ".Lkdump_90:\n"
    "s_setprio 1\n"
    "v_mov_b32 v244, v14\n"
    "s_nop 1\n"
    "v_permlane16_swap_b32 v14, v244\n"
    "v_add_f32 v14, v14, v244\n"
    "v_mov_b32 v244, v14\n"
    "s_nop 1\n"
    "v_permlane32_swap_b32 v14, v244\n"
    "v_add_f32 v14, v14, v244\n"
    "v_mov_b32 v246, v172\n"
    "v_mov_b32 v247, v14\n"
    "s_lshl_b32 s60, s12, 9\n"
    "s_add_u32 s60, s60, 0x1e000\n"
    "v_lshl_add_u32 v244, v1, 3, s60\n"
    "s_lshl_b32 s60, s12, 13\n"
    "s_add_u32 s60, s60, 0x16000\n"
    "v_add_u32 v245, s60, v11\n"
    "s_waitcnt lgkmcnt(0)\n"
    "ds_write_b64 v244, v[246:247]\n"
    "ds_write_b128 v245, v[112:115]\n"
    "ds_write_b128 v245, v[116:119] offset:1024\n"
    "ds_write_b128 v245, v[120:123] offset:2048\n"
    "ds_write_b128 v245, v[124:127] offset:3072\n"
    "ds_write_b128 v245, v[128:131] offset:4096\n"
    "ds_write_b128 v245, v[132:135] offset:5120\n"
    "ds_write_b128 v245, v[136:139] offset:6144\n"
    "ds_write_b128 v245, v[140:143] offset:7168\n"
    "s_cmp_gt_u32 s15, 20\n"
    "s_cbranch_scc1 .Lkdret_91\n"
    "ds_read_b128 v[144:147], v12\n"
    "ds_read_b128 v[148:151], v12 offset:512\n"
    "ds_read_b128 v[152:155], v12 offset:1024\n"
    "ds_read_b128 v[156:159], v12 offset:1536\n"
    "v_mov_b32 v172, 0\n"
    "v_mov_b32 v173, 0\n"
    "v_mov_b32 v174, 0\n"
    "v_mov_b32 v175, 0\n"
    "v_mov_b32 v14, 0\n"
    "s_mov_b32 s70, 0xff800000\n"
    "s_mov_b32 s71, 0xff800000\n"
    "s_mov_b32 s76, 0\n"
    "s_waitcnt lgkmcnt(0)\n"
    "v_mfma_f32_32x32x16_bf16 v[112:127], v[172:175], v[172:175], 0\n"
    "v_mfma_f32_32x32x16_bf16 v[128:143], v[172:175], v[172:175], 0\n"
    "s_branch .Lkdret_91\n"
    ".Lkxrare_92:\n"
    "v_mov_b32 v244, v15\n"
    "s_nop 1\n"
    "v_permlane16_swap_b32 v15, v244\n"
    "v_max_f32 v15, v15, v244\n"
    "v_mov_b32 v244, v15\n"
    "s_nop 1\n"
    "v_permlane32_swap_b32 v15, v244\n"
    "v_max_f32 v15, v15, v244\n"
    "v_max_f32 v15, s71, v15\n"
    "v_ceil_f32 v15, v15\n"
    "v_sub_f32 v160, v160, v15\n"
    "v_sub_f32 v164, v164, v15\n"
    "v_sub_f32 v172, v172, v15\n"
    "v_sub_f32 v161, v161, v15\n"
    "v_sub_f32 v165, v165, v15\n"
    "v_sub_f32 v173, v173, v15\n"
    "v_sub_f32 v162, v162, v15\n"
    "v_sub_f32 v166, v166, v15\n"
    "v_sub_f32 v174, v174, v15\n"
    "v_sub_f32 v163, v163, v15\n"
    "v_sub_f32 v167, v167, v15\n"
    "v_sub_f32 v175, v175, v15\n"
    "s_cmp_lg_u32 s71, 0\n"
    "s_mov_b32 s70, 0x41000000\n"
    "s_mov_b32 s71, 0\n"
    "s_cbranch_scc1 .Lkxret_93\n"
    "v_sub_f32 v244, 0, v15\n"
    "v_exp_f32 v244, v244\n"
    "s_nop 0\n"
    "v_mul_f32 v112, v112, v244\n"
    "v_mul_f32 v113, v113, v244\n"
    "v_mul_f32 v114, v114, v244\n"
    "v_mul_f32 v115, v115, v244\n"
    "v_mul_f32 v116, v116, v244\n"
    "v_mul_f32 v117, v117, v244\n"
    "v_mul_f32 v118, v118, v244\n"
    "v_mul_f32 v119, v119, v244\n"
    "v_mul_f32 v120, v120, v244\n"
    "v_mul_f32 v121, v121, v244\n"
    "v_mul_f32 v122, v122, v244\n"
    "v_mul_f32 v123, v123, v244\n"
    "v_mul_f32 v124, v124, v244\n"
    "v_mul_f32 v125, v125, v244\n"
    "v_mul_f32 v126, v126, v244\n"
    "v_mul_f32 v127, v127, v244\n"
    "v_mul_f32 v128, v128, v244\n"
    "v_mul_f32 v129, v129, v244\n"
    "v_mul_f32 v130, v130, v244\n"
    "v_mul_f32 v131, v131, v244\n"
    "v_mul_f32 v132, v132, v244\n"
    "v_mul_f32 v133, v133, v244\n"
    "v_mul_f32 v134, v134, v244\n"
    "v_mul_f32 v135, v135, v244\n"
    "v_mul_f32 v136, v136, v244\n"
    "v_mul_f32 v137, v137, v244\n"
    "v_mul_f32 v138, v138, v244\n"
    "v_mul_f32 v139, v139, v244\n"
    "v_mul_f32 v140, v140, v244\n"
    "v_mul_f32 v141, v141, v244\n"
    "v_mul_f32 v142, v142, v244\n"
    "v_mul_f32 v143, v143, v244\n"
    "v_mul_f32 v14, v14, v244\n"
    "s_branch .Lkxret_93\n"
    ".Lkdump_94:\n"
    "s_setprio 1\n"
    "v_mov_b32 v244, v14\n"
    "s_nop 1\n"
    "v_permlane16_swap_b32 v14, v244\n"
    "v_add_f32 v14, v14, v244\n"
    "v_mov_b32 v244, v14\n"
    "s_nop 1\n"
    "v_permlane32_swap_b32 v14, v244\n"
    "v_add_f32 v14, v14, v244\n"
    "v_mov_b32 v246, v172\n"
    "v_mov_b32 v247, v14\n"
    "s_lshl_b32 s60, s12, 9\n"
    "s_add_u32 s60, s60, 0x1e000\n"
    "v_lshl_add_u32 v244, v1, 3, s60\n"
    "s_lshl_b32 s60, s12, 13\n"
    "s_add_u32 s60, s60, 0x16000\n"
    "v_add_u32 v245, s60, v11\n"
    "s_waitcnt lgkmcnt(0)\n"
    "ds_write_b64 v244, v[246:247]\n"
    "ds_write_b128 v245, v[112:115]\n"
    "ds_write_b128 v245, v[116:119] offset:1024\n"
    "ds_write_b128 v245, v[120:123] offset:2048\n"
    "ds_write_b128 v245, v[124:127] offset:3072\n"
    "ds_write_b128 v245, v[128:131] offset:4096\n"
    "ds_write_b128 v245, v[132:135] offset:5120\n"
    "ds_write_b128 v245, v[136:139] offset:6144\n"
    "ds_write_b128 v245, v[140:143] offset:7168\n"
    "s_cmp_gt_u32 s15, 20\n"
    "s_cbranch_scc1 .Lkdret_95\n"
    "ds_read_b128 v[144:147], v12\n"
    "ds_read_b128 v[148:151], v12 offset:512\n"
    "ds_read_b128 v[152:155], v12 offset:1024\n"
    "ds_read_b128 v[156:159], v12 offset:1536\n"
    "v_mov_b32 v172, 0\n"
    "v_mov_b32 v173, 0\n"
    "v_mov_b32 v174, 0\n"
    "v_mov_b32 v175, 0\n"
    "v_mov_b32 v14, 0\n"
    "s_mov_b32 s70, 0xff800000\n"
    "s_mov_b32 s71, 0xff800000\n"
    "s_mov_b32 s76, 0\n"
    "s_waitcnt lgkmcnt(0)\n"
    "v_mfma_f32_32x32x16_bf16 v[112:127], v[172:175], v[172:175], 0\n"
    "v_mfma_f32_32x32x16_bf16 v[128:143], v[172:175], v[172:175], 0\n"
    "s_branch .Lkdret_95\n"
    ".Lkxrare_96:\n"
    "v_mov_b32 v244, v15\n"
    "s_nop 1\n"
    "v_permlane16_swap_b32 v15, v244\n"
    "v_max_f32 v15, v15, v244\n"
    "v_mov_b32 v244, v15\n"
    "s_nop 1\n"
    "v_permlane32_swap_b32 v15, v244\n"
    "v_max_f32 v15, v15, v244\n"
    "v_max_f32 v15, s71, v15\n"
    "v_ceil_f32 v15, v15\n"
    "v_sub_f32 v160, v160, v15\n"
    "v_sub_f32 v164, v164, v15\n"
    "v_sub_f32 v172, v172, v15\n"
    "v_sub_f32 v161, v161, v15\n"
    "v_sub_f32 v165, v165, v15\n"
    "v_sub_f32 v173, v173, v15\n"
    "v_sub_f32 v162, v162, v15\n"
    "v_sub_f32 v166, v166, v15\n"
    "v_sub_f32 v174, v174, v15\n"
    "v_sub_f32 v163, v163, v15\n"
    "v_sub_f32 v167, v167, v15\n"
    "v_sub_f32 v175, v175, v15\n"
    "s_cmp_lg_u32 s71, 0\n"
    "s_mov_b32 s70, 0x41000000\n"
    "s_mov_b32 s71, 0\n"
    "s_cbranch_scc1 .Lkxret_97\n"
    "v_sub_f32 v244, 0, v15\n"
    "v_exp_f32 v244, v244\n"
    "s_nop 0\n"
    "v_mul_f32 v112, v112, v244\n"
    "v_mul_f32 v113, v113, v244\n"
    "v_mul_f32 v114, v114, v244\n"
    "v_mul_f32 v115, v115, v244\n"
    "v_mul_f32 v116, v116, v244\n"
    "v_mul_f32 v117, v117, v244\n"
    "v_mul_f32 v118, v118, v244\n"
    "v_mul_f32 v119, v119, v244\n"
    "v_mul_f32 v120, v120, v244\n"
    "v_mul_f32 v121, v121, v244\n"
    "v_mul_f32 v122, v122, v244\n"
    "v_mul_f32 v123, v123, v244\n"
    "v_mul_f32 v124, v124, v244\n"
    "v_mul_f32 v125, v125, v244\n"
    "v_mul_f32 v126, v126, v244\n"
    "v_mul_f32 v127, v127, v244\n"
    "v_mul_f32 v128, v128, v244\n"
    "v_mul_f32 v129, v129, v244\n"
    "v_mul_f32 v130, v130, v244\n"
    "v_mul_f32 v131, v131, v244\n"
    "v_mul_f32 v132, v132, v244\n"
    "v_mul_f32 v133, v133, v244\n"
    "v_mul_f32 v134, v134, v244\n"
    "v_mul_f32 v135, v135, v244\n"
    "v_mul_f32 v136, v136, v244\n"
    "v_mul_f32 v137, v137, v244\n"
    "v_mul_f32 v138, v138, v244\n"
    "v_mul_f32 v139, v139, v244\n"
    "v_mul_f32 v140, v140, v244\n"
    "v_mul_f32 v141, v141, v244\n"
    "v_mul_f32 v142, v142, v244\n"
    "v_mul_f32 v143, v143, v244\n"
    "v_mul_f32 v14, v14, v244\n"
    "s_branch .Lkxret_97\n"
    ".Lkdump_98:\n"
    "s_setprio 1\n"
    "v_mov_b32 v244, v14\n"
    "s_nop 1\n"
    "v_permlane16_swap_b32 v14, v244\n"
    "v_add_f32 v14, v14, v244\n"
    "v_mov_b32 v244, v14\n"
    "s_nop 1\n"
    "v_permlane32_swap_b32 v14, v244\n"
    "v_add_f32 v14, v14, v244\n"
    "v_mov_b32 v246, v172\n"
    "v_mov_b32 v247, v14\n"
    "s_lshl_b32 s60, s12, 9\n"
    "s_add_u32 s60, s60, 0x1e000\n"
    "v_lshl_add_u32 v244, v1, 3, s60\n"
    "s_lshl_b32 s60, s12, 13\n"
    "s_add_u32 s60, s60, 0x16000\n"
    "v_add_u32 v245, s60, v11\n"
    "s_waitcnt lgkmcnt(0)\n"
    "ds_write_b64 v244, v[246:247]\n"
    "ds_write_b128 v245, v[112:115]\n"
    "ds_write_b128 v245, v[116:119] offset:1024\n"
    "ds_write_b128 v245, v[120:123] offset:2048\n"
    "ds_write_b128 v245, v[124:127] offset:3072\n"
    "ds_write_b128 v245, v[128:131] offset:4096\n"
    "ds_write_b128 v245, v[132:135] offset:5120\n"
    "ds_write_b128 v245, v[136:139] offset:6144\n"
    "ds_write_b128 v245, v[140:143] offset:7168\n"
    "s_cmp_gt_u32 s15, 20\n"
    "s_cbranch_scc1 .Lkdret_99\n"
    "ds_read_b128 v[144:147], v12\n"
    "ds_read_b128 v[148:151], v12 offset:512\n"
    "ds_read_b128 v[152:155], v12 offset:1024\n"
    "ds_read_b128 v[156:159], v12 offset:1536\n"
    "v_mov_b32 v172, 0\n"
    "v_mov_b32 v173, 0\n"
    "v_mov_b32 v174, 0\n"
    "v_mov_b32 v175, 0\n"
    "v_mov_b32 v14, 0\n"
    "s_mov_b32 s70, 0xff800000\n"
    "s_mov_b32 s71, 0xff800000\n"
    "s_mov_b32 s76, 0\n"
    "s_waitcnt lgkmcnt(0)\n"
    "v_mfma_f32_32x32x16_bf16 v[112:127], v[172:175], v[172:175], 0\n"
    "v_mfma_f32_32x32x16_bf16 v[128:143], v[172:175], v[172:175], 0\n"
    "s_branch .Lkdret_99\n"
    ".Lkxrare_100:\n"
    "v_mov_b32 v244, v15\n"
    "s_nop 1\n"
    "v_permlane16_swap_b32 v15, v244\n"
    "v_max_f32 v15, v15, v244\n"
    "v_mov_b32 v244, v15\n"
    "s_nop 1\n"
    "v_permlane32_swap_b32 v15, v244\n"
    "v_max_f32 v15, v15, v244\n"
    "v_max_f32 v15, s71, v15\n"
    "v_ceil_f32 v15, v15\n"
    "v_sub_f32 v160, v160, v15\n"
    "v_sub_f32 v164, v164, v15\n"
    "v_sub_f32 v172, v172, v15\n"
    "v_sub_f32 v161, v161, v15\n"
    "v_sub_f32 v165, v165, v15\n"
    "v_sub_f32 v173, v173, v15\n"
    "v_sub_f32 v162, v162, v15\n"
    "v_sub_f32 v166, v166, v15\n"
    "v_sub_f32 v174, v174, v15\n"
    "v_sub_f32 v163, v163, v15\n"
    "v_sub_f32 v167, v167, v15\n"
    "v_sub_f32 v175, v175, v15\n"
    "s_cmp_lg_u32 s71, 0\n"
    "s_mov_b32 s70, 0x41000000\n"
    "s_mov_b32 s71, 0\n"
    "s_cbranch_scc1 .Lkxret_101\n"
    "v_sub_f32 v244, 0, v15\n"
    "v_exp_f32 v244, v244\n"
    "s_nop 0\n"
    "v_mul_f32 v112, v112, v244\n"
    "v_mul_f32 v113, v113, v244\n"
    "v_mul_f32 v114, v114, v244\n"
    "v_mul_f32 v115, v115, v244\n"
    "v_mul_f32 v116, v116, v244\n"
    "v_mul_f32 v117, v117, v244\n"
    "v_mul_f32 v118, v118, v244\n"
    "v_mul_f32 v119, v119, v244\n"
    "v_mul_f32 v120, v120, v244\n"
    "v_mul_f32 v121, v121, v244\n"
    "v_mul_f32 v122, v122, v244\n"
    "v_mul_f32 v123, v123, v244\n"
    "v_mul_f32 v124, v124, v244\n"
    "v_mul_f32 v125, v125, v244\n"
    "v_mul_f32 v126, v126, v244\n"
    "v_mul_f32 v127, v127, v244\n"
    "v_mul_f32 v128, v128, v244\n"
    "v_mul_f32 v129, v129, v244\n"
    "v_mul_f32 v130, v130, v244\n"
    "v_mul_f32 v131, v131, v244\n"
    "v_mul_f32 v132, v132, v244\n"
    "v_mul_f32 v133, v133, v244\n"
    "v_mul_f32 v134, v134, v244\n"
    "v_mul_f32 v135, v135, v244\n"
    "v_mul_f32 v136, v136, v244\n"
    "v_mul_f32 v137, v137, v244\n"
    "v_mul_f32 v138, v138, v244\n"
    "v_mul_f32 v139, v139, v244\n"
    "v_mul_f32 v140, v140, v244\n"
    "v_mul_f32 v141, v141, v244\n"
    "v_mul_f32 v142, v142, v244\n"
    "v_mul_f32 v143, v143, v244\n"
    "v_mul_f32 v14, v14, v244\n"
    "s_branch .Lkxret_101\n"
    ".Lkdump_102:\n"
    "s_setprio 1\n"
    "v_mov_b32 v244, v14\n"
    "s_nop 1\n"
    "v_permlane16_swap_b32 v14, v244\n"
    "v_add_f32 v14, v14, v244\n"
    "v_mov_b32 v244, v14\n"
    "s_nop 1\n"
    "v_permlane32_swap_b32 v14, v244\n"
    "v_add_f32 v14, v14, v244\n"
    "v_mov_b32 v246, v172\n"
    "v_mov_b32 v247, v14\n"
    "s_lshl_b32 s60, s12, 9\n"
    "s_add_u32 s60, s60, 0x1e000\n"
    "v_lshl_add_u32 v244, v1, 3, s60\n"
    "s_lshl_b32 s60, s12, 13\n"
    "s_add_u32 s60, s60, 0x16000\n"
    "v_add_u32 v245, s60, v11\n"
    "s_waitcnt lgkmcnt(0)\n"
    "ds_write_b64 v244, v[246:247]\n"
    "ds_write_b128 v245, v[112:115]\n"
    "ds_write_b128 v245, v[116:119] offset:1024\n"
    "ds_write_b128 v245, v[120:123] offset:2048\n"
    "ds_write_b128 v245, v[124:127] offset:3072\n"
    "ds_write_b128 v245, v[128:131] offset:4096\n"
    "ds_write_b128 v245, v[132:135] offset:5120\n"
    "ds_write_b128 v245, v[136:139] offset:6144\n"
    "ds_write_b128 v245, v[140:143] offset:7168\n"
    "s_cmp_gt_u32 s15, 20\n"
    "s_cbranch_scc1 .Lkdret_103\n"
    "ds_read_b128 v[144:147], v12\n"
    "ds_read_b128 v[148:151], v12 offset:512\n"
    "ds_read_b128 v[152:155], v12 offset:1024\n"
    "ds_read_b128 v[156:159], v12 offset:1536\n"
    "v_mov_b32 v172, 0\n"
    "v_mov_b32 v173, 0\n"
    "v_mov_b32 v174, 0\n"
    "v_mov_b32 v175, 0\n"
    "v_mov_b32 v14, 0\n"
    "s_mov_b32 s70, 0xff800000\n"
    "s_mov_b32 s71, 0xff800000\n"
    "s_mov_b32 s76, 0\n"
    "s_waitcnt lgkmcnt(0)\n"
    "v_mfma_f32_32x32x16_bf16 v[112:127], v[172:175], v[172:175], 0\n"
    "v_mfma_f32_32x32x16_bf16 v[128:143], v[172:175], v[172:175], 0\n"
    "s_branch .Lkdret_103\n"
    ".Lkxrare_104:\n"
    "v_mov_b32 v244, v15\n"
    "s_nop 1\n"
    "v_permlane16_swap_b32 v15, v244\n"
    "v_max_f32 v15, v15, v244\n"
    "v_mov_b32 v244, v15\n"
    "s_nop 1\n"
    "v_permlane32_swap_b32 v15, v244\n"
    "v_max_f32 v15, v15, v244\n"
    "v_max_f32 v15, s71, v15\n"
    "v_ceil_f32 v15, v15\n"
    "v_sub_f32 v160, v160, v15\n"
    "v_sub_f32 v164, v164, v15\n"
    "v_sub_f32 v172, v172, v15\n"
    "v_sub_f32 v161, v161, v15\n"
    "v_sub_f32 v165, v165, v15\n"
    "v_sub_f32 v173, v173, v15\n"
    "v_sub_f32 v162, v162, v15\n"
    "v_sub_f32 v166, v166, v15\n"
    "v_sub_f32 v174, v174, v15\n"
    "v_sub_f32 v163, v163, v15\n"
    "v_sub_f32 v167, v167, v15\n"
    "v_sub_f32 v175, v175, v15\n"
    "s_cmp_lg_u32 s71, 0\n"
    "s_mov_b32 s70, 0x41000000\n"
    "s_mov_b32 s71, 0\n"
    "s_cbranch_scc1 .Lkxret_105\n"
    "v_sub_f32 v244, 0, v15\n"
    "v_exp_f32 v244, v244\n"
    "s_nop 0\n"
    "v_mul_f32 v112, v112, v244\n"
    "v_mul_f32 v113, v113, v244\n"
    "v_mul_f32 v114, v114, v244\n"
    "v_mul_f32 v115, v115, v244\n"
    "v_mul_f32 v116, v116, v244\n"
    "v_mul_f32 v117, v117, v244\n"
    "v_mul_f32 v118, v118, v244\n"
    "v_mul_f32 v119, v119, v244\n"
    "v_mul_f32 v120, v120, v244\n"
    "v_mul_f32 v121, v121, v244\n"
    "v_mul_f32 v122, v122, v244\n"
    "v_mul_f32 v123, v123, v244\n"
    "v_mul_f32 v124, v124, v244\n"
    "v_mul_f32 v125, v125, v244\n"
    "v_mul_f32 v126, v126, v244\n"
    "v_mul_f32 v127, v127, v244\n"
    "v_mul_f32 v128, v128, v244\n"
    "v_mul_f32 v129, v129, v244\n"
    "v_mul_f32 v130, v130, v244\n"
    "v_mul_f32 v131, v131, v244\n"
    "v_mul_f32 v132, v132, v244\n"
    "v_mul_f32 v133, v133, v244\n"
    "v_mul_f32 v134, v134, v244\n"
    "v_mul_f32 v135, v135, v244\n"
    "v_mul_f32 v136, v136, v244\n"
    "v_mul_f32 v137, v137, v244\n"
    "v_mul_f32 v138, v138, v244\n"
    "v_mul_f32 v139, v139, v244\n"
    "v_mul_f32 v140, v140, v244\n"
    "v_mul_f32 v141, v141, v244\n"
    "v_mul_f32 v142, v142, v244\n"
    "v_mul_f32 v143, v143, v244\n"
    "v_mul_f32 v14, v14, v244\n"
    "s_branch .Lkxret_105\n"
    ".Lkdump_106:\n"
    "s_setprio 1\n"
    "v_mov_b32 v244, v14\n"
    "s_nop 1\n"
    "v_permlane16_swap_b32 v14, v244\n"
    "v_add_f32 v14, v14, v244\n"
    "v_mov_b32 v244, v14\n"
    "s_nop 1\n"
    "v_permlane32_swap_b32 v14, v244\n"
    "v_add_f32 v14, v14, v244\n"
    "v_mov_b32 v246, v172\n"
    "v_mov_b32 v247, v14\n"
    "s_lshl_b32 s60, s12, 9\n"
    "s_add_u32 s60, s60, 0x1e000\n"
    "v_lshl_add_u32 v244, v1, 3, s60\n"
    "s_lshl_b32 s60, s12, 13\n"
    "s_add_u32 s60, s60, 0x16000\n"
    "v_add_u32 v245, s60, v11\n"
    "s_waitcnt lgkmcnt(0)\n"
    "ds_write_b64 v244, v[246:247]\n"
    "ds_write_b128 v245, v[112:115]\n"
    "ds_write_b128 v245, v[116:119] offset:1024\n"
    "ds_write_b128 v245, v[120:123] offset:2048\n"
    "ds_write_b128 v245, v[124:127] offset:3072\n"
    "ds_write_b128 v245, v[128:131] offset:4096\n"
    "ds_write_b128 v245, v[132:135] offset:5120\n"
    "ds_write_b128 v245, v[136:139] offset:6144\n"
    "ds_write_b128 v245, v[140:143] offset:7168\n"
    "s_cmp_gt_u32 s15, 20\n"
    "s_cbranch_scc1 .Lkdret_107\n"
    "ds_read_b128 v[144:147], v12\n"
    "ds_read_b128 v[148:151], v12 offset:512\n"
    "ds_read_b128 v[152:155], v12 offset:1024\n"
    "ds_read_b128 v[156:159], v12 offset:1536\n"
    "v_mov_b32 v172, 0\n"
    "v_mov_b32 v173, 0\n"
    "v_mov_b32 v174, 0\n"
    "v_mov_b32 v175, 0\n"
    "v_mov_b32 v14, 0\n"
    "s_mov_b32 s70, 0xff800000\n"
    "s_mov_b32 s71, 0xff800000\n"
    "s_mov_b32 s76, 0\n"
    "s_waitcnt lgkmcnt(0)\n"
    "v_mfma_f32_32x32x16_bf16 v[112:127], v[172:175], v[172:175], 0\n"
    "v_mfma_f32_32x32x16_bf16 v[128:143], v[172:175], v[172:175], 0\n"
    "s_branch .Lkdret_107\n"
    ".Lkxrare_108:\n"
    "v_mov_b32 v244, v15\n"
    "s_nop 1\n"
    "v_permlane16_swap_b32 v15, v244\n"
    "v_max_f32 v15, v15, v244\n"
    "v_mov_b32 v244, v15\n"
    "s_nop 1\n"
    "v_permlane32_swap_b32 v15, v244\n"
    "v_max_f32 v15, v15, v244\n"
    "v_max_f32 v15, s71, v15\n"
    "v_ceil_f32 v15, v15\n"
    "v_sub_f32 v160, v160, v15\n"
    "v_sub_f32 v164, v164, v15\n"
    "v_sub_f32 v172, v172, v15\n"
    "v_sub_f32 v161, v161, v15\n"
    "v_sub_f32 v165, v165, v15\n"
    "v_sub_f32 v173, v173, v15\n"
    "v_sub_f32 v162, v162, v15\n"
    "v_sub_f32 v166, v166, v15\n"
    "v_sub_f32 v174, v174, v15\n"
    "v_sub_f32 v163, v163, v15\n"
    "v_sub_f32 v167, v167, v15\n"
    "v_sub_f32 v175, v175, v15\n"
    "s_cmp_lg_u32 s71, 0\n"
    "s_mov_b32 s70, 0x41000000\n"
    "s_mov_b32 s71, 0\n"
    "s_cbranch_scc1 .Lkxret_109\n"
    "v_sub_f32 v244, 0, v15\n"
    "v_exp_f32 v244, v244\n"
    "s_nop 0\n"
    "v_mul_f32 v112, v112, v244\n"
    "v_mul_f32 v113, v113, v244\n"
    "v_mul_f32 v114, v114, v244\n"
    "v_mul_f32 v115, v115, v244\n"
    "v_mul_f32 v116, v116, v244\n"
    "v_mul_f32 v117, v117, v244\n"
    "v_mul_f32 v118, v118, v244\n"
    "v_mul_f32 v119, v119, v244\n"
    "v_mul_f32 v120, v120, v244\n"
    "v_mul_f32 v121, v121, v244\n"
    "v_mul_f32 v122, v122, v244\n"
    "v_mul_f32 v123, v123, v244\n"
    "v_mul_f32 v124, v124, v244\n"
    "v_mul_f32 v125, v125, v244\n"
    "v_mul_f32 v126, v126, v244\n"
    "v_mul_f32 v127, v127, v244\n"
    "v_mul_f32 v128, v128, v244\n"
    "v_mul_f32 v129, v129, v244\n"
    "v_mul_f32 v130, v130, v244\n"
    "v_mul_f32 v131, v131, v244\n"
    "v_mul_f32 v132, v132, v244\n"
    "v_mul_f32 v133, v133, v244\n"
    "v_mul_f32 v134, v134, v244\n"
    "v_mul_f32 v135, v135, v244\n"
    "v_mul_f32 v136, v136, v244\n"
    "v_mul_f32 v137, v137, v244\n"
    "v_mul_f32 v138, v138, v244\n"
    "v_mul_f32 v139, v139, v244\n"
    "v_mul_f32 v140, v140, v244\n"
    "v_mul_f32 v141, v141, v244\n"
    "v_mul_f32 v142, v142, v244\n"
    "v_mul_f32 v143, v143, v244\n"
    "v_mul_f32 v14, v14, v244\n"
    "s_branch .Lkxret_109\n"
    ".Lkdump_110:\n"
    "s_setprio 1\n"
    "v_mov_b32 v244, v14\n"
    "s_nop 1\n"
    "v_permlane16_swap_b32 v14, v244\n"
    "v_add_f32 v14, v14, v244\n"
    "v_mov_b32 v244, v14\n"
    "s_nop 1\n"
    "v_permlane32_swap_b32 v14, v244\n"
    "v_add_f32 v14, v14, v244\n"
    "v_mov_b32 v246, v172\n"
    "v_mov_b32 v247, v14\n"
    "s_lshl_b32 s60, s12, 9\n"
    "s_add_u32 s60, s60, 0x1e000\n"
    "v_lshl_add_u32 v244, v1, 3, s60\n"
    "s_lshl_b32 s60, s12, 13\n"
    "s_add_u32 s60, s60, 0x16000\n"
    "v_add_u32 v245, s60, v11\n"
    "s_waitcnt lgkmcnt(0)\n"
    "ds_write_b64 v244, v[246:247]\n"
    "ds_write_b128 v245, v[112:115]\n"
    "ds_write_b128 v245, v[116:119] offset:1024\n"
    "ds_write_b128 v245, v[120:123] offset:2048\n"
    "ds_write_b128 v245, v[124:127] offset:3072\n"
    "ds_write_b128 v245, v[128:131] offset:4096\n"
    "ds_write_b128 v245, v[132:135] offset:5120\n"
    "ds_write_b128 v245, v[136:139] offset:6144\n"
    "ds_write_b128 v245, v[140:143] offset:7168\n"
    "s_cmp_gt_u32 s15, 20\n"
    "s_cbranch_scc1 .Lkdret_111\n"
    "ds_read_b128 v[144:147], v12\n"
    "ds_read_b128 v[148:151], v12 offset:512\n"
    "ds_read_b128 v[152:155], v12 offset:1024\n"
    "ds_read_b128 v[156:159], v12 offset:1536\n"
    "v_mov_b32 v172, 0\n"
    "v_mov_b32 v173, 0\n"
    "v_mov_b32 v174, 0\n"
    "v_mov_b32 v175, 0\n"
    "v_mov_b32 v14, 0\n"
    "s_mov_b32 s70, 0xff800000\n"
    "s_mov_b32 s71, 0xff800000\n"
    "s_mov_b32 s76, 0\n"
    "s_waitcnt lgkmcnt(0)\n"
    "v_mfma_f32_32x32x16_bf16 v[112:127], v[172:175], v[172:175], 0\n"
    "v_mfma_f32_32x32x16_bf16 v[128:143], v[172:175], v[172:175], 0\n"
    "s_branch .Lkdret_111\n"
    ".Lkxrare_112:\n"
    "v_mov_b32 v244, v15\n"
    "s_nop 1\n"
    "v_permlane16_swap_b32 v15, v244\n"
    "v_max_f32 v15, v15, v244\n"
    "v_mov_b32 v244, v15\n"
    "s_nop 1\n"
    "v_permlane32_swap_b32 v15, v244\n"
    "v_max_f32 v15, v15, v244\n"
    "v_max_f32 v15, s71, v15\n"
    "v_ceil_f32 v15, v15\n"
    "v_sub_f32 v160, v160, v15\n"
    "v_sub_f32 v164, v164, v15\n"
    "v_sub_f32 v172, v172, v15\n"
    "v_sub_f32 v161, v161, v15\n"
    "v_sub_f32 v165, v165, v15\n"
    "v_sub_f32 v173, v173, v15\n"
    "v_sub_f32 v162, v162, v15\n"
    "v_sub_f32 v166, v166, v15\n"
    "v_sub_f32 v174, v174, v15\n"
    "v_sub_f32 v163, v163, v15\n"
    "v_sub_f32 v167, v167, v15\n"
    "v_sub_f32 v175, v175, v15\n"
    "s_cmp_lg_u32 s71, 0\n"
    "s_mov_b32 s70, 0x41000000\n"
    "s_mov_b32 s71, 0\n"
    "s_cbranch_scc1 .Lkxret_113\n"
    "v_sub_f32 v244, 0, v15\n"
    "v_exp_f32 v244, v244\n"
    "s_nop 0\n"
    "v_mul_f32 v112, v112, v244\n"
    "v_mul_f32 v113, v113, v244\n"
    "v_mul_f32 v114, v114, v244\n"
    "v_mul_f32 v115, v115, v244\n"
    "v_mul_f32 v116, v116, v244\n"
    "v_mul_f32 v117, v117, v244\n"
    "v_mul_f32 v118, v118, v244\n"
    "v_mul_f32 v119, v119, v244\n"
    "v_mul_f32 v120, v120, v244\n"
    "v_mul_f32 v121, v121, v244\n"
    "v_mul_f32 v122, v122, v244\n"
    "v_mul_f32 v123, v123, v244\n"
    "v_mul_f32 v124, v124, v244\n"
    "v_mul_f32 v125, v125, v244\n"
    "v_mul_f32 v126, v126, v244\n"
    "v_mul_f32 v127, v127, v244\n"
    "v_mul_f32 v128, v128, v244\n"
    "v_mul_f32 v129, v129, v244\n"
    "v_mul_f32 v130, v130, v244\n"
    "v_mul_f32 v131, v131, v244\n"
    "v_mul_f32 v132, v132, v244\n"
    "v_mul_f32 v133, v133, v244\n"
    "v_mul_f32 v134, v134, v244\n"
    "v_mul_f32 v135, v135, v244\n"
    "v_mul_f32 v136, v136, v244\n"
    "v_mul_f32 v137, v137, v244\n"
    "v_mul_f32 v138, v138, v244\n"
    "v_mul_f32 v139, v139, v244\n"
    "v_mul_f32 v140, v140, v244\n"
    "v_mul_f32 v141, v141, v244\n"
    "v_mul_f32 v142, v142, v244\n"
    "v_mul_f32 v143, v143, v244\n"
    "v_mul_f32 v14, v14, v244\n"
    "s_branch .Lkxret_113\n"
    ".Lkdump_114:\n"
    "s_setprio 1\n"
    "v_mov_b32 v244, v14\n"
    "s_nop 1\n"
    "v_permlane16_swap_b32 v14, v244\n"
    "v_add_f32 v14, v14, v244\n"
    "v_mov_b32 v244, v14\n"
    "s_nop 1\n"
    "v_permlane32_swap_b32 v14, v244\n"
    "v_add_f32 v14, v14, v244\n"
    "v_mov_b32 v246, v172\n"
    "v_mov_b32 v247, v14\n"
    "s_lshl_b32 s60, s12, 9\n"
    "s_add_u32 s60, s60, 0x1e000\n"
    "v_lshl_add_u32 v244, v1, 3, s60\n"
    "s_lshl_b32 s60, s12, 13\n"
    "s_add_u32 s60, s60, 0x16000\n"
    "v_add_u32 v245, s60, v11\n"
    "s_waitcnt lgkmcnt(0)\n"
    "ds_write_b64 v244, v[246:247]\n"
    "ds_write_b128 v245, v[112:115]\n"
    "ds_write_b128 v245, v[116:119] offset:1024\n"
    "ds_write_b128 v245, v[120:123] offset:2048\n"
    "ds_write_b128 v245, v[124:127] offset:3072\n"
    "ds_write_b128 v245, v[128:131] offset:4096\n"
    "ds_write_b128 v245, v[132:135] offset:5120\n"
    "ds_write_b128 v245, v[136:139] offset:6144\n"
    "ds_write_b128 v245, v[140:143] offset:7168\n"
    "s_cmp_gt_u32 s15, 20\n"
    "s_cbranch_scc1 .Lkdret_115\n"
    "ds_read_b128 v[144:147], v12\n"
    "ds_read_b128 v[148:151], v12 offset:512\n"
    "ds_read_b128 v[152:155], v12 offset:1024\n"
    "ds_read_b128 v[156:159], v12 offset:1536\n"
    "v_mov_b32 v172, 0\n"
    "v_mov_b32 v173, 0\n"
    "v_mov_b32 v174, 0\n"
    "v_mov_b32 v175, 0\n"
    "v_mov_b32 v14, 0\n"
    "s_mov_b32 s70, 0xff800000\n"
    "s_mov_b32 s71, 0xff800000\n"
    "s_mov_b32 s76, 0\n"
    "s_waitcnt lgkmcnt(0)\n"
    "v_mfma_f32_32x32x16_bf16 v[112:127], v[172:175], v[172:175], 0\n"
    "v_mfma_f32_32x32x16_bf16 v[128:143], v[172:175], v[172:175], 0\n"
    "s_branch .Lkdret_115\n"
    ".Lkxrare_116:\n"
    "v_mov_b32 v244, v15\n"
    "s_nop 1\n"
    "v_permlane16_swap_b32 v15, v244\n"
    "v_max_f32 v15, v15, v244\n"
    "v_mov_b32 v244, v15\n"
    "s_nop 1\n"
    "v_permlane32_swap_b32 v15, v244\n"
    "v_max_f32 v15, v15, v244\n"
    "v_max_f32 v15, s71, v15\n"
    "v_ceil_f32 v15, v15\n"
    "v_sub_f32 v160, v160, v15\n"
    "v_sub_f32 v164, v164, v15\n"
    "v_sub_f32 v172, v172, v15\n"
    "v_sub_f32 v161, v161, v15\n"
    "v_sub_f32 v165, v165, v15\n"
    "v_sub_f32 v173, v173, v15\n"
    "v_sub_f32 v162, v162, v15\n"
    "v_sub_f32 v166, v166, v15\n"
    "v_sub_f32 v174, v174, v15\n"
    "v_sub_f32 v163, v163, v15\n"
    "v_sub_f32 v167, v167, v15\n"
    "v_sub_f32 v175, v175, v15\n"
    "s_cmp_lg_u32 s71, 0\n"
    "s_mov_b32 s70, 0x41000000\n"
    "s_mov_b32 s71, 0\n"
    "s_cbranch_scc1 .Lkxret_117\n"
    "v_sub_f32 v244, 0, v15\n"
    "v_exp_f32 v244, v244\n"
    "s_nop 0\n"
    "v_mul_f32 v112, v112, v244\n"
    "v_mul_f32 v113, v113, v244\n"
    "v_mul_f32 v114, v114, v244\n"
    "v_mul_f32 v115, v115, v244\n"
    "v_mul_f32 v116, v116, v244\n"
    "v_mul_f32 v117, v117, v244\n"
    "v_mul_f32 v118, v118, v244\n"
    "v_mul_f32 v119, v119, v244\n"
    "v_mul_f32 v120, v120, v244\n"
    "v_mul_f32 v121, v121, v244\n"
    "v_mul_f32 v122, v122, v244\n"
    "v_mul_f32 v123, v123, v244\n"
    "v_mul_f32 v124, v124, v244\n"
    "v_mul_f32 v125, v125, v244\n"
    "v_mul_f32 v126, v126, v244\n"
    "v_mul_f32 v127, v127, v244\n"
    "v_mul_f32 v128, v128, v244\n"
    "v_mul_f32 v129, v129, v244\n"
    "v_mul_f32 v130, v130, v244\n"
    "v_mul_f32 v131, v131, v244\n"
    "v_mul_f32 v132, v132, v244\n"
    "v_mul_f32 v133, v133, v244\n"
    "v_mul_f32 v134, v134, v244\n"
    "v_mul_f32 v135, v135, v244\n"
    "v_mul_f32 v136, v136, v244\n"
    "v_mul_f32 v137, v137, v244\n"
    "v_mul_f32 v138, v138, v244\n"
    "v_mul_f32 v139, v139, v244\n"
    "v_mul_f32 v140, v140, v244\n"
    "v_mul_f32 v141, v141, v244\n"
    "v_mul_f32 v142, v142, v244\n"
    "v_mul_f32 v143, v143, v244\n"
    "v_mul_f32 v14, v14, v244\n"
    "s_branch .Lkxret_117\n"
    ".Lkdump_118:\n"
    "s_setprio 1\n"
    "v_mov_b32 v244, v14\n"
    "s_nop 1\n"
    "v_permlane16_swap_b32 v14, v244\n"
    "v_add_f32 v14, v14, v244\n"
    "v_mov_b32 v244, v14\n"
    "s_nop 1\n"
    "v_permlane32_swap_b32 v14, v244\n"
    "v_add_f32 v14, v14, v244\n"
    "v_mov_b32 v246, v172\n"
    "v_mov_b32 v247, v14\n"
    "s_lshl_b32 s60, s12, 9\n"
    "s_add_u32 s60, s60, 0x1e000\n"
    "v_lshl_add_u32 v244, v1, 3, s60\n"
    "s_lshl_b32 s60, s12, 13\n"
    "s_add_u32 s60, s60, 0x16000\n"
    "v_add_u32 v245, s60, v11\n"
    "s_waitcnt lgkmcnt(0)\n"
    "ds_write_b64 v244, v[246:247]\n"
    "ds_write_b128 v245, v[112:115]\n"
    "ds_write_b128 v245, v[116:119] offset:1024\n"
    "ds_write_b128 v245, v[120:123] offset:2048\n"
    "ds_write_b128 v245, v[124:127] offset:3072\n"
    "ds_write_b128 v245, v[128:131] offset:4096\n"
    "ds_write_b128 v245, v[132:135] offset:5120\n"
    "ds_write_b128 v245, v[136:139] offset:6144\n"
    "ds_write_b128 v245, v[140:143] offset:7168\n"
    "s_cmp_gt_u32 s15, 20\n"
    "s_cbranch_scc1 .Lkdret_119\n"
    "ds_read_b128 v[144:147], v12\n"
    "ds_read_b128 v[148:151], v12 offset:512\n"
    "ds_read_b128 v[152:155], v12 offset:1024\n"
    "ds_read_b128 v[156:159], v12 offset:1536\n"
    "v_mov_b32 v172, 0\n"
    "v_mov_b32 v173, 0\n"
    "v_mov_b32 v174, 0\n"
    "v_mov_b32 v175, 0\n"
    "v_mov_b32 v14, 0\n"
    "s_mov_b32 s70, 0xff800000\n"
    "s_mov_b32 s71, 0xff800000\n"
    "s_mov_b32 s76, 0\n"
    "s_waitcnt lgkmcnt(0)\n"
    "v_mfma_f32_32x32x16_bf16 v[112:127], v[172:175], v[172:175], 0\n"
    "v_mfma_f32_32x32x16_bf16 v[128:143], v[172:175], v[172:175], 0\n"
    "s_branch .Lkdret_119\n"
    ".Lkxrare_120:\n"
    "v_mov_b32 v244, v15\n"
    "s_nop 1\n"
    "v_permlane16_swap_b32 v15, v244\n"
    "v_max_f32 v15, v15, v244\n"
    "v_mov_b32 v244, v15\n"
    "s_nop 1\n"
    "v_permlane32_swap_b32 v15, v244\n"
    "v_max_f32 v15, v15, v244\n"
    "v_max_f32 v15, s71, v15\n"
    "v_ceil_f32 v15, v15\n"
    "v_sub_f32 v160, v160, v15\n"
    "v_sub_f32 v164, v164, v15\n"
    "v_sub_f32 v172, v172, v15\n"
    "v_sub_f32 v161, v161, v15\n"
    "v_sub_f32 v165, v165, v15\n"
    "v_sub_f32 v173, v173, v15\n"
    "v_sub_f32 v162, v162, v15\n"
    "v_sub_f32 v166, v166, v15\n"
    "v_sub_f32 v174, v174, v15\n"
    "v_sub_f32 v163, v163, v15\n"
    "v_sub_f32 v167, v167, v15\n"
    "v_sub_f32 v175, v175, v15\n"
    "s_cmp_lg_u32 s71, 0\n"
    "s_mov_b32 s70, 0x41000000\n"
    "s_mov_b32 s71, 0\n"
    "s_cbranch_scc1 .Lkxret_121\n"
    "v_sub_f32 v244, 0, v15\n"
    "v_exp_f32 v244, v244\n"
    "s_nop 0\n"
    "v_mul_f32 v112, v112, v244\n"
    "v_mul_f32 v113, v113, v244\n"
    "v_mul_f32 v114, v114, v244\n"
    "v_mul_f32 v115, v115, v244\n"
    "v_mul_f32 v116, v116, v244\n"
    "v_mul_f32 v117, v117, v244\n"
    "v_mul_f32 v118, v118, v244\n"
    "v_mul_f32 v119, v119, v244\n"
    "v_mul_f32 v120, v120, v244\n"
    "v_mul_f32 v121, v121, v244\n"
    "v_mul_f32 v122, v122, v244\n"
    "v_mul_f32 v123, v123, v244\n"
    "v_mul_f32 v124, v124, v244\n"
    "v_mul_f32 v125, v125, v244\n"
    "v_mul_f32 v126, v126, v244\n"
    "v_mul_f32 v127, v127, v244\n"
    "v_mul_f32 v128, v128, v244\n"
    "v_mul_f32 v129, v129, v244\n"
    "v_mul_f32 v130, v130, v244\n"
    "v_mul_f32 v131, v131, v244\n"
    "v_mul_f32 v132, v132, v244\n"
    "v_mul_f32 v133, v133, v244\n"
    "v_mul_f32 v134, v134, v244\n"
    "v_mul_f32 v135, v135, v244\n"
    "v_mul_f32 v136, v136, v244\n"
    "v_mul_f32 v137, v137, v244\n"
    "v_mul_f32 v138, v138, v244\n"
    "v_mul_f32 v139, v139, v244\n"
    "v_mul_f32 v140, v140, v244\n"
    "v_mul_f32 v141, v141, v244\n"
    "v_mul_f32 v142, v142, v244\n"
    "v_mul_f32 v143, v143, v244\n"
    "v_mul_f32 v14, v14, v244\n"
    "s_branch .Lkxret_121\n"
    ".Lkdump_123:\n"
    "s_setprio 1\n"
    "v_mov_b32 v244, v14\n"
    "s_nop 1\n"
    "v_permlane16_swap_b32 v14, v244\n"
    "v_add_f32 v14, v14, v244\n"
    "v_mov_b32 v244, v14\n"
    "s_nop 1\n"
    "v_permlane32_swap_b32 v14, v244\n"
    "v_add_f32 v14, v14, v244\n"
    "v_mov_b32 v246, v172\n"
    "v_mov_b32 v247, v14\n"
    "s_lshl_b32 s60, s12, 9\n"
    "s_add_u32 s60, s60, 0x1e000\n"
    "v_lshl_add_u32 v244, v1, 3, s60\n"
    "s_lshl_b32 s60, s12, 13\n"
    "s_add_u32 s60, s60, 0x16000\n"
    "v_add_u32 v245, s60, v11\n"
    "s_waitcnt lgkmcnt(0)\n"
    "ds_write_b64 v244, v[246:247]\n"
    "ds_write_b128 v245, v[112:115]\n"
    "ds_write_b128 v245, v[116:119] offset:1024\n"
    "ds_write_b128 v245, v[120:123] offset:2048\n"
    "ds_write_b128 v245, v[124:127] offset:3072\n"
    "ds_write_b128 v245, v[128:131] offset:4096\n"
    "ds_write_b128 v245, v[132:135] offset:5120\n"
    "ds_write_b128 v245, v[136:139] offset:6144\n"
    "ds_write_b128 v245, v[140:143] offset:7168\n"
    "s_cmp_gt_u32 s15, 20\n"
    "s_cbranch_scc1 .Lkdret_124\n"
    "ds_read_b128 v[144:147], v12\n"
    "ds_read_b128 v[148:151], v12 offset:512\n"
    "ds_read_b128 v[152:155], v12 offset:1024\n"
    "ds_read_b128 v[156:159], v12 offset:1536\n"
    "v_mov_b32 v172, 0\n"
    "v_mov_b32 v173, 0\n"
    "v_mov_b32 v174, 0\n"
    "v_mov_b32 v175, 0\n"
    "v_mov_b32 v14, 0\n"
    "s_mov_b32 s70, 0xff800000\n"
    "s_mov_b32 s71, 0xff800000\n"
    "s_mov_b32 s76, 0\n"
    "s_waitcnt lgkmcnt(0)\n"
    "v_mfma_f32_32x32x16_bf16 v[112:127], v[172:175], v[172:175], 0\n"
    "v_mfma_f32_32x32x16_bf16 v[128:143], v[172:175], v[172:175], 0\n"
    "s_branch .Lkdret_124\n"
    ".Lkxrare_127:\n"
    "v_mov_b32 v244, v15\n"
    "s_nop 1\n"
    "v_permlane16_swap_b32 v15, v244\n"
    "v_max_f32 v15, v15, v244\n"
    "v_mov_b32 v244, v15\n"
    "s_nop 1\n"
    "v_permlane32_swap_b32 v15, v244\n"
    "v_max_f32 v15, v15, v244\n"
    "v_max_f32 v15, s71, v15\n"
    "v_ceil_f32 v15, v15\n"
    "v_sub_f32 v160, v160, v15\n"
    "v_sub_f32 v164, v164, v15\n"
    "v_sub_f32 v172, v172, v15\n"
    "v_sub_f32 v161, v161, v15\n"
    "v_sub_f32 v165, v165, v15\n"
    "v_sub_f32 v173, v173, v15\n"
    "v_sub_f32 v162, v162, v15\n"
    "v_sub_f32 v166, v166, v15\n"
    "v_sub_f32 v174, v174, v15\n"
    "v_sub_f32 v163, v163, v15\n"
    "v_sub_f32 v167, v167, v15\n"
    "v_sub_f32 v175, v175, v15\n"
    "s_cmp_lg_u32 s71, 0\n"
    "s_mov_b32 s70, 0x41000000\n"
    "s_mov_b32 s71, 0\n"
    "s_cbranch_scc1 .Lkxret_128\n"
    "v_sub_f32 v244, 0, v15\n"
    "v_exp_f32 v244, v244\n"
    "s_nop 0\n"
    "v_mul_f32 v112, v112, v244\n"
    "v_mul_f32 v113, v113, v244\n"
    "v_mul_f32 v114, v114, v244\n"
    "v_mul_f32 v115, v115, v244\n"
    "v_mul_f32 v116, v116, v244\n"
    "v_mul_f32 v117, v117, v244\n"
    "v_mul_f32 v118, v118, v244\n"
    "v_mul_f32 v119, v119, v244\n"
    "v_mul_f32 v120, v120, v244\n"
    "v_mul_f32 v121, v121, v244\n"
    "v_mul_f32 v122, v122, v244\n"
    "v_mul_f32 v123, v123, v244\n"
    "v_mul_f32 v124, v124, v244\n"
    "v_mul_f32 v125, v125, v244\n"
    "v_mul_f32 v126, v126, v244\n"
    "v_mul_f32 v127, v127, v244\n"
    "v_mul_f32 v128, v128, v244\n"
    "v_mul_f32 v129, v129, v244\n"
    "v_mul_f32 v130, v130, v244\n"
    "v_mul_f32 v131, v131, v244\n"
    "v_mul_f32 v132, v132, v244\n"
    "v_mul_f32 v133, v133, v244\n"
    "v_mul_f32 v134, v134, v244\n"
    "v_mul_f32 v135, v135, v244\n"
    "v_mul_f32 v136, v136, v244\n"
    "v_mul_f32 v137, v137, v244\n"
    "v_mul_f32 v138, v138, v244\n"
    "v_mul_f32 v139, v139, v244\n"
    "v_mul_f32 v140, v140, v244\n"
    "v_mul_f32 v141, v141, v244\n"
    "v_mul_f32 v142, v142, v244\n"
    "v_mul_f32 v143, v143, v244\n"
    "v_mul_f32 v14, v14, v244\n"
    "s_branch .Lkxret_128\n"
    ".Lkxrare_133:\n"
    "v_mov_b32 v244, v15\n"
    "s_nop 1\n"
    "v_permlane16_swap_b32 v15, v244\n"
    "v_max_f32 v15, v15, v244\n"
    "v_mov_b32 v244, v15\n"
    "s_nop 1\n"
    "v_permlane32_swap_b32 v15, v244\n"
    "v_max_f32 v15, v15, v244\n"
    "v_max_f32 v15, s71, v15\n"
    "v_ceil_f32 v15, v15\n"
    "v_sub_f32 v160, v160, v15\n"
    "v_sub_f32 v164, v164, v15\n"
    "v_sub_f32 v172, v172, v15\n"
    "v_sub_f32 v161, v161, v15\n"
    "v_sub_f32 v165, v165, v15\n"
    "v_sub_f32 v173, v173, v15\n"
    "v_sub_f32 v162, v162, v15\n"
    "v_sub_f32 v166, v166, v15\n"
    "v_sub_f32 v174, v174, v15\n"
    "v_sub_f32 v163, v163, v15\n"
    "v_sub_f32 v167, v167, v15\n"
    "v_sub_f32 v175, v175, v15\n"
    "s_cmp_lg_u32 s71, 0\n"
    "s_mov_b32 s70, 0x41000000\n"
    "s_mov_b32 s71, 0\n"
    "s_cbranch_scc1 .Lkxret_134\n"
    "v_sub_f32 v244, 0, v15\n"
    "v_exp_f32 v244, v244\n"
    "s_nop 0\n"
    "v_mul_f32 v112, v112, v244\n"
    "v_mul_f32 v113, v113, v244\n"
    "v_mul_f32 v114, v114, v244\n"
    "v_mul_f32 v115, v115, v244\n"
    "v_mul_f32 v116, v116, v244\n"
    "v_mul_f32 v117, v117, v244\n"
    "v_mul_f32 v118, v118, v244\n"
    "v_mul_f32 v119, v119, v244\n"
    "v_mul_f32 v120, v120, v244\n"
    "v_mul_f32 v121, v121, v244\n"
    "v_mul_f32 v122, v122, v244\n"
    "v_mul_f32 v123, v123, v244\n"
    "v_mul_f32 v124, v124, v244\n"
    "v_mul_f32 v125, v125, v244\n"
    "v_mul_f32 v126, v126, v244\n"
    "v_mul_f32 v127, v127, v244\n"
    "v_mul_f32 v128, v128, v244\n"
    "v_mul_f32 v129, v129, v244\n"
    "v_mul_f32 v130, v130, v244\n"
    "v_mul_f32 v131, v131, v244\n"
    "v_mul_f32 v132, v132, v244\n"
    "v_mul_f32 v133, v133, v244\n"
    "v_mul_f32 v134, v134, v244\n"
    "v_mul_f32 v135, v135, v244\n"
    "v_mul_f32 v136, v136, v244\n"
    "v_mul_f32 v137, v137, v244\n"
    "v_mul_f32 v138, v138, v244\n"
    "v_mul_f32 v139, v139, v244\n"
    "v_mul_f32 v140, v140, v244\n"
    "v_mul_f32 v141, v141, v244\n"
    "v_mul_f32 v142, v142, v244\n"
    "v_mul_f32 v143, v143, v244\n"
    "v_mul_f32 v14, v14, v244\n"
    "s_branch .Lkxret_134\n"
    ".Lkss_129:\n"
    "s_waitcnt lgkmcnt(4)\n"
    "v_readfirstlane_b32 s60, v246\n"
    "v_readfirstlane_b32 s61, v247\n"
    "s_and_b32 s62, s15, 1\n"
    "s_cselect_b32 s60, s61, s60\n"
    "s_cmp_lg_u32 s60, 1\n"
    "s_cbranch_scc1 .Lkssr_130\n"
    "buffer_load_dwordx4 v[192:195], v11, s[36:39], s96 offen sc1\n"
    "buffer_load_dwordx4 v[196:199], v6, s[36:39], s96 offen sc1\n"
    "buffer_load_dwordx4 v[200:203], v12, s[36:39], s96 offen sc1\n"
    "s_mov_b32 s83, 2\n"
    "s_branch .Lkssr_130\n"
    ".Lkse_131:\n"
    "s_cmp_eq_u32 s83, 2\n"
    "s_cbranch_scc1 .Lkego_174\n"
    "s_cmp_lg_u32 s12, 0\n"
    "s_cbranch_scc1 .Lkser_132\n"
    "v_cmp_ne_u64 vcc, s[68:69], v[250:251]\n"
    "s_nop 0\n"
    "s_cmp_eq_u64 vcc, 0\n"
    "s_cselect_b32 s62, 1, 0\n"
    "s_and_b32 s60, s15, 1\n"
    "s_xor_b32 s60, s60, 1\n"
    "s_lshl2_add_u32 s60, s60, 0x27800\n"
    "v_mov_b32 v244, s60\n"
    "v_mov_b32 v245, s62\n"
    "s_mov_b64 exec, 1\n"
    "s_nop 0\n"
    "ds_write_b32 v244, v245\n"
    "s_mov_b64 exec, -1\n"
    "s_mov_b32 s63, 0x1000\n"
    "buffer_load_dwordx2 v[250:251], v13, s[44:47], s63 offen sc1\n"
    "s_branch .Lkser_132\n"
    ".Lkego_174:\n"
    "buffer_load_dwordx4 v[204:207], v11, s[36:39], s96 offen offset:1024 sc1\n"
    "buffer_load_dwordx4 v[208:211], v6, s[36:39], s96 offen offset:1024 sc1\n"
    "buffer_load_dwordx4 v[212:215], v12, s[36:39], s96 offen offset:1024 sc1\n"
    "s_add_u32 s67, s52, 0x8000\n"
    "s_sub_u32 s56, s52, 0xe000\n"
    "s_cselect_b32 s56, s67, s56\n"
    "s_add_u32 s67, s53, 0x8000\n"
    "s_sub_u32 s57, s53, 0xe000\n"
    "s_cselect_b32 s57, s67, s57\n"
    "s_add_u32 s67, s54, 0x8000\n"
    "s_sub_u32 s58, s54, 0xe000\n"
    "s_cselect_b32 s58, s67, s58\n"
    "s_add_u32 s67, s55, 0x8000\n"
    "s_sub_u32 s59, s55, 0xe000\n"
    "s_cselect_b32 s59, s67, s59\n"
    "s_add_u32 s15, s15, 1\n"
    "s_add_u32 s86, s86, 0x4000\n"
    "s_mov_b32 s77, 1\n"
    "s_mov_b32 s83, 0\n"
    "s_waitcnt vmcnt(11) lgkmcnt(0)\n"
    "s_setprio 0\n"
    "s_barrier\n"
    "s_branch .Lkd1_55\n"
    ".Lkdump_136:\n"
    "s_setprio 1\n"
    "v_mov_b32 v244, v14\n"
    "s_nop 1\n"
    "v_permlane16_swap_b32 v14, v244\n"
    "v_add_f32 v14, v14, v244\n"
    "v_mov_b32 v244, v14\n"
    "s_nop 1\n"
    "v_permlane32_swap_b32 v14, v244\n"
    "v_add_f32 v14, v14, v244\n"
    "v_mov_b32 v246, v172\n"
    "v_mov_b32 v247, v14\n"
    "s_lshl_b32 s60, s12, 9\n"
    "s_add_u32 s60, s60, 0x1e000\n"
    "v_lshl_add_u32 v244, v1, 3, s60\n"
    "s_lshl_b32 s60, s12, 13\n"
    "s_add_u32 s60, s60, 0x16000\n"
    "v_add_u32 v245, s60, v11\n"
    "s_waitcnt lgkmcnt(0)\n"
    "ds_write_b64 v244, v[246:247]\n"
    "ds_write_b128 v245, v[112:115]\n"
    "ds_write_b128 v245, v[116:119] offset:1024\n"
    "ds_write_b128 v245, v[120:123] offset:2048\n"
    "ds_write_b128 v245, v[124:127] offset:3072\n"
    "ds_write_b128 v245, v[128:131] offset:4096\n"
    "ds_write_b128 v245, v[132:135] offset:5120\n"
    "ds_write_b128 v245, v[136:139] offset:6144\n"
    "ds_write_b128 v245, v[140:143] offset:7168\n"
    "s_cmp_gt_u32 s15, 20\n"
    "s_cbranch_scc1 .Lkdret_137\n"
    "ds_read_b128 v[144:147], v12\n"
    "ds_read_b128 v[148:151], v12 offset:512\n"
    "ds_read_b128 v[152:155], v12 offset:1024\n"
    "ds_read_b128 v[156:159], v12 offset:1536\n"
    "v_mov_b32 v172, 0\n"
    "v_mov_b32 v173, 0\n"
    "v_mov_b32 v174, 0\n"
    "v_mov_b32 v175, 0\n"
    "v_mov_b32 v14, 0\n"
    "s_mov_b32 s70, 0xff800000\n"
    "s_mov_b32 s71, 0xff800000\n"
    "s_mov_b32 s76, 0\n"
    "s_waitcnt lgkmcnt(0)\n"
    "v_mfma_f32_32x32x16_bf16 v[112:127], v[172:175], v[172:175], 0\n"
    "v_mfma_f32_32x32x16_bf16 v[128:143], v[172:175], v[172:175], 0\n"
    "s_branch .Lkdret_137\n"
    ".Lkdump_142:\n"
    "s_setprio 1\n"
    "v_mov_b32 v244, v14\n"
    "s_nop 1\n"
    "v_permlane16_swap_b32 v14, v244\n"
    "v_add_f32 v14, v14, v244\n"
    "v_mov_b32 v244, v14\n"
    "s_nop 1\n"
    "v_permlane32_swap_b32 v14, v244\n"
    "v_add_f32 v14, v14, v244\n"
    "v_mov_b32 v246, v172\n"
    "v_mov_b32 v247, v14\n"
    "s_lshl_b32 s60, s12, 9\n"
    "s_add_u32 s60, s60, 0x1e000\n"
    "v_lshl_add_u32 v244, v1, 3, s60\n"
    "s_lshl_b32 s60, s12, 13\n"
    "s_add_u32 s60, s60, 0x16000\n"
    "v_add_u32 v245, s60, v11\n"
    "s_waitcnt lgkmcnt(0)\n"
    "ds_write_b64 v244, v[246:247]\n"
    "ds_write_b128 v245, v[112:115]\n"
    "ds_write_b128 v245, v[116:119] offset:1024\n"
    "ds_write_b128 v245, v[120:123] offset:2048\n"
    "ds_write_b128 v245, v[124:127] offset:3072\n"
    "ds_write_b128 v245, v[128:131] offset:4096\n"
    "ds_write_b128 v245, v[132:135] offset:5120\n"
    "ds_write_b128 v245, v[136:139] offset:6144\n"
    "ds_write_b128 v245, v[140:143] offset:7168\n"
    "s_cmp_gt_u32 s15, 20\n"
    "s_cbranch_scc1 .Lkdret_143\n"
    "ds_read_b128 v[144:147], v12\n"
    "ds_read_b128 v[148:151], v12 offset:512\n"
    "ds_read_b128 v[152:155], v12 offset:1024\n"
    "ds_read_b128 v[156:159], v12 offset:1536\n"
    "v_mov_b32 v172, 0\n"
    "v_mov_b32 v173, 0\n"
    "v_mov_b32 v174, 0\n"
    "v_mov_b32 v175, 0\n"
    "v_mov_b32 v14, 0\n"
    "s_mov_b32 s70, 0xff800000\n"
    "s_mov_b32 s71, 0xff800000\n"
    "s_mov_b32 s76, 0\n"
    "s_waitcnt lgkmcnt(0)\n"
    "v_mfma_f32_32x32x16_bf16 v[112:127], v[172:175], v[172:175], 0\n"
    "v_mfma_f32_32x32x16_bf16 v[128:143], v[172:175], v[172:175], 0\n"
    "s_branch .Lkdret_143\n"
    ".Lkss_138:\n"
    "s_waitcnt lgkmcnt(8)\n"
    "v_readfirstlane_b32 s60, v246\n"
    "v_readfirstlane_b32 s61, v247\n"
    "s_and_b32 s62, s15, 1\n"
    "s_cselect_b32 s60, s61, s60\n"
    "s_cmp_lg_u32 s60, 1\n"
    "s_cbranch_scc1 .Lkssr_139\n"
    "buffer_load_dwordx4 v[192:195], v11, s[36:39], s96 offen sc1\n"
    "buffer_load_dwordx4 v[196:199], v6, s[36:39], s96 offen sc1\n"
    "buffer_load_dwordx4 v[200:203], v12, s[36:39], s96 offen sc1\n"
    "s_mov_b32 s83, 2\n"
    "s_branch .Lkssr_139\n"
    ".Lkse_140:\n"
    "s_cmp_eq_u32 s83, 2\n"
    "s_cbranch_scc1 .Lkego_175\n"
    "s_cmp_lg_u32 s12, 0\n"
    "s_cbranch_scc1 .Lkser_141\n"
    "v_cmp_ne_u64 vcc, s[68:69], v[250:251]\n"
    "s_nop 0\n"
    "s_cmp_eq_u64 vcc, 0\n"
    "s_cselect_b32 s62, 1, 0\n"
    "s_and_b32 s60, s15, 1\n"
    "s_xor_b32 s60, s60, 1\n"
    "s_lshl2_add_u32 s60, s60, 0x27800\n"
    "v_mov_b32 v244, s60\n"
    "v_mov_b32 v245, s62\n"
    "s_mov_b64 exec, 1\n"
    "s_nop 0\n"
    "ds_write_b32 v244, v245\n"
    "s_mov_b64 exec, -1\n"
    "s_mov_b32 s63, 0x1000\n"
    "buffer_load_dwordx2 v[250:251], v13, s[44:47], s63 offen sc1\n"
    "s_branch .Lkser_141\n"
    ".Lkego_175:\n"
    "buffer_load_dwordx4 v[204:207], v11, s[36:39], s96 offen offset:1024 sc1\n"
    "buffer_load_dwordx4 v[208:211], v6, s[36:39], s96 offen offset:1024 sc1\n"
    "buffer_load_dwordx4 v[212:215], v12, s[36:39], s96 offen offset:1024 sc1\n"
    "s_add_u32 s67, s56, 0x8000\n"
    "s_sub_u32 s52, s56, 0xe000\n"
    "s_cselect_b32 s52, s67, s52\n"
    "s_add_u32 s67, s57, 0x8000\n"
    "s_sub_u32 s53, s57, 0xe000\n"
    "s_cselect_b32 s53, s67, s53\n"
    "s_add_u32 s67, s58, 0x8000\n"
    "s_sub_u32 s54, s58, 0xe000\n"
    "s_cselect_b32 s54, s67, s54\n"
    "s_add_u32 s67, s59, 0x8000\n"
    "s_sub_u32 s55, s59, 0xe000\n"
    "s_cselect_b32 s55, s67, s55\n"
    "s_add_u32 s15, s15, 1\n"
    "s_add_u32 s86, s86, 0x4000\n"
    "s_mov_b32 s77, 1\n"
    "s_mov_b32 s83, 0\n"
    "s_waitcnt vmcnt(11) lgkmcnt(0)\n"
    "s_setprio 0\n"
    "s_barrier\n"
    "s_branch .Lkd0_54\n"
    ".Lkxrare_144:\n"
    "v_mov_b32 v244, v15\n"
    "s_nop 1\n"
    "v_permlane16_swap_b32 v15, v244\n"
    "v_max_f32 v15, v15, v244\n"
    "v_mov_b32 v244, v15\n"
    "s_nop 1\n"
    "v_permlane32_swap_b32 v15, v244\n"
    "v_max_f32 v15, v15, v244\n"
    "v_max_f32 v15, s71, v15\n"
    "v_ceil_f32 v15, v15\n"
    "v_sub_f32 v160, v160, v15\n"
    "v_sub_f32 v164, v164, v15\n"
    "v_sub_f32 v172, v172, v15\n"
    "v_sub_f32 v161, v161, v15\n"
    "v_sub_f32 v165, v165, v15\n"
    "v_sub_f32 v173, v173, v15\n"
    "v_sub_f32 v162, v162, v15\n"
    "v_sub_f32 v166, v166, v15\n"
    "v_sub_f32 v174, v174, v15\n"
    "v_sub_f32 v163, v163, v15\n"
    "v_sub_f32 v167, v167, v15\n"
    "v_sub_f32 v175, v175, v15\n"
    "s_cmp_lg_u32 s71, 0\n"
    "s_mov_b32 s70, 0x41000000\n"
    "s_mov_b32 s71, 0\n"
    "s_cbranch_scc1 .Lkxret_145\n"
    "v_sub_f32 v244, 0, v15\n"
    "v_exp_f32 v244, v244\n"
    "s_nop 0\n"
    "v_mul_f32 v112, v112, v244\n"
    "v_mul_f32 v113, v113, v244\n"
    "v_mul_f32 v114, v114, v244\n"
    "v_mul_f32 v115, v115, v244\n"
    "v_mul_f32 v116, v116, v244\n"
    "v_mul_f32 v117, v117, v244\n"
    "v_mul_f32 v118, v118, v244\n"
    "v_mul_f32 v119, v119, v244\n"
    "v_mul_f32 v120, v120, v244\n"
    "v_mul_f32 v121, v121, v244\n"
    "v_mul_f32 v122, v122, v244\n"
    "v_mul_f32 v123, v123, v244\n"
    "v_mul_f32 v124, v124, v244\n"
    "v_mul_f32 v125, v125, v244\n"
    "v_mul_f32 v126, v126, v244\n"
    "v_mul_f32 v127, v127, v244\n"
    "v_mul_f32 v128, v128, v244\n"
    "v_mul_f32 v129, v129, v244\n"
    "v_mul_f32 v130, v130, v244\n"
    "v_mul_f32 v131, v131, v244\n"
    "v_mul_f32 v132, v132, v244\n"
    "v_mul_f32 v133, v133, v244\n"
    "v_mul_f32 v134, v134, v244\n"
    "v_mul_f32 v135, v135, v244\n"
    "v_mul_f32 v136, v136, v244\n"
    "v_mul_f32 v137, v137, v244\n"
    "v_mul_f32 v138, v138, v244\n"
    "v_mul_f32 v139, v139, v244\n"
    "v_mul_f32 v140, v140, v244\n"
    "v_mul_f32 v141, v141, v244\n"
    "v_mul_f32 v142, v142, v244\n"
    "v_mul_f32 v143, v143, v244\n"
    "v_mul_f32 v14, v14, v244\n"
    "s_branch .Lkxret_145\n"
    ".Lkdump_146:\n"
    "s_setprio 1\n"
    "v_mov_b32 v244, v14\n"
    "s_nop 1\n"
    "v_permlane16_swap_b32 v14, v244\n"
    "v_add_f32 v14, v14, v244\n"
    "v_mov_b32 v244, v14\n"
    "s_nop 1\n"
    "v_permlane32_swap_b32 v14, v244\n"
    "v_add_f32 v14, v14, v244\n"
    "v_mov_b32 v246, v172\n"
    "v_mov_b32 v247, v14\n"
    "s_lshl_b32 s60, s12, 9\n"
    "s_add_u32 s60, s60, 0x1e000\n"
    "v_lshl_add_u32 v244, v1, 3, s60\n"
    "s_lshl_b32 s60, s12, 13\n"
    "s_add_u32 s60, s60, 0x16000\n"
    "v_add_u32 v245, s60, v11\n"
    "s_waitcnt lgkmcnt(0)\n"
    "ds_write_b64 v244, v[246:247]\n"
    "ds_write_b128 v245, v[112:115]\n"
    "ds_write_b128 v245, v[116:119] offset:1024\n"
    "ds_write_b128 v245, v[120:123] offset:2048\n"
    "ds_write_b128 v245, v[124:127] offset:3072\n"
    "ds_write_b128 v245, v[128:131] offset:4096\n"
    "ds_write_b128 v245, v[132:135] offset:5120\n"
    "ds_write_b128 v245, v[136:139] offset:6144\n"
    "ds_write_b128 v245, v[140:143] offset:7168\n"
    "s_cmp_gt_u32 s15, 20\n"
    "s_cbranch_scc1 .Lkdret_147\n"
    "ds_read_b128 v[144:147], v12\n"
    "ds_read_b128 v[148:151], v12 offset:512\n"
    "ds_read_b128 v[152:155], v12 offset:1024\n"
    "ds_read_b128 v[156:159], v12 offset:1536\n"
    "v_mov_b32 v172, 0\n"
    "v_mov_b32 v173, 0\n"
    "v_mov_b32 v174, 0\n"
    "v_mov_b32 v175, 0\n"
    "v_mov_b32 v14, 0\n"
    "s_mov_b32 s70, 0xff800000\n"
    "s_mov_b32 s71, 0xff800000\n"
    "s_mov_b32 s76, 0\n"
    "s_waitcnt lgkmcnt(0)\n"
    "v_mfma_f32_32x32x16_bf16 v[112:127], v[172:175], v[172:175], 0\n"
    "v_mfma_f32_32x32x16_bf16 v[128:143], v[172:175], v[172:175], 0\n"
    "s_branch .Lkdret_147\n"
    ".Lkxrare_148:\n"
    "v_mov_b32 v244, v15\n"
    "s_nop 1\n"
    "v_permlane16_swap_b32 v15, v244\n"
    "v_max_f32 v15, v15, v244\n"
    "v_mov_b32 v244, v15\n"
    "s_nop 1\n"
    "v_permlane32_swap_b32 v15, v244\n"
    "v_max_f32 v15, v15, v244\n"
    "v_max_f32 v15, s71, v15\n"
    "v_ceil_f32 v15, v15\n"
    "v_sub_f32 v160, v160, v15\n"
    "v_sub_f32 v164, v164, v15\n"
    "v_sub_f32 v172, v172, v15\n"
    "v_sub_f32 v161, v161, v15\n"
    "v_sub_f32 v165, v165, v15\n"
    "v_sub_f32 v173, v173, v15\n"
    "v_sub_f32 v162, v162, v15\n"
    "v_sub_f32 v166, v166, v15\n"
    "v_sub_f32 v174, v174, v15\n"
    "v_sub_f32 v163, v163, v15\n"
    "v_sub_f32 v167, v167, v15\n"
    "v_sub_f32 v175, v175, v15\n"
    "s_cmp_lg_u32 s71, 0\n"
    "s_mov_b32 s70, 0x41000000\n"
    "s_mov_b32 s71, 0\n"
    "s_cbranch_scc1 .Lkxret_149\n"
    "v_sub_f32 v244, 0, v15\n"
    "v_exp_f32 v244, v244\n"
    "s_nop 0\n"
    "v_mul_f32 v112, v112, v244\n"
    "v_mul_f32 v113, v113, v244\n"
    "v_mul_f32 v114, v114, v244\n"
    "v_mul_f32 v115, v115, v244\n"
    "v_mul_f32 v116, v116, v244\n"
    "v_mul_f32 v117, v117, v244\n"
    "v_mul_f32 v118, v118, v244\n"
    "v_mul_f32 v119, v119, v244\n"
    "v_mul_f32 v120, v120, v244\n"
    "v_mul_f32 v121, v121, v244\n"
    "v_mul_f32 v122, v122, v244\n"
    "v_mul_f32 v123, v123, v244\n"
    "v_mul_f32 v124, v124, v244\n"
    "v_mul_f32 v125, v125, v244\n"
    "v_mul_f32 v126, v126, v244\n"
    "v_mul_f32 v127, v127, v244\n"
    "v_mul_f32 v128, v128, v244\n"
    "v_mul_f32 v129, v129, v244\n"
    "v_mul_f32 v130, v130, v244\n"
    "v_mul_f32 v131, v131, v244\n"
    "v_mul_f32 v132, v132, v244\n"
    "v_mul_f32 v133, v133, v244\n"
    "v_mul_f32 v134, v134, v244\n"
    "v_mul_f32 v135, v135, v244\n"
    "v_mul_f32 v136, v136, v244\n"
    "v_mul_f32 v137, v137, v244\n"
    "v_mul_f32 v138, v138, v244\n"
    "v_mul_f32 v139, v139, v244\n"
    "v_mul_f32 v140, v140, v244\n"
    "v_mul_f32 v141, v141, v244\n"
    "v_mul_f32 v142, v142, v244\n"
    "v_mul_f32 v143, v143, v244\n"
    "v_mul_f32 v14, v14, v244\n"
    "s_branch .Lkxret_149\n"
    ".Lkdump_151:\n"
    "s_setprio 1\n"
    "v_mov_b32 v244, v14\n"
    "s_nop 1\n"
    "v_permlane16_swap_b32 v14, v244\n"
    "v_add_f32 v14, v14, v244\n"
    "v_mov_b32 v244, v14\n"
    "s_nop 1\n"
    "v_permlane32_swap_b32 v14, v244\n"
    "v_add_f32 v14, v14, v244\n"
    "v_mov_b32 v246, v172\n"
    "v_mov_b32 v247, v14\n"
    "s_lshl_b32 s60, s12, 9\n"
    "s_add_u32 s60, s60, 0x1e000\n"
    "v_lshl_add_u32 v244, v1, 3, s60\n"
    "s_lshl_b32 s60, s12, 13\n"
    "s_add_u32 s60, s60, 0x16000\n"
    "v_add_u32 v245, s60, v11\n"
    "s_waitcnt lgkmcnt(0)\n"
    "ds_write_b64 v244, v[246:247]\n"
    "ds_write_b128 v245, v[112:115]\n"
    "ds_write_b128 v245, v[116:119] offset:1024\n"
    "ds_write_b128 v245, v[120:123] offset:2048\n"
    "ds_write_b128 v245, v[124:127] offset:3072\n"
    "ds_write_b128 v245, v[128:131] offset:4096\n"
    "ds_write_b128 v245, v[132:135] offset:5120\n"
    "ds_write_b128 v245, v[136:139] offset:6144\n"
    "ds_write_b128 v245, v[140:143] offset:7168\n"
    "s_cmp_gt_u32 s15, 20\n"
    "s_cbranch_scc1 .Lkdret_152\n"
    "ds_read_b128 v[144:147], v12\n"
    "ds_read_b128 v[148:151], v12 offset:512\n"
    "ds_read_b128 v[152:155], v12 offset:1024\n"
    "ds_read_b128 v[156:159], v12 offset:1536\n"
    "v_mov_b32 v172, 0\n"
    "v_mov_b32 v173, 0\n"
    "v_mov_b32 v174, 0\n"
    "v_mov_b32 v175, 0\n"
    "v_mov_b32 v14, 0\n"
    "s_mov_b32 s70, 0xff800000\n"
    "s_mov_b32 s71, 0xff800000\n"
    "s_mov_b32 s76, 0\n"
    "s_waitcnt lgkmcnt(0)\n"
    "v_mfma_f32_32x32x16_bf16 v[112:127], v[172:175], v[172:175], 0\n"
    "v_mfma_f32_32x32x16_bf16 v[128:143], v[172:175], v[172:175], 0\n"
    "s_branch .Lkdret_152\n"
    ".Lkxrare_155:\n"
    "v_mov_b32 v244, v15\n"
    "s_nop 1\n"
    "v_permlane16_swap_b32 v15, v244\n"
    "v_max_f32 v15, v15, v244\n"
    "v_mov_b32 v244, v15\n"
    "s_nop 1\n"
    "v_permlane32_swap_b32 v15, v244\n"
    "v_max_f32 v15, v15, v244\n"
    "v_max_f32 v15, s71, v15\n"
    "v_ceil_f32 v15, v15\n"
    "v_sub_f32 v160, v160, v15\n"
    "v_sub_f32 v164, v164, v15\n"
    "v_sub_f32 v172, v172, v15\n"
    "v_sub_f32 v161, v161, v15\n"
    "v_sub_f32 v165, v165, v15\n"
    "v_sub_f32 v173, v173, v15\n"
    "v_sub_f32 v162, v162, v15\n"
    "v_sub_f32 v166, v166, v15\n"
    "v_sub_f32 v174, v174, v15\n"
    "v_sub_f32 v163, v163, v15\n"
    "v_sub_f32 v167, v167, v15\n"
    "v_sub_f32 v175, v175, v15\n"
    "s_cmp_lg_u32 s71, 0\n"
    "s_mov_b32 s70, 0x41000000\n"
    "s_mov_b32 s71, 0\n"
    "s_cbranch_scc1 .Lkxret_156\n"
    "v_sub_f32 v244, 0, v15\n"
    "v_exp_f32 v244, v244\n"
    "s_nop 0\n"
    "v_mul_f32 v112, v112, v244\n"
    "v_mul_f32 v113, v113, v244\n"
    "v_mul_f32 v114, v114, v244\n"
    "v_mul_f32 v115, v115, v244\n"
    "v_mul_f32 v116, v116, v244\n"
    "v_mul_f32 v117, v117, v244\n"
    "v_mul_f32 v118, v118, v244\n"
    "v_mul_f32 v119, v119, v244\n"
    "v_mul_f32 v120, v120, v244\n"
    "v_mul_f32 v121, v121, v244\n"
    "v_mul_f32 v122, v122, v244\n"
    "v_mul_f32 v123, v123, v244\n"
    "v_mul_f32 v124, v124, v244\n"
    "v_mul_f32 v125, v125, v244\n"
    "v_mul_f32 v126, v126, v244\n"
    "v_mul_f32 v127, v127, v244\n"
    "v_mul_f32 v128, v128, v244\n"
    "v_mul_f32 v129, v129, v244\n"
    "v_mul_f32 v130, v130, v244\n"
    "v_mul_f32 v131, v131, v244\n"
    "v_mul_f32 v132, v132, v244\n"
    "v_mul_f32 v133, v133, v244\n"
    "v_mul_f32 v134, v134, v244\n"
    "v_mul_f32 v135, v135, v244\n"
    "v_mul_f32 v136, v136, v244\n"
    "v_mul_f32 v137, v137, v244\n"
    "v_mul_f32 v138, v138, v244\n"
    "v_mul_f32 v139, v139, v244\n"
    "v_mul_f32 v140, v140, v244\n"
    "v_mul_f32 v141, v141, v244\n"
    "v_mul_f32 v142, v142, v244\n"
    "v_mul_f32 v143, v143, v244\n"
    "v_mul_f32 v14, v14, v244\n"
    "s_branch .Lkxret_156\n"
    ".Lkdump_157:\n"
    "s_setprio 1\n"
    "v_mov_b32 v244, v14\n"
    "s_nop 1\n"
    "v_permlane16_swap_b32 v14, v244\n"
    "v_add_f32 v14, v14, v244\n"
    "v_mov_b32 v244, v14\n"
    "s_nop 1\n"
    "v_permlane32_swap_b32 v14, v244\n"
    "v_add_f32 v14, v14, v244\n"
    "v_mov_b32 v246, v172\n"
    "v_mov_b32 v247, v14\n"
    "s_lshl_b32 s60, s12, 9\n"
    "s_add_u32 s60, s60, 0x1e000\n"
    "v_lshl_add_u32 v244, v1, 3, s60\n"
    "s_lshl_b32 s60, s12, 13\n"
    "s_add_u32 s60, s60, 0x16000\n"
    "v_add_u32 v245, s60, v11\n"
    "s_waitcnt lgkmcnt(0)\n"
    "ds_write_b64 v244, v[246:247]\n"
    "ds_write_b128 v245, v[112:115]\n"
    "ds_write_b128 v245, v[116:119] offset:1024\n"
    "ds_write_b128 v245, v[120:123] offset:2048\n"
    "ds_write_b128 v245, v[124:127] offset:3072\n"
    "ds_write_b128 v245, v[128:131] offset:4096\n"
    "ds_write_b128 v245, v[132:135] offset:5120\n"
    "ds_write_b128 v245, v[136:139] offset:6144\n"
    "ds_write_b128 v245, v[140:143] offset:7168\n"
    "s_cmp_gt_u32 s15, 20\n"
    "s_cbranch_scc1 .Lkdret_158\n"
    "ds_read_b128 v[144:147], v12\n"
    "ds_read_b128 v[148:151], v12 offset:512\n"
    "ds_read_b128 v[152:155], v12 offset:1024\n"
    "ds_read_b128 v[156:159], v12 offset:1536\n"
    "v_mov_b32 v172, 0\n"
    "v_mov_b32 v173, 0\n"
    "v_mov_b32 v174, 0\n"
    "v_mov_b32 v175, 0\n"
    "v_mov_b32 v14, 0\n"
    "s_mov_b32 s70, 0xff800000\n"
    "s_mov_b32 s71, 0xff800000\n"
    "s_mov_b32 s76, 0\n"
    "s_waitcnt lgkmcnt(0)\n"
    "v_mfma_f32_32x32x16_bf16 v[112:127], v[172:175], v[172:175], 0\n"
    "v_mfma_f32_32x32x16_bf16 v[128:143], v[172:175], v[172:175], 0\n"
    "s_branch .Lkdret_158\n"
    ".Lkdump_159:\n"
    "s_setprio 1\n"
    "v_mov_b32 v244, v14\n"
    "s_nop 1\n"
    "v_permlane16_swap_b32 v14, v244\n"
    "v_add_f32 v14, v14, v244\n"
    "v_mov_b32 v244, v14\n"
    "s_nop 1\n"
    "v_permlane32_swap_b32 v14, v244\n"
    "v_add_f32 v14, v14, v244\n"
    "v_mov_b32 v246, v172\n"
    "v_mov_b32 v247, v14\n"
    "s_lshl_b32 s60, s12, 9\n"
    "s_add_u32 s60, s60, 0x1e000\n"
    "v_lshl_add_u32 v244, v1, 3, s60\n"
    "s_lshl_b32 s60, s12, 13\n"
    "s_add_u32 s60, s60, 0x16000\n"
    "v_add_u32 v245, s60, v11\n"
    "s_waitcnt lgkmcnt(0)\n"
    "ds_write_b64 v244, v[246:247]\n"
    "ds_write_b128 v245, v[112:115]\n"
    "ds_write_b128 v245, v[116:119] offset:1024\n"
    "ds_write_b128 v245, v[120:123] offset:2048\n"
    "ds_write_b128 v245, v[124:127] offset:3072\n"
    "ds_write_b128 v245, v[128:131] offset:4096\n"
    "ds_write_b128 v245, v[132:135] offset:5120\n"
    "ds_write_b128 v245, v[136:139] offset:6144\n"
    "ds_write_b128 v245, v[140:143] offset:7168\n"
    "s_cmp_gt_u32 s15, 20\n"
    "s_cbranch_scc1 .Lkdret_160\n"
    "ds_read_b128 v[144:147], v12\n"
    "ds_read_b128 v[148:151], v12 offset:512\n"
    "ds_read_b128 v[152:155], v12 offset:1024\n"
    "ds_read_b128 v[156:159], v12 offset:1536\n"
    "v_mov_b32 v172, 0\n"
    "v_mov_b32 v173, 0\n"
    "v_mov_b32 v174, 0\n"
    "v_mov_b32 v175, 0\n"
    "v_mov_b32 v14, 0\n"
    "s_mov_b32 s70, 0xff800000\n"
    "s_mov_b32 s71, 0xff800000\n"
    "s_mov_b32 s76, 0\n"
    "s_waitcnt lgkmcnt(0)\n"
    "v_mfma_f32_32x32x16_bf16 v[112:127], v[172:175], v[172:175], 0\n"
    "v_mfma_f32_32x32x16_bf16 v[128:143], v[172:175], v[172:175], 0\n"
    "s_branch .Lkdret_160\n"
    ".Lkhotend_2:\n"
    ".Lkrare_14:\n"
    "s_nop 7\n"
    "v_max3_f32 v244, v80, v81, v82\n"
    "v_max3_f32 v245, v83, v84, v85\n"
    "v_max3_f32 v244, v244, v86, v87\n"
    "v_max_f32 v244, v244, v245\n"
    "v_mov_b32 v245, v244\n"
    "s_nop 1\n"
    "v_permlane16_swap_b32 v244, v245\n"
    "v_max_f32 v244, v244, v245\n"
    "v_mov_b32 v245, v244\n"
    "s_nop 1\n"
    "v_permlane32_swap_b32 v244, v245\n"
    "v_max_f32 v244, v244, v245\n"
    "v_max_f32 v244, 0, v244\n"
    "v_ceil_f32 v244, v244\n"
    "v_sub_f32 v245, 0, v244\n"
    "v_exp_f32 v245, v245\n"
    "s_nop 0\n"
    "v_mul_f32 v228, v228, v245\n"
    "v_sub_f32 v80, v80, v244\n"
    "v_sub_f32 v81, v81, v244\n"
    "v_sub_f32 v82, v82, v244\n"
    "v_sub_f32 v83, v83, v244\n"
    "v_sub_f32 v84, v84, v244\n"
    "v_sub_f32 v85, v85, v244\n"
    "v_sub_f32 v86, v86, v244\n"
    "v_sub_f32 v87, v87, v244\n"
    "v_sub_f32 v96, v96, v244\n"
    "v_sub_f32 v97, v97, v244\n"
    "v_sub_f32 v98, v98, v244\n"
    "v_sub_f32 v99, v99, v244\n"
    "v_sub_f32 v100, v100, v244\n"
    "v_sub_f32 v101, v101, v244\n"
    "v_sub_f32 v102, v102, v244\n"
    "v_sub_f32 v103, v103, v244\n"
    "v_sub_f32 v112, v112, v244\n"
    "v_sub_f32 v113, v113, v244\n"
    "v_sub_f32 v114, v114, v244\n"
    "v_sub_f32 v115, v115, v244\n"
    "v_max3_f32 v244, v88, v89, v90\n"
    "v_max3_f32 v245, v91, v92, v93\n"
    "v_max3_f32 v244, v244, v94, v95\n"
    "v_max_f32 v244, v244, v245\n"
    "v_mov_b32 v245, v244\n"
    "s_nop 1\n"
    "v_permlane16_swap_b32 v244, v245\n"
    "v_max_f32 v244, v244, v245\n"
    "v_mov_b32 v245, v244\n"
    "s_nop 1\n"
    "v_permlane32_swap_b32 v244, v245\n"
    "v_max_f32 v244, v244, v245\n"
    "v_max_f32 v244, 0, v244\n"
    "v_ceil_f32 v244, v244\n"
    "v_sub_f32 v245, 0, v244\n"
    "v_exp_f32 v245, v245\n"
    "s_nop 0\n"
    "v_mul_f32 v229, v229, v245\n"
    "v_sub_f32 v88, v88, v244\n"
    "v_sub_f32 v89, v89, v244\n"
    "v_sub_f32 v90, v90, v244\n"
    "v_sub_f32 v91, v91, v244\n"
    "v_sub_f32 v92, v92, v244\n"
    "v_sub_f32 v93, v93, v244\n"
    "v_sub_f32 v94, v94, v244\n"
    "v_sub_f32 v95, v95, v244\n"
    "v_sub_f32 v104, v104, v244\n"
    "v_sub_f32 v105, v105, v244\n"
    "v_sub_f32 v106, v106, v244\n"
    "v_sub_f32 v107, v107, v244\n"
    "v_sub_f32 v108, v108, v244\n"
    "v_sub_f32 v109, v109, v244\n"
    "v_sub_f32 v110, v110, v244\n"
    "v_sub_f32 v111, v111, v244\n"
    "v_sub_f32 v116, v116, v244\n"
    "v_sub_f32 v117, v117, v244\n"
    "v_sub_f32 v118, v118, v244\n"
    "v_sub_f32 v119, v119, v244\n"
    "v_exp_f32 v12, v80\n"
    "v_exp_f32 v13, v81\n"
    "v_exp_f32 v14, v82\n"
    "v_add_f32 v230, v12, v13\n"
    "v_cvt_pk_bf16_f32 v160, v12, v13\n"
    "v_exp_f32 v15, v83\n"
    "v_add_f32 v230, v230, v14\n"
    "v_exp_f32 v12, v84\n"
    "v_add_f32 v230, v230, v15\n"
    "v_cvt_pk_bf16_f32 v161, v14, v15\n"
    "v_exp_f32 v13, v85\n"
    "v_add_f32 v230, v230, v12\n"
    "v_exp_f32 v14, v86\n"
    "v_add_f32 v230, v230, v13\n"
    "v_cvt_pk_bf16_f32 v162, v12, v13\n"
    "v_exp_f32 v15, v87\n"
    "v_add_f32 v230, v230, v14\n"
    "v_exp_f32 v12, v88\n"
    "v_add_f32 v230, v230, v15\n"
    "v_cvt_pk_bf16_f32 v163, v14, v15\n"
    "v_exp_f32 v13, v89\n"
    "v_exp_f32 v14, v90\n"
    "v_add_f32 v231, v12, v13\n"
    "v_cvt_pk_bf16_f32 v164, v12, v13\n"
    "v_exp_f32 v15, v91\n"
    "v_add_f32 v231, v231, v14\n"
    "v_exp_f32 v12, v92\n"
    "v_add_f32 v231, v231, v15\n"
    "v_cvt_pk_bf16_f32 v165, v14, v15\n"
    "v_exp_f32 v13, v93\n"
    "v_add_f32 v231, v231, v12\n"
    "v_exp_f32 v14, v94\n"
    "v_add_f32 v231, v231, v13\n"
    "v_cvt_pk_bf16_f32 v166, v12, v13\n"
    "v_exp_f32 v15, v95\n"
    "v_add_f32 v231, v231, v14\n"
    "v_add_f32 v231, v231, v15\n"
    "v_cvt_pk_bf16_f32 v167, v14, v15\n"
    "v_max_f32 v248, v230, v231\n"
    "v_cmp_nge_f32 vcc, s70, v248\n"
    "s_nop 1\n"
    "s_branch .Lkrret_15\n"
    ".Lkrare_17:\n"
    "s_nop 7\n"
    "v_max3_f32 v244, v96, v97, v98\n"
    "v_max3_f32 v245, v99, v100, v101\n"
    "v_max3_f32 v244, v244, v102, v103\n"
    "v_max_f32 v244, v244, v245\n"
    "v_mov_b32 v245, v244\n"
    "s_nop 1\n"
    "v_permlane16_swap_b32 v244, v245\n"
    "v_max_f32 v244, v244, v245\n"
    "v_mov_b32 v245, v244\n"
    "s_nop 1\n"
    "v_permlane32_swap_b32 v244, v245\n"
    "v_max_f32 v244, v244, v245\n"
    "v_max_f32 v244, 0, v244\n"
    "v_ceil_f32 v244, v244\n"
    "v_sub_f32 v245, 0, v244\n"
    "v_exp_f32 v245, v245\n"
    "s_nop 0\n"
    "v_mul_f32 v16, v16, v245\n"
    "v_mul_f32 v17, v17, v245\n"
    "v_mul_f32 v18, v18, v245\n"
    "v_mul_f32 v19, v19, v245\n"
    "v_mul_f32 v24, v24, v245\n"
    "v_mul_f32 v25, v25, v245\n"
    "v_mul_f32 v26, v26, v245\n"
    "v_mul_f32 v27, v27, v245\n"
    "v_mul_f32 v32, v32, v245\n"
    "v_mul_f32 v33, v33, v245\n"
    "v_mul_f32 v34, v34, v245\n"
    "v_mul_f32 v35, v35, v245\n"
    "v_mul_f32 v40, v40, v245\n"
    "v_mul_f32 v41, v41, v245\n"
    "v_mul_f32 v42, v42, v245\n"
    "v_mul_f32 v43, v43, v245\n"
    "v_mul_f32 v48, v48, v245\n"
    "v_mul_f32 v49, v49, v245\n"
    "v_mul_f32 v50, v50, v245\n"
    "v_mul_f32 v51, v51, v245\n"
    "v_mul_f32 v56, v56, v245\n"
    "v_mul_f32 v57, v57, v245\n"
    "v_mul_f32 v58, v58, v245\n"
    "v_mul_f32 v59, v59, v245\n"
    "v_mul_f32 v64, v64, v245\n"
    "v_mul_f32 v65, v65, v245\n"
    "v_mul_f32 v66, v66, v245\n"
    "v_mul_f32 v67, v67, v245\n"
    "v_mul_f32 v72, v72, v245\n"
    "v_mul_f32 v73, v73, v245\n"
    "v_mul_f32 v74, v74, v245\n"
    "v_mul_f32 v75, v75, v245\n"
    "v_mul_f32 v228, v228, v245\n"
    "v_sub_f32 v96, v96, v244\n"
    "v_sub_f32 v97, v97, v244\n"
    "v_sub_f32 v98, v98, v244\n"
    "v_sub_f32 v99, v99, v244\n"
    "v_sub_f32 v100, v100, v244\n"
    "v_sub_f32 v101, v101, v244\n"
    "v_sub_f32 v102, v102, v244\n"
    "v_sub_f32 v103, v103, v244\n"
    "v_sub_f32 v80, v80, v244\n"
    "v_sub_f32 v81, v81, v244\n"
    "v_sub_f32 v82, v82, v244\n"
    "v_sub_f32 v83, v83, v244\n"
    "v_sub_f32 v84, v84, v244\n"
    "v_sub_f32 v85, v85, v244\n"
    "v_sub_f32 v86, v86, v244\n"
    "v_sub_f32 v87, v87, v244\n"
    "v_sub_f32 v112, v112, v244\n"
    "v_sub_f32 v113, v113, v244\n"
    "v_sub_f32 v114, v114, v244\n"
    "v_sub_f32 v115, v115, v244\n"
    "v_max3_f32 v244, v104, v105, v106\n"
    "v_max3_f32 v245, v107, v108, v109\n"
    "v_max3_f32 v244, v244, v110, v111\n"
    "v_max_f32 v244, v244, v245\n"
    "v_mov_b32 v245, v244\n"
    "s_nop 1\n"
    "v_permlane16_swap_b32 v244, v245\n"
    "v_max_f32 v244, v244, v245\n"
    "v_mov_b32 v245, v244\n"
    "s_nop 1\n"
    "v_permlane32_swap_b32 v244, v245\n"
    "v_max_f32 v244, v244, v245\n"
    "v_max_f32 v244, 0, v244\n"
    "v_ceil_f32 v244, v244\n"
    "v_sub_f32 v245, 0, v244\n"
    "v_exp_f32 v245, v245\n"
    "s_nop 0\n"
    "v_mul_f32 v20, v20, v245\n"
    "v_mul_f32 v21, v21, v245\n"
    "v_mul_f32 v22, v22, v245\n"
    "v_mul_f32 v23, v23, v245\n"
    "v_mul_f32 v28, v28, v245\n"
    "v_mul_f32 v29, v29, v245\n"
    "v_mul_f32 v30, v30, v245\n"
    "v_mul_f32 v31, v31, v245\n"
    "v_mul_f32 v36, v36, v245\n"
    "v_mul_f32 v37, v37, v245\n"
    "v_mul_f32 v38, v38, v245\n"
    "v_mul_f32 v39, v39, v245\n"
    "v_mul_f32 v44, v44, v245\n"
    "v_mul_f32 v45, v45, v245\n"
    "v_mul_f32 v46, v46, v245\n"
    "v_mul_f32 v47, v47, v245\n"
    "v_mul_f32 v52, v52, v245\n"
    "v_mul_f32 v53, v53, v245\n"
    "v_mul_f32 v54, v54, v245\n"
    "v_mul_f32 v55, v55, v245\n"
    "v_mul_f32 v60, v60, v245\n"
    "v_mul_f32 v61, v61, v245\n"
    "v_mul_f32 v62, v62, v245\n"
    "v_mul_f32 v63, v63, v245\n"
    "v_mul_f32 v68, v68, v245\n"
    "v_mul_f32 v69, v69, v245\n"
    "v_mul_f32 v70, v70, v245\n"
    "v_mul_f32 v71, v71, v245\n"
    "v_mul_f32 v76, v76, v245\n"
    "v_mul_f32 v77, v77, v245\n"
    "v_mul_f32 v78, v78, v245\n"
    "v_mul_f32 v79, v79, v245\n"
    "v_mul_f32 v229, v229, v245\n"
    "v_sub_f32 v104, v104, v244\n"
    "v_sub_f32 v105, v105, v244\n"
    "v_sub_f32 v106, v106, v244\n"
    "v_sub_f32 v107, v107, v244\n"
    "v_sub_f32 v108, v108, v244\n"
    "v_sub_f32 v109, v109, v244\n"
    "v_sub_f32 v110, v110, v244\n"
    "v_sub_f32 v111, v111, v244\n"
    "v_sub_f32 v88, v88, v244\n"
    "v_sub_f32 v89, v89, v244\n"
    "v_sub_f32 v90, v90, v244\n"
    "v_sub_f32 v91, v91, v244\n"
    "v_sub_f32 v92, v92, v244\n"
    "v_sub_f32 v93, v93, v244\n"
    "v_sub_f32 v94, v94, v244\n"
    "v_sub_f32 v95, v95, v244\n"
    "v_sub_f32 v116, v116, v244\n"
    "v_sub_f32 v117, v117, v244\n"
    "v_sub_f32 v118, v118, v244\n"
    "v_sub_f32 v119, v119, v244\n"
    "v_exp_f32 v12, v96\n"
    "v_exp_f32 v13, v97\n"
    "v_exp_f32 v14, v98\n"
    "v_add_f32 v230, v12, v13\n"
    "v_cvt_pk_bf16_f32 v168, v12, v13\n"
    "v_exp_f32 v15, v99\n"
    "v_add_f32 v230, v230, v14\n"
    "v_exp_f32 v12, v100\n"
    "v_add_f32 v230, v230, v15\n"
    "v_cvt_pk_bf16_f32 v169, v14, v15\n"
    "v_exp_f32 v13, v101\n"
    "v_add_f32 v230, v230, v12\n"
    "v_exp_f32 v14, v102\n"
    "v_add_f32 v230, v230, v13\n"
    "v_cvt_pk_bf16_f32 v170, v12, v13\n"
    "v_exp_f32 v15, v103\n"
    "v_add_f32 v230, v230, v14\n"
    "v_exp_f32 v12, v104\n"
    "v_add_f32 v230, v230, v15\n"
    "v_cvt_pk_bf16_f32 v171, v14, v15\n"
    "v_exp_f32 v13, v105\n"
    "v_exp_f32 v14, v106\n"
    "v_add_f32 v231, v12, v13\n"
    "v_cvt_pk_bf16_f32 v172, v12, v13\n"
    "v_exp_f32 v15, v107\n"
    "v_add_f32 v231, v231, v14\n"
    "v_exp_f32 v12, v108\n"
    "v_add_f32 v231, v231, v15\n"
    "v_cvt_pk_bf16_f32 v173, v14, v15\n"
    "v_exp_f32 v13, v109\n"
    "v_add_f32 v231, v231, v12\n"
    "v_exp_f32 v14, v110\n"
    "v_add_f32 v231, v231, v13\n"
    "v_cvt_pk_bf16_f32 v174, v12, v13\n"
    "v_exp_f32 v15, v111\n"
    "v_add_f32 v231, v231, v14\n"
    "v_add_f32 v231, v231, v15\n"
    "v_cvt_pk_bf16_f32 v175, v14, v15\n"
    "v_max_f32 v248, v230, v231\n"
    "v_cmp_nge_f32 vcc, s70, v248\n"
    "s_nop 1\n"
    "s_branch .Lkrret_18\n"
    ".Lkrare_19:\n"
    "s_nop 7\n"
    "v_max3_f32 v244, v80, v81, v82\n"
    "v_max3_f32 v245, v83, v84, v85\n"
    "v_max3_f32 v244, v244, v86, v87\n"
    "v_max_f32 v244, v244, v245\n"
    "v_mov_b32 v245, v244\n"
    "s_nop 1\n"
    "v_permlane16_swap_b32 v244, v245\n"
    "v_max_f32 v244, v244, v245\n"
    "v_mov_b32 v245, v244\n"
    "s_nop 1\n"
    "v_permlane32_swap_b32 v244, v245\n"
    "v_max_f32 v244, v244, v245\n"
    "v_max_f32 v244, 0, v244\n"
    "v_ceil_f32 v244, v244\n"
    "v_sub_f32 v245, 0, v244\n"
    "v_exp_f32 v245, v245\n"
    "s_nop 0\n"
    "v_mul_f32 v16, v16, v245\n"
    "v_mul_f32 v17, v17, v245\n"
    "v_mul_f32 v18, v18, v245\n"
    "v_mul_f32 v19, v19, v245\n"
    "v_mul_f32 v24, v24, v245\n"
    "v_mul_f32 v25, v25, v245\n"
    "v_mul_f32 v26, v26, v245\n"
    "v_mul_f32 v27, v27, v245\n"
    "v_mul_f32 v32, v32, v245\n"
    "v_mul_f32 v33, v33, v245\n"
    "v_mul_f32 v34, v34, v245\n"
    "v_mul_f32 v35, v35, v245\n"
    "v_mul_f32 v40, v40, v245\n"
    "v_mul_f32 v41, v41, v245\n"
    "v_mul_f32 v42, v42, v245\n"
    "v_mul_f32 v43, v43, v245\n"
    "v_mul_f32 v48, v48, v245\n"
    "v_mul_f32 v49, v49, v245\n"
    "v_mul_f32 v50, v50, v245\n"
    "v_mul_f32 v51, v51, v245\n"
    "v_mul_f32 v56, v56, v245\n"
    "v_mul_f32 v57, v57, v245\n"
    "v_mul_f32 v58, v58, v245\n"
    "v_mul_f32 v59, v59, v245\n"
    "v_mul_f32 v64, v64, v245\n"
    "v_mul_f32 v65, v65, v245\n"
    "v_mul_f32 v66, v66, v245\n"
    "v_mul_f32 v67, v67, v245\n"
    "v_mul_f32 v72, v72, v245\n"
    "v_mul_f32 v73, v73, v245\n"
    "v_mul_f32 v74, v74, v245\n"
    "v_mul_f32 v75, v75, v245\n"
    "v_mul_f32 v228, v228, v245\n"
    "v_sub_f32 v80, v80, v244\n"
    "v_sub_f32 v81, v81, v244\n"
    "v_sub_f32 v82, v82, v244\n"
    "v_sub_f32 v83, v83, v244\n"
    "v_sub_f32 v84, v84, v244\n"
    "v_sub_f32 v85, v85, v244\n"
    "v_sub_f32 v86, v86, v244\n"
    "v_sub_f32 v87, v87, v244\n"
    "v_sub_f32 v96, v96, v244\n"
    "v_sub_f32 v97, v97, v244\n"
    "v_sub_f32 v98, v98, v244\n"
    "v_sub_f32 v99, v99, v244\n"
    "v_sub_f32 v100, v100, v244\n"
    "v_sub_f32 v101, v101, v244\n"
    "v_sub_f32 v102, v102, v244\n"
    "v_sub_f32 v103, v103, v244\n"
    "v_sub_f32 v112, v112, v244\n"
    "v_sub_f32 v113, v113, v244\n"
    "v_sub_f32 v114, v114, v244\n"
    "v_sub_f32 v115, v115, v244\n"
    "v_max3_f32 v244, v88, v89, v90\n"
    "v_max3_f32 v245, v91, v92, v93\n"
    "v_max3_f32 v244, v244, v94, v95\n"
    "v_max_f32 v244, v244, v245\n"
    "v_mov_b32 v245, v244\n"
    "s_nop 1\n"
    "v_permlane16_swap_b32 v244, v245\n"
    "v_max_f32 v244, v244, v245\n"
    "v_mov_b32 v245, v244\n"
    "s_nop 1\n"
    "v_permlane32_swap_b32 v244, v245\n"
    "v_max_f32 v244, v244, v245\n"
    "v_max_f32 v244, 0, v244\n"
    "v_ceil_f32 v244, v244\n"
    "v_sub_f32 v245, 0, v244\n"
    "v_exp_f32 v245, v245\n"
    "s_nop 0\n"
    "v_mul_f32 v20, v20, v245\n"
    "v_mul_f32 v21, v21, v245\n"
    "v_mul_f32 v22, v22, v245\n"
    "v_mul_f32 v23, v23, v245\n"
    "v_mul_f32 v28, v28, v245\n"
    "v_mul_f32 v29, v29, v245\n"
    "v_mul_f32 v30, v30, v245\n"
    "v_mul_f32 v31, v31, v245\n"
    "v_mul_f32 v36, v36, v245\n"
    "v_mul_f32 v37, v37, v245\n"
    "v_mul_f32 v38, v38, v245\n"
    "v_mul_f32 v39, v39, v245\n"
    "v_mul_f32 v44, v44, v245\n"
    "v_mul_f32 v45, v45, v245\n"
    "v_mul_f32 v46, v46, v245\n"
    "v_mul_f32 v47, v47, v245\n"
    "v_mul_f32 v52, v52, v245\n"
    "v_mul_f32 v53, v53, v245\n"
    "v_mul_f32 v54, v54, v245\n"
    "v_mul_f32 v55, v55, v245\n"
    "v_mul_f32 v60, v60, v245\n"
    "v_mul_f32 v61, v61, v245\n"
    "v_mul_f32 v62, v62, v245\n"
    "v_mul_f32 v63, v63, v245\n"
    "v_mul_f32 v68, v68, v245\n"
    "v_mul_f32 v69, v69, v245\n"
    "v_mul_f32 v70, v70, v245\n"
    "v_mul_f32 v71, v71, v245\n"
    "v_mul_f32 v76, v76, v245\n"
    "v_mul_f32 v77, v77, v245\n"
    "v_mul_f32 v78, v78, v245\n"
    "v_mul_f32 v79, v79, v245\n"
    "v_mul_f32 v229, v229, v245\n"
    "v_sub_f32 v88, v88, v244\n"
    "v_sub_f32 v89, v89, v244\n"
    "v_sub_f32 v90, v90, v244\n"
    "v_sub_f32 v91, v91, v244\n"
    "v_sub_f32 v92, v92, v244\n"
    "v_sub_f32 v93, v93, v244\n"
    "v_sub_f32 v94, v94, v244\n"
    "v_sub_f32 v95, v95, v244\n"
    "v_sub_f32 v104, v104, v244\n"
    "v_sub_f32 v105, v105, v244\n"
    "v_sub_f32 v106, v106, v244\n"
    "v_sub_f32 v107, v107, v244\n"
    "v_sub_f32 v108, v108, v244\n"
    "v_sub_f32 v109, v109, v244\n"
    "v_sub_f32 v110, v110, v244\n"
    "v_sub_f32 v111, v111, v244\n"
    "v_sub_f32 v116, v116, v244\n"
    "v_sub_f32 v117, v117, v244\n"
    "v_sub_f32 v118, v118, v244\n"
    "v_sub_f32 v119, v119, v244\n"
    "v_exp_f32 v12, v80\n"
    "v_exp_f32 v13, v81\n"
    "v_exp_f32 v14, v82\n"
    "v_add_f32 v230, v12, v13\n"
    "v_cvt_pk_bf16_f32 v160, v12, v13\n"
    "v_exp_f32 v15, v83\n"
    "v_add_f32 v230, v230, v14\n"
    "v_exp_f32 v12, v84\n"
    "v_add_f32 v230, v230, v15\n"
    "v_cvt_pk_bf16_f32 v161, v14, v15\n"
    "v_exp_f32 v13, v85\n"
    "v_add_f32 v230, v230, v12\n"
    "v_exp_f32 v14, v86\n"
    "v_add_f32 v230, v230, v13\n"
    "v_cvt_pk_bf16_f32 v162, v12, v13\n"
    "v_exp_f32 v15, v87\n"
    "v_add_f32 v230, v230, v14\n"
    "v_exp_f32 v12, v88\n"
    "v_add_f32 v230, v230, v15\n"
    "v_cvt_pk_bf16_f32 v163, v14, v15\n"
    "v_exp_f32 v13, v89\n"
    "v_exp_f32 v14, v90\n"
    "v_add_f32 v231, v12, v13\n"
    "v_cvt_pk_bf16_f32 v164, v12, v13\n"
    "v_exp_f32 v15, v91\n"
    "v_add_f32 v231, v231, v14\n"
    "v_exp_f32 v12, v92\n"
    "v_add_f32 v231, v231, v15\n"
    "v_cvt_pk_bf16_f32 v165, v14, v15\n"
    "v_exp_f32 v13, v93\n"
    "v_add_f32 v231, v231, v12\n"
    "v_exp_f32 v14, v94\n"
    "v_add_f32 v231, v231, v13\n"
    "v_cvt_pk_bf16_f32 v166, v12, v13\n"
    "v_exp_f32 v15, v95\n"
    "v_add_f32 v231, v231, v14\n"
    "v_add_f32 v231, v231, v15\n"
    "v_cvt_pk_bf16_f32 v167, v14, v15\n"
    "v_max_f32 v248, v230, v231\n"
    "v_cmp_nge_f32 vcc, s70, v248\n"
    "s_nop 1\n"
    "s_branch .Lkrret_20\n"
    ".Lkrare_21:\n"
    "s_nop 7\n"
    "v_max3_f32 v244, v96, v97, v98\n"
    "v_max3_f32 v245, v99, v100, v101\n"
    "v_max3_f32 v244, v244, v102, v103\n"
    "v_max_f32 v244, v244, v245\n"
    "v_mov_b32 v245, v244\n"
    "s_nop 1\n"
    "v_permlane16_swap_b32 v244, v245\n"
    "v_max_f32 v244, v244, v245\n"
    "v_mov_b32 v245, v244\n"
    "s_nop 1\n"
    "v_permlane32_swap_b32 v244, v245\n"
    "v_max_f32 v244, v244, v245\n"
    "v_max_f32 v244, 0, v244\n"
    "v_ceil_f32 v244, v244\n"
    "v_sub_f32 v245, 0, v244\n"
    "v_exp_f32 v245, v245\n"
    "s_nop 0\n"
    "v_mul_f32 v16, v16, v245\n"
    "v_mul_f32 v17, v17, v245\n"
    "v_mul_f32 v18, v18, v245\n"
    "v_mul_f32 v19, v19, v245\n"
    "v_mul_f32 v24, v24, v245\n"
    "v_mul_f32 v25, v25, v245\n"
    "v_mul_f32 v26, v26, v245\n"
    "v_mul_f32 v27, v27, v245\n"
    "v_mul_f32 v32, v32, v245\n"
    "v_mul_f32 v33, v33, v245\n"
    "v_mul_f32 v34, v34, v245\n"
    "v_mul_f32 v35, v35, v245\n"
    "v_mul_f32 v40, v40, v245\n"
    "v_mul_f32 v41, v41, v245\n"
    "v_mul_f32 v42, v42, v245\n"
    "v_mul_f32 v43, v43, v245\n"
    "v_mul_f32 v48, v48, v245\n"
    "v_mul_f32 v49, v49, v245\n"
    "v_mul_f32 v50, v50, v245\n"
    "v_mul_f32 v51, v51, v245\n"
    "v_mul_f32 v56, v56, v245\n"
    "v_mul_f32 v57, v57, v245\n"
    "v_mul_f32 v58, v58, v245\n"
    "v_mul_f32 v59, v59, v245\n"
    "v_mul_f32 v64, v64, v245\n"
    "v_mul_f32 v65, v65, v245\n"
    "v_mul_f32 v66, v66, v245\n"
    "v_mul_f32 v67, v67, v245\n"
    "v_mul_f32 v72, v72, v245\n"
    "v_mul_f32 v73, v73, v245\n"
    "v_mul_f32 v74, v74, v245\n"
    "v_mul_f32 v75, v75, v245\n"
    "v_mul_f32 v228, v228, v245\n"
    "v_sub_f32 v96, v96, v244\n"
    "v_sub_f32 v97, v97, v244\n"
    "v_sub_f32 v98, v98, v244\n"
    "v_sub_f32 v99, v99, v244\n"
    "v_sub_f32 v100, v100, v244\n"
    "v_sub_f32 v101, v101, v244\n"
    "v_sub_f32 v102, v102, v244\n"
    "v_sub_f32 v103, v103, v244\n"
    "v_sub_f32 v80, v80, v244\n"
    "v_sub_f32 v81, v81, v244\n"
    "v_sub_f32 v82, v82, v244\n"
    "v_sub_f32 v83, v83, v244\n"
    "v_sub_f32 v84, v84, v244\n"
    "v_sub_f32 v85, v85, v244\n"
    "v_sub_f32 v86, v86, v244\n"
    "v_sub_f32 v87, v87, v244\n"
    "v_sub_f32 v112, v112, v244\n"
    "v_sub_f32 v113, v113, v244\n"
    "v_sub_f32 v114, v114, v244\n"
    "v_sub_f32 v115, v115, v244\n"
    "v_max3_f32 v244, v104, v105, v106\n"
    "v_max3_f32 v245, v107, v108, v109\n"
    "v_max3_f32 v244, v244, v110, v111\n"
    "v_max_f32 v244, v244, v245\n"
    "v_mov_b32 v245, v244\n"
    "s_nop 1\n"
    "v_permlane16_swap_b32 v244, v245\n"
    "v_max_f32 v244, v244, v245\n"
    "v_mov_b32 v245, v244\n"
    "s_nop 1\n"
    "v_permlane32_swap_b32 v244, v245\n"
    "v_max_f32 v244, v244, v245\n"
    "v_max_f32 v244, 0, v244\n"
    "v_ceil_f32 v244, v244\n"
    "v_sub_f32 v245, 0, v244\n"
    "v_exp_f32 v245, v245\n"
    "s_nop 0\n"
    "v_mul_f32 v20, v20, v245\n"
    "v_mul_f32 v21, v21, v245\n"
    "v_mul_f32 v22, v22, v245\n"
    "v_mul_f32 v23, v23, v245\n"
    "v_mul_f32 v28, v28, v245\n"
    "v_mul_f32 v29, v29, v245\n"
    "v_mul_f32 v30, v30, v245\n"
    "v_mul_f32 v31, v31, v245\n"
    "v_mul_f32 v36, v36, v245\n"
    "v_mul_f32 v37, v37, v245\n"
    "v_mul_f32 v38, v38, v245\n"
    "v_mul_f32 v39, v39, v245\n"
    "v_mul_f32 v44, v44, v245\n"
    "v_mul_f32 v45, v45, v245\n"
    "v_mul_f32 v46, v46, v245\n"
    "v_mul_f32 v47, v47, v245\n"
    "v_mul_f32 v52, v52, v245\n"
    "v_mul_f32 v53, v53, v245\n"
    "v_mul_f32 v54, v54, v245\n"
    "v_mul_f32 v55, v55, v245\n"
    "v_mul_f32 v60, v60, v245\n"
    "v_mul_f32 v61, v61, v245\n"
    "v_mul_f32 v62, v62, v245\n"
    "v_mul_f32 v63, v63, v245\n"
    "v_mul_f32 v68, v68, v245\n"
    "v_mul_f32 v69, v69, v245\n"
    "v_mul_f32 v70, v70, v245\n"
    "v_mul_f32 v71, v71, v245\n"
    "v_mul_f32 v76, v76, v245\n"
    "v_mul_f32 v77, v77, v245\n"
    "v_mul_f32 v78, v78, v245\n"
    "v_mul_f32 v79, v79, v245\n"
    "v_mul_f32 v229, v229, v245\n"
    "v_sub_f32 v104, v104, v244\n"
    "v_sub_f32 v105, v105, v244\n"
    "v_sub_f32 v106, v106, v244\n"
    "v_sub_f32 v107, v107, v244\n"
    "v_sub_f32 v108, v108, v244\n"
    "v_sub_f32 v109, v109, v244\n"
    "v_sub_f32 v110, v110, v244\n"
    "v_sub_f32 v111, v111, v244\n"
    "v_sub_f32 v88, v88, v244\n"
    "v_sub_f32 v89, v89, v244\n"
    "v_sub_f32 v90, v90, v244\n"
    "v_sub_f32 v91, v91, v244\n"
    "v_sub_f32 v92, v92, v244\n"
    "v_sub_f32 v93, v93, v244\n"
    "v_sub_f32 v94, v94, v244\n"
    "v_sub_f32 v95, v95, v244\n"
    "v_sub_f32 v116, v116, v244\n"
    "v_sub_f32 v117, v117, v244\n"
    "v_sub_f32 v118, v118, v244\n"
    "v_sub_f32 v119, v119, v244\n"
    "v_exp_f32 v12, v96\n"
    "v_exp_f32 v13, v97\n"
    "v_exp_f32 v14, v98\n"
    "v_add_f32 v230, v12, v13\n"
    "v_cvt_pk_bf16_f32 v168, v12, v13\n"
    "v_exp_f32 v15, v99\n"
    "v_add_f32 v230, v230, v14\n"
    "v_exp_f32 v12, v100\n"
    "v_add_f32 v230, v230, v15\n"
    "v_cvt_pk_bf16_f32 v169, v14, v15\n"
    "v_exp_f32 v13, v101\n"
    "v_add_f32 v230, v230, v12\n"
    "v_exp_f32 v14, v102\n"
    "v_add_f32 v230, v230, v13\n"
    "v_cvt_pk_bf16_f32 v170, v12, v13\n"
    "v_exp_f32 v15, v103\n"
    "v_add_f32 v230, v230, v14\n"
    "v_exp_f32 v12, v104\n"
    "v_add_f32 v230, v230, v15\n"
    "v_cvt_pk_bf16_f32 v171, v14, v15\n"
    "v_exp_f32 v13, v105\n"
    "v_exp_f32 v14, v106\n"
    "v_add_f32 v231, v12, v13\n"
    "v_cvt_pk_bf16_f32 v172, v12, v13\n"
    "v_exp_f32 v15, v107\n"
    "v_add_f32 v231, v231, v14\n"
    "v_exp_f32 v12, v108\n"
    "v_add_f32 v231, v231, v15\n"
    "v_cvt_pk_bf16_f32 v173, v14, v15\n"
    "v_exp_f32 v13, v109\n"
    "v_add_f32 v231, v231, v12\n"
    "v_exp_f32 v14, v110\n"
    "v_add_f32 v231, v231, v13\n"
    "v_cvt_pk_bf16_f32 v174, v12, v13\n"
    "v_exp_f32 v15, v111\n"
    "v_add_f32 v231, v231, v14\n"
    "v_add_f32 v231, v231, v15\n"
    "v_cvt_pk_bf16_f32 v175, v14, v15\n"
    "v_max_f32 v248, v230, v231\n"
    "v_cmp_nge_f32 vcc, s70, v248\n"
    "s_nop 1\n"
    "s_branch .Lkrret_22\n"
    ".Lkrare_23:\n"
    "s_nop 7\n"
    "v_max3_f32 v244, v80, v81, v82\n"
    "v_max3_f32 v245, v83, v84, v85\n"
    "v_max3_f32 v244, v244, v86, v87\n"
    "v_max_f32 v244, v244, v245\n"
    "v_mov_b32 v245, v244\n"
    "s_nop 1\n"
    "v_permlane16_swap_b32 v244, v245\n"
    "v_max_f32 v244, v244, v245\n"
    "v_mov_b32 v245, v244\n"
    "s_nop 1\n"
    "v_permlane32_swap_b32 v244, v245\n"
    "v_max_f32 v244, v244, v245\n"
    "v_max_f32 v244, 0, v244\n"
    "v_ceil_f32 v244, v244\n"
    "v_sub_f32 v245, 0, v244\n"
    "v_exp_f32 v245, v245\n"
    "s_nop 0\n"
    "v_mul_f32 v16, v16, v245\n"
    "v_mul_f32 v17, v17, v245\n"
    "v_mul_f32 v18, v18, v245\n"
    "v_mul_f32 v19, v19, v245\n"
    "v_mul_f32 v24, v24, v245\n"
    "v_mul_f32 v25, v25, v245\n"
    "v_mul_f32 v26, v26, v245\n"
    "v_mul_f32 v27, v27, v245\n"
    "v_mul_f32 v32, v32, v245\n"
    "v_mul_f32 v33, v33, v245\n"
    "v_mul_f32 v34, v34, v245\n"
    "v_mul_f32 v35, v35, v245\n"
    "v_mul_f32 v40, v40, v245\n"
    "v_mul_f32 v41, v41, v245\n"
    "v_mul_f32 v42, v42, v245\n"
    "v_mul_f32 v43, v43, v245\n"
    "v_mul_f32 v48, v48, v245\n"
    "v_mul_f32 v49, v49, v245\n"
    "v_mul_f32 v50, v50, v245\n"
    "v_mul_f32 v51, v51, v245\n"
    "v_mul_f32 v56, v56, v245\n"
    "v_mul_f32 v57, v57, v245\n"
    "v_mul_f32 v58, v58, v245\n"
    "v_mul_f32 v59, v59, v245\n"
    "v_mul_f32 v64, v64, v245\n"
    "v_mul_f32 v65, v65, v245\n"
    "v_mul_f32 v66, v66, v245\n"
    "v_mul_f32 v67, v67, v245\n"
    "v_mul_f32 v72, v72, v245\n"
    "v_mul_f32 v73, v73, v245\n"
    "v_mul_f32 v74, v74, v245\n"
    "v_mul_f32 v75, v75, v245\n"
    "v_mul_f32 v228, v228, v245\n"
    "v_sub_f32 v80, v80, v244\n"
    "v_sub_f32 v81, v81, v244\n"
    "v_sub_f32 v82, v82, v244\n"
    "v_sub_f32 v83, v83, v244\n"
    "v_sub_f32 v84, v84, v244\n"
    "v_sub_f32 v85, v85, v244\n"
    "v_sub_f32 v86, v86, v244\n"
    "v_sub_f32 v87, v87, v244\n"
    "v_sub_f32 v96, v96, v244\n"
    "v_sub_f32 v97, v97, v244\n"
    "v_sub_f32 v98, v98, v244\n"
    "v_sub_f32 v99, v99, v244\n"
    "v_sub_f32 v100, v100, v244\n"
    "v_sub_f32 v101, v101, v244\n"
    "v_sub_f32 v102, v102, v244\n"
    "v_sub_f32 v103, v103, v244\n"
    "v_sub_f32 v112, v112, v244\n"
    "v_sub_f32 v113, v113, v244\n"
    "v_sub_f32 v114, v114, v244\n"
    "v_sub_f32 v115, v115, v244\n"
    "v_max3_f32 v244, v88, v89, v90\n"
    "v_max3_f32 v245, v91, v92, v93\n"
    "v_max3_f32 v244, v244, v94, v95\n"
    "v_max_f32 v244, v244, v245\n"
    "v_mov_b32 v245, v244\n"
    "s_nop 1\n"
    "v_permlane16_swap_b32 v244, v245\n"
    "v_max_f32 v244, v244, v245\n"
    "v_mov_b32 v245, v244\n"
    "s_nop 1\n"
    "v_permlane32_swap_b32 v244, v245\n"
    "v_max_f32 v244, v244, v245\n"
    "v_max_f32 v244, 0, v244\n"
    "v_ceil_f32 v244, v244\n"
    "v_sub_f32 v245, 0, v244\n"
    "v_exp_f32 v245, v245\n"
    "s_nop 0\n"
    "v_mul_f32 v20, v20, v245\n"
    "v_mul_f32 v21, v21, v245\n"
    "v_mul_f32 v22, v22, v245\n"
    "v_mul_f32 v23, v23, v245\n"
    "v_mul_f32 v28, v28, v245\n"
    "v_mul_f32 v29, v29, v245\n"
    "v_mul_f32 v30, v30, v245\n"
    "v_mul_f32 v31, v31, v245\n"
    "v_mul_f32 v36, v36, v245\n"
    "v_mul_f32 v37, v37, v245\n"
    "v_mul_f32 v38, v38, v245\n"
    "v_mul_f32 v39, v39, v245\n"
    "v_mul_f32 v44, v44, v245\n"
    "v_mul_f32 v45, v45, v245\n"
    "v_mul_f32 v46, v46, v245\n"
    "v_mul_f32 v47, v47, v245\n"
    "v_mul_f32 v52, v52, v245\n"
    "v_mul_f32 v53, v53, v245\n"
    "v_mul_f32 v54, v54, v245\n"
    "v_mul_f32 v55, v55, v245\n"
    "v_mul_f32 v60, v60, v245\n"
    "v_mul_f32 v61, v61, v245\n"
    "v_mul_f32 v62, v62, v245\n"
    "v_mul_f32 v63, v63, v245\n"
    "v_mul_f32 v68, v68, v245\n"
    "v_mul_f32 v69, v69, v245\n"
    "v_mul_f32 v70, v70, v245\n"
    "v_mul_f32 v71, v71, v245\n"
    "v_mul_f32 v76, v76, v245\n"
    "v_mul_f32 v77, v77, v245\n"
    "v_mul_f32 v78, v78, v245\n"
    "v_mul_f32 v79, v79, v245\n"
    "v_mul_f32 v229, v229, v245\n"
    "v_sub_f32 v88, v88, v244\n"
    "v_sub_f32 v89, v89, v244\n"
    "v_sub_f32 v90, v90, v244\n"
    "v_sub_f32 v91, v91, v244\n"
    "v_sub_f32 v92, v92, v244\n"
    "v_sub_f32 v93, v93, v244\n"
    "v_sub_f32 v94, v94, v244\n"
    "v_sub_f32 v95, v95, v244\n"
    "v_sub_f32 v104, v104, v244\n"
    "v_sub_f32 v105, v105, v244\n"
    "v_sub_f32 v106, v106, v244\n"
    "v_sub_f32 v107, v107, v244\n"
    "v_sub_f32 v108, v108, v244\n"
    "v_sub_f32 v109, v109, v244\n"
    "v_sub_f32 v110, v110, v244\n"
    "v_sub_f32 v111, v111, v244\n"
    "v_sub_f32 v116, v116, v244\n"
    "v_sub_f32 v117, v117, v244\n"
    "v_sub_f32 v118, v118, v244\n"
    "v_sub_f32 v119, v119, v244\n"
    "v_exp_f32 v12, v80\n"
    "v_exp_f32 v13, v81\n"
    "v_exp_f32 v14, v82\n"
    "v_add_f32 v230, v12, v13\n"
    "v_cvt_pk_bf16_f32 v160, v12, v13\n"
    "v_exp_f32 v15, v83\n"
    "v_add_f32 v230, v230, v14\n"
    "v_exp_f32 v12, v84\n"
    "v_add_f32 v230, v230, v15\n"
    "v_cvt_pk_bf16_f32 v161, v14, v15\n"
    "v_exp_f32 v13, v85\n"
    "v_add_f32 v230, v230, v12\n"
    "v_exp_f32 v14, v86\n"
    "v_add_f32 v230, v230, v13\n"
    "v_cvt_pk_bf16_f32 v162, v12, v13\n"
    "v_exp_f32 v15, v87\n"
    "v_add_f32 v230, v230, v14\n"
    "v_exp_f32 v12, v88\n"
    "v_add_f32 v230, v230, v15\n"
    "v_cvt_pk_bf16_f32 v163, v14, v15\n"
    "v_exp_f32 v13, v89\n"
    "v_exp_f32 v14, v90\n"
    "v_add_f32 v231, v12, v13\n"
    "v_cvt_pk_bf16_f32 v164, v12, v13\n"
    "v_exp_f32 v15, v91\n"
    "v_add_f32 v231, v231, v14\n"
    "v_exp_f32 v12, v92\n"
    "v_add_f32 v231, v231, v15\n"
    "v_cvt_pk_bf16_f32 v165, v14, v15\n"
    "v_exp_f32 v13, v93\n"
    "v_add_f32 v231, v231, v12\n"
    "v_exp_f32 v14, v94\n"
    "v_add_f32 v231, v231, v13\n"
    "v_cvt_pk_bf16_f32 v166, v12, v13\n"
    "v_exp_f32 v15, v95\n"
    "v_add_f32 v231, v231, v14\n"
    "v_add_f32 v231, v231, v15\n"
    "v_cvt_pk_bf16_f32 v167, v14, v15\n"
    "v_max_f32 v248, v230, v231\n"
    "v_cmp_nge_f32 vcc, s70, v248\n"
    "s_nop 1\n"
    "s_branch .Lkrret_24\n"
    ".Lkrare_25:\n"
    "s_nop 7\n"
    "v_max3_f32 v244, v96, v97, v98\n"
    "v_max3_f32 v245, v99, v100, v101\n"
    "v_max3_f32 v244, v244, v102, v103\n"
    "v_max_f32 v244, v244, v245\n"
    "v_mov_b32 v245, v244\n"
    "s_nop 1\n"
    "v_permlane16_swap_b32 v244, v245\n"
    "v_max_f32 v244, v244, v245\n"
    "v_mov_b32 v245, v244\n"
    "s_nop 1\n"
    "v_permlane32_swap_b32 v244, v245\n"
    "v_max_f32 v244, v244, v245\n"
    "v_max_f32 v244, 0, v244\n"
    "v_ceil_f32 v244, v244\n"
    "v_sub_f32 v245, 0, v244\n"
    "v_exp_f32 v245, v245\n"
    "s_nop 0\n"
    "v_mul_f32 v16, v16, v245\n"
    "v_mul_f32 v17, v17, v245\n"
    "v_mul_f32 v18, v18, v245\n"
    "v_mul_f32 v19, v19, v245\n"
    "v_mul_f32 v24, v24, v245\n"
    "v_mul_f32 v25, v25, v245\n"
    "v_mul_f32 v26, v26, v245\n"
    "v_mul_f32 v27, v27, v245\n"
    "v_mul_f32 v32, v32, v245\n"
    "v_mul_f32 v33, v33, v245\n"
    "v_mul_f32 v34, v34, v245\n"
    "v_mul_f32 v35, v35, v245\n"
    "v_mul_f32 v40, v40, v245\n"
    "v_mul_f32 v41, v41, v245\n"
    "v_mul_f32 v42, v42, v245\n"
    "v_mul_f32 v43, v43, v245\n"
    "v_mul_f32 v48, v48, v245\n"
    "v_mul_f32 v49, v49, v245\n"
    "v_mul_f32 v50, v50, v245\n"
    "v_mul_f32 v51, v51, v245\n"
    "v_mul_f32 v56, v56, v245\n"
    "v_mul_f32 v57, v57, v245\n"
    "v_mul_f32 v58, v58, v245\n"
    "v_mul_f32 v59, v59, v245\n"
    "v_mul_f32 v64, v64, v245\n"
    "v_mul_f32 v65, v65, v245\n"
    "v_mul_f32 v66, v66, v245\n"
    "v_mul_f32 v67, v67, v245\n"
    "v_mul_f32 v72, v72, v245\n"
    "v_mul_f32 v73, v73, v245\n"
    "v_mul_f32 v74, v74, v245\n"
    "v_mul_f32 v75, v75, v245\n"
    "v_mul_f32 v228, v228, v245\n"
    "v_sub_f32 v96, v96, v244\n"
    "v_sub_f32 v97, v97, v244\n"
    "v_sub_f32 v98, v98, v244\n"
    "v_sub_f32 v99, v99, v244\n"
    "v_sub_f32 v100, v100, v244\n"
    "v_sub_f32 v101, v101, v244\n"
    "v_sub_f32 v102, v102, v244\n"
    "v_sub_f32 v103, v103, v244\n"
    "v_max3_f32 v244, v104, v105, v106\n"
    "v_max3_f32 v245, v107, v108, v109\n"
    "v_max3_f32 v244, v244, v110, v111\n"
    "v_max_f32 v244, v244, v245\n"
    "v_mov_b32 v245, v244\n"
    "s_nop 1\n"
    "v_permlane16_swap_b32 v244, v245\n"
    "v_max_f32 v244, v244, v245\n"
    "v_mov_b32 v245, v244\n"
    "s_nop 1\n"
    "v_permlane32_swap_b32 v244, v245\n"
    "v_max_f32 v244, v244, v245\n"
    "v_max_f32 v244, 0, v244\n"
    "v_ceil_f32 v244, v244\n"
    "v_sub_f32 v245, 0, v244\n"
    "v_exp_f32 v245, v245\n"
    "s_nop 0\n"
    "v_mul_f32 v20, v20, v245\n"
    "v_mul_f32 v21, v21, v245\n"
    "v_mul_f32 v22, v22, v245\n"
    "v_mul_f32 v23, v23, v245\n"
    "v_mul_f32 v28, v28, v245\n"
    "v_mul_f32 v29, v29, v245\n"
    "v_mul_f32 v30, v30, v245\n"
    "v_mul_f32 v31, v31, v245\n"
    "v_mul_f32 v36, v36, v245\n"
    "v_mul_f32 v37, v37, v245\n"
    "v_mul_f32 v38, v38, v245\n"
    "v_mul_f32 v39, v39, v245\n"
    "v_mul_f32 v44, v44, v245\n"
    "v_mul_f32 v45, v45, v245\n"
    "v_mul_f32 v46, v46, v245\n"
    "v_mul_f32 v47, v47, v245\n"
    "v_mul_f32 v52, v52, v245\n"
    "v_mul_f32 v53, v53, v245\n"
    "v_mul_f32 v54, v54, v245\n"
    "v_mul_f32 v55, v55, v245\n"
    "v_mul_f32 v60, v60, v245\n"
    "v_mul_f32 v61, v61, v245\n"
    "v_mul_f32 v62, v62, v245\n"
    "v_mul_f32 v63, v63, v245\n"
    "v_mul_f32 v68, v68, v245\n"
    "v_mul_f32 v69, v69, v245\n"
    "v_mul_f32 v70, v70, v245\n"
    "v_mul_f32 v71, v71, v245\n"
    "v_mul_f32 v76, v76, v245\n"
    "v_mul_f32 v77, v77, v245\n"
    "v_mul_f32 v78, v78, v245\n"
    "v_mul_f32 v79, v79, v245\n"
    "v_mul_f32 v229, v229, v245\n"
    "v_sub_f32 v104, v104, v244\n"
    "v_sub_f32 v105, v105, v244\n"
    "v_sub_f32 v106, v106, v244\n"
    "v_sub_f32 v107, v107, v244\n"
    "v_sub_f32 v108, v108, v244\n"
    "v_sub_f32 v109, v109, v244\n"
    "v_sub_f32 v110, v110, v244\n"
    "v_sub_f32 v111, v111, v244\n"
    "v_exp_f32 v12, v96\n"
    "v_exp_f32 v13, v97\n"
    "v_exp_f32 v14, v98\n"
    "v_add_f32 v230, v12, v13\n"
    "v_cvt_pk_bf16_f32 v168, v12, v13\n"
    "v_exp_f32 v15, v99\n"
    "v_add_f32 v230, v230, v14\n"
    "v_exp_f32 v12, v100\n"
    "v_add_f32 v230, v230, v15\n"
    "v_cvt_pk_bf16_f32 v169, v14, v15\n"
    "v_exp_f32 v13, v101\n"
    "v_add_f32 v230, v230, v12\n"
    "v_exp_f32 v14, v102\n"
    "v_add_f32 v230, v230, v13\n"
    "v_cvt_pk_bf16_f32 v170, v12, v13\n"
    "v_exp_f32 v15, v103\n"
    "v_add_f32 v230, v230, v14\n"
    "v_exp_f32 v12, v104\n"
    "v_add_f32 v230, v230, v15\n"
    "v_cvt_pk_bf16_f32 v171, v14, v15\n"
    "v_exp_f32 v13, v105\n"
    "v_exp_f32 v14, v106\n"
    "v_add_f32 v231, v12, v13\n"
    "v_cvt_pk_bf16_f32 v172, v12, v13\n"
    "v_exp_f32 v15, v107\n"
    "v_add_f32 v231, v231, v14\n"
    "v_exp_f32 v12, v108\n"
    "v_add_f32 v231, v231, v15\n"
    "v_cvt_pk_bf16_f32 v173, v14, v15\n"
    "v_exp_f32 v13, v109\n"
    "v_add_f32 v231, v231, v12\n"
    "v_exp_f32 v14, v110\n"
    "v_add_f32 v231, v231, v13\n"
    "v_cvt_pk_bf16_f32 v174, v12, v13\n"
    "v_exp_f32 v15, v111\n"
    "v_add_f32 v231, v231, v14\n"
    "v_add_f32 v231, v231, v15\n"
    "v_cvt_pk_bf16_f32 v175, v14, v15\n"
    "v_max_f32 v248, v230, v231\n"
    "v_cmp_nge_f32 vcc, s70, v248\n"
    "s_nop 1\n"
    "s_branch .Lkrret_26\n"
    ".Lkrare_27:\n"
    "s_nop 7\n"
    "v_max3_f32 v244, v80, v81, v82\n"
    "v_max3_f32 v245, v83, v84, v85\n"
    "v_max3_f32 v244, v244, v86, v87\n"
    "v_max_f32 v244, v244, v245\n"
    "v_mov_b32 v245, v244\n"
    "s_nop 1\n"
    "v_permlane16_swap_b32 v244, v245\n"
    "v_max_f32 v244, v244, v245\n"
    "v_mov_b32 v245, v244\n"
    "s_nop 1\n"
    "v_permlane32_swap_b32 v244, v245\n"
    "v_max_f32 v244, v244, v245\n"
    "v_max_f32 v244, 0, v244\n"
    "v_ceil_f32 v244, v244\n"
    "v_sub_f32 v245, 0, v244\n"
    "v_exp_f32 v245, v245\n"
    "s_nop 0\n"
    "v_mul_f32 v228, v228, v245\n"
    "v_sub_f32 v80, v80, v244\n"
    "v_sub_f32 v81, v81, v244\n"
    "v_sub_f32 v82, v82, v244\n"
    "v_sub_f32 v83, v83, v244\n"
    "v_sub_f32 v84, v84, v244\n"
    "v_sub_f32 v85, v85, v244\n"
    "v_sub_f32 v86, v86, v244\n"
    "v_sub_f32 v87, v87, v244\n"
    "v_sub_f32 v96, v96, v244\n"
    "v_sub_f32 v97, v97, v244\n"
    "v_sub_f32 v98, v98, v244\n"
    "v_sub_f32 v99, v99, v244\n"
    "v_sub_f32 v100, v100, v244\n"
    "v_sub_f32 v101, v101, v244\n"
    "v_sub_f32 v102, v102, v244\n"
    "v_sub_f32 v103, v103, v244\n"
    "v_sub_f32 v112, v112, v244\n"
    "v_sub_f32 v113, v113, v244\n"
    "v_sub_f32 v114, v114, v244\n"
    "v_sub_f32 v115, v115, v244\n"
    "v_max3_f32 v244, v88, v89, v90\n"
    "v_max3_f32 v245, v91, v92, v93\n"
    "v_max3_f32 v244, v244, v94, v95\n"
    "v_max_f32 v244, v244, v245\n"
    "v_mov_b32 v245, v244\n"
    "s_nop 1\n"
    "v_permlane16_swap_b32 v244, v245\n"
    "v_max_f32 v244, v244, v245\n"
    "v_mov_b32 v245, v244\n"
    "s_nop 1\n"
    "v_permlane32_swap_b32 v244, v245\n"
    "v_max_f32 v244, v244, v245\n"
    "v_max_f32 v244, 0, v244\n"
    "v_ceil_f32 v244, v244\n"
    "v_sub_f32 v245, 0, v244\n"
    "v_exp_f32 v245, v245\n"
    "s_nop 0\n"
    "v_mul_f32 v229, v229, v245\n"
    "v_sub_f32 v88, v88, v244\n"
    "v_sub_f32 v89, v89, v244\n"
    "v_sub_f32 v90, v90, v244\n"
    "v_sub_f32 v91, v91, v244\n"
    "v_sub_f32 v92, v92, v244\n"
    "v_sub_f32 v93, v93, v244\n"
    "v_sub_f32 v94, v94, v244\n"
    "v_sub_f32 v95, v95, v244\n"
    "v_sub_f32 v104, v104, v244\n"
    "v_sub_f32 v105, v105, v244\n"
    "v_sub_f32 v106, v106, v244\n"
    "v_sub_f32 v107, v107, v244\n"
    "v_sub_f32 v108, v108, v244\n"
    "v_sub_f32 v109, v109, v244\n"
    "v_sub_f32 v110, v110, v244\n"
    "v_sub_f32 v111, v111, v244\n"
    "v_sub_f32 v116, v116, v244\n"
    "v_sub_f32 v117, v117, v244\n"
    "v_sub_f32 v118, v118, v244\n"
    "v_sub_f32 v119, v119, v244\n"
    "v_exp_f32 v12, v80\n"
    "v_exp_f32 v13, v81\n"
    "v_exp_f32 v14, v82\n"
    "v_add_f32 v230, v12, v13\n"
    "v_cvt_pk_bf16_f32 v160, v12, v13\n"
    "v_exp_f32 v15, v83\n"
    "v_add_f32 v230, v230, v14\n"
    "v_exp_f32 v12, v84\n"
    "v_add_f32 v230, v230, v15\n"
    "v_cvt_pk_bf16_f32 v161, v14, v15\n"
    "v_exp_f32 v13, v85\n"
    "v_add_f32 v230, v230, v12\n"
    "v_exp_f32 v14, v86\n"
    "v_add_f32 v230, v230, v13\n"
    "v_cvt_pk_bf16_f32 v162, v12, v13\n"
    "v_exp_f32 v15, v87\n"
    "v_add_f32 v230, v230, v14\n"
    "v_exp_f32 v12, v88\n"
    "v_add_f32 v230, v230, v15\n"
    "v_cvt_pk_bf16_f32 v163, v14, v15\n"
    "v_exp_f32 v13, v89\n"
    "v_exp_f32 v14, v90\n"
    "v_add_f32 v231, v12, v13\n"
    "v_cvt_pk_bf16_f32 v164, v12, v13\n"
    "v_exp_f32 v15, v91\n"
    "v_add_f32 v231, v231, v14\n"
    "v_exp_f32 v12, v92\n"
    "v_add_f32 v231, v231, v15\n"
    "v_cvt_pk_bf16_f32 v165, v14, v15\n"
    "v_exp_f32 v13, v93\n"
    "v_add_f32 v231, v231, v12\n"
    "v_exp_f32 v14, v94\n"
    "v_add_f32 v231, v231, v13\n"
    "v_cvt_pk_bf16_f32 v166, v12, v13\n"
    "v_exp_f32 v15, v95\n"
    "v_add_f32 v231, v231, v14\n"
    "v_add_f32 v231, v231, v15\n"
    "v_cvt_pk_bf16_f32 v167, v14, v15\n"
    "v_max_f32 v248, v230, v231\n"
    "v_cmp_nge_f32 vcc, s70, v248\n"
    "s_nop 1\n"
    "s_branch .Lkrret_28\n"
    ".Lkrare_29:\n"
    "s_nop 7\n"
    "v_max3_f32 v244, v96, v97, v98\n"
    "v_max3_f32 v245, v99, v100, v101\n"
    "v_max3_f32 v244, v244, v102, v103\n"
    "v_max_f32 v244, v244, v245\n"
    "v_mov_b32 v245, v244\n"
    "s_nop 1\n"
    "v_permlane16_swap_b32 v244, v245\n"
    "v_max_f32 v244, v244, v245\n"
    "v_mov_b32 v245, v244\n"
    "s_nop 1\n"
    "v_permlane32_swap_b32 v244, v245\n"
    "v_max_f32 v244, v244, v245\n"
    "v_max_f32 v244, 0, v244\n"
    "v_ceil_f32 v244, v244\n"
    "v_sub_f32 v245, 0, v244\n"
    "v_exp_f32 v245, v245\n"
    "s_nop 0\n"
    "v_mul_f32 v16, v16, v245\n"
    "v_mul_f32 v17, v17, v245\n"
    "v_mul_f32 v18, v18, v245\n"
    "v_mul_f32 v19, v19, v245\n"
    "v_mul_f32 v24, v24, v245\n"
    "v_mul_f32 v25, v25, v245\n"
    "v_mul_f32 v26, v26, v245\n"
    "v_mul_f32 v27, v27, v245\n"
    "v_mul_f32 v32, v32, v245\n"
    "v_mul_f32 v33, v33, v245\n"
    "v_mul_f32 v34, v34, v245\n"
    "v_mul_f32 v35, v35, v245\n"
    "v_mul_f32 v40, v40, v245\n"
    "v_mul_f32 v41, v41, v245\n"
    "v_mul_f32 v42, v42, v245\n"
    "v_mul_f32 v43, v43, v245\n"
    "v_mul_f32 v48, v48, v245\n"
    "v_mul_f32 v49, v49, v245\n"
    "v_mul_f32 v50, v50, v245\n"
    "v_mul_f32 v51, v51, v245\n"
    "v_mul_f32 v56, v56, v245\n"
    "v_mul_f32 v57, v57, v245\n"
    "v_mul_f32 v58, v58, v245\n"
    "v_mul_f32 v59, v59, v245\n"
    "v_mul_f32 v64, v64, v245\n"
    "v_mul_f32 v65, v65, v245\n"
    "v_mul_f32 v66, v66, v245\n"
    "v_mul_f32 v67, v67, v245\n"
    "v_mul_f32 v72, v72, v245\n"
    "v_mul_f32 v73, v73, v245\n"
    "v_mul_f32 v74, v74, v245\n"
    "v_mul_f32 v75, v75, v245\n"
    "v_mul_f32 v228, v228, v245\n"
    "v_sub_f32 v96, v96, v244\n"
    "v_sub_f32 v97, v97, v244\n"
    "v_sub_f32 v98, v98, v244\n"
    "v_sub_f32 v99, v99, v244\n"
    "v_sub_f32 v100, v100, v244\n"
    "v_sub_f32 v101, v101, v244\n"
    "v_sub_f32 v102, v102, v244\n"
    "v_sub_f32 v103, v103, v244\n"
    "v_sub_f32 v80, v80, v244\n"
    "v_sub_f32 v81, v81, v244\n"
    "v_sub_f32 v82, v82, v244\n"
    "v_sub_f32 v83, v83, v244\n"
    "v_sub_f32 v84, v84, v244\n"
    "v_sub_f32 v85, v85, v244\n"
    "v_sub_f32 v86, v86, v244\n"
    "v_sub_f32 v87, v87, v244\n"
    "v_sub_f32 v112, v112, v244\n"
    "v_sub_f32 v113, v113, v244\n"
    "v_sub_f32 v114, v114, v244\n"
    "v_sub_f32 v115, v115, v244\n"
    "v_max3_f32 v244, v104, v105, v106\n"
    "v_max3_f32 v245, v107, v108, v109\n"
    "v_max3_f32 v244, v244, v110, v111\n"
    "v_max_f32 v244, v244, v245\n"
    "v_mov_b32 v245, v244\n"
    "s_nop 1\n"
    "v_permlane16_swap_b32 v244, v245\n"
    "v_max_f32 v244, v244, v245\n"
    "v_mov_b32 v245, v244\n"
    "s_nop 1\n"
    "v_permlane32_swap_b32 v244, v245\n"
    "v_max_f32 v244, v244, v245\n"
    "v_max_f32 v244, 0, v244\n"
    "v_ceil_f32 v244, v244\n"
    "v_sub_f32 v245, 0, v244\n"
    "v_exp_f32 v245, v245\n"
    "s_nop 0\n"
    "v_mul_f32 v20, v20, v245\n"
    "v_mul_f32 v21, v21, v245\n"
    "v_mul_f32 v22, v22, v245\n"
    "v_mul_f32 v23, v23, v245\n"
    "v_mul_f32 v28, v28, v245\n"
    "v_mul_f32 v29, v29, v245\n"
    "v_mul_f32 v30, v30, v245\n"
    "v_mul_f32 v31, v31, v245\n"
    "v_mul_f32 v36, v36, v245\n"
    "v_mul_f32 v37, v37, v245\n"
    "v_mul_f32 v38, v38, v245\n"
    "v_mul_f32 v39, v39, v245\n"
    "v_mul_f32 v44, v44, v245\n"
    "v_mul_f32 v45, v45, v245\n"
    "v_mul_f32 v46, v46, v245\n"
    "v_mul_f32 v47, v47, v245\n"
    "v_mul_f32 v52, v52, v245\n"
    "v_mul_f32 v53, v53, v245\n"
    "v_mul_f32 v54, v54, v245\n"
    "v_mul_f32 v55, v55, v245\n"
    "v_mul_f32 v60, v60, v245\n"
    "v_mul_f32 v61, v61, v245\n"
    "v_mul_f32 v62, v62, v245\n"
    "v_mul_f32 v63, v63, v245\n"
    "v_mul_f32 v68, v68, v245\n"
    "v_mul_f32 v69, v69, v245\n"
    "v_mul_f32 v70, v70, v245\n"
    "v_mul_f32 v71, v71, v245\n"
    "v_mul_f32 v76, v76, v245\n"
    "v_mul_f32 v77, v77, v245\n"
    "v_mul_f32 v78, v78, v245\n"
    "v_mul_f32 v79, v79, v245\n"
    "v_mul_f32 v229, v229, v245\n"
    "v_sub_f32 v104, v104, v244\n"
    "v_sub_f32 v105, v105, v244\n"
    "v_sub_f32 v106, v106, v244\n"
    "v_sub_f32 v107, v107, v244\n"
    "v_sub_f32 v108, v108, v244\n"
    "v_sub_f32 v109, v109, v244\n"
    "v_sub_f32 v110, v110, v244\n"
    "v_sub_f32 v111, v111, v244\n"
    "v_sub_f32 v88, v88, v244\n"
    "v_sub_f32 v89, v89, v244\n"
    "v_sub_f32 v90, v90, v244\n"
    "v_sub_f32 v91, v91, v244\n"
    "v_sub_f32 v92, v92, v244\n"
    "v_sub_f32 v93, v93, v244\n"
    "v_sub_f32 v94, v94, v244\n"
    "v_sub_f32 v95, v95, v244\n"
    "v_sub_f32 v116, v116, v244\n"
    "v_sub_f32 v117, v117, v244\n"
    "v_sub_f32 v118, v118, v244\n"
    "v_sub_f32 v119, v119, v244\n"
    "v_exp_f32 v12, v96\n"
    "v_exp_f32 v13, v97\n"
    "v_exp_f32 v14, v98\n"
    "v_add_f32 v230, v12, v13\n"
    "v_cvt_pk_bf16_f32 v168, v12, v13\n"
    "v_exp_f32 v15, v99\n"
    "v_add_f32 v230, v230, v14\n"
    "v_exp_f32 v12, v100\n"
    "v_add_f32 v230, v230, v15\n"
    "v_cvt_pk_bf16_f32 v169, v14, v15\n"
    "v_exp_f32 v13, v101\n"
    "v_add_f32 v230, v230, v12\n"
    "v_exp_f32 v14, v102\n"
    "v_add_f32 v230, v230, v13\n"
    "v_cvt_pk_bf16_f32 v170, v12, v13\n"
    "v_exp_f32 v15, v103\n"
    "v_add_f32 v230, v230, v14\n"
    "v_exp_f32 v12, v104\n"
    "v_add_f32 v230, v230, v15\n"
    "v_cvt_pk_bf16_f32 v171, v14, v15\n"
    "v_exp_f32 v13, v105\n"
    "v_exp_f32 v14, v106\n"
    "v_add_f32 v231, v12, v13\n"
    "v_cvt_pk_bf16_f32 v172, v12, v13\n"
    "v_exp_f32 v15, v107\n"
    "v_add_f32 v231, v231, v14\n"
    "v_exp_f32 v12, v108\n"
    "v_add_f32 v231, v231, v15\n"
    "v_cvt_pk_bf16_f32 v173, v14, v15\n"
    "v_exp_f32 v13, v109\n"
    "v_add_f32 v231, v231, v12\n"
    "v_exp_f32 v14, v110\n"
    "v_add_f32 v231, v231, v13\n"
    "v_cvt_pk_bf16_f32 v174, v12, v13\n"
    "v_exp_f32 v15, v111\n"
    "v_add_f32 v231, v231, v14\n"
    "v_add_f32 v231, v231, v15\n"
    "v_cvt_pk_bf16_f32 v175, v14, v15\n"
    "v_max_f32 v248, v230, v231\n"
    "v_cmp_nge_f32 vcc, s70, v248\n"
    "s_nop 1\n"
    "s_branch .Lkrret_30\n"
    ".Lkrare_31:\n"
    "s_nop 7\n"
    "v_max3_f32 v244, v80, v81, v82\n"
    "v_max3_f32 v245, v83, v84, v85\n"
    "v_max3_f32 v244, v244, v86, v87\n"
    "v_max_f32 v244, v244, v245\n"
    "v_mov_b32 v245, v244\n"
    "s_nop 1\n"
    "v_permlane16_swap_b32 v244, v245\n"
    "v_max_f32 v244, v244, v245\n"
    "v_mov_b32 v245, v244\n"
    "s_nop 1\n"
    "v_permlane32_swap_b32 v244, v245\n"
    "v_max_f32 v244, v244, v245\n"
    "v_max_f32 v244, 0, v244\n"
    "v_ceil_f32 v244, v244\n"
    "v_sub_f32 v245, 0, v244\n"
    "v_exp_f32 v245, v245\n"
    "s_nop 0\n"
    "v_mul_f32 v16, v16, v245\n"
    "v_mul_f32 v17, v17, v245\n"
    "v_mul_f32 v18, v18, v245\n"
    "v_mul_f32 v19, v19, v245\n"
    "v_mul_f32 v24, v24, v245\n"
    "v_mul_f32 v25, v25, v245\n"
    "v_mul_f32 v26, v26, v245\n"
    "v_mul_f32 v27, v27, v245\n"
    "v_mul_f32 v32, v32, v245\n"
    "v_mul_f32 v33, v33, v245\n"
    "v_mul_f32 v34, v34, v245\n"
    "v_mul_f32 v35, v35, v245\n"
    "v_mul_f32 v40, v40, v245\n"
    "v_mul_f32 v41, v41, v245\n"
    "v_mul_f32 v42, v42, v245\n"
    "v_mul_f32 v43, v43, v245\n"
    "v_mul_f32 v48, v48, v245\n"
    "v_mul_f32 v49, v49, v245\n"
    "v_mul_f32 v50, v50, v245\n"
    "v_mul_f32 v51, v51, v245\n"
    "v_mul_f32 v56, v56, v245\n"
    "v_mul_f32 v57, v57, v245\n"
    "v_mul_f32 v58, v58, v245\n"
    "v_mul_f32 v59, v59, v245\n"
    "v_mul_f32 v64, v64, v245\n"
    "v_mul_f32 v65, v65, v245\n"
    "v_mul_f32 v66, v66, v245\n"
    "v_mul_f32 v67, v67, v245\n"
    "v_mul_f32 v72, v72, v245\n"
    "v_mul_f32 v73, v73, v245\n"
    "v_mul_f32 v74, v74, v245\n"
    "v_mul_f32 v75, v75, v245\n"
    "v_mul_f32 v228, v228, v245\n"
    "v_sub_f32 v80, v80, v244\n"
    "v_sub_f32 v81, v81, v244\n"
    "v_sub_f32 v82, v82, v244\n"
    "v_sub_f32 v83, v83, v244\n"
    "v_sub_f32 v84, v84, v244\n"
    "v_sub_f32 v85, v85, v244\n"
    "v_sub_f32 v86, v86, v244\n"
    "v_sub_f32 v87, v87, v244\n"
    "v_sub_f32 v96, v96, v244\n"
    "v_sub_f32 v97, v97, v244\n"
    "v_sub_f32 v98, v98, v244\n"
    "v_sub_f32 v99, v99, v244\n"
    "v_sub_f32 v100, v100, v244\n"
    "v_sub_f32 v101, v101, v244\n"
    "v_sub_f32 v102, v102, v244\n"
    "v_sub_f32 v103, v103, v244\n"
    "v_sub_f32 v112, v112, v244\n"
    "v_sub_f32 v113, v113, v244\n"
    "v_sub_f32 v114, v114, v244\n"
    "v_sub_f32 v115, v115, v244\n"
    "v_max3_f32 v244, v88, v89, v90\n"
    "v_max3_f32 v245, v91, v92, v93\n"
    "v_max3_f32 v244, v244, v94, v95\n"
    "v_max_f32 v244, v244, v245\n"
    "v_mov_b32 v245, v244\n"
    "s_nop 1\n"
    "v_permlane16_swap_b32 v244, v245\n"
    "v_max_f32 v244, v244, v245\n"
    "v_mov_b32 v245, v244\n"
    "s_nop 1\n"
    "v_permlane32_swap_b32 v244, v245\n"
    "v_max_f32 v244, v244, v245\n"
    "v_max_f32 v244, 0, v244\n"
    "v_ceil_f32 v244, v244\n"
    "v_sub_f32 v245, 0, v244\n"
    "v_exp_f32 v245, v245\n"
    "s_nop 0\n"
    "v_mul_f32 v20, v20, v245\n"
    "v_mul_f32 v21, v21, v245\n"
    "v_mul_f32 v22, v22, v245\n"
    "v_mul_f32 v23, v23, v245\n"
    "v_mul_f32 v28, v28, v245\n"
    "v_mul_f32 v29, v29, v245\n"
    "v_mul_f32 v30, v30, v245\n"
    "v_mul_f32 v31, v31, v245\n"
    "v_mul_f32 v36, v36, v245\n"
    "v_mul_f32 v37, v37, v245\n"
    "v_mul_f32 v38, v38, v245\n"
    "v_mul_f32 v39, v39, v245\n"
    "v_mul_f32 v44, v44, v245\n"
    "v_mul_f32 v45, v45, v245\n"
    "v_mul_f32 v46, v46, v245\n"
    "v_mul_f32 v47, v47, v245\n"
    "v_mul_f32 v52, v52, v245\n"
    "v_mul_f32 v53, v53, v245\n"
    "v_mul_f32 v54, v54, v245\n"
    "v_mul_f32 v55, v55, v245\n"
    "v_mul_f32 v60, v60, v245\n"
    "v_mul_f32 v61, v61, v245\n"
    "v_mul_f32 v62, v62, v245\n"
    "v_mul_f32 v63, v63, v245\n"
    "v_mul_f32 v68, v68, v245\n"
    "v_mul_f32 v69, v69, v245\n"
    "v_mul_f32 v70, v70, v245\n"
    "v_mul_f32 v71, v71, v245\n"
    "v_mul_f32 v76, v76, v245\n"
    "v_mul_f32 v77, v77, v245\n"
    "v_mul_f32 v78, v78, v245\n"
    "v_mul_f32 v79, v79, v245\n"
    "v_mul_f32 v229, v229, v245\n"
    "v_sub_f32 v88, v88, v244\n"
    "v_sub_f32 v89, v89, v244\n"
    "v_sub_f32 v90, v90, v244\n"
    "v_sub_f32 v91, v91, v244\n"
    "v_sub_f32 v92, v92, v244\n"
    "v_sub_f32 v93, v93, v244\n"
    "v_sub_f32 v94, v94, v244\n"
    "v_sub_f32 v95, v95, v244\n"
    "v_sub_f32 v104, v104, v244\n"
    "v_sub_f32 v105, v105, v244\n"
    "v_sub_f32 v106, v106, v244\n"
    "v_sub_f32 v107, v107, v244\n"
    "v_sub_f32 v108, v108, v244\n"
    "v_sub_f32 v109, v109, v244\n"
    "v_sub_f32 v110, v110, v244\n"
    "v_sub_f32 v111, v111, v244\n"
    "v_sub_f32 v116, v116, v244\n"
    "v_sub_f32 v117, v117, v244\n"
    "v_sub_f32 v118, v118, v244\n"
    "v_sub_f32 v119, v119, v244\n"
    "v_exp_f32 v12, v80\n"
    "v_exp_f32 v13, v81\n"
    "v_exp_f32 v14, v82\n"
    "v_add_f32 v230, v12, v13\n"
    "v_cvt_pk_bf16_f32 v160, v12, v13\n"
    "v_exp_f32 v15, v83\n"
    "v_add_f32 v230, v230, v14\n"
    "v_exp_f32 v12, v84\n"
    "v_add_f32 v230, v230, v15\n"
    "v_cvt_pk_bf16_f32 v161, v14, v15\n"
    "v_exp_f32 v13, v85\n"
    "v_add_f32 v230, v230, v12\n"
    "v_exp_f32 v14, v86\n"
    "v_add_f32 v230, v230, v13\n"
    "v_cvt_pk_bf16_f32 v162, v12, v13\n"
    "v_exp_f32 v15, v87\n"
    "v_add_f32 v230, v230, v14\n"
    "v_exp_f32 v12, v88\n"
    "v_add_f32 v230, v230, v15\n"
    "v_cvt_pk_bf16_f32 v163, v14, v15\n"
    "v_exp_f32 v13, v89\n"
    "v_exp_f32 v14, v90\n"
    "v_add_f32 v231, v12, v13\n"
    "v_cvt_pk_bf16_f32 v164, v12, v13\n"
    "v_exp_f32 v15, v91\n"
    "v_add_f32 v231, v231, v14\n"
    "v_exp_f32 v12, v92\n"
    "v_add_f32 v231, v231, v15\n"
    "v_cvt_pk_bf16_f32 v165, v14, v15\n"
    "v_exp_f32 v13, v93\n"
    "v_add_f32 v231, v231, v12\n"
    "v_exp_f32 v14, v94\n"
    "v_add_f32 v231, v231, v13\n"
    "v_cvt_pk_bf16_f32 v166, v12, v13\n"
    "v_exp_f32 v15, v95\n"
    "v_add_f32 v231, v231, v14\n"
    "v_add_f32 v231, v231, v15\n"
    "v_cvt_pk_bf16_f32 v167, v14, v15\n"
    "v_max_f32 v248, v230, v231\n"
    "v_cmp_nge_f32 vcc, s70, v248\n"
    "s_nop 1\n"
    "s_branch .Lkrret_32\n"
    ".Lkrare_35:\n"
    "s_nop 7\n"
    "v_max3_f32 v244, v96, v97, v98\n"
    "v_max3_f32 v245, v99, v100, v101\n"
    "v_max3_f32 v244, v244, v102, v103\n"
    "v_max_f32 v244, v244, v245\n"
    "v_mov_b32 v245, v244\n"
    "s_nop 1\n"
    "v_permlane16_swap_b32 v244, v245\n"
    "v_max_f32 v244, v244, v245\n"
    "v_mov_b32 v245, v244\n"
    "s_nop 1\n"
    "v_permlane32_swap_b32 v244, v245\n"
    "v_max_f32 v244, v244, v245\n"
    "v_max_f32 v244, 0, v244\n"
    "v_ceil_f32 v244, v244\n"
    "v_sub_f32 v245, 0, v244\n"
    "v_exp_f32 v245, v245\n"
    "s_nop 0\n"
    "v_mul_f32 v16, v16, v245\n"
    "v_mul_f32 v17, v17, v245\n"
    "v_mul_f32 v18, v18, v245\n"
    "v_mul_f32 v19, v19, v245\n"
    "v_mul_f32 v24, v24, v245\n"
    "v_mul_f32 v25, v25, v245\n"
    "v_mul_f32 v26, v26, v245\n"
    "v_mul_f32 v27, v27, v245\n"
    "v_mul_f32 v32, v32, v245\n"
    "v_mul_f32 v33, v33, v245\n"
    "v_mul_f32 v34, v34, v245\n"
    "v_mul_f32 v35, v35, v245\n"
    "v_mul_f32 v40, v40, v245\n"
    "v_mul_f32 v41, v41, v245\n"
    "v_mul_f32 v42, v42, v245\n"
    "v_mul_f32 v43, v43, v245\n"
    "v_mul_f32 v48, v48, v245\n"
    "v_mul_f32 v49, v49, v245\n"
    "v_mul_f32 v50, v50, v245\n"
    "v_mul_f32 v51, v51, v245\n"
    "v_mul_f32 v56, v56, v245\n"
    "v_mul_f32 v57, v57, v245\n"
    "v_mul_f32 v58, v58, v245\n"
    "v_mul_f32 v59, v59, v245\n"
    "v_mul_f32 v64, v64, v245\n"
    "v_mul_f32 v65, v65, v245\n"
    "v_mul_f32 v66, v66, v245\n"
    "v_mul_f32 v67, v67, v245\n"
    "v_mul_f32 v72, v72, v245\n"
    "v_mul_f32 v73, v73, v245\n"
    "v_mul_f32 v74, v74, v245\n"
    "v_mul_f32 v75, v75, v245\n"
    "v_mul_f32 v228, v228, v245\n"
    "v_sub_f32 v96, v96, v244\n"
    "v_sub_f32 v97, v97, v244\n"
    "v_sub_f32 v98, v98, v244\n"
    "v_sub_f32 v99, v99, v244\n"
    "v_sub_f32 v100, v100, v244\n"
    "v_sub_f32 v101, v101, v244\n"
    "v_sub_f32 v102, v102, v244\n"
    "v_sub_f32 v103, v103, v244\n"
    "v_sub_f32 v80, v80, v244\n"
    "v_sub_f32 v81, v81, v244\n"
    "v_sub_f32 v82, v82, v244\n"
    "v_sub_f32 v83, v83, v244\n"
    "v_sub_f32 v84, v84, v244\n"
    "v_sub_f32 v85, v85, v244\n"
    "v_sub_f32 v86, v86, v244\n"
    "v_sub_f32 v87, v87, v244\n"
    "v_sub_f32 v112, v112, v244\n"
    "v_sub_f32 v113, v113, v244\n"
    "v_sub_f32 v114, v114, v244\n"
    "v_sub_f32 v115, v115, v244\n"
    "v_max3_f32 v244, v104, v105, v106\n"
    "v_max3_f32 v245, v107, v108, v109\n"
    "v_max3_f32 v244, v244, v110, v111\n"
    "v_max_f32 v244, v244, v245\n"
    "v_mov_b32 v245, v244\n"
    "s_nop 1\n"
    "v_permlane16_swap_b32 v244, v245\n"
    "v_max_f32 v244, v244, v245\n"
    "v_mov_b32 v245, v244\n"
    "s_nop 1\n"
    "v_permlane32_swap_b32 v244, v245\n"
    "v_max_f32 v244, v244, v245\n"
    "v_max_f32 v244, 0, v244\n"
    "v_ceil_f32 v244, v244\n"
    "v_sub_f32 v245, 0, v244\n"
    "v_exp_f32 v245, v245\n"
    "s_nop 0\n"
    "v_mul_f32 v20, v20, v245\n"
    "v_mul_f32 v21, v21, v245\n"
    "v_mul_f32 v22, v22, v245\n"
    "v_mul_f32 v23, v23, v245\n"
    "v_mul_f32 v28, v28, v245\n"
    "v_mul_f32 v29, v29, v245\n"
    "v_mul_f32 v30, v30, v245\n"
    "v_mul_f32 v31, v31, v245\n"
    "v_mul_f32 v36, v36, v245\n"
    "v_mul_f32 v37, v37, v245\n"
    "v_mul_f32 v38, v38, v245\n"
    "v_mul_f32 v39, v39, v245\n"
    "v_mul_f32 v44, v44, v245\n"
    "v_mul_f32 v45, v45, v245\n"
    "v_mul_f32 v46, v46, v245\n"
    "v_mul_f32 v47, v47, v245\n"
    "v_mul_f32 v52, v52, v245\n"
    "v_mul_f32 v53, v53, v245\n"
    "v_mul_f32 v54, v54, v245\n"
    "v_mul_f32 v55, v55, v245\n"
    "v_mul_f32 v60, v60, v245\n"
    "v_mul_f32 v61, v61, v245\n"
    "v_mul_f32 v62, v62, v245\n"
    "v_mul_f32 v63, v63, v245\n"
    "v_mul_f32 v68, v68, v245\n"
    "v_mul_f32 v69, v69, v245\n"
    "v_mul_f32 v70, v70, v245\n"
    "v_mul_f32 v71, v71, v245\n"
    "v_mul_f32 v76, v76, v245\n"
    "v_mul_f32 v77, v77, v245\n"
    "v_mul_f32 v78, v78, v245\n"
    "v_mul_f32 v79, v79, v245\n"
    "v_mul_f32 v229, v229, v245\n"
    "v_sub_f32 v104, v104, v244\n"
    "v_sub_f32 v105, v105, v244\n"
    "v_sub_f32 v106, v106, v244\n"
    "v_sub_f32 v107, v107, v244\n"
    "v_sub_f32 v108, v108, v244\n"
    "v_sub_f32 v109, v109, v244\n"
    "v_sub_f32 v110, v110, v244\n"
    "v_sub_f32 v111, v111, v244\n"
    "v_sub_f32 v88, v88, v244\n"
    "v_sub_f32 v89, v89, v244\n"
    "v_sub_f32 v90, v90, v244\n"
    "v_sub_f32 v91, v91, v244\n"
    "v_sub_f32 v92, v92, v244\n"
    "v_sub_f32 v93, v93, v244\n"
    "v_sub_f32 v94, v94, v244\n"
    "v_sub_f32 v95, v95, v244\n"
    "v_sub_f32 v116, v116, v244\n"
    "v_sub_f32 v117, v117, v244\n"
    "v_sub_f32 v118, v118, v244\n"
    "v_sub_f32 v119, v119, v244\n"
    "v_exp_f32 v12, v96\n"
    "v_exp_f32 v13, v97\n"
    "v_exp_f32 v14, v98\n"
    "v_add_f32 v230, v12, v13\n"
    "v_cvt_pk_bf16_f32 v168, v12, v13\n"
    "v_exp_f32 v15, v99\n"
    "v_add_f32 v230, v230, v14\n"
    "v_exp_f32 v12, v100\n"
    "v_add_f32 v230, v230, v15\n"
    "v_cvt_pk_bf16_f32 v169, v14, v15\n"
    "v_exp_f32 v13, v101\n"
    "v_add_f32 v230, v230, v12\n"
    "v_exp_f32 v14, v102\n"
    "v_add_f32 v230, v230, v13\n"
    "v_cvt_pk_bf16_f32 v170, v12, v13\n"
    "v_exp_f32 v15, v103\n"
    "v_add_f32 v230, v230, v14\n"
    "v_exp_f32 v12, v104\n"
    "v_add_f32 v230, v230, v15\n"
    "v_cvt_pk_bf16_f32 v171, v14, v15\n"
    "v_exp_f32 v13, v105\n"
    "v_exp_f32 v14, v106\n"
    "v_add_f32 v231, v12, v13\n"
    "v_cvt_pk_bf16_f32 v172, v12, v13\n"
    "v_exp_f32 v15, v107\n"
    "v_add_f32 v231, v231, v14\n"
    "v_exp_f32 v12, v108\n"
    "v_add_f32 v231, v231, v15\n"
    "v_cvt_pk_bf16_f32 v173, v14, v15\n"
    "v_exp_f32 v13, v109\n"
    "v_add_f32 v231, v231, v12\n"
    "v_exp_f32 v14, v110\n"
    "v_add_f32 v231, v231, v13\n"
    "v_cvt_pk_bf16_f32 v174, v12, v13\n"
    "v_exp_f32 v15, v111\n"
    "v_add_f32 v231, v231, v14\n"
    "v_add_f32 v231, v231, v15\n"
    "v_cvt_pk_bf16_f32 v175, v14, v15\n"
    "v_max_f32 v248, v230, v231\n"
    "v_cmp_nge_f32 vcc, s70, v248\n"
    "s_nop 1\n"
    "s_branch .Lkrret_36\n"
    ".Lkrare_37:\n"
    "s_nop 7\n"
    "v_max3_f32 v244, v80, v81, v82\n"
    "v_max3_f32 v245, v83, v84, v85\n"
    "v_max3_f32 v244, v244, v86, v87\n"
    "v_max_f32 v244, v244, v245\n"
    "v_mov_b32 v245, v244\n"
    "s_nop 1\n"
    "v_permlane16_swap_b32 v244, v245\n"
    "v_max_f32 v244, v244, v245\n"
    "v_mov_b32 v245, v244\n"
    "s_nop 1\n"
    "v_permlane32_swap_b32 v244, v245\n"
    "v_max_f32 v244, v244, v245\n"
    "v_max_f32 v244, 0, v244\n"
    "v_ceil_f32 v244, v244\n"
    "v_sub_f32 v245, 0, v244\n"
    "v_exp_f32 v245, v245\n"
    "s_nop 0\n"
    "v_mul_f32 v16, v16, v245\n"
    "v_mul_f32 v17, v17, v245\n"
    "v_mul_f32 v18, v18, v245\n"
    "v_mul_f32 v19, v19, v245\n"
    "v_mul_f32 v24, v24, v245\n"
    "v_mul_f32 v25, v25, v245\n"
    "v_mul_f32 v26, v26, v245\n"
    "v_mul_f32 v27, v27, v245\n"
    "v_mul_f32 v32, v32, v245\n"
    "v_mul_f32 v33, v33, v245\n"
    "v_mul_f32 v34, v34, v245\n"
    "v_mul_f32 v35, v35, v245\n"
    "v_mul_f32 v40, v40, v245\n"
    "v_mul_f32 v41, v41, v245\n"
    "v_mul_f32 v42, v42, v245\n"
    "v_mul_f32 v43, v43, v245\n"
    "v_mul_f32 v48, v48, v245\n"
    "v_mul_f32 v49, v49, v245\n"
    "v_mul_f32 v50, v50, v245\n"
    "v_mul_f32 v51, v51, v245\n"
    "v_mul_f32 v56, v56, v245\n"
    "v_mul_f32 v57, v57, v245\n"
    "v_mul_f32 v58, v58, v245\n"
    "v_mul_f32 v59, v59, v245\n"
    "v_mul_f32 v64, v64, v245\n"
    "v_mul_f32 v65, v65, v245\n"
    "v_mul_f32 v66, v66, v245\n"
    "v_mul_f32 v67, v67, v245\n"
    "v_mul_f32 v72, v72, v245\n"
    "v_mul_f32 v73, v73, v245\n"
    "v_mul_f32 v74, v74, v245\n"
    "v_mul_f32 v75, v75, v245\n"
    "v_mul_f32 v228, v228, v245\n"
    "v_sub_f32 v80, v80, v244\n"
    "v_sub_f32 v81, v81, v244\n"
    "v_sub_f32 v82, v82, v244\n"
    "v_sub_f32 v83, v83, v244\n"
    "v_sub_f32 v84, v84, v244\n"
    "v_sub_f32 v85, v85, v244\n"
    "v_sub_f32 v86, v86, v244\n"
    "v_sub_f32 v87, v87, v244\n"
    "v_sub_f32 v96, v96, v244\n"
    "v_sub_f32 v97, v97, v244\n"
    "v_sub_f32 v98, v98, v244\n"
    "v_sub_f32 v99, v99, v244\n"
    "v_sub_f32 v100, v100, v244\n"
    "v_sub_f32 v101, v101, v244\n"
    "v_sub_f32 v102, v102, v244\n"
    "v_sub_f32 v103, v103, v244\n"
    "v_sub_f32 v112, v112, v244\n"
    "v_sub_f32 v113, v113, v244\n"
    "v_sub_f32 v114, v114, v244\n"
    "v_sub_f32 v115, v115, v244\n"
    "v_max3_f32 v244, v88, v89, v90\n"
    "v_max3_f32 v245, v91, v92, v93\n"
    "v_max3_f32 v244, v244, v94, v95\n"
    "v_max_f32 v244, v244, v245\n"
    "v_mov_b32 v245, v244\n"
    "s_nop 1\n"
    "v_permlane16_swap_b32 v244, v245\n"
    "v_max_f32 v244, v244, v245\n"
    "v_mov_b32 v245, v244\n"
    "s_nop 1\n"
    "v_permlane32_swap_b32 v244, v245\n"
    "v_max_f32 v244, v244, v245\n"
    "v_max_f32 v244, 0, v244\n"
    "v_ceil_f32 v244, v244\n"
    "v_sub_f32 v245, 0, v244\n"
    "v_exp_f32 v245, v245\n"
    "s_nop 0\n"
    "v_mul_f32 v20, v20, v245\n"
    "v_mul_f32 v21, v21, v245\n"
    "v_mul_f32 v22, v22, v245\n"
    "v_mul_f32 v23, v23, v245\n"
    "v_mul_f32 v28, v28, v245\n"
    "v_mul_f32 v29, v29, v245\n"
    "v_mul_f32 v30, v30, v245\n"
    "v_mul_f32 v31, v31, v245\n"
    "v_mul_f32 v36, v36, v245\n"
    "v_mul_f32 v37, v37, v245\n"
    "v_mul_f32 v38, v38, v245\n"
    "v_mul_f32 v39, v39, v245\n"
    "v_mul_f32 v44, v44, v245\n"
    "v_mul_f32 v45, v45, v245\n"
    "v_mul_f32 v46, v46, v245\n"
    "v_mul_f32 v47, v47, v245\n"
    "v_mul_f32 v52, v52, v245\n"
    "v_mul_f32 v53, v53, v245\n"
    "v_mul_f32 v54, v54, v245\n"
    "v_mul_f32 v55, v55, v245\n"
    "v_mul_f32 v60, v60, v245\n"
    "v_mul_f32 v61, v61, v245\n"
    "v_mul_f32 v62, v62, v245\n"
    "v_mul_f32 v63, v63, v245\n"
    "v_mul_f32 v68, v68, v245\n"
    "v_mul_f32 v69, v69, v245\n"
    "v_mul_f32 v70, v70, v245\n"
    "v_mul_f32 v71, v71, v245\n"
    "v_mul_f32 v76, v76, v245\n"
    "v_mul_f32 v77, v77, v245\n"
    "v_mul_f32 v78, v78, v245\n"
    "v_mul_f32 v79, v79, v245\n"
    "v_mul_f32 v229, v229, v245\n"
    "v_sub_f32 v88, v88, v244\n"
    "v_sub_f32 v89, v89, v244\n"
    "v_sub_f32 v90, v90, v244\n"
    "v_sub_f32 v91, v91, v244\n"
    "v_sub_f32 v92, v92, v244\n"
    "v_sub_f32 v93, v93, v244\n"
    "v_sub_f32 v94, v94, v244\n"
    "v_sub_f32 v95, v95, v244\n"
    "v_sub_f32 v104, v104, v244\n"
    "v_sub_f32 v105, v105, v244\n"
    "v_sub_f32 v106, v106, v244\n"
    "v_sub_f32 v107, v107, v244\n"
    "v_sub_f32 v108, v108, v244\n"
    "v_sub_f32 v109, v109, v244\n"
    "v_sub_f32 v110, v110, v244\n"
    "v_sub_f32 v111, v111, v244\n"
    "v_sub_f32 v116, v116, v244\n"
    "v_sub_f32 v117, v117, v244\n"
    "v_sub_f32 v118, v118, v244\n"
    "v_sub_f32 v119, v119, v244\n"
    "v_exp_f32 v12, v80\n"
    "v_exp_f32 v13, v81\n"
    "v_exp_f32 v14, v82\n"
    "v_add_f32 v230, v12, v13\n"
    "v_cvt_pk_bf16_f32 v160, v12, v13\n"
    "v_exp_f32 v15, v83\n"
    "v_add_f32 v230, v230, v14\n"
    "v_exp_f32 v12, v84\n"
    "v_add_f32 v230, v230, v15\n"
    "v_cvt_pk_bf16_f32 v161, v14, v15\n"
    "v_exp_f32 v13, v85\n"
    "v_add_f32 v230, v230, v12\n"
    "v_exp_f32 v14, v86\n"
    "v_add_f32 v230, v230, v13\n"
    "v_cvt_pk_bf16_f32 v162, v12, v13\n"
    "v_exp_f32 v15, v87\n"
    "v_add_f32 v230, v230, v14\n"
    "v_exp_f32 v12, v88\n"
    "v_add_f32 v230, v230, v15\n"
    "v_cvt_pk_bf16_f32 v163, v14, v15\n"
    "v_exp_f32 v13, v89\n"
    "v_exp_f32 v14, v90\n"
    "v_add_f32 v231, v12, v13\n"
    "v_cvt_pk_bf16_f32 v164, v12, v13\n"
    "v_exp_f32 v15, v91\n"
    "v_add_f32 v231, v231, v14\n"
    "v_exp_f32 v12, v92\n"
    "v_add_f32 v231, v231, v15\n"
    "v_cvt_pk_bf16_f32 v165, v14, v15\n"
    "v_exp_f32 v13, v93\n"
    "v_add_f32 v231, v231, v12\n"
    "v_exp_f32 v14, v94\n"
    "v_add_f32 v231, v231, v13\n"
    "v_cvt_pk_bf16_f32 v166, v12, v13\n"
    "v_exp_f32 v15, v95\n"
    "v_add_f32 v231, v231, v14\n"
    "v_add_f32 v231, v231, v15\n"
    "v_cvt_pk_bf16_f32 v167, v14, v15\n"
    "v_max_f32 v248, v230, v231\n"
    "v_cmp_nge_f32 vcc, s70, v248\n"
    "s_nop 1\n"
    "s_branch .Lkrret_38\n"
    ".Lkrare_39:\n"
    "s_nop 7\n"
    "v_max3_f32 v244, v96, v97, v98\n"
    "v_max3_f32 v245, v99, v100, v101\n"
    "v_max3_f32 v244, v244, v102, v103\n"
    "v_max_f32 v244, v244, v245\n"
    "v_mov_b32 v245, v244\n"
    "s_nop 1\n"
    "v_permlane16_swap_b32 v244, v245\n"
    "v_max_f32 v244, v244, v245\n"
    "v_mov_b32 v245, v244\n"
    "s_nop 1\n"
    "v_permlane32_swap_b32 v244, v245\n"
    "v_max_f32 v244, v244, v245\n"
    "v_max_f32 v244, 0, v244\n"
    "v_ceil_f32 v244, v244\n"
    "v_sub_f32 v245, 0, v244\n"
    "v_exp_f32 v245, v245\n"
    "s_nop 0\n"
    "v_mul_f32 v16, v16, v245\n"
    "v_mul_f32 v17, v17, v245\n"
    "v_mul_f32 v18, v18, v245\n"
    "v_mul_f32 v19, v19, v245\n"
    "v_mul_f32 v24, v24, v245\n"
    "v_mul_f32 v25, v25, v245\n"
    "v_mul_f32 v26, v26, v245\n"
    "v_mul_f32 v27, v27, v245\n"
    "v_mul_f32 v32, v32, v245\n"
    "v_mul_f32 v33, v33, v245\n"
    "v_mul_f32 v34, v34, v245\n"
    "v_mul_f32 v35, v35, v245\n"
    "v_mul_f32 v40, v40, v245\n"
    "v_mul_f32 v41, v41, v245\n"
    "v_mul_f32 v42, v42, v245\n"
    "v_mul_f32 v43, v43, v245\n"
    "v_mul_f32 v48, v48, v245\n"
    "v_mul_f32 v49, v49, v245\n"
    "v_mul_f32 v50, v50, v245\n"
    "v_mul_f32 v51, v51, v245\n"
    "v_mul_f32 v56, v56, v245\n"
    "v_mul_f32 v57, v57, v245\n"
    "v_mul_f32 v58, v58, v245\n"
    "v_mul_f32 v59, v59, v245\n"
    "v_mul_f32 v64, v64, v245\n"
    "v_mul_f32 v65, v65, v245\n"
    "v_mul_f32 v66, v66, v245\n"
    "v_mul_f32 v67, v67, v245\n"
    "v_mul_f32 v72, v72, v245\n"
    "v_mul_f32 v73, v73, v245\n"
    "v_mul_f32 v74, v74, v245\n"
    "v_mul_f32 v75, v75, v245\n"
    "v_mul_f32 v228, v228, v245\n"
    "v_sub_f32 v96, v96, v244\n"
    "v_sub_f32 v97, v97, v244\n"
    "v_sub_f32 v98, v98, v244\n"
    "v_sub_f32 v99, v99, v244\n"
    "v_sub_f32 v100, v100, v244\n"
    "v_sub_f32 v101, v101, v244\n"
    "v_sub_f32 v102, v102, v244\n"
    "v_sub_f32 v103, v103, v244\n"
    "v_sub_f32 v80, v80, v244\n"
    "v_sub_f32 v81, v81, v244\n"
    "v_sub_f32 v82, v82, v244\n"
    "v_sub_f32 v83, v83, v244\n"
    "v_sub_f32 v84, v84, v244\n"
    "v_sub_f32 v85, v85, v244\n"
    "v_sub_f32 v86, v86, v244\n"
    "v_sub_f32 v87, v87, v244\n"
    "v_sub_f32 v112, v112, v244\n"
    "v_sub_f32 v113, v113, v244\n"
    "v_sub_f32 v114, v114, v244\n"
    "v_sub_f32 v115, v115, v244\n"
    "v_max3_f32 v244, v104, v105, v106\n"
    "v_max3_f32 v245, v107, v108, v109\n"
    "v_max3_f32 v244, v244, v110, v111\n"
    "v_max_f32 v244, v244, v245\n"
    "v_mov_b32 v245, v244\n"
    "s_nop 1\n"
    "v_permlane16_swap_b32 v244, v245\n"
    "v_max_f32 v244, v244, v245\n"
    "v_mov_b32 v245, v244\n"
    "s_nop 1\n"
    "v_permlane32_swap_b32 v244, v245\n"
    "v_max_f32 v244, v244, v245\n"
    "v_max_f32 v244, 0, v244\n"
    "v_ceil_f32 v244, v244\n"
    "v_sub_f32 v245, 0, v244\n"
    "v_exp_f32 v245, v245\n"
    "s_nop 0\n"
    "v_mul_f32 v20, v20, v245\n"
    "v_mul_f32 v21, v21, v245\n"
    "v_mul_f32 v22, v22, v245\n"
    "v_mul_f32 v23, v23, v245\n"
    "v_mul_f32 v28, v28, v245\n"
    "v_mul_f32 v29, v29, v245\n"
    "v_mul_f32 v30, v30, v245\n"
    "v_mul_f32 v31, v31, v245\n"
    "v_mul_f32 v36, v36, v245\n"
    "v_mul_f32 v37, v37, v245\n"
    "v_mul_f32 v38, v38, v245\n"
    "v_mul_f32 v39, v39, v245\n"
    "v_mul_f32 v44, v44, v245\n"
    "v_mul_f32 v45, v45, v245\n"
    "v_mul_f32 v46, v46, v245\n"
    "v_mul_f32 v47, v47, v245\n"
    "v_mul_f32 v52, v52, v245\n"
    "v_mul_f32 v53, v53, v245\n"
    "v_mul_f32 v54, v54, v245\n"
    "v_mul_f32 v55, v55, v245\n"
    "v_mul_f32 v60, v60, v245\n"
    "v_mul_f32 v61, v61, v245\n"
    "v_mul_f32 v62, v62, v245\n"
    "v_mul_f32 v63, v63, v245\n"
    "v_mul_f32 v68, v68, v245\n"
    "v_mul_f32 v69, v69, v245\n"
    "v_mul_f32 v70, v70, v245\n"
    "v_mul_f32 v71, v71, v245\n"
    "v_mul_f32 v76, v76, v245\n"
    "v_mul_f32 v77, v77, v245\n"
    "v_mul_f32 v78, v78, v245\n"
    "v_mul_f32 v79, v79, v245\n"
    "v_mul_f32 v229, v229, v245\n"
    "v_sub_f32 v104, v104, v244\n"
    "v_sub_f32 v105, v105, v244\n"
    "v_sub_f32 v106, v106, v244\n"
    "v_sub_f32 v107, v107, v244\n"
    "v_sub_f32 v108, v108, v244\n"
    "v_sub_f32 v109, v109, v244\n"
    "v_sub_f32 v110, v110, v244\n"
    "v_sub_f32 v111, v111, v244\n"
    "v_sub_f32 v88, v88, v244\n"
    "v_sub_f32 v89, v89, v244\n"
    "v_sub_f32 v90, v90, v244\n"
    "v_sub_f32 v91, v91, v244\n"
    "v_sub_f32 v92, v92, v244\n"
    "v_sub_f32 v93, v93, v244\n"
    "v_sub_f32 v94, v94, v244\n"
    "v_sub_f32 v95, v95, v244\n"
    "v_sub_f32 v116, v116, v244\n"
    "v_sub_f32 v117, v117, v244\n"
    "v_sub_f32 v118, v118, v244\n"
    "v_sub_f32 v119, v119, v244\n"
    "v_exp_f32 v12, v96\n"
    "v_exp_f32 v13, v97\n"
    "v_exp_f32 v14, v98\n"
    "v_add_f32 v230, v12, v13\n"
    "v_cvt_pk_bf16_f32 v168, v12, v13\n"
    "v_exp_f32 v15, v99\n"
    "v_add_f32 v230, v230, v14\n"
    "v_exp_f32 v12, v100\n"
    "v_add_f32 v230, v230, v15\n"
    "v_cvt_pk_bf16_f32 v169, v14, v15\n"
    "v_exp_f32 v13, v101\n"
    "v_add_f32 v230, v230, v12\n"
    "v_exp_f32 v14, v102\n"
    "v_add_f32 v230, v230, v13\n"
    "v_cvt_pk_bf16_f32 v170, v12, v13\n"
    "v_exp_f32 v15, v103\n"
    "v_add_f32 v230, v230, v14\n"
    "v_exp_f32 v12, v104\n"
    "v_add_f32 v230, v230, v15\n"
    "v_cvt_pk_bf16_f32 v171, v14, v15\n"
    "v_exp_f32 v13, v105\n"
    "v_exp_f32 v14, v106\n"
    "v_add_f32 v231, v12, v13\n"
    "v_cvt_pk_bf16_f32 v172, v12, v13\n"
    "v_exp_f32 v15, v107\n"
    "v_add_f32 v231, v231, v14\n"
    "v_exp_f32 v12, v108\n"
    "v_add_f32 v231, v231, v15\n"
    "v_cvt_pk_bf16_f32 v173, v14, v15\n"
    "v_exp_f32 v13, v109\n"
    "v_add_f32 v231, v231, v12\n"
    "v_exp_f32 v14, v110\n"
    "v_add_f32 v231, v231, v13\n"
    "v_cvt_pk_bf16_f32 v174, v12, v13\n"
    "v_exp_f32 v15, v111\n"
    "v_add_f32 v231, v231, v14\n"
    "v_add_f32 v231, v231, v15\n"
    "v_cvt_pk_bf16_f32 v175, v14, v15\n"
    "v_max_f32 v248, v230, v231\n"
    "v_cmp_nge_f32 vcc, s70, v248\n"
    "s_nop 1\n"
    "s_branch .Lkrret_40\n"
    ".Lkrare_41:\n"
    "s_nop 7\n"
    "v_max3_f32 v244, v80, v81, v82\n"
    "v_max3_f32 v245, v83, v84, v85\n"
    "v_max3_f32 v244, v244, v86, v87\n"
    "v_max_f32 v244, v244, v245\n"
    "v_mov_b32 v245, v244\n"
    "s_nop 1\n"
    "v_permlane16_swap_b32 v244, v245\n"
    "v_max_f32 v244, v244, v245\n"
    "v_mov_b32 v245, v244\n"
    "s_nop 1\n"
    "v_permlane32_swap_b32 v244, v245\n"
    "v_max_f32 v244, v244, v245\n"
    "v_max_f32 v244, 0, v244\n"
    "v_ceil_f32 v244, v244\n"
    "v_sub_f32 v245, 0, v244\n"
    "v_exp_f32 v245, v245\n"
    "s_nop 0\n"
    "v_mul_f32 v16, v16, v245\n"
    "v_mul_f32 v17, v17, v245\n"
    "v_mul_f32 v18, v18, v245\n"
    "v_mul_f32 v19, v19, v245\n"
    "v_mul_f32 v24, v24, v245\n"
    "v_mul_f32 v25, v25, v245\n"
    "v_mul_f32 v26, v26, v245\n"
    "v_mul_f32 v27, v27, v245\n"
    "v_mul_f32 v32, v32, v245\n"
    "v_mul_f32 v33, v33, v245\n"
    "v_mul_f32 v34, v34, v245\n"
    "v_mul_f32 v35, v35, v245\n"
    "v_mul_f32 v40, v40, v245\n"
    "v_mul_f32 v41, v41, v245\n"
    "v_mul_f32 v42, v42, v245\n"
    "v_mul_f32 v43, v43, v245\n"
    "v_mul_f32 v48, v48, v245\n"
    "v_mul_f32 v49, v49, v245\n"
    "v_mul_f32 v50, v50, v245\n"
    "v_mul_f32 v51, v51, v245\n"
    "v_mul_f32 v56, v56, v245\n"
    "v_mul_f32 v57, v57, v245\n"
    "v_mul_f32 v58, v58, v245\n"
    "v_mul_f32 v59, v59, v245\n"
    "v_mul_f32 v64, v64, v245\n"
    "v_mul_f32 v65, v65, v245\n"
    "v_mul_f32 v66, v66, v245\n"
    "v_mul_f32 v67, v67, v245\n"
    "v_mul_f32 v72, v72, v245\n"
    "v_mul_f32 v73, v73, v245\n"
    "v_mul_f32 v74, v74, v245\n"
    "v_mul_f32 v75, v75, v245\n"
    "v_mul_f32 v228, v228, v245\n"
    "v_sub_f32 v80, v80, v244\n"
    "v_sub_f32 v81, v81, v244\n"
    "v_sub_f32 v82, v82, v244\n"
    "v_sub_f32 v83, v83, v244\n"
    "v_sub_f32 v84, v84, v244\n"
    "v_sub_f32 v85, v85, v244\n"
    "v_sub_f32 v86, v86, v244\n"
    "v_sub_f32 v87, v87, v244\n"
    "v_sub_f32 v96, v96, v244\n"
    "v_sub_f32 v97, v97, v244\n"
    "v_sub_f32 v98, v98, v244\n"
    "v_sub_f32 v99, v99, v244\n"
    "v_sub_f32 v100, v100, v244\n"
    "v_sub_f32 v101, v101, v244\n"
    "v_sub_f32 v102, v102, v244\n"
    "v_sub_f32 v103, v103, v244\n"
    "v_sub_f32 v112, v112, v244\n"
    "v_sub_f32 v113, v113, v244\n"
    "v_sub_f32 v114, v114, v244\n"
    "v_sub_f32 v115, v115, v244\n"
    "v_max3_f32 v244, v88, v89, v90\n"
    "v_max3_f32 v245, v91, v92, v93\n"
    "v_max3_f32 v244, v244, v94, v95\n"
    "v_max_f32 v244, v244, v245\n"
    "v_mov_b32 v245, v244\n"
    "s_nop 1\n"
    "v_permlane16_swap_b32 v244, v245\n"
    "v_max_f32 v244, v244, v245\n"
    "v_mov_b32 v245, v244\n"
    "s_nop 1\n"
    "v_permlane32_swap_b32 v244, v245\n"
    "v_max_f32 v244, v244, v245\n"
    "v_max_f32 v244, 0, v244\n"
    "v_ceil_f32 v244, v244\n"
    "v_sub_f32 v245, 0, v244\n"
    "v_exp_f32 v245, v245\n"
    "s_nop 0\n"
    "v_mul_f32 v20, v20, v245\n"
    "v_mul_f32 v21, v21, v245\n"
    "v_mul_f32 v22, v22, v245\n"
    "v_mul_f32 v23, v23, v245\n"
    "v_mul_f32 v28, v28, v245\n"
    "v_mul_f32 v29, v29, v245\n"
    "v_mul_f32 v30, v30, v245\n"
    "v_mul_f32 v31, v31, v245\n"
    "v_mul_f32 v36, v36, v245\n"
    "v_mul_f32 v37, v37, v245\n"
    "v_mul_f32 v38, v38, v245\n"
    "v_mul_f32 v39, v39, v245\n"
    "v_mul_f32 v44, v44, v245\n"
    "v_mul_f32 v45, v45, v245\n"
    "v_mul_f32 v46, v46, v245\n"
    "v_mul_f32 v47, v47, v245\n"
    "v_mul_f32 v52, v52, v245\n"
    "v_mul_f32 v53, v53, v245\n"
    "v_mul_f32 v54, v54, v245\n"
    "v_mul_f32 v55, v55, v245\n"
    "v_mul_f32 v60, v60, v245\n"
    "v_mul_f32 v61, v61, v245\n"
    "v_mul_f32 v62, v62, v245\n"
    "v_mul_f32 v63, v63, v245\n"
    "v_mul_f32 v68, v68, v245\n"
    "v_mul_f32 v69, v69, v245\n"
    "v_mul_f32 v70, v70, v245\n"
    "v_mul_f32 v71, v71, v245\n"
    "v_mul_f32 v76, v76, v245\n"
    "v_mul_f32 v77, v77, v245\n"
    "v_mul_f32 v78, v78, v245\n"
    "v_mul_f32 v79, v79, v245\n"
    "v_mul_f32 v229, v229, v245\n"
    "v_sub_f32 v88, v88, v244\n"
    "v_sub_f32 v89, v89, v244\n"
    "v_sub_f32 v90, v90, v244\n"
    "v_sub_f32 v91, v91, v244\n"
    "v_sub_f32 v92, v92, v244\n"
    "v_sub_f32 v93, v93, v244\n"
    "v_sub_f32 v94, v94, v244\n"
    "v_sub_f32 v95, v95, v244\n"
    "v_sub_f32 v104, v104, v244\n"
    "v_sub_f32 v105, v105, v244\n"
    "v_sub_f32 v106, v106, v244\n"
    "v_sub_f32 v107, v107, v244\n"
    "v_sub_f32 v108, v108, v244\n"
    "v_sub_f32 v109, v109, v244\n"
    "v_sub_f32 v110, v110, v244\n"
    "v_sub_f32 v111, v111, v244\n"
    "v_sub_f32 v116, v116, v244\n"
    "v_sub_f32 v117, v117, v244\n"
    "v_sub_f32 v118, v118, v244\n"
    "v_sub_f32 v119, v119, v244\n"
    "v_exp_f32 v12, v80\n"
    "v_exp_f32 v13, v81\n"
    "v_exp_f32 v14, v82\n"
    "v_add_f32 v230, v12, v13\n"
    "v_cvt_pk_bf16_f32 v160, v12, v13\n"
    "v_exp_f32 v15, v83\n"
    "v_add_f32 v230, v230, v14\n"
    "v_exp_f32 v12, v84\n"
    "v_add_f32 v230, v230, v15\n"
    "v_cvt_pk_bf16_f32 v161, v14, v15\n"
    "v_exp_f32 v13, v85\n"
    "v_add_f32 v230, v230, v12\n"
    "v_exp_f32 v14, v86\n"
    "v_add_f32 v230, v230, v13\n"
    "v_cvt_pk_bf16_f32 v162, v12, v13\n"
    "v_exp_f32 v15, v87\n"
    "v_add_f32 v230, v230, v14\n"
    "v_exp_f32 v12, v88\n"
    "v_add_f32 v230, v230, v15\n"
    "v_cvt_pk_bf16_f32 v163, v14, v15\n"
    "v_exp_f32 v13, v89\n"
    "v_exp_f32 v14, v90\n"
    "v_add_f32 v231, v12, v13\n"
    "v_cvt_pk_bf16_f32 v164, v12, v13\n"
    "v_exp_f32 v15, v91\n"
    "v_add_f32 v231, v231, v14\n"
    "v_exp_f32 v12, v92\n"
    "v_add_f32 v231, v231, v15\n"
    "v_cvt_pk_bf16_f32 v165, v14, v15\n"
    "v_exp_f32 v13, v93\n"
    "v_add_f32 v231, v231, v12\n"
    "v_exp_f32 v14, v94\n"
    "v_add_f32 v231, v231, v13\n"
    "v_cvt_pk_bf16_f32 v166, v12, v13\n"
    "v_exp_f32 v15, v95\n"
    "v_add_f32 v231, v231, v14\n"
    "v_add_f32 v231, v231, v15\n"
    "v_cvt_pk_bf16_f32 v167, v14, v15\n"
    "v_max_f32 v248, v230, v231\n"
    "v_cmp_nge_f32 vcc, s70, v248\n"
    "s_nop 1\n"
    "s_branch .Lkrret_42\n"
    ".Lkrare_43:\n"
    "s_nop 7\n"
    "v_max3_f32 v244, v96, v97, v98\n"
    "v_max3_f32 v245, v99, v100, v101\n"
    "v_max3_f32 v244, v244, v102, v103\n"
    "v_max_f32 v244, v244, v245\n"
    "v_mov_b32 v245, v244\n"
    "s_nop 1\n"
    "v_permlane16_swap_b32 v244, v245\n"
    "v_max_f32 v244, v244, v245\n"
    "v_mov_b32 v245, v244\n"
    "s_nop 1\n"
    "v_permlane32_swap_b32 v244, v245\n"
    "v_max_f32 v244, v244, v245\n"
    "v_max_f32 v244, 0, v244\n"
    "v_ceil_f32 v244, v244\n"
    "v_sub_f32 v245, 0, v244\n"
    "v_exp_f32 v245, v245\n"
    "s_nop 0\n"
    "v_mul_f32 v16, v16, v245\n"
    "v_mul_f32 v17, v17, v245\n"
    "v_mul_f32 v18, v18, v245\n"
    "v_mul_f32 v19, v19, v245\n"
    "v_mul_f32 v24, v24, v245\n"
    "v_mul_f32 v25, v25, v245\n"
    "v_mul_f32 v26, v26, v245\n"
    "v_mul_f32 v27, v27, v245\n"
    "v_mul_f32 v32, v32, v245\n"
    "v_mul_f32 v33, v33, v245\n"
    "v_mul_f32 v34, v34, v245\n"
    "v_mul_f32 v35, v35, v245\n"
    "v_mul_f32 v40, v40, v245\n"
    "v_mul_f32 v41, v41, v245\n"
    "v_mul_f32 v42, v42, v245\n"
    "v_mul_f32 v43, v43, v245\n"
    "v_mul_f32 v48, v48, v245\n"
    "v_mul_f32 v49, v49, v245\n"
    "v_mul_f32 v50, v50, v245\n"
    "v_mul_f32 v51, v51, v245\n"
    "v_mul_f32 v56, v56, v245\n"
    "v_mul_f32 v57, v57, v245\n"
    "v_mul_f32 v58, v58, v245\n"
    "v_mul_f32 v59, v59, v245\n"
    "v_mul_f32 v64, v64, v245\n"
    "v_mul_f32 v65, v65, v245\n"
    "v_mul_f32 v66, v66, v245\n"
    "v_mul_f32 v67, v67, v245\n"
    "v_mul_f32 v72, v72, v245\n"
    "v_mul_f32 v73, v73, v245\n"
    "v_mul_f32 v74, v74, v245\n"
    "v_mul_f32 v75, v75, v245\n"
    "v_mul_f32 v228, v228, v245\n"
    "v_sub_f32 v96, v96, v244\n"
    "v_sub_f32 v97, v97, v244\n"
    "v_sub_f32 v98, v98, v244\n"
    "v_sub_f32 v99, v99, v244\n"
    "v_sub_f32 v100, v100, v244\n"
    "v_sub_f32 v101, v101, v244\n"
    "v_sub_f32 v102, v102, v244\n"
    "v_sub_f32 v103, v103, v244\n"
    "v_max3_f32 v244, v104, v105, v106\n"
    "v_max3_f32 v245, v107, v108, v109\n"
    "v_max3_f32 v244, v244, v110, v111\n"
    "v_max_f32 v244, v244, v245\n"
    "v_mov_b32 v245, v244\n"
    "s_nop 1\n"
    "v_permlane16_swap_b32 v244, v245\n"
    "v_max_f32 v244, v244, v245\n"
    "v_mov_b32 v245, v244\n"
    "s_nop 1\n"
    "v_permlane32_swap_b32 v244, v245\n"
    "v_max_f32 v244, v244, v245\n"
    "v_max_f32 v244, 0, v244\n"
    "v_ceil_f32 v244, v244\n"
    "v_sub_f32 v245, 0, v244\n"
    "v_exp_f32 v245, v245\n"
    "s_nop 0\n"
    "v_mul_f32 v20, v20, v245\n"
    "v_mul_f32 v21, v21, v245\n"
    "v_mul_f32 v22, v22, v245\n"
    "v_mul_f32 v23, v23, v245\n"
    "v_mul_f32 v28, v28, v245\n"
    "v_mul_f32 v29, v29, v245\n"
    "v_mul_f32 v30, v30, v245\n"
    "v_mul_f32 v31, v31, v245\n"
    "v_mul_f32 v36, v36, v245\n"
    "v_mul_f32 v37, v37, v245\n"
    "v_mul_f32 v38, v38, v245\n"
    "v_mul_f32 v39, v39, v245\n"
    "v_mul_f32 v44, v44, v245\n"
    "v_mul_f32 v45, v45, v245\n"
    "v_mul_f32 v46, v46, v245\n"
    "v_mul_f32 v47, v47, v245\n"
    "v_mul_f32 v52, v52, v245\n"
    "v_mul_f32 v53, v53, v245\n"
    "v_mul_f32 v54, v54, v245\n"
    "v_mul_f32 v55, v55, v245\n"
    "v_mul_f32 v60, v60, v245\n"
    "v_mul_f32 v61, v61, v245\n"
    "v_mul_f32 v62, v62, v245\n"
    "v_mul_f32 v63, v63, v245\n"
    "v_mul_f32 v68, v68, v245\n"
    "v_mul_f32 v69, v69, v245\n"
    "v_mul_f32 v70, v70, v245\n"
    "v_mul_f32 v71, v71, v245\n"
    "v_mul_f32 v76, v76, v245\n"
    "v_mul_f32 v77, v77, v245\n"
    "v_mul_f32 v78, v78, v245\n"
    "v_mul_f32 v79, v79, v245\n"
    "v_mul_f32 v229, v229, v245\n"
    "v_sub_f32 v104, v104, v244\n"
    "v_sub_f32 v105, v105, v244\n"
    "v_sub_f32 v106, v106, v244\n"
    "v_sub_f32 v107, v107, v244\n"
    "v_sub_f32 v108, v108, v244\n"
    "v_sub_f32 v109, v109, v244\n"
    "v_sub_f32 v110, v110, v244\n"
    "v_sub_f32 v111, v111, v244\n"
    "v_exp_f32 v12, v96\n"
    "v_exp_f32 v13, v97\n"
    "v_exp_f32 v14, v98\n"
    "v_add_f32 v230, v12, v13\n"
    "v_cvt_pk_bf16_f32 v168, v12, v13\n"
    "v_exp_f32 v15, v99\n"
    "v_add_f32 v230, v230, v14\n"
    "v_exp_f32 v12, v100\n"
    "v_add_f32 v230, v230, v15\n"
    "v_cvt_pk_bf16_f32 v169, v14, v15\n"
    "v_exp_f32 v13, v101\n"
    "v_add_f32 v230, v230, v12\n"
    "v_exp_f32 v14, v102\n"
    "v_add_f32 v230, v230, v13\n"
    "v_cvt_pk_bf16_f32 v170, v12, v13\n"
    "v_exp_f32 v15, v103\n"
    "v_add_f32 v230, v230, v14\n"
    "v_exp_f32 v12, v104\n"
    "v_add_f32 v230, v230, v15\n"
    "v_cvt_pk_bf16_f32 v171, v14, v15\n"
    "v_exp_f32 v13, v105\n"
    "v_exp_f32 v14, v106\n"
    "v_add_f32 v231, v12, v13\n"
    "v_cvt_pk_bf16_f32 v172, v12, v13\n"
    "v_exp_f32 v15, v107\n"
    "v_add_f32 v231, v231, v14\n"
    "v_exp_f32 v12, v108\n"
    "v_add_f32 v231, v231, v15\n"
    "v_cvt_pk_bf16_f32 v173, v14, v15\n"
    "v_exp_f32 v13, v109\n"
    "v_add_f32 v231, v231, v12\n"
    "v_exp_f32 v14, v110\n"
    "v_add_f32 v231, v231, v13\n"
    "v_cvt_pk_bf16_f32 v174, v12, v13\n"
    "v_exp_f32 v15, v111\n"
    "v_add_f32 v231, v231, v14\n"
    "v_add_f32 v231, v231, v15\n"
    "v_cvt_pk_bf16_f32 v175, v14, v15\n"
    "v_max_f32 v248, v230, v231\n"
    "v_cmp_nge_f32 vcc, s70, v248\n"
    "s_nop 1\n"
    "s_branch .Lkrret_44\n"
    ".Lkleft_163:\n"
    "s_mov_b32 s83, 0\n"
    ".Lkspin_176:\n"
    "s_mov_b32 s63, 0x1000\n"
    "buffer_load_dwordx2 v[250:251], v13, s[44:47], s63 offen sc1\n"
    "s_waitcnt vmcnt(0)\n"
    "v_cmp_ne_u64 vcc, s[68:69], v[250:251]\n"
    "s_nop 0\n"
    "s_cmp_eq_u64 vcc, 0\n"
    "s_cselect_b32 s78, 1, 0\n"
    "s_cmp_eq_u32 s78, 1\n"
    "s_cbranch_scc1 .Lkok_177\n"
    "s_sleep 1\n"
    "s_add_u32 s83, s83, 1\n"
    "s_cmp_lt_u32 s83, 0x9c40\n"
    "s_cbranch_scc1 .Lkspin_176\n"
    ".Lkok_177:\n"
    ".Lkleft2_178:\n"
    "s_add_u32 s60, s98, s79\n"
    "s_add_u32 s60, s60, 0\n"
    "s_and_b32 s61, s60, 12\n"
    "s_and_b32 s60, s60, 3\n"
    "s_lshl1_add_u32 s60, s61, s60\n"
    "s_mul_i32 s61, s60, 0xa000\n"
    "s_lshl_b32 s60, s60, 10\n"
    "s_add_u32 s96, s60, s81\n"
    "s_add_u32 s97, s61, s75\n"
    "s_cmp_lt_u32 s79, 16\n"
    "s_cselect_b32 s96, s96, 0x7ff00000\n"
    "s_cselect_b32 s97, s97, 0x7ff00000\n"
    "buffer_load_dwordx4 v[192:195], v11, s[36:39], s96 offen sc1\n"
    "buffer_load_dwordx4 v[196:199], v6, s[36:39], s96 offen sc1\n"
    "buffer_load_dwordx4 v[200:203], v12, s[36:39], s96 offen sc1\n"
    "buffer_load_dwordx4 v[204:207], v11, s[36:39], s96 offen offset:1024 sc1\n"
    "buffer_load_dwordx4 v[208:211], v6, s[36:39], s96 offen offset:1024 sc1\n"
    "buffer_load_dwordx4 v[212:215], v12, s[36:39], s96 offen offset:1024 sc1\n"
    "s_add_u32 s60, s98, s79\n"
    "s_add_u32 s60, s60, 2\n"
    "s_and_b32 s61, s60, 12\n"
    "s_and_b32 s60, s60, 3\n"
    "s_lshl1_add_u32 s60, s61, s60\n"
    "s_mul_i32 s61, s60, 0xa000\n"
    "s_lshl_b32 s60, s60, 10\n"
    "s_add_u32 s96, s60, s81\n"
    "s_add_u32 s97, s61, s75\n"
    "s_cmp_lt_u32 s79, 14\n"
    "s_cselect_b32 s96, s96, 0x7ff00000\n"
    "s_cselect_b32 s97, s97, 0x7ff00000\n"
    "s_xor_b32 s63, s74, 0x3c000\n"
    "s_add_u32 m0, s73, s63\n"
    "s_add_u32 s61, s97, 0\n"
    "buffer_load_dwordx4 v4, s[32:35], s61 offen lds\n"
    "buffer_load_dwordx4 v5, s[32:35], s61 offen offset:1024 lds\n"
    "s_add_u32 s63, s63, 0x2000\n"
    "s_add_u32 m0, s73, s63\n"
    "s_add_u32 s61, s97, 0xa000\n"
    "buffer_load_dwordx4 v4, s[32:35], s61 offen lds\n"
    "buffer_load_dwordx4 v5, s[32:35], s61 offen offset:1024 lds\n"
    "v_add_u32 v234, s74, v238\n"
    "v_add_u32 v235, s74, v239\n"
    "s_waitcnt vmcnt(4)\n"
    "ds_read_b64_tr_b16 v[176:177], v234\n"
    "ds_read_b64_tr_b16 v[178:179], v234 offset:4096\n"
    "ds_read_b64_tr_b16 v[180:181], v235\n"
    "ds_read_b64_tr_b16 v[182:183], v235 offset:4096\n"
    "ds_read_b64_tr_b16 v[184:185], v234 offset:512\n"
    "ds_read_b64_tr_b16 v[186:187], v234 offset:4608\n"
    "s_waitcnt lgkmcnt(2)\n"
    "v_mfma_f32_16x16x32_bf16 v[16:19], v[176:179], v[192:195], v[16:19]\n"
    "ds_read_b64_tr_b16 v[188:189], v235 offset:512\n"
    "ds_read_b64_tr_b16 v[190:191], v235 offset:4608\n"
    "v_mfma_f32_16x16x32_bf16 v[48:51], v[176:179], v[196:199], v[48:51]\n"
    "v_mfma_f32_16x16x32_bf16 v[80:83], v[176:179], v[200:203], v[80:83]\n"
    "v_mfma_f32_16x16x32_bf16 v[20:23], v[180:183], v[192:195], v[20:23]\n"
    "ds_read_b64_tr_b16 v[176:177], v234 offset:1024\n"
    "ds_read_b64_tr_b16 v[178:179], v234 offset:5120\n"
    "v_mfma_f32_16x16x32_bf16 v[52:55], v[180:183], v[196:199], v[52:55]\n"
    "v_mfma_f32_16x16x32_bf16 v[84:87], v[180:183], v[200:203], v[84:87]\n"
    "s_waitcnt lgkmcnt(2)\n"
    "v_mfma_f32_16x16x32_bf16 v[24:27], v[184:187], v[192:195], v[24:27]\n"
    "ds_read_b64_tr_b16 v[180:181], v235 offset:1024\n"
    "ds_read_b64_tr_b16 v[182:183], v235 offset:5120\n"
    "v_mfma_f32_16x16x32_bf16 v[56:59], v[184:187], v[196:199], v[56:59]\n"
    "v_mfma_f32_16x16x32_bf16 v[88:91], v[184:187], v[200:203], v[88:91]\n"
    "v_mfma_f32_16x16x32_bf16 v[28:31], v[188:191], v[192:195], v[28:31]\n"
    "ds_read_b64_tr_b16 v[184:185], v234 offset:1536\n"
    "ds_read_b64_tr_b16 v[186:187], v234 offset:5632\n"
    "v_mfma_f32_16x16x32_bf16 v[60:63], v[188:191], v[196:199], v[60:63]\n"
    "v_mfma_f32_16x16x32_bf16 v[92:95], v[188:191], v[200:203], v[92:95]\n"
    "s_waitcnt lgkmcnt(2)\n"
    "v_mfma_f32_16x16x32_bf16 v[32:35], v[176:179], v[192:195], v[32:35]\n"
    "ds_read_b64_tr_b16 v[188:189], v235 offset:1536\n"
    "ds_read_b64_tr_b16 v[190:191], v235 offset:5632\n"
    "v_mfma_f32_16x16x32_bf16 v[64:67], v[176:179], v[196:199], v[64:67]\n"
    "v_mfma_f32_16x16x32_bf16 v[96:99], v[176:179], v[200:203], v[96:99]\n"
    "v_mfma_f32_16x16x32_bf16 v[36:39], v[180:183], v[192:195], v[36:39]\n"
    "ds_read_b64_tr_b16 v[176:177], v234 offset:8192\n"
    "ds_read_b64_tr_b16 v[178:179], v234 offset:12288\n"
    "v_mfma_f32_16x16x32_bf16 v[68:71], v[180:183], v[196:199], v[68:71]\n"
    "v_mfma_f32_16x16x32_bf16 v[100:103], v[180:183], v[200:203], v[100:103]\n"
    "s_waitcnt lgkmcnt(2)\n"
    "v_mfma_f32_16x16x32_bf16 v[40:43], v[184:187], v[192:195], v[40:43]\n"
    "ds_read_b64_tr_b16 v[180:181], v235 offset:8192\n"
    "ds_read_b64_tr_b16 v[182:183], v235 offset:12288\n"
    "v_mfma_f32_16x16x32_bf16 v[72:75], v[184:187], v[196:199], v[72:75]\n"
    "v_mfma_f32_16x16x32_bf16 v[104:107], v[184:187], v[200:203], v[104:107]\n"
    "v_mfma_f32_16x16x32_bf16 v[44:47], v[188:191], v[192:195], v[44:47]\n"
    "ds_read_b64_tr_b16 v[184:185], v234 offset:8704\n"
    "ds_read_b64_tr_b16 v[186:187], v234 offset:12800\n"
    "v_mfma_f32_16x16x32_bf16 v[76:79], v[188:191], v[196:199], v[76:79]\n"
    "v_mfma_f32_16x16x32_bf16 v[108:111], v[188:191], v[200:203], v[108:111]\n"
    "s_waitcnt lgkmcnt(2)\n"
    "v_mfma_f32_16x16x32_bf16 v[16:19], v[176:179], v[204:207], v[16:19]\n"
    "ds_read_b64_tr_b16 v[188:189], v235 offset:8704\n"
    "ds_read_b64_tr_b16 v[190:191], v235 offset:12800\n"
    "v_mfma_f32_16x16x32_bf16 v[48:51], v[176:179], v[208:211], v[48:51]\n"
    "v_mfma_f32_16x16x32_bf16 v[80:83], v[176:179], v[212:215], v[80:83]\n"
    "v_mfma_f32_16x16x32_bf16 v[20:23], v[180:183], v[204:207], v[20:23]\n"
    "ds_read_b64_tr_b16 v[176:177], v234 offset:9216\n"
    "ds_read_b64_tr_b16 v[178:179], v234 offset:13312\n"
    "v_mfma_f32_16x16x32_bf16 v[52:55], v[180:183], v[208:211], v[52:55]\n"
    "v_mfma_f32_16x16x32_bf16 v[84:87], v[180:183], v[212:215], v[84:87]\n"
    "s_waitcnt lgkmcnt(2)\n"
    "v_mfma_f32_16x16x32_bf16 v[24:27], v[184:187], v[204:207], v[24:27]\n"
    "ds_read_b64_tr_b16 v[180:181], v235 offset:9216\n"
    "ds_read_b64_tr_b16 v[182:183], v235 offset:13312\n"
    "v_mfma_f32_16x16x32_bf16 v[56:59], v[184:187], v[208:211], v[56:59]\n"
    "v_mfma_f32_16x16x32_bf16 v[88:91], v[184:187], v[212:215], v[88:91]\n"
    "v_mfma_f32_16x16x32_bf16 v[28:31], v[188:191], v[204:207], v[28:31]\n"
    "ds_read_b64_tr_b16 v[184:185], v234 offset:9728\n"
    "ds_read_b64_tr_b16 v[186:187], v234 offset:13824\n"
    "v_mfma_f32_16x16x32_bf16 v[60:63], v[188:191], v[208:211], v[60:63]\n"
    "v_mfma_f32_16x16x32_bf16 v[92:95], v[188:191], v[212:215], v[92:95]\n"
    "s_waitcnt lgkmcnt(2)\n"
    "v_mfma_f32_16x16x32_bf16 v[32:35], v[176:179], v[204:207], v[32:35]\n"
    "ds_read_b64_tr_b16 v[188:189], v235 offset:9728\n"
    "ds_read_b64_tr_b16 v[190:191], v235 offset:13824\n"
    "v_mfma_f32_16x16x32_bf16 v[64:67], v[176:179], v[208:211], v[64:67]\n"
    "v_mfma_f32_16x16x32_bf16 v[96:99], v[176:179], v[212:215], v[96:99]\n"
    "v_mfma_f32_16x16x32_bf16 v[36:39], v[180:183], v[204:207], v[36:39]\n"
    "v_mfma_f32_16x16x32_bf16 v[68:71], v[180:183], v[208:211], v[68:71]\n"
    "v_mfma_f32_16x16x32_bf16 v[100:103], v[180:183], v[212:215], v[100:103]\n"
    "s_waitcnt lgkmcnt(0)\n"
    "v_mfma_f32_16x16x32_bf16 v[40:43], v[184:187], v[204:207], v[40:43]\n"
    "v_mfma_f32_16x16x32_bf16 v[72:75], v[184:187], v[208:211], v[72:75]\n"
    "v_mfma_f32_16x16x32_bf16 v[104:107], v[184:187], v[212:215], v[104:107]\n"
    "v_mfma_f32_16x16x32_bf16 v[44:47], v[188:191], v[204:207], v[44:47]\n"
    "v_mfma_f32_16x16x32_bf16 v[76:79], v[188:191], v[208:211], v[76:79]\n"
    "v_mfma_f32_16x16x32_bf16 v[108:111], v[188:191], v[212:215], v[108:111]\n"
    "s_add_u32 s79, s79, 2\n"
    "s_xor_b32 s74, s74, 0x3c000\n"
    "s_waitcnt vmcnt(0) lgkmcnt(0)\n"
    "s_barrier\n"
    "s_cmp_lt_u32 s79, 16\n"
    "s_cbranch_scc1 .Lkleft2_178\n"
    "s_add_u32 m0, s73, 0x1e800\n"
    "s_add_u32 s61, s28, 0x1e000\n"
    "buffer_load_dwordx4 v4, s[32:35], s61 offen lds\n"
    "buffer_load_dwordx4 v5, s[32:35], s61 offen offset:1024 lds\n"
    "s_add_u32 m0, s73, 0x20800\n"
    "s_add_u32 s61, s29, 0\n"
    "buffer_load_dwordx4 v4, s[32:35], s61 offen lds\n"
    "buffer_load_dwordx4 v5, s[32:35], s61 offen offset:1024 lds\n"
    "s_add_u32 m0, s73, 0x22800\n"
    "s_add_u32 s61, s29, 0xa000\n"
    "buffer_load_dwordx4 v4, s[32:35], s61 offen lds\n"
    "buffer_load_dwordx4 v5, s[32:35], s61 offen offset:1024 lds\n"
    "s_add_u32 m0, s73, 0x24800\n"
    "s_add_u32 s61, s29, 0x14000\n"
    "buffer_load_dwordx4 v4, s[32:35], s61 offen lds\n"
    "buffer_load_dwordx4 v5, s[32:35], s61 offen offset:1024 lds\n"
    "s_waitcnt vmcnt(0)\n"
    "s_barrier\n"
    "s_branch .Lkleftr_164\n"
    : "+v"(tid)
    : "s"(__builtin_amdgcn_kernarg_segment_ptr()), "s"(blockIdx.x)
    : "memory", "vcc", "scc",
      "s8", "s9", "s10", "s11", "s12", "s13", "s14", "s15", "s16", "s17", "s18", "s19", "s20", "s21", "s22", "s23", "s24", "s25", "s26", "s27", "s28", "s29", "s30", "s31", "s33", "s34", "s35", "s36", "s37", "s38", "s39", "s40", "s41", "s42", "s43", "s44", "s45", "s46", "s47", "s48", "s49", "s50", "s51", "s52", "s53", "s54", "s55", "s56", "s57", "s58", "s59", "s60", "s61", "s62", "s63", "s64", "s65", "s66", "s67", "s68", "s69", "s70", "s71", "s72", "s73", "s74", "s75", "s76", "s77", "s78", "s79", "s80", "s81", "s82", "s83", "s84", "s85", "s86", "s87", "s88", "s89", "s90", "s91", "s92", "s93", "s94", "s95", "s96", "s97", "s98", "s99",
      "v1", "v2", "v3", "v4", "v5", "v6", "v7", "v8", "v9", "v10", "v11", "v12", "v13", "v14", "v15", "v16", "v17", "v18", "v19", "v20", "v21", "v22", "v23", "v24", "v25", "v26", "v27", "v28", "v29", "v30", "v31", "v32", "v33", "v34", "v35", "v36", "v37", "v38", "v39", "v40", "v41", "v42", "v43", "v44", "v45", "v46", "v47", "v48", "v49", "v50", "v51", "v52", "v53", "v54", "v55", "v56", "v57", "v58", "v59", "v60", "v61", "v62", "v63", "v64", "v65", "v66", "v67", "v68", "v69", "v70", "v71", "v72", "v73", "v74", "v75", "v76", "v77", "v78", "v79", "v80", "v81", "v82", "v83", "v84", "v85", "v86", "v87", "v88", "v89", "v90", "v91", "v92", "v93", "v94", "v95", "v96", "v97", "v98", "v99", "v100", "v101", "v102", "v103", "v104", "v105", "v106", "v107", "v108", "v109", "v110", "v111", "v112", "v113", "v114", "v115", "v116", "v117", "v118", "v119", "v120", "v121", "v122", "v123", "v124", "v125", "v126", "v127", "v128", "v129", "v130", "v131", "v132", "v133", "v134", "v135", "v136", "v137", "v138", "v139", "v140", "v141", "v142", "v143", "v144", "v145", "v146", "v147", "v148", "v149", "v150", "v151", "v152", "v153", "v154", "v155", "v156", "v157", "v158", "v159", "v160", "v161", "v162", "v163", "v164", "v165", "v166", "v167", "v168", "v169", "v170", "v171", "v172", "v173", "v174", "v175", "v176", "v177", "v178", "v179", "v180", "v181", "v182", "v183", "v184", "v185", "v186", "v187", "v188", "v189", "v190", "v191", "v192", "v193", "v194", "v195", "v196", "v197", "v198", "v199", "v200", "v201", "v202", "v203", "v204", "v205", "v206", "v207", "v208", "v209", "v210", "v211", "v212", "v213", "v214", "v215", "v216", "v217", "v218", "v219", "v220", "v221", "v222", "v223", "v224", "v225", "v226", "v227", "v228", "v229", "v230", "v231", "v232", "v233", "v234", "v235", "v236", "v237", "v238", "v239", "v240", "v241", "v242", "v243", "v244", "v245", "v246", "v247", "v248", "v249", "v250", "v251", "v252", "v253", "v254", "v255");
}

extern "C" void kernel_launch(void* const* d_in, const int* in_sizes, int n_in, void* d_out, int out_size, void* d_ws, size_t ws_size,
                              hipStream_t stream) {
  Args a;
  for (int i = 0; i < 10; ++i) a.p[i] = d_in[i];
  a.p[10] = d_out;
  a.p[11] = d_ws;
  (void)hipFuncSetAttribute((const void*)fused_attn_block, hipFuncAttributeMaxDynamicSharedMemorySize, 163840);
  fused_attn_block<<<256, 512, 163840, stream>>>(a);
}
```
